```python
import torch

def _softmax(x, axis=-1):
    return torch.softmax(x, dim=axis)

def _gelu(x):
    return torch.nn.functional.gelu(x, approximate='tanh')

def _silu(x):
    return torch.nn.functional.silu(x)

def _sigmoid(x):
    return torch.sigmoid(x)

def _layernorm(x, axis=-1, eps=1e-05):
    m = x.mean(dim=axis, keepdim=True)
    v = x.var(dim=axis, keepdim=True, unbiased=False)
    return (x - m) / torch.sqrt(v + eps)

def _scatter_add(tgt, idx, src, axis=0):
    return tgt.clone().index_add_(axis, idx, src)

def _segment_sum(src, seg, n):
    out = torch.zeros((n,) + src.shape[1:], dtype=src.dtype)
    return out.index_add_(0, seg, src)

def _pad(x, pw):
    flat = tuple((v for pair in reversed(pw) for v in pair))
    return torch.nn.functional.pad(x, flat)
kernel_name = 'standalone__kv_cache_quant_pre_rope_vs_post_rope__decode'

def setup_inputs():

    _w = lambda a: (lambda t: t.to(torch.bfloat16) if t.is_floating_point() else t)(torch.as_tensor(a))
    B, Sq, Skv = (1, 1, 16384)
    H, Dh0 = (32, 192)
    G = -1 if -1 > 0 else Dh0
    G = max(2, G // 2 * 2)
    Dh = max(G, Dh0 // G * G)
    Skv = max(Skv, Sq)
    ng = Dh // G
    bits = 3 if 3 > 1 else 4
    bits = max(2, min(8, bits))
    qmax = (1 << bits) - 1
    half = Dh // 2
    inv = 1.0 / 10000.0 ** (torch.arange(half) / max(1, half))
    ang_k = torch.arange(Skv)[:, None] * inv[None, :]
    ang_q = torch.arange(Skv - Sq, Skv)[:, None] * inv[None, :]
    kc = torch.cat([torch.cos(ang_k), torch.cos(ang_k)], dim=-1)
    ks = torch.cat([torch.sin(ang_k), torch.sin(ang_k)], dim=-1)
    qc = torch.cat([torch.cos(ang_q), torch.cos(ang_q)], dim=-1)
    qs = torch.cat([torch.sin(ang_q), torch.sin(ang_q)], dim=-1)
    qk_i = torch.randint(0, qmax + 1, (B, H, Skv, Dh)).to(torch.int32)
    qv_i = torch.randint(0, qmax + 1, (B, H, Skv, Dh)).to(torch.int32)
    ksc = torch.abs((lambda *s: torch.randn(*s, dtype=torch.bfloat16) * 0.4)(B, H, Skv, ng)) * 0.1 + 0.01
    kzp = torch.full((B, H, Skv, ng), qmax / 2.0)
    vsc = torch.abs((lambda *s: torch.randn(*s, dtype=torch.bfloat16) * 0.4)(B, H, Skv, ng)) * 0.1 + 0.01
    vzp = torch.full((B, H, Skv, ng), qmax / 2.0)
    return {'q': _w((lambda *s: torch.randn(*s, dtype=torch.bfloat16) * 6.4)(B, H, Sq, Dh) * 0.5), 'qk': _w(qk_i), 'k_scales': _w(ksc), 'k_zeros': _w(kzp), 'qv': _w(qv_i), 'v_scales': _w(vsc), 'v_zeros': _w(vzp), 'q_cos': _w(qc), 'q_sin': _w(qs), 'k_cos': _w(kc), 'k_sin': _w(ks)}

def reference(q, qk, k_scales, k_zeros, qv, v_scales, v_zeros, q_cos, q_sin, k_cos, k_sin):
    B, H, Sq, Dh = q.shape
    ng = k_scales.shape[-1]
    G = Dh // ng
    half = Dh // 2
    ks = (lambda x, n, axis: torch.repeat_interleave(x, n, dim=axis))(k_scales, G, axis=-1)
    kz = (lambda x, n, axis: torch.repeat_interleave(x, n, dim=axis))(k_zeros, G, axis=-1)
    k = ((lambda x, dt: x.to(dt))(qk, q.dtype) - kz) * ks
    vs = (lambda x, n, axis: torch.repeat_interleave(x, n, dim=axis))(v_scales, G, axis=-1)
    vz = (lambda x, n, axis: torch.repeat_interleave(x, n, dim=axis))(v_zeros, G, axis=-1)
    v = ((lambda x, dt: x.to(dt))(qv, q.dtype) - vz) * vs
    k1 = k[..., :half].clone()
    k2 = k[..., half:].clone()
    krh = torch.concatenate([-k2, k1], axis=-1)
    kr = k * k_cos[None, None, :, :] + krh * k_sin[None, None, :, :]
    q1 = q[..., :half].clone()
    q2 = q[..., half:].clone()
    qrh = torch.concatenate([-q2, q1], axis=-1)
    qr = q * q_cos[None, None, :, :] + qrh * q_sin[None, None, :, :]
    logits = torch.einsum('bhqd,bhkd->bhqk', qr, kr) * float(Dh) ** (-0.5)
    attn = _softmax(logits, axis=-1)
    out = torch.einsum('bhqk,bhkd->bhqd', attn, v)
    return out
```

```cpp
#include <hip/hip_runtime.h>
#include <hip/hip_bf16.h>
#include <cstdint>
#include <cstdio>

using bf16 = __hip_bfloat16;
using i32x4 = __attribute__((ext_vector_type(4))) int;
using u32x2 = __attribute__((ext_vector_type(2))) unsigned;
using f32x2 = __attribute__((ext_vector_type(2))) float;

constexpr int DH = 192;
constexpr int HALF = 96;
constexpr int NSPL = 8;

__device__ __forceinline__ float bflo(unsigned u) { return __uint_as_float(u << 16); }
__device__ __forceinline__ float bfhi(unsigned u) { return __uint_as_float(u & 0xffff0000u); }
__device__ __forceinline__ float rbf(float x) { return __bfloat162float(__float2bfloat16(x)); }

template <int CTRL> __device__ __forceinline__ float dpp(float x) {
    return __builtin_bit_cast(float, __builtin_amdgcn_mov_dpp(__builtin_bit_cast(int, x), CTRL, 0xf, 0xf, true));
}
__device__ __forceinline__ float row16_allsum(float x) {
    x += dpp<0x128>(x);
    x += dpp<0x124>(x);
    x += dpp<0x122>(x);
    x += dpp<0x121>(x);
    return x;
}
__device__ __forceinline__ float xrow_max(float x) {
    auto s = __builtin_amdgcn_permlane16_swap(__float_as_uint(x), __float_as_uint(x), false, false);
    x = fmaxf(__uint_as_float(s[0]), __uint_as_float(s[1]));
    auto t = __builtin_amdgcn_permlane32_swap(__float_as_uint(x), __float_as_uint(x), false, false);
    return fmaxf(__uint_as_float(t[0]), __uint_as_float(t[1]));
}
__device__ __forceinline__ float xrow_sum(float x) {
    auto s = __builtin_amdgcn_permlane16_swap(__float_as_uint(x), __float_as_uint(x), false, false);
    x = __uint_as_float(s[0]) + __uint_as_float(s[1]);
    auto t = __builtin_amdgcn_permlane32_swap(__float_as_uint(x), __float_as_uint(x), false, false);
    return __uint_as_float(t[0]) + __uint_as_float(t[1]);
}

struct Grp {
    i32x4 k[3];
    i32x4 v[3];
    u32x2 cs[3];
    u32x2 sn[3];
};

#define SB() __builtin_amdgcn_sched_barrier(0)

template <int NW, int D, int MODE>
__global__ __launch_bounds__(NW * 64) void kv5_kernel(
    const bf16* __restrict__ q, const int* __restrict__ qk, const unsigned short* __restrict__ ksc,
    const unsigned short* __restrict__ kzp, const int* __restrict__ qv, const unsigned short* __restrict__ vsc,
    const unsigned short* __restrict__ vzp, const bf16* __restrict__ q_cos, const bf16* __restrict__ q_sin,
    const bf16* __restrict__ k_cos, const bf16* __restrict__ k_sin, float* __restrict__ part,
    int Skv, float scale_log2)
{
    static_assert(D <= 4, "");
    constexpr int RB = NW * D * 4;
    __shared__ float s_qr[DH];
    __shared__ float s_red[NW * DH];
    __shared__ float s_ml[2 * NW];

    const int j = blockIdx.x & (NSPL - 1);
    const int h = blockIdx.x / NSPL;
    const int tid = threadIdx.x;
    const int lane = tid & 63;
    const int wave = __builtin_amdgcn_readfirstlane(tid >> 6);
    const int r = lane >> 4;
    const int c = lane & 15;
    const size_t hbase = (size_t)h * Skv;
    const int nblk = Skv / (RB * NSPL);

    unsigned short q0 = 0, q1 = 0, qc = 0, qs = 0;
    if (tid < DH) {
        q0 = reinterpret_cast<const unsigned short*>(q)[h * DH + tid];
        q1 = reinterpret_cast<const unsigned short*>(q)[h * DH + (tid < HALF ? tid + HALF : tid - HALF)];
        qc = reinterpret_cast<const unsigned short*>(q_cos)[tid];
        qs = reinterpret_cast<const unsigned short*>(q_sin)[tid];
    }
    SB();

    const int rowl = j * RB + wave * 4 + r;
    const int* kp = qk + (hbase + rowl) * DH + 4 * c;
    const int* vp = qv + (hbase + rowl) * DH + 4 * c;
    const bf16* cp = k_cos + (size_t)rowl * DH + 4 * c;
    const bf16* sp = k_sin + (size_t)rowl * DH + 4 * c;
    const int so1 = c < 8 ? 64 + HALF : 64 - HALF;
    const unsigned short* sarr = (c >> 2) == 0 ? ksc : (c >> 2) == 1 ? kzp : (c >> 2) == 2 ? vsc : vzp;
    const unsigned short* scp = sarr + hbase + j * RB + wave * 4 + r + ((c & 3) < D ? (c & 3) : 0) * (NW * 4);

    auto issue = [&](Grp& g, int blk, int d) {
        const size_t ro = (size_t)blk * (RB * NSPL) + d * (NW * 4);
        const int* k_ = kp + ro * DH;
        const int* v_ = vp + ro * DH;
        const bf16* c_ = cp + ro * DH;
        const bf16* s_ = sp + ro * DH;
#pragma unroll
        for (int x = 0; x < 3; ++x) g.k[x] = __builtin_nontemporal_load(reinterpret_cast<const i32x4*>(k_ + 64 * x));
        if constexpr (!(MODE & 1)) {
            if constexpr (MODE & 2) {
#pragma unroll
                for (int x = 0; x < 3; ++x) g.cs[x] = __builtin_nontemporal_load(reinterpret_cast<const u32x2*>(c_ + 64 * x));
                g.sn[0] = __builtin_nontemporal_load(reinterpret_cast<const u32x2*>(s_ + HALF));
                g.sn[1] = __builtin_nontemporal_load(reinterpret_cast<const u32x2*>(s_ + so1));
                g.sn[2] = __builtin_nontemporal_load(reinterpret_cast<const u32x2*>(s_ + 128 - HALF));
            } else {
#pragma unroll
                for (int x = 0; x < 3; ++x) g.cs[x] = *reinterpret_cast<const u32x2*>(c_ + 64 * x);
                g.sn[0] = *reinterpret_cast<const u32x2*>(s_ + HALF);
                g.sn[1] = *reinterpret_cast<const u32x2*>(s_ + so1);
                g.sn[2] = *reinterpret_cast<const u32x2*>(s_ + 128 - HALF);
            }
        } else {
#pragma unroll
            for (int x = 0; x < 3; ++x) { g.cs[x] = u32x2{0x3f803f80u, 0x3f803f80u}; g.sn[x] = u32x2{0x3f803f80u + blk, 0x3f803f80u}; }
        }
#pragma unroll
        for (int x = 0; x < 3; ++x) g.v[x] = __builtin_nontemporal_load(reinterpret_cast<const i32x4*>(v_ + 64 * x));
    };

    Grp buf[D];
    unsigned sc_cur = __builtin_nontemporal_load(scp);
    SB();
#pragma unroll
    for (int d = 0; d < D; ++d) { issue(buf[d], 0, d); SB(); }

    if (tid < DH) {
        const float qd = bflo(q0), qp = bflo(q1);
        const float rot = tid < HALF ? -qp : qp;
        const float a = rbf(qd * bflo(qc));
        const float b = rbf(rot * bflo(qs));
        s_qr[tid] = rbf(a + b);
    }
    __syncthreads();

    f32x2 qa[3][2], qb[3][2];
#pragma unroll
    for (int x = 0; x < 3; ++x) {
        const int d0 = 4 * (c + 16 * x);
        const int p0 = d0 < HALF ? d0 + HALF : d0 - HALF;
        const float sg = d0 < HALF ? 1.f : -1.f;
#pragma unroll
        for (int e = 0; e < 2; ++e) {
            qa[x][e] = f32x2{s_qr[d0 + 2 * e], s_qr[d0 + 2 * e + 1]};
            qb[x][e] = f32x2{sg * s_qr[p0 + 2 * e], sg * s_qr[p0 + 2 * e + 1]};
        }
    }

    float m = -INFINITY, l = 0.f;
    f32x2 acc[3][2];
#pragma unroll
    for (int x = 0; x < 3; ++x) { acc[x][0] = f32x2{0.f, 0.f}; acc[x][1] = f32x2{0.f, 0.f}; }

    auto consume = [&](const Grp& g, float ks, float kz, float vs, float vz) {
        if constexpr (MODE & 4) {
            int a = 0;
#pragma unroll
            for (int x = 0; x < 3; ++x) a += g.k[x][0] ^ g.k[x][1] ^ g.k[x][2] ^ g.k[x][3] ^ g.v[x][0] ^ g.v[x][1] ^ g.v[x][2] ^ g.v[x][3] ^ g.cs[x][0] ^ g.cs[x][1] ^ g.sn[x][0] ^ g.sn[x][1];
            l += __int_as_float(a) + ks + kz + vs + vz;
            return;
        }
        const f32x2 kz2 = f32x2{kz, kz};
        f32x2 a2 = f32x2{0.f, 0.f};
#pragma unroll
        for (int x = 0; x < 3; ++x) {
#pragma unroll
            for (int e = 0; e < 2; ++e) {
                const f32x2 cc = f32x2{bflo(g.cs[x][e]), bfhi(g.cs[x][e])};
                const f32x2 ss = f32x2{bflo(g.sn[x][e]), bfhi(g.sn[x][e])};
                const f32x2 w = __builtin_elementwise_fma(qb[x][e], ss, qa[x][e] * cc);
                const f32x2 kf = f32x2{(float)g.k[x][2 * e], (float)g.k[x][2 * e + 1]} - kz2;
                a2 = __builtin_elementwise_fma(kf, w, a2);
            }
        }
        float a = row16_allsum(a2[0] + a2[1]);
        const float s = a * (ks * scale_log2);
        if (__builtin_amdgcn_ballot_w64(s > m) != 0) {
            const float mn = fmaxf(m, s);
            const float alpha = __builtin_amdgcn_exp2f(m - mn);
            m = mn;
            l *= alpha;
            const f32x2 al2 = f32x2{alpha, alpha};
#pragma unroll
            for (int x = 0; x < 3; ++x) { acc[x][0] *= al2; acc[x][1] *= al2; }
        }
        const float p = __builtin_amdgcn_exp2f(s - m);
        l += p;
        const float pw = p * vs;
        const f32x2 pw2 = f32x2{pw, pw};
        const f32x2 vz2 = f32x2{vz, vz};
#pragma unroll
        for (int x = 0; x < 3; ++x)
#pragma unroll
            for (int e = 0; e < 2; ++e) {
                const f32x2 vf = f32x2{(float)g.v[x][2 * e], (float)g.v[x][2 * e + 1]} - vz2;
                acc[x][e] = __builtin_elementwise_fma(pw2, vf, acc[x][e]);
            }
    };
#define BC(val, N) dpp<0x150 + (N)>(val)
#define CONSUME(d) consume(buf[d], BC(scf, (d)), BC(scf, 4 + (d)), BC(scf, 8 + (d)), BC(scf, 12 + (d)))

    for (int b = 0; b < nblk - 1; ++b) {
        const float scf = bflo(sc_cur);
        SB();
        sc_cur = __builtin_nontemporal_load(scp + (size_t)(b + 1) * (RB * NSPL));
        SB();
        CONSUME(0); SB(); issue(buf[0], b + 1, 0); SB();
        if constexpr (D > 1) { CONSUME(1); SB(); issue(buf[1], b + 1, 1); SB(); }
        if constexpr (D > 2) { CONSUME(2); SB(); issue(buf[2], b + 1, 2); SB(); }
        if constexpr (D > 3) { CONSUME(3); SB(); issue(buf[3], b + 1, 3); SB(); }
    }
    {
        const float scf = bflo(sc_cur);
        CONSUME(0);
        if constexpr (D > 1) { CONSUME(1); }
        if constexpr (D > 2) { CONSUME(2); }
        if constexpr (D > 3) { CONSUME(3); }
    }

    float mw = xrow_max(m);
    if (lane == 0) s_ml[wave] = mw;
    __syncthreads();
    float M = s_ml[0];
#pragma unroll
    for (int w = 1; w < NW; ++w) M = fmaxf(M, s_ml[w]);
    const float f = __builtin_amdgcn_exp2f(m - M);
    l = xrow_sum(l * f);
    float o[3][4];
#pragma unroll
    for (int x = 0; x < 3; ++x)
#pragma unroll
        for (int e = 0; e < 4; ++e) o[x][e] = xrow_sum(acc[x][e >> 1][e & 1] * f);
    if (r == 0) {
#pragma unroll
        for (int x = 0; x < 3; ++x)
            *reinterpret_cast<float4*>(&s_red[wave * DH + 4 * (c + 16 * x)]) = make_float4(o[x][0], o[x][1], o[x][2], o[x][3]);
    }
    if (lane == 0) s_ml[NW + wave] = l;
    __syncthreads();
    float* pp = part + ((size_t)h * NSPL + j) * (DH + 2);
    if (tid < DH) {
        float a = 0.f;
#pragma unroll
        for (int w = 0; w < NW; ++w) a += s_red[w * DH + tid];
        pp[tid] = a;
    } else if (tid == DH) {
        pp[DH] = M;
    } else if (tid == DH + 1) {
        float a = 0.f;
#pragma unroll
        for (int w = 0; w < NW; ++w) a += s_ml[NW + w];
        pp[DH + 1] = a;
    }
}


template <int NS>
__global__ __launch_bounds__(DH) void kv_combine_t(const float* __restrict__ part, bf16* __restrict__ out)
{
    const int h = blockIdx.x;
    const int d = threadIdx.x;
    const float* pp = part + (size_t)h * NS * (DH + 2);
    float mm[NS], ll[NS], oo[NS];
#pragma unroll
    for (int s = 0; s < NS; ++s) {
        mm[s] = pp[s * (DH + 2) + DH];
        ll[s] = pp[s * (DH + 2) + DH + 1];
        oo[s] = pp[s * (DH + 2) + d];
    }
    float M = mm[0];
#pragma unroll
    for (int s = 1; s < NS; ++s) M = fmaxf(M, mm[s]);
    float L = 0.f, o = 0.f;
#pragma unroll
    for (int s = 0; s < NS; ++s) {
        const float f = exp2f(mm[s] - M);
        L += f * ll[s];
        o += f * oo[s];
    }
    out[h * DH + d] = __float2bfloat16(o / L);
}

template <int NW, int D, int MODE>
static void launch5(void* const* d_in, const int* in_sizes, void* d_out, void* d_ws, hipStream_t stream, bool comb = true) {
    const int H = in_sizes[0] / DH;
    const int Skv = in_sizes[9] / DH;
    float* part = (float*)d_ws;
    const float scale_log2 = 1.4426950408889634f / sqrtf((float)DH);
    kv5_kernel<NW, D, MODE><<<H * NSPL, NW * 64, 0, stream>>>((const bf16*)d_in[0], (const int*)d_in[1], (const unsigned short*)d_in[2],
        (const unsigned short*)d_in[3], (const int*)d_in[4], (const unsigned short*)d_in[5], (const unsigned short*)d_in[6],
        (const bf16*)d_in[7], (const bf16*)d_in[8], (const bf16*)d_in[9], (const bf16*)d_in[10], part, Skv, scale_log2);
    if (comb) kv_combine_t<8><<<H, DH, 0, stream>>>(part, (bf16*)d_out);
}


extern "C" void kernel_launch(void* const* d_in, const int* in_sizes, int n_in, void* d_out, int out_size, void* d_ws, size_t ws_size, hipStream_t stream) {
    for (int i = 0; i < n_in; ++i) fprintf(stderr, "in%d %p %d\n", i, d_in[i], in_sizes[i]); fprintf(stderr, "out %p ws %p\n", d_out, d_ws); { int dev; hipGetDevice(&dev); char bus[64]; hipDeviceGetPCIBusId(bus, 64, dev); fprintf(stderr, "dev %d %s\n", dev, bus); }
    launch5<4, 4, 0>(d_in, in_sizes, d_out, d_ws, stream);
}
```

```cpp
#include <hip/hip_runtime.h>
#include <hip/hip_bf16.h>
#include <cstdint>
using bf16 = __hip_bfloat16;
using i32x4 = __attribute__((ext_vector_type(4))) int;
using u32x2 = __attribute__((ext_vector_type(2))) unsigned;
using f32x2 = __attribute__((ext_vector_type(2))) float;
using f32x4 = __attribute__((ext_vector_type(4))) float;

constexpr int DH = 192;
constexpr int HALF = 96;
constexpr int NSPL = 8;

__device__ __forceinline__ float bflo(unsigned u) { return __uint_as_float(u << 16); }
__device__ __forceinline__ float bfhi(unsigned u) { return __uint_as_float(u & 0xffff0000u); }
__device__ __forceinline__ float rbf(float x) { return __bfloat162float(__float2bfloat16(x)); }

template <int CTRL> __device__ __forceinline__ float dpp(float x) {
    return __builtin_bit_cast(float, __builtin_amdgcn_mov_dpp(__builtin_bit_cast(int, x), CTRL, 0xf, 0xf, true));
}
__device__ __forceinline__ float row16_allsum(float x) {
    x += dpp<0x128>(x);
    x += dpp<0x124>(x);
    x += dpp<0x122>(x);
    x += dpp<0x121>(x);
    return x;
}
__device__ __forceinline__ float xrow_max(float x) {
    auto s = __builtin_amdgcn_permlane16_swap(__float_as_uint(x), __float_as_uint(x), false, false);
    x = fmaxf(__uint_as_float(s[0]), __uint_as_float(s[1]));
    auto t = __builtin_amdgcn_permlane32_swap(__float_as_uint(x), __float_as_uint(x), false, false);
    return fmaxf(__uint_as_float(t[0]), __uint_as_float(t[1]));
}
__device__ __forceinline__ float xrow_sum(float x) {
    auto s = __builtin_amdgcn_permlane16_swap(__float_as_uint(x), __float_as_uint(x), false, false);
    x = __uint_as_float(s[0]) + __uint_as_float(s[1]);
    auto t = __builtin_amdgcn_permlane32_swap(__float_as_uint(x), __float_as_uint(x), false, false);
    return __uint_as_float(t[0]) + __uint_as_float(t[1]);
}

struct Grp { i32x4 k[3]; i32x4 v[3]; u32x2 cs[3]; u32x2 sn[3]; };

#define SB() __builtin_amdgcn_sched_barrier(0)
typedef __amdgpu_buffer_rsrc_t rsrc_t;
__device__ __forceinline__ rsrc_t mkrsrc(const void* p, unsigned bytes) {
    return __builtin_amdgcn_make_buffer_rsrc(const_cast<void*>(p), 0, bytes, 0x00020000);
}
template <int AUX> __device__ __forceinline__ i32x4 bld128(rsrc_t r, unsigned voff, unsigned soff) {
    return __builtin_bit_cast(i32x4, __builtin_amdgcn_raw_buffer_load_b128(r, voff, soff, AUX));
}
template <int AUX> __device__ __forceinline__ u32x2 bld64(rsrc_t r, unsigned voff, unsigned soff) {
    return __builtin_bit_cast(u32x2, __builtin_amdgcn_raw_buffer_load_b64(r, voff, soff, AUX));
}
template <int AUX> __device__ __forceinline__ unsigned bld32(rsrc_t r, unsigned voff, unsigned soff) {
    return __builtin_amdgcn_raw_buffer_load_b32(r, voff, soff, AUX);
}

template <int NW, int D, int CSA, int PFA, int PFB, int KVA>
__global__ __launch_bounds__((NW + (PFA >= 0 ? 1 : 0)) * 64) void kv7_kernel(
    const unsigned short* __restrict__ q, const int* __restrict__ qk, const unsigned short* __restrict__ ksc,
    const unsigned short* __restrict__ kzp, const int* __restrict__ qv, const unsigned short* __restrict__ vsc,
    const unsigned short* __restrict__ vzp, const unsigned short* __restrict__ q_cos, const unsigned short* __restrict__ q_sin,
    const unsigned short* __restrict__ k_cos, const unsigned short* __restrict__ k_sin, float* __restrict__ part,
    int Skv, float scale_log2)
{
    static_assert(D <= 4, "");
    constexpr int RB = NW * D * 4;
    __shared__ __attribute__((aligned(16))) float s_qr[2 * DH];
    __shared__ __attribute__((aligned(16))) float s_red[NW * DH];
    __shared__ float s_ml[2 * NW];

    const int j = blockIdx.x & (NSPL - 1);
    const int h = blockIdx.x / NSPL;
    const int tid = threadIdx.x;
    const int lane = tid & 63;
    const int wave = __builtin_amdgcn_readfirstlane(tid >> 6);
    const int r = lane >> 4;
    const int c = lane & 15;
    const int nblk = Skv / (RB * NSPL);
    const rsrc_t cr = mkrsrc(k_cos, (unsigned)Skv * DH * 2);
    const rsrc_t sr = mkrsrc(k_sin, (unsigned)Skv * DH * 2);

    if constexpr (PFA >= 0) {
        if (wave == NW) {
            const int H = gridDim.x / NSPL;
            unsigned dummy = 0;
            __builtin_amdgcn_s_barrier();
            for (int i = h; i < nblk; i += H) {
                const unsigned so = (unsigned)(j + NSPL * i) * RB * DH * 2;
                constexpr int NP = RB * DH * 2 / 8192;
                unsigned t[2 * NP];
#pragma unroll
                for (int x = 0; x < NP; ++x) { t[2 * x] = bld32<PFA>(cr, lane * 128 + x * 8192, so); t[2 * x + 1] = bld32<PFA>(sr, lane * 128 + x * 8192, so); }
#pragma unroll
                for (int x = 0; x < 2 * NP; ++x) dummy ^= t[x];
                asm volatile("" : "+v"(dummy) :: "memory");
                if constexpr (PFB >= 0) {
#pragma unroll
                    for (int x = 0; x < NP; ++x) { t[2 * x] = bld32<PFB>(cr, lane * 128 + x * 8192 + 4, so); t[2 * x + 1] = bld32<PFB>(sr, lane * 128 + x * 8192 + 4, so); }
#pragma unroll
                    for (int x = 0; x < 2 * NP; ++x) dummy ^= t[x];
                }
            }
            asm volatile("" :: "v"(dummy));
            __syncthreads();
            return;
        }
    }

    const int tq = tid < DH ? tid : tid - DH;
    const unsigned q0 = q[h * DH + tq];
    const unsigned q1 = q[h * DH + (tq < HALF ? tq + HALF : tq - HALF)];
    const unsigned qc = q_cos[tq];
    const unsigned qs = q_sin[tq];
    SB();

    const unsigned rl = wave * 4 + r;
    const unsigned kvoff = rl * (DH * 4) + c * 16;
    const unsigned csoff = rl * (DH * 2) + c * 8;
    const unsigned snoff1 = csoff + (c < 8 ? (64 + HALF) * 2 : (64 - HALF) * 2);
    const unsigned hstride = (unsigned)Skv * DH * 4;
    const rsrc_t kr = mkrsrc((const char*)qk + (size_t)h * hstride, hstride);
    const rsrc_t vr = mkrsrc((const char*)qv + (size_t)h * hstride, hstride);
    const unsigned short* sarr = (c >> 2) == 0 ? ksc : (c >> 2) == 1 ? kzp : (c >> 2) == 2 ? vsc : vzp;
    const unsigned short* scp = sarr + (size_t)h * Skv + j * RB + rl + ((c & 3) < D ? (c & 3) : 0) * (NW * 4);

    auto issue = [&](Grp& g, int blk, int d) {
        const unsigned row = (unsigned)(blk * NSPL + j) * RB + d * (NW * 4);
        const unsigned so = row * (DH * 4), so2 = row * (DH * 2);
#pragma unroll
        for (int x = 0; x < 3; ++x) g.k[x] = bld128<KVA>(kr, kvoff + 256 * x, so);
        if constexpr (CSA >= 0) {
#pragma unroll
            for (int x = 0; x < 3; ++x) g.cs[x] = bld64<CSA>(cr, csoff + 128 * x, so2);
            g.sn[0] = bld64<CSA>(sr, csoff + HALF * 2, so2);
            g.sn[1] = bld64<CSA>(sr, snoff1, so2);
            g.sn[2] = bld64<CSA>(sr, csoff + (128 - HALF) * 2, so2);
        } else {
#pragma unroll
            for (int x = 0; x < 3; ++x) { g.cs[x] = u32x2{0x3f803f80u, 0x3f803f80u}; g.sn[x] = u32x2{0x3f803f80u + blk, 0x3f803f80u}; }
        }
#pragma unroll
        for (int x = 0; x < 3; ++x) g.v[x] = bld128<KVA>(vr, kvoff + 256 * x, so);
    };

    Grp buf[D];
    unsigned sc_cur = __builtin_nontemporal_load(scp);
    SB();
#pragma unroll
    for (int d = 0; d < D; ++d) { issue(buf[d], 0, d); SB(); }

    {
        const float qd = bflo(q0), qp = bflo(q1);
        const float rot = tq < HALF ? -qp : qp;
        const float a = rbf(qd * bflo(qc));
        const float b = rbf(rot * bflo(qs));
        const float qr = rbf(a + b);
        if (NW > 6 ? tid < DH : true) {
            s_qr[tq] = qr;
            const int p = tq < HALF ? tq + HALF : tq - HALF;
            s_qr[DH + p] = p < HALF ? qr : -qr;
        }
    }
    __syncthreads();

    f32x2 qa[3][2], qb[3][2];
#pragma unroll
    for (int x = 0; x < 3; ++x) {
        const f32x4 a = *reinterpret_cast<const f32x4*>(&s_qr[4 * (c + 16 * x)]);
        const f32x4 b = *reinterpret_cast<const f32x4*>(&s_qr[DH + 4 * (c + 16 * x)]);
        qa[x][0] = f32x2{a[0], a[1]}; qa[x][1] = f32x2{a[2], a[3]};
        qb[x][0] = f32x2{b[0], b[1]}; qb[x][1] = f32x2{b[2], b[3]};
    }

    float m = -INFINITY, l = 0.f;
    f32x2 acc[3][2];
#pragma unroll
    for (int x = 0; x < 3; ++x) { acc[x][0] = f32x2{0.f, 0.f}; acc[x][1] = f32x2{0.f, 0.f}; }

    auto consume = [&](const Grp& g, float ks, float kz, float vs, float vz) {
        const f32x2 kz2 = f32x2{kz, kz};
        f32x2 a2 = f32x2{0.f, 0.f};
#pragma unroll
        for (int x = 0; x < 3; ++x) {
#pragma unroll
            for (int e = 0; e < 2; ++e) {
                const f32x2 cc = f32x2{bflo(g.cs[x][e]), bfhi(g.cs[x][e])};
                const f32x2 ss = f32x2{bflo(g.sn[x][e]), bfhi(g.sn[x][e])};
                const f32x2 w = __builtin_elementwise_fma(qb[x][e], ss, qa[x][e] * cc);
                const f32x2 kf = f32x2{(float)g.k[x][2 * e], (float)g.k[x][2 * e + 1]} - kz2;
                a2 = __builtin_elementwise_fma(kf, w, a2);
            }
        }
        const float a = row16_allsum(a2[0] + a2[1]);
        const float s = a * (ks * scale_log2);
        if (__builtin_amdgcn_ballot_w64(s > m) != 0) {
            const float mn = fmaxf(m, s);
            const float alpha = __builtin_amdgcn_exp2f(m - mn);
            m = mn;
            l *= alpha;
            const f32x2 al2 = f32x2{alpha, alpha};
#pragma unroll
            for (int x = 0; x < 3; ++x) { acc[x][0] *= al2; acc[x][1] *= al2; }
        }
        const float p = __builtin_amdgcn_exp2f(s - m);
        l += p;
        const float pw = p * vs;
        const f32x2 pw2 = f32x2{pw, pw};
        const f32x2 vz2 = f32x2{vz, vz};
#pragma unroll
        for (int x = 0; x < 3; ++x)
#pragma unroll
            for (int e = 0; e < 2; ++e) {
                const f32x2 vf = f32x2{(float)g.v[x][2 * e], (float)g.v[x][2 * e + 1]} - vz2;
                acc[x][e] = __builtin_elementwise_fma(pw2, vf, acc[x][e]);
            }
        asm volatile("" : "+v"(acc[0][0]), "+v"(acc[0][1]), "+v"(acc[1][0]), "+v"(acc[1][1]), "+v"(acc[2][0]), "+v"(acc[2][1]) :: "memory");
    };
#define BC(val, N) dpp<0x150 + (N)>(val)
#define CONSUME(d) consume(buf[d], BC(scf, (d)), BC(scf, 4 + (d)), BC(scf, 8 + (d)), BC(scf, 12 + (d)))

    for (int b = 0; b < nblk - 1; ++b) {
        float scf = bflo(sc_cur);
        asm volatile("" : "+v"(scf) :: "memory");
        SB();
        sc_cur = __builtin_nontemporal_load(scp + (size_t)(b + 1) * (RB * NSPL));
        SB();
        CONSUME(0); SB(); issue(buf[0], b + 1, 0); SB();
        if constexpr (D > 1) { CONSUME(1); SB(); issue(buf[1], b + 1, 1); SB(); }
        if constexpr (D > 2) { CONSUME(2); SB(); issue(buf[2], b + 1, 2); SB(); }
        if constexpr (D > 3) { CONSUME(3); SB(); issue(buf[3], b + 1, 3); SB(); }
    }
    {
        const float scf = bflo(sc_cur);
        CONSUME(0);
        if constexpr (D > 1) { CONSUME(1); }
        if constexpr (D > 2) { CONSUME(2); }
        if constexpr (D > 3) { CONSUME(3); }
    }

    const float mw = xrow_max(m);
    const float f = __builtin_amdgcn_exp2f(m - mw);
    l = xrow_sum(l * f);
    float o[3][4];
#pragma unroll
    for (int x = 0; x < 3; ++x)
#pragma unroll
        for (int e = 0; e < 4; ++e) o[x][e] = xrow_sum(acc[x][e >> 1][e & 1] * f);
    if (r == 0) {
#pragma unroll
        for (int x = 0; x < 3; ++x)
            *reinterpret_cast<f32x4*>(&s_red[wave * DH + 4 * (c + 16 * x)]) = f32x4{o[x][0], o[x][1], o[x][2], o[x][3]};
    }
    if (lane == 0) { s_ml[2 * wave] = mw; s_ml[2 * wave + 1] = l; }
    __syncthreads();
    if (tid < DH) {
        float M = s_ml[0];
#pragma unroll
        for (int w = 1; w < NW; ++w) M = fmaxf(M, s_ml[2 * w]);
        float a = 0.f, L = 0.f;
#pragma unroll
        for (int w = 0; w < NW; ++w) {
            const float fw = __builtin_amdgcn_exp2f(s_ml[2 * w] - M);
            a += fw * s_red[w * DH + tid];
            L += fw * s_ml[2 * w + 1];
        }
        float* pp = part + ((size_t)h * NSPL + j) * (DH + 2);
        pp[tid] = a;
        if (tid == 0) { pp[DH] = M; pp[DH + 1] = L; }
    }
}

template <int NS>
__global__ __launch_bounds__(DH) void kv_combine_t(const float* __restrict__ part, bf16* __restrict__ out)
{
    const int h = blockIdx.x;
    const int d = threadIdx.x;
    const float* pp = part + (size_t)h * NS * (DH + 2);
    float mm[NS], ll[NS], oo[NS];
#pragma unroll
    for (int s = 0; s < NS; ++s) {
        mm[s] = pp[s * (DH + 2) + DH];
        ll[s] = pp[s * (DH + 2) + DH + 1];
        oo[s] = pp[s * (DH + 2) + d];
    }
    float M = mm[0];
#pragma unroll
    for (int s = 1; s < NS; ++s) M = fmaxf(M, mm[s]);
    float L = 0.f, o = 0.f;
#pragma unroll
    for (int s = 0; s < NS; ++s) {
        const float f = __builtin_amdgcn_exp2f(mm[s] - M);
        L += f * ll[s];
        o += f * oo[s];
    }
    out[h * DH + d] = __float2bfloat16(o / L);
}

template <int NW, int D, int CSA, int PFA, int PFB, int KVA>
static void launch7(void* const* d_in, const int* in_sizes, void* d_out, void* d_ws, hipStream_t stream, bool comb = true) {
    const int H = in_sizes[0] / DH;
    const int Skv = in_sizes[9] / DH;
    float* part = (float*)d_ws;
    const float scale_log2 = 1.4426950408889634f / sqrtf((float)DH);
    typedef const unsigned short* P;
    kv7_kernel<NW, D, CSA, PFA, PFB, KVA><<<H * NSPL, (NW + (PFA >= 0 ? 1 : 0)) * 64, 0, stream>>>((P)d_in[0], (const int*)d_in[1], (P)d_in[2],
        (P)d_in[3], (const int*)d_in[4], (P)d_in[5], (P)d_in[6], (P)d_in[7], (P)d_in[8], (P)d_in[9], (P)d_in[10], part, Skv, scale_log2);
    if (comb) kv_combine_t<NSPL><<<H, DH, 0, stream>>>(part, (bf16*)d_out);
}

extern "C" void kernel_launch(void* const* d_in, const int* in_sizes, int n_in, void* d_out, int out_size, void* d_ws, size_t ws_size, hipStream_t stream) {
    launch7<4,4,0,0,-1,2>(d_in, in_sizes, d_out, d_ws, stream);
}
```

```cpp
#include <hip/hip_runtime.h>
#include <hip/hip_bf16.h>
#include <cstdint>
using bf16 = __hip_bfloat16;
using i32x4 = __attribute__((ext_vector_type(4))) int;
using u32x4 = __attribute__((ext_vector_type(4))) unsigned;
using u32x2 = __attribute__((ext_vector_type(2))) unsigned;
using f32x2 = __attribute__((ext_vector_type(2))) float;
using f32x4 = __attribute__((ext_vector_type(4))) float;

constexpr int DH = 192;
constexpr int HALF = 96;
constexpr int RW = 64;
constexpr int MAXH = 32;

__device__ __forceinline__ float bflo(unsigned u) { return __uint_as_float(u << 16); }
__device__ __forceinline__ float bfhi(unsigned u) { return __uint_as_float(u & 0xffff0000u); }
__device__ __forceinline__ float rbf(float x) { return __bfloat162float(__float2bfloat16(x)); }

template <int CTRL> __device__ __forceinline__ float dpp(float x) {
    return __builtin_bit_cast(float, __builtin_amdgcn_mov_dpp(__builtin_bit_cast(int, x), CTRL, 0xf, 0xf, true));
}
__device__ __forceinline__ float row16_allsum(float x) {
    x += dpp<0x128>(x);
    x += dpp<0x124>(x);
    x += dpp<0x122>(x);
    x += dpp<0x121>(x);
    return x;
}
__device__ __forceinline__ float row16_allmax(float x) {
    x = fmaxf(x, dpp<0x128>(x));
    x = fmaxf(x, dpp<0x124>(x));
    x = fmaxf(x, dpp<0x122>(x));
    x = fmaxf(x, dpp<0x121>(x));
    return x;
}
__device__ __forceinline__ float xrow_max(float x) {
    auto s = __builtin_amdgcn_permlane16_swap(__float_as_uint(x), __float_as_uint(x), false, false);
    x = fmaxf(__uint_as_float(s[0]), __uint_as_float(s[1]));
    auto t = __builtin_amdgcn_permlane32_swap(__float_as_uint(x), __float_as_uint(x), false, false);
    return fmaxf(__uint_as_float(t[0]), __uint_as_float(t[1]));
}
__device__ __forceinline__ float xrow_sum(float x) {
    auto s = __builtin_amdgcn_permlane16_swap(__float_as_uint(x), __float_as_uint(x), false, false);
    x = __uint_as_float(s[0]) + __uint_as_float(s[1]);
    auto t = __builtin_amdgcn_permlane32_swap(__float_as_uint(x), __float_as_uint(x), false, false);
    return __uint_as_float(t[0]) + __uint_as_float(t[1]);
}
__device__ __forceinline__ float sw32(float a, float b) {
    auto t = __builtin_amdgcn_permlane32_swap(__float_as_uint(a), __float_as_uint(b), false, false);
    return __uint_as_float(t[0]) + __uint_as_float(t[1]);
}
__device__ __forceinline__ float sw16(float a, float b) {
    auto t = __builtin_amdgcn_permlane16_swap(__float_as_uint(a), __float_as_uint(b), false, false);
    return __uint_as_float(t[0]) + __uint_as_float(t[1]);
}

#define SB() __builtin_amdgcn_sched_barrier(0)
typedef __amdgpu_buffer_rsrc_t rsrc_t;
__device__ __forceinline__ rsrc_t mkrsrc(const void* p, unsigned bytes) {
    return __builtin_amdgcn_make_buffer_rsrc(const_cast<void*>(p), 0, bytes, 0x00020000);
}
template <int AUX> __device__ __forceinline__ i32x4 bld128(rsrc_t r, unsigned voff, unsigned soff) {
    return __builtin_bit_cast(i32x4, __builtin_amdgcn_raw_buffer_load_b128(r, voff, soff, AUX));
}
template <int AUX> __device__ __forceinline__ unsigned bld16(rsrc_t r, unsigned voff, unsigned soff) {
    return __builtin_amdgcn_raw_buffer_load_b16(r, voff, soff, AUX);
}

struct HG { i32x4 d[3]; };

template <int STA, int MODE, int TP, int ADV>
__global__ __launch_bounds__(256) void kv8_kernel(
    const unsigned short* __restrict__ q, const int* __restrict__ qk, const unsigned short* __restrict__ ksc,
    const unsigned short* __restrict__ kzp, const int* __restrict__ qv, const unsigned short* __restrict__ vsc,
    const unsigned short* __restrict__ vzp, const unsigned short* __restrict__ q_cos, const unsigned short* __restrict__ q_sin,
    const unsigned short* __restrict__ k_cos, const unsigned short* __restrict__ k_sin, float* __restrict__ part,
    int Skv, int H, float scale_log2)
{
    __shared__ __attribute__((aligned(16))) float s_cs[RW * 2 * DH];
    __shared__ __attribute__((aligned(16))) float s_q[MAXH * 2 * DH];

    const unsigned long long tstart = __builtin_amdgcn_s_memrealtime() - ADV;
    const int b = blockIdx.x;
    const int nb = gridDim.x;
    const int tid = threadIdx.x;
    const int lane = tid & 63;
    const int wave = __builtin_amdgcn_readfirstlane(tid >> 6);
    const int r = lane >> 4;
    const int c = lane & 15;
    const unsigned r0 = b * RW;
    const unsigned hstride = (unsigned)Skv * DH * 4;
    const int nh = H >> 2;

    u32x4 pc[6], ps[6];
    {
        const u32x4* c4 = reinterpret_cast<const u32x4*>(k_cos + (size_t)r0 * DH) + tid;
        const u32x4* s4 = reinterpret_cast<const u32x4*>(k_sin + (size_t)r0 * DH) + tid;
#pragma unroll
        for (int i = 0; i < 6; ++i) { pc[i] = __builtin_nontemporal_load(c4 + 256 * i); ps[i] = __builtin_nontemporal_load(s4 + 256 * i); }
    }
    u32x4 qlo[2], qhi[2], qcl, qch, qsl, qsh;
    const int ch = tid % 12;
    {
#pragma unroll
        for (int i = 0; i < 2; ++i) {
            const int it = min(tid + 252 * i, H * 12 - 12 + ch);
            const int hh = it / 12;
            qlo[i] = *reinterpret_cast<const u32x4*>(q + hh * DH + 8 * ch);
            qhi[i] = *reinterpret_cast<const u32x4*>(q + hh * DH + HALF + 8 * ch);
        }
        qcl = *reinterpret_cast<const u32x4*>(q_cos + 8 * ch);
        qch = *reinterpret_cast<const u32x4*>(q_cos + HALF + 8 * ch);
        qsl = *reinterpret_cast<const u32x4*>(q_sin + 8 * ch);
        qsh = *reinterpret_cast<const u32x4*>(q_sin + HALF + 8 * ch);
    }
    SB();

    const rsrc_t kr = mkrsrc(qk, (unsigned)H * hstride);
    const rsrc_t vr = mkrsrc(qv, (unsigned)H * hstride);
    const rsrc_t nr = mkrsrc(qk, 0);
    const unsigned scbytes = (unsigned)H * Skv * 2;
    const rsrc_t r_ks = mkrsrc(ksc, scbytes), r_kz = mkrsrc(kzp, scbytes), r_vs = mkrsrc(vsc, scbytes), r_vz = mkrsrc(vzp, scbytes);
    const unsigned kvoff = r * (DH * 4) + c * 16;
    const unsigned scoff = (4 * c + r) * 2;

    auto issue = [&](HG& g, rsrc_t rs, unsigned so) {
#pragma unroll
        for (int x = 0; x < 3; ++x) g.d[x] = bld128<2>(rs, kvoff + 256 * x, so);
    };
    unsigned sc_ks, sc_kz, sc_vs, sc_vz;
    auto issue_sc = [&](int head) {
        const unsigned so = ((unsigned)head * Skv + r0) * 2;
        sc_ks = bld16<2>(r_ks, scoff, so); sc_kz = bld16<2>(r_kz, scoff, so);
        sc_vs = bld16<2>(r_vs, scoff, so); sc_vz = bld16<2>(r_vz, scoff, so);
    };

    auto pace = [&](int u) {
        if constexpr (TP > 0) {
            const unsigned long long tgt = tstart + (unsigned)((u * TP) >> 5);
            while (__builtin_amdgcn_s_memrealtime() < tgt) __builtin_amdgcn_s_sleep(2);
        }
    };
    HG buf[8];
    issue_sc(wave); SB();
    {
        const unsigned so = (unsigned)wave * hstride + r0 * (DH * 4);
#pragma unroll
        for (int g = 0; g < 8; ++g) { issue(buf[g], kr, so + g * (4 * DH * 4)); SB(); }
    }

#pragma unroll
    for (int i = 0; i < 6; ++i) {
        const int e0 = (tid + 256 * i) * 8;
        const int row = e0 / DH, d = e0 - row * DH;
        const int p = d < HALF ? d + HALF : d - HALF;
        float* cd = &s_cs[row * 2 * DH + d];
        float* sd = &s_cs[row * 2 * DH + DH + p];
        *reinterpret_cast<f32x4*>(cd) = f32x4{bflo(pc[i][0]), bfhi(pc[i][0]), bflo(pc[i][1]), bfhi(pc[i][1])};
        *reinterpret_cast<f32x4*>(cd + 4) = f32x4{bflo(pc[i][2]), bfhi(pc[i][2]), bflo(pc[i][3]), bfhi(pc[i][3])};
        *reinterpret_cast<f32x4*>(sd) = f32x4{bflo(ps[i][0]), bfhi(ps[i][0]), bflo(ps[i][1]), bfhi(ps[i][1])};
        *reinterpret_cast<f32x4*>(sd + 4) = f32x4{bflo(ps[i][2]), bfhi(ps[i][2]), bflo(ps[i][3]), bfhi(ps[i][3])};
    }
#pragma unroll
    for (int i = 0; i < 2; ++i) {
        const int it = min(tid + 252 * i, H * 12 - 12 + ch);
        {
            const int hh = it / 12;
            float lo[8], hi[8];
#pragma unroll
            for (int e = 0; e < 8; ++e) {
                const float a = (e & 1) ? bfhi(qlo[i][e >> 1]) : bflo(qlo[i][e >> 1]);
                const float bb = (e & 1) ? bfhi(qhi[i][e >> 1]) : bflo(qhi[i][e >> 1]);
                const float cl = (e & 1) ? bfhi(qcl[e >> 1]) : bflo(qcl[e >> 1]);
                const float chh = (e & 1) ? bfhi(qch[e >> 1]) : bflo(qch[e >> 1]);
                const float sl = (e & 1) ? bfhi(qsl[e >> 1]) : bflo(qsl[e >> 1]);
                const float sh = (e & 1) ? bfhi(qsh[e >> 1]) : bflo(qsh[e >> 1]);
                lo[e] = rbf(rbf(a * cl) + rbf(-bb * sl));
                hi[e] = rbf(rbf(bb * chh) + rbf(a * sh));
            }
            float* qa = &s_q[hh * 2 * DH];
            float* qb = qa + DH;
            *reinterpret_cast<f32x4*>(qa + 8 * ch) = f32x4{lo[0], lo[1], lo[2], lo[3]};
            *reinterpret_cast<f32x4*>(qa + 8 * ch + 4) = f32x4{lo[4], lo[5], lo[6], lo[7]};
            *reinterpret_cast<f32x4*>(qa + HALF + 8 * ch) = f32x4{hi[0], hi[1], hi[2], hi[3]};
            *reinterpret_cast<f32x4*>(qa + HALF + 8 * ch + 4) = f32x4{hi[4], hi[5], hi[6], hi[7]};
            *reinterpret_cast<f32x4*>(qb + 8 * ch) = f32x4{hi[0], hi[1], hi[2], hi[3]};
            *reinterpret_cast<f32x4*>(qb + 8 * ch + 4) = f32x4{hi[4], hi[5], hi[6], hi[7]};
            *reinterpret_cast<f32x4*>(qb + HALF + 8 * ch) = f32x4{-lo[0], -lo[1], -lo[2], -lo[3]};
            *reinterpret_cast<f32x4*>(qb + HALF + 8 * ch + 4) = f32x4{-lo[4], -lo[5], -lo[6], -lo[7]};
        }
    }
    __syncthreads();

    float* po = part;
    float* pml = part + (size_t)H * nb * DH;

    for (int i = 0; i < nh; ++i) {
        const int head = 4 * i + wave;
        const unsigned hso = (unsigned)head * hstride + r0 * (DH * 4);
        f32x4 qa[3], qb[3];
#pragma unroll
        for (int x = 0; x < 3; ++x) {
            qa[x] = *reinterpret_cast<const f32x4*>(&s_q[head * 2 * DH + 4 * (c + 16 * x)]);
            qb[x] = *reinterpret_cast<const f32x4*>(&s_q[head * 2 * DH + DH + 4 * (c + 16 * x)]);
        }
        float ksf = bflo(sc_ks), kzr = bflo(sc_kz), vsf = bflo(sc_vs), vzr = bflo(sc_vz);
        asm volatile("" : "+v"(ksf), "+v"(kzr), "+v"(vsf), "+v"(vzr) :: "memory");
        SB();
        float sreg = 0.f;
#pragma unroll 1
        for (int half = 0; half < 2; ++half) {
            const rsrc_t nx = half ? vr : kr;
            const unsigned nso = half ? hso : hso + 8 * (4 * DH * 4);
            const float* csb = &s_cs[(half * 32 + r) * 2 * DH + 4 * c];
            const int cg = c - 8 * half;
            const int ub = 32 * i + 8 * half + 8;
#define KSTEP(g) { \
                const float kz = dpp<0x150 + (g)>(kzr); \
                const f32x2 kz2 = f32x2{kz, kz}; \
                f32x2 a2 = f32x2{0.f, 0.f}; \
                _Pragma("unroll") for (int x = 0; x < 3; ++x) { \
                    const f32x4 cc = *reinterpret_cast<const f32x4*>(csb + (g) * (4 * 2 * DH) + 64 * x); \
                    const f32x4 ss = *reinterpret_cast<const f32x4*>(csb + (g) * (4 * 2 * DH) + DH + 64 * x); \
                    _Pragma("unroll") for (int e = 0; e < 2; ++e) { \
                        const f32x2 w = __builtin_elementwise_fma(f32x2{qb[x][2 * e], qb[x][2 * e + 1]}, f32x2{ss[2 * e], ss[2 * e + 1]}, \
                                                                  f32x2{qa[x][2 * e], qa[x][2 * e + 1]} * f32x2{cc[2 * e], cc[2 * e + 1]}); \
                        const f32x2 kf = f32x2{(float)buf[g].d[x][2 * e], (float)buf[g].d[x][2 * e + 1]} - kz2; \
                        a2 = __builtin_elementwise_fma(kf, w, a2); \
                    } \
                } \
                float a = a2[0] + a2[1]; \
                asm volatile("" : "+v"(a) :: "memory"); \
                SB(); if (((g) & 1) == 0) pace(ub + (g)); issue(buf[g], nx, nso + (g) * (4 * DH * 4)); SB(); \
                a = row16_allsum(a); \
                sreg = (cg == (g)) ? a : sreg; }
            KSTEP(0) KSTEP(1) KSTEP(2) KSTEP(3) KSTEP(4) KSTEP(5) KSTEP(6) KSTEP(7)
            kzr = dpp<0x128>(kzr);
        }
        const float s = sreg * (ksf * scale_log2);
        const float m = xrow_max(row16_allmax(s));
        const float p = __builtin_amdgcn_exp2f(s - m);
        const float l = xrow_sum(row16_allsum(p));
        float pwr = p * vsf;
        const bool more = i + 1 < nh;
        if (more) issue_sc(head + 4);
        SB();
        f32x2 acc[3][2];
#pragma unroll
        for (int x = 0; x < 3; ++x) { acc[x][0] = f32x2{0.f, 0.f}; acc[x][1] = f32x2{0.f, 0.f}; }
#pragma unroll 1
        for (int half = 0; half < 2; ++half) {
            const rsrc_t nx = half ? (more ? kr : nr) : vr;
            const unsigned nso = half ? hso + 4 * hstride : hso + 8 * (4 * DH * 4);
            const int ub = (half && !more) ? 0 : 32 * i + 8 * half + 24;
#define VSTEP(g) { \
                const float pw = dpp<0x150 + (g)>(pwr); \
                const float vz = dpp<0x150 + (g)>(vzr); \
                const f32x2 pw2 = f32x2{pw, pw}, vz2 = f32x2{vz, vz}; \
                _Pragma("unroll") for (int x = 0; x < 3; ++x) \
                    _Pragma("unroll") for (int e = 0; e < 2; ++e) { \
                        const f32x2 vf = f32x2{(float)buf[g].d[x][2 * e], (float)buf[g].d[x][2 * e + 1]} - vz2; \
                        acc[x][e] = __builtin_elementwise_fma(pw2, vf, acc[x][e]); \
                    } \
                asm volatile("" : "+v"(acc[0][0]), "+v"(acc[0][1]), "+v"(acc[1][0]), "+v"(acc[1][1]), "+v"(acc[2][0]), "+v"(acc[2][1]) :: "memory"); \
                SB(); if (((g) & 1) == 0) pace(ub + (g)); issue(buf[g], nx, nso + (g) * (4 * DH * 4)); SB(); }
            VSTEP(0) VSTEP(1) VSTEP(2) VSTEP(3) VSTEP(4) VSTEP(5) VSTEP(6) VSTEP(7)
            pwr = dpp<0x128>(pwr);
            vzr = dpp<0x128>(vzr);
        }
        f32x4 o;
#pragma unroll
        for (int e = 0; e < 4; ++e) {
            const float t0 = sw32(acc[0][e >> 1][e & 1], acc[2][e >> 1][e & 1]);
            const float t1 = sw32(acc[1][e >> 1][e & 1], acc[1][e >> 1][e & 1]);
            o[e] = sw16(t0, t1);
        }
        if (r == 3) o = f32x4{m, l, 0.f, 0.f};
        if constexpr (MODE & 1) {
            *reinterpret_cast<f32x4*>(&s_q[head * 2 * DH + 4 * lane]) = o;
        } else if constexpr (STA >= 0) {
            float* dst = r < 3 ? po + ((size_t)head * nb + b) * DH + 4 * lane : pml + ((size_t)head * nb + b) * 4;
            if (lane <= 48) {
                if constexpr (STA == 0) *reinterpret_cast<f32x4*>(dst) = o;
                else if constexpr (STA == 2) __builtin_nontemporal_store(o, reinterpret_cast<f32x4*>(dst));
                else asm volatile("global_store_dwordx4 %0, %1, off sc1\n\ts_nop 1" :: "v"(dst), "v"(o) : "memory");
            }
        } else {
            asm volatile("" :: "v"(o));
        }
    }
    if constexpr (MODE & 1) {
        if (lane <= 48) {
            for (int i = 0; i < nh; ++i) {
                const int head = 4 * i + wave;
                const f32x4 o = *reinterpret_cast<const f32x4*>(&s_q[head * 2 * DH + 4 * lane]);
                float* dst = r < 3 ? po + ((size_t)head * nb + b) * DH + 4 * lane : pml + ((size_t)head * nb + b) * 4;
                if constexpr (STA == 0) *reinterpret_cast<f32x4*>(dst) = o;
                else if constexpr (STA == 2) __builtin_nontemporal_store(o, reinterpret_cast<f32x4*>(dst));
                else asm volatile("global_store_dwordx4 %0, %1, off sc1\n\ts_nop 1" :: "v"(dst), "v"(o) : "memory");
            }
        }
    }
}

__global__ __launch_bounds__(256) void kv8_combine(const float* __restrict__ part, bf16* __restrict__ out, int H)
{
    constexpr int NB = 256;
    __shared__ float s_f[NB];
    __shared__ float s_red[8];
    __shared__ __attribute__((aligned(16))) float s_o[4 * 32];
    const int chunk = blockIdx.x, h = blockIdx.y;
    const int tid = threadIdx.x, lane = tid & 63, wave = tid >> 6;
    const float* po = part + (size_t)h * NB * DH + chunk * 32 + (tid & 7) * 4;
    const float* pml = part + (size_t)H * NB * DH + (size_t)h * NB * 4;
    const f32x2 ml = *reinterpret_cast<const f32x2*>(pml + tid * 4);
    f32x4 v[8];
#pragma unroll
    for (int i = 0; i < 8; ++i) v[i] = *reinterpret_cast<const f32x4*>(po + (size_t)((tid >> 3) + 32 * i) * DH);
    float mw = xrow_max(row16_allmax(ml[0]));
    if (lane == 0) s_red[wave] = mw;
    __syncthreads();
    const float M = fmaxf(fmaxf(s_red[0], s_red[1]), fmaxf(s_red[2], s_red[3]));
    const float f = __builtin_amdgcn_exp2f(ml[0] - M);
    s_f[tid] = f;
    const float lw = xrow_sum(row16_allsum(f * ml[1]));
    if (lane == 0) s_red[4 + wave] = lw;
    __syncthreads();
    f32x4 o = f32x4{0.f, 0.f, 0.f, 0.f};
#pragma unroll
    for (int i = 0; i < 8; ++i) o += s_f[(tid >> 3) + 32 * i] * v[i];
#pragma unroll
    for (int e = 0; e < 4; ++e) { float t = o[e]; t += dpp<0x128>(t); o[e] = xrow_sum(t); }
    if (lane < 8) *reinterpret_cast<f32x4*>(&s_o[wave * 32 + lane * 4]) = o;
    __syncthreads();
    if (tid < 32) {
        const float L = (s_red[4] + s_red[5]) + (s_red[6] + s_red[7]);
        const float t = (s_o[tid] + s_o[32 + tid]) + (s_o[64 + tid] + s_o[96 + tid]);
        out[h * DH + chunk * 32 + tid] = __float2bfloat16(t / L);
    }
}

template <int STA, int MODE, int TP, int ADV>
static void launch8(void* const* d_in, const int* in_sizes, void* d_out, void* d_ws, hipStream_t stream, bool comb = true) {
    const int H = in_sizes[0] / DH;
    const int Skv = in_sizes[9] / DH;
    float* part = (float*)d_ws;
    const float scale_log2 = 1.4426950408889634f / sqrtf((float)DH);
    typedef const unsigned short* P;
    kv8_kernel<STA, MODE, TP, ADV><<<Skv / RW, 256, 0, stream>>>((P)d_in[0], (const int*)d_in[1], (P)d_in[2],
        (P)d_in[3], (const int*)d_in[4], (P)d_in[5], (P)d_in[6], (P)d_in[7], (P)d_in[8], (P)d_in[9], (P)d_in[10], part, Skv, H, scale_log2);
    if (comb) kv8_combine<<<dim3(6, H), 256, 0, stream>>>(part, (bf16*)d_out, H);
}

extern "C" void kernel_launch(void* const* d_in, const int* in_sizes, int n_in, void* d_out, int out_size, void* d_ws, size_t ws_size, hipStream_t stream) {
    launch8<0,1,86*16,-150>(d_in, in_sizes, d_out, d_ws, stream);
}
```

```cpp
#include <hip/hip_runtime.h>
#include <hip/hip_bf16.h>
#include <cstdint>
#include <cstdio>
using bf16 = __hip_bfloat16;
using i32x4 = __attribute__((ext_vector_type(4))) int;
using u32x4 = __attribute__((ext_vector_type(4))) unsigned;
using u32x2 = __attribute__((ext_vector_type(2))) unsigned;
using f32x2 = __attribute__((ext_vector_type(2))) float;
using f32x4 = __attribute__((ext_vector_type(4))) float;

constexpr int DH = 192;
constexpr int HALF = 96;
constexpr int RW = 64;
constexpr int MAXH = 32;

__device__ __forceinline__ float bflo(unsigned u) { return __uint_as_float(u << 16); }
__device__ __forceinline__ float bfhi(unsigned u) { return __uint_as_float(u & 0xffff0000u); }
__device__ __forceinline__ float rbf(float x) { return __bfloat162float(__float2bfloat16(x)); }

template <int CTRL> __device__ __forceinline__ float dpp(float x) {
    return __builtin_bit_cast(float, __builtin_amdgcn_mov_dpp(__builtin_bit_cast(int, x), CTRL, 0xf, 0xf, true));
}
__device__ __forceinline__ float row16_allsum(float x) {
    x += dpp<0x128>(x);
    x += dpp<0x124>(x);
    x += dpp<0x122>(x);
    x += dpp<0x121>(x);
    return x;
}
__device__ __forceinline__ float row16_allmax(float x) {
    x = fmaxf(x, dpp<0x128>(x));
    x = fmaxf(x, dpp<0x124>(x));
    x = fmaxf(x, dpp<0x122>(x));
    x = fmaxf(x, dpp<0x121>(x));
    return x;
}
__device__ __forceinline__ float xrow_max(float x) {
    auto s = __builtin_amdgcn_permlane16_swap(__float_as_uint(x), __float_as_uint(x), false, false);
    x = fmaxf(__uint_as_float(s[0]), __uint_as_float(s[1]));
    auto t = __builtin_amdgcn_permlane32_swap(__float_as_uint(x), __float_as_uint(x), false, false);
    return fmaxf(__uint_as_float(t[0]), __uint_as_float(t[1]));
}
__device__ __forceinline__ float xrow_sum(float x) {
    auto s = __builtin_amdgcn_permlane16_swap(__float_as_uint(x), __float_as_uint(x), false, false);
    x = __uint_as_float(s[0]) + __uint_as_float(s[1]);
    auto t = __builtin_amdgcn_permlane32_swap(__float_as_uint(x), __float_as_uint(x), false, false);
    return __uint_as_float(t[0]) + __uint_as_float(t[1]);
}
__device__ __forceinline__ float sw32(float a, float b) {
    auto t = __builtin_amdgcn_permlane32_swap(__float_as_uint(a), __float_as_uint(b), false, false);
    return __uint_as_float(t[0]) + __uint_as_float(t[1]);
}
__device__ __forceinline__ float sw16(float a, float b) {
    auto t = __builtin_amdgcn_permlane16_swap(__float_as_uint(a), __float_as_uint(b), false, false);
    return __uint_as_float(t[0]) + __uint_as_float(t[1]);
}

#define SB() __builtin_amdgcn_sched_barrier(0)
typedef __amdgpu_buffer_rsrc_t rsrc_t;
__device__ __forceinline__ rsrc_t mkrsrc(const void* p, unsigned bytes) {
    return __builtin_amdgcn_make_buffer_rsrc(const_cast<void*>(p), 0, bytes, 0x00020000);
}
template <int AUX> __device__ __forceinline__ i32x4 bld128(rsrc_t r, unsigned voff, unsigned soff) {
    return __builtin_bit_cast(i32x4, __builtin_amdgcn_raw_buffer_load_b128(r, voff, soff, AUX));
}
template <int AUX> __device__ __forceinline__ unsigned bld16(rsrc_t r, unsigned voff, unsigned soff) {
    return __builtin_amdgcn_raw_buffer_load_b16(r, voff, soff, AUX);
}

struct HG { i32x4 d[3]; };

template <int STA, int MODE, int TP, int ADV>
__global__ __launch_bounds__(256) void kv8_kernel(
    const unsigned short* __restrict__ q, const int* __restrict__ qk, const unsigned short* __restrict__ ksc,
    const unsigned short* __restrict__ kzp, const int* __restrict__ qv, const unsigned short* __restrict__ vsc,
    const unsigned short* __restrict__ vzp, const unsigned short* __restrict__ q_cos, const unsigned short* __restrict__ q_sin,
    const unsigned short* __restrict__ k_cos, const unsigned short* __restrict__ k_sin, float* __restrict__ part,
    int Skv, int H, float scale_log2)
{
    __shared__ __attribute__((aligned(16))) float s_cs[RW * 2 * DH];
    __shared__ __attribute__((aligned(16))) float s_q[MAXH * 2 * DH];

    const unsigned long long tstart = __builtin_amdgcn_s_memrealtime() - ADV;
    const int b = blockIdx.x;
    const int nb = gridDim.x;
    const int tid = threadIdx.x;
    const int lane = tid & 63;
    const int wave = __builtin_amdgcn_readfirstlane(tid >> 6);
    const int r = lane >> 4;
    const int c = lane & 15;
    const unsigned r0 = b * RW;
    const unsigned hstride = (unsigned)Skv * DH * 4;
    const int nh = H >> 2;

    u32x4 pc[6], ps[6];
    {
        const u32x4* c4 = reinterpret_cast<const u32x4*>(k_cos + (size_t)r0 * DH) + tid;
        const u32x4* s4 = reinterpret_cast<const u32x4*>(k_sin + (size_t)r0 * DH) + tid;
#pragma unroll
        for (int i = 0; i < 6; ++i) { pc[i] = __builtin_nontemporal_load(c4 + 256 * i); ps[i] = __builtin_nontemporal_load(s4 + 256 * i); }
    }
    u32x4 qlo[2], qhi[2], qcl, qch, qsl, qsh;
    const int ch = tid % 12;
    {
#pragma unroll
        for (int i = 0; i < 2; ++i) {
            const int it = min(tid + 252 * i, H * 12 - 12 + ch);
            const int hh = it / 12;
            qlo[i] = *reinterpret_cast<const u32x4*>(q + hh * DH + 8 * ch);
            qhi[i] = *reinterpret_cast<const u32x4*>(q + hh * DH + HALF + 8 * ch);
        }
        qcl = *reinterpret_cast<const u32x4*>(q_cos + 8 * ch);
        qch = *reinterpret_cast<const u32x4*>(q_cos + HALF + 8 * ch);
        qsl = *reinterpret_cast<const u32x4*>(q_sin + 8 * ch);
        qsh = *reinterpret_cast<const u32x4*>(q_sin + HALF + 8 * ch);
    }
    SB();

    const rsrc_t kr = mkrsrc(qk, (unsigned)H * hstride);
    const rsrc_t vr = mkrsrc(qv, (unsigned)H * hstride);
    const rsrc_t nr = mkrsrc(qk, 0);
    const unsigned scbytes = (unsigned)H * Skv * 2;
    const rsrc_t r_ks = mkrsrc(ksc, scbytes), r_kz = mkrsrc(kzp, scbytes), r_vs = mkrsrc(vsc, scbytes), r_vz = mkrsrc(vzp, scbytes);
    const unsigned kvoff = r * (DH * 4) + c * 16;
    const unsigned scoff = (4 * c + r) * 2;

    auto issue = [&](HG& g, rsrc_t rs, unsigned so) {
#pragma unroll
        for (int x = 0; x < 3; ++x) g.d[x] = bld128<2>(rs, kvoff + 256 * x, so);
    };
    unsigned sc_ks, sc_kz, sc_vs, sc_vz;
    auto issue_sc = [&](int head) {
        const unsigned so = ((unsigned)head * Skv + r0) * 2;
        sc_ks = bld16<2>(r_ks, scoff, so); sc_kz = bld16<2>(r_kz, scoff, so);
        sc_vs = bld16<2>(r_vs, scoff, so); sc_vz = bld16<2>(r_vz, scoff, so);
    };

    auto pace = [&](int u) {
        if constexpr (TP > 0) {
            const unsigned long long tgt = tstart + (unsigned)((u * TP) >> 5);
            while (__builtin_amdgcn_s_memrealtime() < tgt) __builtin_amdgcn_s_sleep(2);
        }
    };
    HG buf[8];
    issue_sc(wave); SB();
    {
        const unsigned so = (unsigned)wave * hstride + r0 * (DH * 4);
#pragma unroll
        for (int g = 0; g < 8; ++g) { issue(buf[g], kr, so + g * (4 * DH * 4)); SB(); }
    }

#pragma unroll
    for (int i = 0; i < 6; ++i) {
        const int e0 = (tid + 256 * i) * 8;
        const int row = e0 / DH, d = e0 - row * DH;
        const int p = d < HALF ? d + HALF : d - HALF;
        float* cd = &s_cs[row * 2 * DH + d];
        float* sd = &s_cs[row * 2 * DH + DH + p];
        *reinterpret_cast<f32x4*>(cd) = f32x4{bflo(pc[i][0]), bfhi(pc[i][0]), bflo(pc[i][1]), bfhi(pc[i][1])};
        *reinterpret_cast<f32x4*>(cd + 4) = f32x4{bflo(pc[i][2]), bfhi(pc[i][2]), bflo(pc[i][3]), bfhi(pc[i][3])};
        *reinterpret_cast<f32x4*>(sd) = f32x4{bflo(ps[i][0]), bfhi(ps[i][0]), bflo(ps[i][1]), bfhi(ps[i][1])};
        *reinterpret_cast<f32x4*>(sd + 4) = f32x4{bflo(ps[i][2]), bfhi(ps[i][2]), bflo(ps[i][3]), bfhi(ps[i][3])};
    }
#pragma unroll
    for (int i = 0; i < 2; ++i) {
        const int it = min(tid + 252 * i, H * 12 - 12 + ch);
        {
            const int hh = it / 12;
            float lo[8], hi[8];
#pragma unroll
            for (int e = 0; e < 8; ++e) {
                const float a = (e & 1) ? bfhi(qlo[i][e >> 1]) : bflo(qlo[i][e >> 1]);
                const float bb = (e & 1) ? bfhi(qhi[i][e >> 1]) : bflo(qhi[i][e >> 1]);
                const float cl = (e & 1) ? bfhi(qcl[e >> 1]) : bflo(qcl[e >> 1]);
                const float chh = (e & 1) ? bfhi(qch[e >> 1]) : bflo(qch[e >> 1]);
                const float sl = (e & 1) ? bfhi(qsl[e >> 1]) : bflo(qsl[e >> 1]);
                const float sh = (e & 1) ? bfhi(qsh[e >> 1]) : bflo(qsh[e >> 1]);
                lo[e] = rbf(rbf(a * cl) + rbf(-bb * sl));
                hi[e] = rbf(rbf(bb * chh) + rbf(a * sh));
            }
            float* qa = &s_q[hh * 2 * DH];
            float* qb = qa + DH;
            *reinterpret_cast<f32x4*>(qa + 8 * ch) = f32x4{lo[0], lo[1], lo[2], lo[3]};
            *reinterpret_cast<f32x4*>(qa + 8 * ch + 4) = f32x4{lo[4], lo[5], lo[6], lo[7]};
            *reinterpret_cast<f32x4*>(qa + HALF + 8 * ch) = f32x4{hi[0], hi[1], hi[2], hi[3]};
            *reinterpret_cast<f32x4*>(qa + HALF + 8 * ch + 4) = f32x4{hi[4], hi[5], hi[6], hi[7]};
            *reinterpret_cast<f32x4*>(qb + 8 * ch) = f32x4{hi[0], hi[1], hi[2], hi[3]};
            *reinterpret_cast<f32x4*>(qb + 8 * ch + 4) = f32x4{hi[4], hi[5], hi[6], hi[7]};
            *reinterpret_cast<f32x4*>(qb + HALF + 8 * ch) = f32x4{-lo[0], -lo[1], -lo[2], -lo[3]};
            *reinterpret_cast<f32x4*>(qb + HALF + 8 * ch + 4) = f32x4{-lo[4], -lo[5], -lo[6], -lo[7]};
        }
    }
    __syncthreads();

    unsigned long long* stp = reinterpret_cast<unsigned long long*>(part + (size_t)H * nb * (DH + 4)) + (size_t)(b * 4 + wave) * 8;
    const unsigned long long tbar = __builtin_amdgcn_s_memrealtime();
    unsigned long long th1 = 0, th4 = 0;
    float* po = part;
    float* pml = part + (size_t)H * nb * DH;

    for (int i = 0; i < nh; ++i) {
        const int head = 4 * i + wave;
        const unsigned hso = (unsigned)head * hstride + r0 * (DH * 4);
        f32x4 qa[3], qb[3];
#pragma unroll
        for (int x = 0; x < 3; ++x) {
            qa[x] = *reinterpret_cast<const f32x4*>(&s_q[head * 2 * DH + 4 * (c + 16 * x)]);
            qb[x] = *reinterpret_cast<const f32x4*>(&s_q[head * 2 * DH + DH + 4 * (c + 16 * x)]);
        }
        float ksf = bflo(sc_ks), kzr = bflo(sc_kz), vsf = bflo(sc_vs), vzr = bflo(sc_vz);
        asm volatile("" : "+v"(ksf), "+v"(kzr), "+v"(vsf), "+v"(vzr) :: "memory");
        SB();
        float sreg = 0.f;
#pragma unroll 1
        for (int half = 0; half < 2; ++half) {
            const rsrc_t nx = half ? vr : kr;
            const unsigned nso = half ? hso : hso + 8 * (4 * DH * 4);
            const float* csb = &s_cs[(half * 32 + r) * 2 * DH + 4 * c];
            const int cg = c - 8 * half;
            const int ub = 32 * i + 8 * half + 8;
#define KSTEP(g) { \
                const float kz = dpp<0x150 + (g)>(kzr); \
                const f32x2 kz2 = f32x2{kz, kz}; \
                f32x2 a2 = f32x2{0.f, 0.f}; \
                _Pragma("unroll") for (int x = 0; x < 3; ++x) { \
                    const f32x4 cc = *reinterpret_cast<const f32x4*>(csb + (g) * (4 * 2 * DH) + 64 * x); \
                    const f32x4 ss = *reinterpret_cast<const f32x4*>(csb + (g) * (4 * 2 * DH) + DH + 64 * x); \
                    _Pragma("unroll") for (int e = 0; e < 2; ++e) { \
                        const f32x2 w = __builtin_elementwise_fma(f32x2{qb[x][2 * e], qb[x][2 * e + 1]}, f32x2{ss[2 * e], ss[2 * e + 1]}, \
                                                                  f32x2{qa[x][2 * e], qa[x][2 * e + 1]} * f32x2{cc[2 * e], cc[2 * e + 1]}); \
                        const f32x2 kf = f32x2{(float)buf[g].d[x][2 * e], (float)buf[g].d[x][2 * e + 1]} - kz2; \
                        a2 = __builtin_elementwise_fma(kf, w, a2); \
                    } \
                } \
                float a = a2[0] + a2[1]; \
                asm volatile("" : "+v"(a) :: "memory"); \
                SB(); if (((g) & 1) == 0) pace(ub + (g)); issue(buf[g], nx, nso + (g) * (4 * DH * 4)); SB(); \
                a = row16_allsum(a); \
                sreg = (cg == (g)) ? a : sreg; }
            KSTEP(0) KSTEP(1) KSTEP(2) KSTEP(3) KSTEP(4) KSTEP(5) KSTEP(6) KSTEP(7)
            kzr = dpp<0x128>(kzr);
        }
        const float s = sreg * (ksf * scale_log2);
        const float m = xrow_max(row16_allmax(s));
        const float p = __builtin_amdgcn_exp2f(s - m);
        const float l = xrow_sum(row16_allsum(p));
        float pwr = p * vsf;
        const bool more = i + 1 < nh;
        if (more) issue_sc(head + 4);
        SB();
        f32x2 acc[3][2];
#pragma unroll
        for (int x = 0; x < 3; ++x) { acc[x][0] = f32x2{0.f, 0.f}; acc[x][1] = f32x2{0.f, 0.f}; }
#pragma unroll 1
        for (int half = 0; half < 2; ++half) {
            const rsrc_t nx = half ? (more ? kr : nr) : vr;
            const unsigned nso = half ? hso + 4 * hstride : hso + 8 * (4 * DH * 4);
            const int ub = (half && !more) ? 0 : 32 * i + 8 * half + 24;
#define VSTEP(g) { \
                const float pw = dpp<0x150 + (g)>(pwr); \
                const float vz = dpp<0x150 + (g)>(vzr); \
                const f32x2 pw2 = f32x2{pw, pw}, vz2 = f32x2{vz, vz}; \
                _Pragma("unroll") for (int x = 0; x < 3; ++x) \
                    _Pragma("unroll") for (int e = 0; e < 2; ++e) { \
                        const f32x2 vf = f32x2{(float)buf[g].d[x][2 * e], (float)buf[g].d[x][2 * e + 1]} - vz2; \
                        acc[x][e] = __builtin_elementwise_fma(pw2, vf, acc[x][e]); \
                    } \
                asm volatile("" : "+v"(acc[0][0]), "+v"(acc[0][1]), "+v"(acc[1][0]), "+v"(acc[1][1]), "+v"(acc[2][0]), "+v"(acc[2][1]) :: "memory"); \
                SB(); if (((g) & 1) == 0) pace(ub + (g)); issue(buf[g], nx, nso + (g) * (4 * DH * 4)); SB(); }
            VSTEP(0) VSTEP(1) VSTEP(2) VSTEP(3) VSTEP(4) VSTEP(5) VSTEP(6) VSTEP(7)
            pwr = dpp<0x128>(pwr);
            vzr = dpp<0x128>(vzr);
        }
        f32x4 o;
#pragma unroll
        for (int e = 0; e < 4; ++e) {
            const float t0 = sw32(acc[0][e >> 1][e & 1], acc[2][e >> 1][e & 1]);
            const float t1 = sw32(acc[1][e >> 1][e & 1], acc[1][e >> 1][e & 1]);
            o[e] = sw16(t0, t1);
        }
        if (r == 3) o = f32x4{m, l, 0.f, 0.f};
        if (i == 0) th1 = __builtin_amdgcn_s_memrealtime();
        if (i == 3) th4 = __builtin_amdgcn_s_memrealtime();
        if constexpr (MODE & 1) {
            *reinterpret_cast<f32x4*>(&s_q[head * 2 * DH + 4 * lane]) = o;
        } else if constexpr (STA >= 0) {
            float* dst = r < 3 ? po + ((size_t)head * nb + b) * DH + 4 * lane : pml + ((size_t)head * nb + b) * 4;
            if (lane <= 48) {
                if constexpr (STA == 0) *reinterpret_cast<f32x4*>(dst) = o;
                else if constexpr (STA == 2) __builtin_nontemporal_store(o, reinterpret_cast<f32x4*>(dst));
                else asm volatile("global_store_dwordx4 %0, %1, off sc1\n\ts_nop 1" :: "v"(dst), "v"(o) : "memory");
            }
        } else {
            asm volatile("" :: "v"(o));
        }
    }
    const unsigned long long tend = __builtin_amdgcn_s_memrealtime();
    if constexpr (MODE & 1) {
        if (lane <= 48) {
            for (int i = 0; i < nh; ++i) {
                const int head = 4 * i + wave;
                const f32x4 o = *reinterpret_cast<const f32x4*>(&s_q[head * 2 * DH + 4 * lane]);
                float* dst = r < 3 ? po + ((size_t)head * nb + b) * DH + 4 * lane : pml + ((size_t)head * nb + b) * 4;
                if constexpr (STA == 0) *reinterpret_cast<f32x4*>(dst) = o;
                else if constexpr (STA == 2) __builtin_nontemporal_store(o, reinterpret_cast<f32x4*>(dst));
                else asm volatile("global_store_dwordx4 %0, %1, off sc1\n\ts_nop 1" :: "v"(dst), "v"(o) : "memory");
            }
        }
    }
    asm volatile("s_waitcnt vmcnt(0)" ::: "memory");
    const unsigned long long tst = __builtin_amdgcn_s_memrealtime();
    if (lane == 0) { stp[0] = tstart + ADV; stp[1] = tbar; stp[2] = th1; stp[3] = th4; stp[4] = tend; stp[5] = tst; }
}

__global__ __launch_bounds__(256) void kv8_combine(const float* __restrict__ part, bf16* __restrict__ out, int H)
{
    constexpr int NB = 256;
    const unsigned long long tc0 = __builtin_amdgcn_s_memrealtime();
    __shared__ float s_f[NB];
    __shared__ float s_red[8];
    __shared__ __attribute__((aligned(16))) float s_o[4 * 32];
    const int chunk = blockIdx.x, h = blockIdx.y;
    const int tid = threadIdx.x, lane = tid & 63, wave = tid >> 6;
    const float* po = part + (size_t)h * NB * DH + chunk * 32 + (tid & 7) * 4;
    const float* pml = part + (size_t)H * NB * DH + (size_t)h * NB * 4;
    const f32x2 ml = *reinterpret_cast<const f32x2*>(pml + tid * 4);
    f32x4 v[8];
#pragma unroll
    for (int i = 0; i < 8; ++i) v[i] = *reinterpret_cast<const f32x4*>(po + (size_t)((tid >> 3) + 32 * i) * DH);
    float mw = xrow_max(row16_allmax(ml[0]));
    if (lane == 0) s_red[wave] = mw;
    __syncthreads();
    const float M = fmaxf(fmaxf(s_red[0], s_red[1]), fmaxf(s_red[2], s_red[3]));
    const float f = __builtin_amdgcn_exp2f(ml[0] - M);
    s_f[tid] = f;
    const float lw = xrow_sum(row16_allsum(f * ml[1]));
    if (lane == 0) s_red[4 + wave] = lw;
    __syncthreads();
    f32x4 o = f32x4{0.f, 0.f, 0.f, 0.f};
#pragma unroll
    for (int i = 0; i < 8; ++i) o += s_f[(tid >> 3) + 32 * i] * v[i];
#pragma unroll
    for (int e = 0; e < 4; ++e) { float t = o[e]; t += dpp<0x128>(t); o[e] = xrow_sum(t); }
    if (lane < 8) *reinterpret_cast<f32x4*>(&s_o[wave * 32 + lane * 4]) = o;
    __syncthreads();
    if (tid < 32) {
        const float L = (s_red[4] + s_red[5]) + (s_red[6] + s_red[7]);
        const float t = (s_o[tid] + s_o[32 + tid]) + (s_o[64 + tid] + s_o[96 + tid]);
        out[h * DH + chunk * 32 + tid] = __float2bfloat16(t / L);
    }
    if (chunk == 0 && h == 0 && tid == 0) {
        const unsigned long long tc1 = __builtin_amdgcn_s_memrealtime();
        const unsigned long long* stp = reinterpret_cast<const unsigned long long*>(part + (size_t)H * NB * (DH + 4));
        unsigned long long mn[6], mx[6];
        for (int k = 0; k < 6; ++k) { mn[k] = ~0ull; mx[k] = 0; }
        for (int i = 0; i < 1024; ++i) for (int k = 0; k < 6; ++k) { unsigned long long v = stp[i * 8 + k]; mn[k] = v < mn[k] ? v : mn[k]; mx[k] = v > mx[k] ? v : mx[k]; }
        printf("STAMP start %d..%d bar %d..%d h1 %d..%d h4 %d..%d end %d..%d st %d..%d comb %d..%d\n", 0, (int)(mx[0] - mn[0]), (int)(mn[1] - mn[0]), (int)(mx[1] - mn[0]),
            (int)(mn[2] - mn[0]), (int)(mx[2] - mn[0]), (int)(mn[3] - mn[0]), (int)(mx[3] - mn[0]), (int)(mn[4] - mn[0]), (int)(mx[4] - mn[0]), (int)(mn[5] - mn[0]), (int)(mx[5] - mn[0]), (int)(tc0 - mn[0]), (int)(tc1 - mn[0]));
    }
}

template <int STA, int MODE, int TP, int ADV>
static void launch8(void* const* d_in, const int* in_sizes, void* d_out, void* d_ws, hipStream_t stream, bool comb = true) {
    const int H = in_sizes[0] / DH;
    const int Skv = in_sizes[9] / DH;
    float* part = (float*)d_ws;
    const float scale_log2 = 1.4426950408889634f / sqrtf((float)DH);
    typedef const unsigned short* P;
    kv8_kernel<STA, MODE, TP, ADV><<<Skv / RW, 256, 0, stream>>>((P)d_in[0], (const int*)d_in[1], (P)d_in[2],
        (P)d_in[3], (const int*)d_in[4], (P)d_in[5], (P)d_in[6], (P)d_in[7], (P)d_in[8], (P)d_in[9], (P)d_in[10], part, Skv, H, scale_log2);
    if (comb) kv8_combine<<<dim3(6, H), 256, 0, stream>>>(part, (bf16*)d_out, H);
}

extern "C" void kernel_launch(void* const* d_in, const int* in_sizes, int n_in, void* d_out, int out_size, void* d_ws, size_t ws_size, hipStream_t stream) {
    launch8<0,1,86*16,-150>(d_in, in_sizes, d_out, d_ws, stream);
}
```

```cpp
#include <hip/hip_runtime.h>
#include <hip/hip_bf16.h>
#include <cstdint>
#include <cstdio>
using bf16 = __hip_bfloat16;
using i32x4 = __attribute__((ext_vector_type(4))) int;
using u32x4 = __attribute__((ext_vector_type(4))) unsigned;
using u32x2 = __attribute__((ext_vector_type(2))) unsigned;
using f32x2 = __attribute__((ext_vector_type(2))) float;
using f32x4 = __attribute__((ext_vector_type(4))) float;

constexpr int DH = 192;
constexpr int HALF = 96;
constexpr int RW = 64;
constexpr int MAXH = 32;

__device__ __forceinline__ float bflo(unsigned u) { return __uint_as_float(u << 16); }
__device__ __forceinline__ float bfhi(unsigned u) { return __uint_as_float(u & 0xffff0000u); }
__device__ __forceinline__ float rbf(float x) { return __bfloat162float(__float2bfloat16(x)); }

template <int CTRL> __device__ __forceinline__ float dpp(float x) {
    return __builtin_bit_cast(float, __builtin_amdgcn_mov_dpp(__builtin_bit_cast(int, x), CTRL, 0xf, 0xf, true));
}
__device__ __forceinline__ float row16_allsum(float x) {
    x += dpp<0x128>(x);
    x += dpp<0x124>(x);
    x += dpp<0x122>(x);
    x += dpp<0x121>(x);
    return x;
}
__device__ __forceinline__ float row16_allmax(float x) {
    x = fmaxf(x, dpp<0x128>(x));
    x = fmaxf(x, dpp<0x124>(x));
    x = fmaxf(x, dpp<0x122>(x));
    x = fmaxf(x, dpp<0x121>(x));
    return x;
}
__device__ __forceinline__ float xrow_max(float x) {
    auto s = __builtin_amdgcn_permlane16_swap(__float_as_uint(x), __float_as_uint(x), false, false);
    x = fmaxf(__uint_as_float(s[0]), __uint_as_float(s[1]));
    auto t = __builtin_amdgcn_permlane32_swap(__float_as_uint(x), __float_as_uint(x), false, false);
    return fmaxf(__uint_as_float(t[0]), __uint_as_float(t[1]));
}
__device__ __forceinline__ float xrow_sum(float x) {
    auto s = __builtin_amdgcn_permlane16_swap(__float_as_uint(x), __float_as_uint(x), false, false);
    x = __uint_as_float(s[0]) + __uint_as_float(s[1]);
    auto t = __builtin_amdgcn_permlane32_swap(__float_as_uint(x), __float_as_uint(x), false, false);
    return __uint_as_float(t[0]) + __uint_as_float(t[1]);
}
__device__ __forceinline__ float sw32(float a, float b) {
    auto t = __builtin_amdgcn_permlane32_swap(__float_as_uint(a), __float_as_uint(b), false, false);
    return __uint_as_float(t[0]) + __uint_as_float(t[1]);
}
__device__ __forceinline__ float sw16(float a, float b) {
    auto t = __builtin_amdgcn_permlane16_swap(__float_as_uint(a), __float_as_uint(b), false, false);
    return __uint_as_float(t[0]) + __uint_as_float(t[1]);
}

#define SB() __builtin_amdgcn_sched_barrier(0)
typedef __amdgpu_buffer_rsrc_t rsrc_t;
__device__ __forceinline__ rsrc_t mkrsrc(const void* p, unsigned bytes) {
    return __builtin_amdgcn_make_buffer_rsrc(const_cast<void*>(p), 0, bytes, 0x00020000);
}
template <int AUX> __device__ __forceinline__ i32x4 bld128(rsrc_t r, unsigned voff, unsigned soff) {
    return __builtin_bit_cast(i32x4, __builtin_amdgcn_raw_buffer_load_b128(r, voff, soff, AUX));
}
template <int AUX> __device__ __forceinline__ unsigned bld16(rsrc_t r, unsigned voff, unsigned soff) {
    return __builtin_amdgcn_raw_buffer_load_b16(r, voff, soff, AUX);
}

struct HG { i32x4 d[3]; };

template <int STA, int MODE, int TP, int ADV>
__global__ __launch_bounds__(256) void kv8_kernel(
    const unsigned short* __restrict__ q, const int* __restrict__ qk, const unsigned short* __restrict__ ksc,
    const unsigned short* __restrict__ kzp, const int* __restrict__ qv, const unsigned short* __restrict__ vsc,
    const unsigned short* __restrict__ vzp, const unsigned short* __restrict__ q_cos, const unsigned short* __restrict__ q_sin,
    const unsigned short* __restrict__ k_cos, const unsigned short* __restrict__ k_sin, float* __restrict__ part,
    int Skv, int H, float scale_log2)
{
    __shared__ __attribute__((aligned(16))) float s_cs[RW * 2 * DH];
    __shared__ __attribute__((aligned(16))) float s_q[MAXH * 2 * DH];

    const unsigned long long tstart = __builtin_amdgcn_s_memrealtime() - ADV;
    const int b = blockIdx.x;
    const int nb = gridDim.x;
    const int tid = threadIdx.x;
    const int lane = tid & 63;
    const int wave = __builtin_amdgcn_readfirstlane(tid >> 6);
    const int r = lane >> 4;
    const int c = lane & 15;
    const unsigned r0 = b * RW;
    const unsigned hstride = (unsigned)Skv * DH * 4;
    const int nh = H >> 2;

    u32x4 pc[6], ps[6];
    {
        const u32x4* c4 = reinterpret_cast<const u32x4*>(k_cos + (size_t)r0 * DH) + tid;
        const u32x4* s4 = reinterpret_cast<const u32x4*>(k_sin + (size_t)r0 * DH) + tid;
#pragma unroll
        for (int i = 0; i < 6; ++i) { pc[i] = __builtin_nontemporal_load(c4 + 256 * i); ps[i] = __builtin_nontemporal_load(s4 + 256 * i); }
    }
    u32x4 qlo[2], qhi[2], qcl, qch, qsl, qsh;
    const int ch = tid % 12;
    {
#pragma unroll
        for (int i = 0; i < 2; ++i) {
            const int it = min(tid + 252 * i, H * 12 - 12 + ch);
            const int hh = it / 12;
            qlo[i] = *reinterpret_cast<const u32x4*>(q + hh * DH + 8 * ch);
            qhi[i] = *reinterpret_cast<const u32x4*>(q + hh * DH + HALF + 8 * ch);
        }
        qcl = *reinterpret_cast<const u32x4*>(q_cos + 8 * ch);
        qch = *reinterpret_cast<const u32x4*>(q_cos + HALF + 8 * ch);
        qsl = *reinterpret_cast<const u32x4*>(q_sin + 8 * ch);
        qsh = *reinterpret_cast<const u32x4*>(q_sin + HALF + 8 * ch);
    }
    SB();

    const rsrc_t kr = mkrsrc(qk, (unsigned)H * hstride);
    const rsrc_t vr = mkrsrc(qv, (unsigned)H * hstride);
    const rsrc_t nr = mkrsrc(qk, 0);
    const unsigned scbytes = (unsigned)H * Skv * 2;
    const rsrc_t r_ks = mkrsrc(ksc, scbytes), r_kz = mkrsrc(kzp, scbytes), r_vs = mkrsrc(vsc, scbytes), r_vz = mkrsrc(vzp, scbytes);
    const unsigned kvoff = r * (DH * 4) + c * 16;
    const unsigned scoff = (4 * c + r) * 2;

    auto issue = [&](HG& g, rsrc_t rs, unsigned so) {
#pragma unroll
        for (int x = 0; x < 3; ++x) g.d[x] = bld128<2>(rs, kvoff + 256 * x, so);
    };
    unsigned sc_ks, sc_kz, sc_vs, sc_vz;
    auto issue_sc = [&](int head) {
        const unsigned so = ((unsigned)head * Skv + r0) * 2;
        sc_ks = bld16<2>(r_ks, scoff, so); sc_kz = bld16<2>(r_kz, scoff, so);
        sc_vs = bld16<2>(r_vs, scoff, so); sc_vz = bld16<2>(r_vz, scoff, so);
    };

    auto pace = [&](int u) {
        if constexpr (TP > 0) {
            const unsigned long long tgt = tstart + (unsigned)((u * TP) >> 5);
            while (__builtin_amdgcn_s_memrealtime() < tgt) __builtin_amdgcn_s_sleep(2);
        }
    };
    HG buf[8];
    issue_sc(wave); SB();
    {
        const unsigned so = (unsigned)wave * hstride + r0 * (DH * 4);
#pragma unroll
        for (int g = 0; g < 8; ++g) { issue(buf[g], kr, so + g * (4 * DH * 4)); SB(); }
    }

#pragma unroll
    for (int i = 0; i < 6; ++i) {
        const int e0 = (tid + 256 * i) * 8;
        const int row = e0 / DH, d = e0 - row * DH;
        const int p = d < HALF ? d + HALF : d - HALF;
        float* cd = &s_cs[row * 2 * DH + d];
        float* sd = &s_cs[row * 2 * DH + DH + p];
        *reinterpret_cast<f32x4*>(cd) = f32x4{bflo(pc[i][0]), bfhi(pc[i][0]), bflo(pc[i][1]), bfhi(pc[i][1])};
        *reinterpret_cast<f32x4*>(cd + 4) = f32x4{bflo(pc[i][2]), bfhi(pc[i][2]), bflo(pc[i][3]), bfhi(pc[i][3])};
        *reinterpret_cast<f32x4*>(sd) = f32x4{bflo(ps[i][0]), bfhi(ps[i][0]), bflo(ps[i][1]), bfhi(ps[i][1])};
        *reinterpret_cast<f32x4*>(sd + 4) = f32x4{bflo(ps[i][2]), bfhi(ps[i][2]), bflo(ps[i][3]), bfhi(ps[i][3])};
    }
#pragma unroll
    for (int i = 0; i < 2; ++i) {
        const int it = min(tid + 252 * i, H * 12 - 12 + ch);
        {
            const int hh = it / 12;
            float lo[8], hi[8];
#pragma unroll
            for (int e = 0; e < 8; ++e) {
                const float a = (e & 1) ? bfhi(qlo[i][e >> 1]) : bflo(qlo[i][e >> 1]);
                const float bb = (e & 1) ? bfhi(qhi[i][e >> 1]) : bflo(qhi[i][e >> 1]);
                const float cl = (e & 1) ? bfhi(qcl[e >> 1]) : bflo(qcl[e >> 1]);
                const float chh = (e & 1) ? bfhi(qch[e >> 1]) : bflo(qch[e >> 1]);
                const float sl = (e & 1) ? bfhi(qsl[e >> 1]) : bflo(qsl[e >> 1]);
                const float sh = (e & 1) ? bfhi(qsh[e >> 1]) : bflo(qsh[e >> 1]);
                lo[e] = rbf(rbf(a * cl) + rbf(-bb * sl));
                hi[e] = rbf(rbf(bb * chh) + rbf(a * sh));
            }
            float* qa = &s_q[hh * 2 * DH];
            float* qb = qa + DH;
            *reinterpret_cast<f32x4*>(qa + 8 * ch) = f32x4{lo[0], lo[1], lo[2], lo[3]};
            *reinterpret_cast<f32x4*>(qa + 8 * ch + 4) = f32x4{lo[4], lo[5], lo[6], lo[7]};
            *reinterpret_cast<f32x4*>(qa + HALF + 8 * ch) = f32x4{hi[0], hi[1], hi[2], hi[3]};
            *reinterpret_cast<f32x4*>(qa + HALF + 8 * ch + 4) = f32x4{hi[4], hi[5], hi[6], hi[7]};
            *reinterpret_cast<f32x4*>(qb + 8 * ch) = f32x4{hi[0], hi[1], hi[2], hi[3]};
            *reinterpret_cast<f32x4*>(qb + 8 * ch + 4) = f32x4{hi[4], hi[5], hi[6], hi[7]};
            *reinterpret_cast<f32x4*>(qb + HALF + 8 * ch) = f32x4{-lo[0], -lo[1], -lo[2], -lo[3]};
            *reinterpret_cast<f32x4*>(qb + HALF + 8 * ch + 4) = f32x4{-lo[4], -lo[5], -lo[6], -lo[7]};
        }
    }
    __syncthreads();

    unsigned long long* stp = reinterpret_cast<unsigned long long*>(part + (size_t)H * nb * (DH + 4)) + (size_t)(b * 4 + wave) * 8;
    const unsigned long long tbar = __builtin_amdgcn_s_memrealtime();
    unsigned long long th1 = 0, th4 = 0;
    float* po = part;
    float* pml = part + (size_t)H * nb * DH;

    for (int i = 0; i < nh; ++i) {
        const int head = 4 * i + wave;
        const unsigned hso = (unsigned)head * hstride + r0 * (DH * 4);
        f32x4 qa[3], qb[3];
#pragma unroll
        for (int x = 0; x < 3; ++x) {
            qa[x] = *reinterpret_cast<const f32x4*>(&s_q[head * 2 * DH + 4 * (c + 16 * x)]);
            qb[x] = *reinterpret_cast<const f32x4*>(&s_q[head * 2 * DH + DH + 4 * (c + 16 * x)]);
        }
        float ksf = bflo(sc_ks), kzr = bflo(sc_kz), vsf = bflo(sc_vs), vzr = bflo(sc_vz);
        asm volatile("" : "+v"(ksf), "+v"(kzr), "+v"(vsf), "+v"(vzr) :: "memory");
        SB();
        float sreg = 0.f;
#pragma unroll 1
        for (int half = 0; half < 2; ++half) {
            const rsrc_t nx = half ? vr : kr;
            const unsigned nso = half ? hso : hso + 8 * (4 * DH * 4);
            const float* csb = &s_cs[(half * 32 + r) * 2 * DH + 4 * c];
            const int cg = c - 8 * half;
            const int ub = 32 * i + 8 * half + 8;
#define KSTEP(g) { \
                const float kz = dpp<0x150 + (g)>(kzr); \
                const f32x2 kz2 = f32x2{kz, kz}; \
                f32x2 a2 = f32x2{0.f, 0.f}; \
                _Pragma("unroll") for (int x = 0; x < 3; ++x) { \
                    const f32x4 cc = *reinterpret_cast<const f32x4*>(csb + (g) * (4 * 2 * DH) + 64 * x); \
                    const f32x4 ss = *reinterpret_cast<const f32x4*>(csb + (g) * (4 * 2 * DH) + DH + 64 * x); \
                    _Pragma("unroll") for (int e = 0; e < 2; ++e) { \
                        const f32x2 w = __builtin_elementwise_fma(f32x2{qb[x][2 * e], qb[x][2 * e + 1]}, f32x2{ss[2 * e], ss[2 * e + 1]}, \
                                                                  f32x2{qa[x][2 * e], qa[x][2 * e + 1]} * f32x2{cc[2 * e], cc[2 * e + 1]}); \
                        const f32x2 kf = f32x2{(float)buf[g].d[x][2 * e], (float)buf[g].d[x][2 * e + 1]} - kz2; \
                        a2 = __builtin_elementwise_fma(kf, w, a2); \
                    } \
                } \
                float a = a2[0] + a2[1]; \
                asm volatile("" : "+v"(a) :: "memory"); \
                SB(); if (((g) & 1) == 0) pace(ub + (g)); issue(buf[g], nx, nso + (g) * (4 * DH * 4)); SB(); \
                a = row16_allsum(a); \
                sreg = (cg == (g)) ? a : sreg; }
            KSTEP(0) KSTEP(1) KSTEP(2) KSTEP(3) KSTEP(4) KSTEP(5) KSTEP(6) KSTEP(7)
            kzr = dpp<0x128>(kzr);
        }
        const float s = sreg * (ksf * scale_log2);
        const float m = xrow_max(row16_allmax(s));
        const float p = __builtin_amdgcn_exp2f(s - m);
        const float l = xrow_sum(row16_allsum(p));
        float pwr = p * vsf;
        const bool more = i + 1 < nh;
        if (more) issue_sc(head + 4);
        SB();
        f32x2 acc[3][2];
#pragma unroll
        for (int x = 0; x < 3; ++x) { acc[x][0] = f32x2{0.f, 0.f}; acc[x][1] = f32x2{0.f, 0.f}; }
#pragma unroll 1
        for (int half = 0; half < 2; ++half) {
            const rsrc_t nx = half ? (more ? kr : nr) : vr;
            const unsigned nso = half ? hso + 4 * hstride : hso + 8 * (4 * DH * 4);
            const int ub = (half && !more) ? 0 : 32 * i + 8 * half + 24;
#define VSTEP(g) { \
                const float pw = dpp<0x150 + (g)>(pwr); \
                const float vz = dpp<0x150 + (g)>(vzr); \
                const f32x2 pw2 = f32x2{pw, pw}, vz2 = f32x2{vz, vz}; \
                _Pragma("unroll") for (int x = 0; x < 3; ++x) \
                    _Pragma("unroll") for (int e = 0; e < 2; ++e) { \
                        const f32x2 vf = f32x2{(float)buf[g].d[x][2 * e], (float)buf[g].d[x][2 * e + 1]} - vz2; \
                        acc[x][e] = __builtin_elementwise_fma(pw2, vf, acc[x][e]); \
                    } \
                asm volatile("" : "+v"(acc[0][0]), "+v"(acc[0][1]), "+v"(acc[1][0]), "+v"(acc[1][1]), "+v"(acc[2][0]), "+v"(acc[2][1]) :: "memory"); \
                SB(); if (((g) & 1) == 0) pace(ub + (g)); issue(buf[g], nx, nso + (g) * (4 * DH * 4)); SB(); }
            VSTEP(0) VSTEP(1) VSTEP(2) VSTEP(3) VSTEP(4) VSTEP(5) VSTEP(6) VSTEP(7)
            pwr = dpp<0x128>(pwr);
            vzr = dpp<0x128>(vzr);
        }
        f32x4 o;
#pragma unroll
        for (int e = 0; e < 4; ++e) {
            const float t0 = sw32(acc[0][e >> 1][e & 1], acc[2][e >> 1][e & 1]);
            const float t1 = sw32(acc[1][e >> 1][e & 1], acc[1][e >> 1][e & 1]);
            o[e] = sw16(t0, t1);
        }
        if (r == 3) o = f32x4{m, l, 0.f, 0.f};
        if (i == 0) th1 = __builtin_amdgcn_s_memrealtime();
        if (i == 3) th4 = __builtin_amdgcn_s_memrealtime();
        if constexpr (MODE & 1) {
            *reinterpret_cast<f32x4*>(&s_q[head * 2 * DH + 4 * lane]) = o;
        } else if constexpr (STA >= 0) {
            float* dst = r < 3 ? po + ((size_t)head * nb + b) * DH + 4 * lane : pml + ((size_t)head * nb + b) * 4;
            if (lane <= 48) {
                if constexpr (STA == 0) *reinterpret_cast<f32x4*>(dst) = o;
                else if constexpr (STA == 2) __builtin_nontemporal_store(o, reinterpret_cast<f32x4*>(dst));
                else asm volatile("global_store_dwordx4 %0, %1, off sc1\n\ts_nop 1" :: "v"(dst), "v"(o) : "memory");
            }
        } else {
            asm volatile("" :: "v"(o));
        }
    }
    const unsigned long long tend = __builtin_amdgcn_s_memrealtime();
    if constexpr (MODE & 1) {
        if (lane <= 48) {
            for (int i = 0; i < nh; ++i) {
                const int head = 4 * i + wave;
                const f32x4 o = *reinterpret_cast<const f32x4*>(&s_q[head * 2 * DH + 4 * lane]);
                float* dst = r < 3 ? po + ((size_t)head * nb + b) * DH + 4 * lane : pml + ((size_t)head * nb + b) * 4;
                if constexpr (STA == 0) *reinterpret_cast<f32x4*>(dst) = o;
                else if constexpr (STA == 2) __builtin_nontemporal_store(o, reinterpret_cast<f32x4*>(dst));
                else asm volatile("global_store_dwordx4 %0, %1, off sc1\n\ts_nop 1" :: "v"(dst), "v"(o) : "memory");
            }
        }
    }
    asm volatile("s_waitcnt vmcnt(0)" ::: "memory");
    const unsigned long long tst = __builtin_amdgcn_s_memrealtime();
    if (lane == 0) { stp[0] = tstart + ADV; stp[1] = tbar; stp[2] = th1; stp[3] = th4; stp[4] = tend; stp[5] = tst; }
}

__global__ __launch_bounds__(256) void kv8_combine(const float* __restrict__ part, bf16* __restrict__ out, int H)
{
    constexpr int NB = 256;
    const unsigned long long tc0 = __builtin_amdgcn_s_memrealtime();
    __shared__ float s_f[NB];
    __shared__ float s_red[8];
    __shared__ __attribute__((aligned(16))) float s_o[4 * 32];
    const int chunk = blockIdx.x, h = blockIdx.y;
    const int tid = threadIdx.x, lane = tid & 63, wave = tid >> 6;
    const float* po = part + (size_t)h * NB * DH + chunk * 32 + (tid & 7) * 4;
    const float* pml = part + (size_t)H * NB * DH + (size_t)h * NB * 4;
    const f32x2 ml = *reinterpret_cast<const f32x2*>(pml + tid * 4);
    f32x4 v[8];
#pragma unroll
    for (int i = 0; i < 8; ++i) v[i] = *reinterpret_cast<const f32x4*>(po + (size_t)((tid >> 3) + 32 * i) * DH);
    float mw = xrow_max(row16_allmax(ml[0]));
    if (lane == 0) s_red[wave] = mw;
    __syncthreads();
    const float M = fmaxf(fmaxf(s_red[0], s_red[1]), fmaxf(s_red[2], s_red[3]));
    const float f = __builtin_amdgcn_exp2f(ml[0] - M);
    s_f[tid] = f;
    const float lw = xrow_sum(row16_allsum(f * ml[1]));
    if (lane == 0) s_red[4 + wave] = lw;
    __syncthreads();
    f32x4 o = f32x4{0.f, 0.f, 0.f, 0.f};
#pragma unroll
    for (int i = 0; i < 8; ++i) o += s_f[(tid >> 3) + 32 * i] * v[i];
#pragma unroll
    for (int e = 0; e < 4; ++e) { float t = o[e]; t += dpp<0x128>(t); o[e] = xrow_sum(t); }
    if (lane < 8) *reinterpret_cast<f32x4*>(&s_o[wave * 32 + lane * 4]) = o;
    __syncthreads();
    if (tid < 32) {
        const float L = (s_red[4] + s_red[5]) + (s_red[6] + s_red[7]);
        const float t = (s_o[tid] + s_o[32 + tid]) + (s_o[64 + tid] + s_o[96 + tid]);
        out[h * DH + chunk * 32 + tid] = __float2bfloat16(t / L);
    }
    if (chunk == 0 && h == 0 && tid < 64) {
        const unsigned long long tc1 = __builtin_amdgcn_s_memrealtime();
        const unsigned long long* stp = reinterpret_cast<const unsigned long long*>(part + (size_t)H * NB * (DH + 4));
        unsigned long long mn0 = ~0ull, mxq = 0, mnq = ~0ull;
        constexpr int Q = 9;
        for (int i = tid; i < 1024; i += 64) { unsigned long long v = stp[i * 8]; mn0 = v < mn0 ? v : mn0; v = stp[i * 8 + ((Q % 10) < 6 ? (Q % 10) : 0)]; mxq = v > mxq ? v : mxq; mnq = v < mnq ? v : mnq; }
        for (int o = 1; o < 64; o <<= 1) { unsigned long long v = __shfl_xor(mn0, o); mn0 = v < mn0 ? v : mn0; v = __shfl_xor(mxq, o); mxq = v > mxq ? v : mxq; v = __shfl_xor(mnq, o); mnq = v < mnq ? v : mnq; }
        unsigned long long X = Q == 9 ? 0 : Q == 6 ? tc0 - mn0 : Q == 7 ? tc1 - mn0 : Q >= 10 ? mnq - mn0 : mxq - mn0;
        if (X > 100000) X = 100000;
        unsigned long long tgt = mn0 + 15000 + X;
        if (tgt > tc1 + 200000) tgt = tc1;
        while (__builtin_amdgcn_s_memrealtime() < tgt) __builtin_amdgcn_s_sleep(1);
    }
}

template <int STA, int MODE, int TP, int ADV>
static void launch8(void* const* d_in, const int* in_sizes, void* d_out, void* d_ws, hipStream_t stream, bool comb = true) {
    const int H = in_sizes[0] / DH;
    const int Skv = in_sizes[9] / DH;
    float* part = (float*)d_ws;
    const float scale_log2 = 1.4426950408889634f / sqrtf((float)DH);
    typedef const unsigned short* P;
    kv8_kernel<STA, MODE, TP, ADV><<<Skv / RW, 256, 0, stream>>>((P)d_in[0], (const int*)d_in[1], (P)d_in[2],
        (P)d_in[3], (const int*)d_in[4], (P)d_in[5], (P)d_in[6], (P)d_in[7], (P)d_in[8], (P)d_in[9], (P)d_in[10], part, Skv, H, scale_log2);
    if (comb) kv8_combine<<<dim3(6, H), 256, 0, stream>>>(part, (bf16*)d_out, H);
}

extern "C" void kernel_launch(void* const* d_in, const int* in_sizes, int n_in, void* d_out, int out_size, void* d_ws, size_t ws_size, hipStream_t stream) {
    launch8<0,1,86*16,-150>(d_in, in_sizes, d_out, d_ws, stream);
}
```

```cpp
#include <hip/hip_runtime.h>
#include <hip/hip_bf16.h>
#include <cstdint>
using bf16 = __hip_bfloat16;
using i32x4 = __attribute__((ext_vector_type(4))) int;
using u32x4 = __attribute__((ext_vector_type(4))) unsigned;
using u32x2 = __attribute__((ext_vector_type(2))) unsigned;
using f32x2 = __attribute__((ext_vector_type(2))) float;
using f32x4 = __attribute__((ext_vector_type(4))) float;

constexpr int DH = 192;
constexpr int HALF = 96;
constexpr int RW = 64;
constexpr int MAXH = 32;

__device__ __forceinline__ float bflo(unsigned u) { return __uint_as_float(u << 16); }
__device__ __forceinline__ float bfhi(unsigned u) { return __uint_as_float(u & 0xffff0000u); }
__device__ __forceinline__ float rbf(float x) { return __bfloat162float(__float2bfloat16(x)); }

template <int CTRL> __device__ __forceinline__ float dpp(float x) {
    return __builtin_bit_cast(float, __builtin_amdgcn_mov_dpp(__builtin_bit_cast(int, x), CTRL, 0xf, 0xf, true));
}
__device__ __forceinline__ float row16_allsum(float x) {
    x += dpp<0x128>(x);
    x += dpp<0x124>(x);
    x += dpp<0x122>(x);
    x += dpp<0x121>(x);
    return x;
}
__device__ __forceinline__ float row16_allmax(float x) {
    x = fmaxf(x, dpp<0x128>(x));
    x = fmaxf(x, dpp<0x124>(x));
    x = fmaxf(x, dpp<0x122>(x));
    x = fmaxf(x, dpp<0x121>(x));
    return x;
}
__device__ __forceinline__ float xrow_max(float x) {
    auto s = __builtin_amdgcn_permlane16_swap(__float_as_uint(x), __float_as_uint(x), false, false);
    x = fmaxf(__uint_as_float(s[0]), __uint_as_float(s[1]));
    auto t = __builtin_amdgcn_permlane32_swap(__float_as_uint(x), __float_as_uint(x), false, false);
    return fmaxf(__uint_as_float(t[0]), __uint_as_float(t[1]));
}
__device__ __forceinline__ float xrow_sum(float x) {
    auto s = __builtin_amdgcn_permlane16_swap(__float_as_uint(x), __float_as_uint(x), false, false);
    x = __uint_as_float(s[0]) + __uint_as_float(s[1]);
    auto t = __builtin_amdgcn_permlane32_swap(__float_as_uint(x), __float_as_uint(x), false, false);
    return __uint_as_float(t[0]) + __uint_as_float(t[1]);
}
__device__ __forceinline__ float sw32(float a, float b) {
    auto t = __builtin_amdgcn_permlane32_swap(__float_as_uint(a), __float_as_uint(b), false, false);
    return __uint_as_float(t[0]) + __uint_as_float(t[1]);
}
__device__ __forceinline__ float sw16(float a, float b) {
    auto t = __builtin_amdgcn_permlane16_swap(__float_as_uint(a), __float_as_uint(b), false, false);
    return __uint_as_float(t[0]) + __uint_as_float(t[1]);
}

#define SB() __builtin_amdgcn_sched_barrier(0)
typedef __amdgpu_buffer_rsrc_t rsrc_t;
__device__ __forceinline__ rsrc_t mkrsrc(const void* p, unsigned bytes) {
    return __builtin_amdgcn_make_buffer_rsrc(const_cast<void*>(p), 0, bytes, 0x00020000);
}
template <int AUX> __device__ __forceinline__ i32x4 bld128(rsrc_t r, unsigned voff, unsigned soff) {
    return __builtin_bit_cast(i32x4, __builtin_amdgcn_raw_buffer_load_b128(r, voff, soff, AUX));
}
template <int AUX> __device__ __forceinline__ unsigned bld16(rsrc_t r, unsigned voff, unsigned soff) {
    return __builtin_amdgcn_raw_buffer_load_b16(r, voff, soff, AUX);
}

struct HG { i32x4 d[3]; };
typedef unsigned long long u64;
typedef __attribute__((address_space(3))) unsigned char lds_u8;

__device__ __forceinline__ unsigned pk_bf16(float lo, float hi) {
    unsigned r;
    asm("v_cvt_pk_bf16_f32 %0, %1, %2" : "=v"(r) : "v"(lo), "v"(hi));
    return r;
}
__device__ __forceinline__ unsigned now32() { return (unsigned)__builtin_amdgcn_s_memrealtime(); }

constexpr int LDS_COS = 0, LDS_SIN = RW * DH * 2, LDS_Q = 2 * RW * DH * 2;
constexpr int LDS_REC = LDS_Q + 4 * DH * 4;
constexpr int LDS_BYTES = 84 * 1024;
static_assert(LDS_REC + MAXH * 512 <= LDS_BYTES, "");

struct Args {
    const int* qk; const int* qv;
    const unsigned short* k_cos; const unsigned short* k_sin;
    const unsigned short* q; const unsigned short* q_cos; const unsigned short* q_sin;
    const unsigned short* ksc; const unsigned short* kzp; const unsigned short* vsc; const unsigned short* vzp;
    unsigned char* part;
    int Skv; float scale_log2;
};

template <int TP, int ADV, int TMIN, int DBG>
__global__ __launch_bounds__(256) void kv9_kernel(Args a)
{
    __shared__ __attribute__((aligned(16))) unsigned char smem[LDS_BYTES];
    const unsigned tstart = now32();
    constexpr int H = MAXH;
    const int b = blockIdx.x;
    const int tid = threadIdx.x;
    const int lane = tid & 63;
    const int wave = __builtin_amdgcn_readfirstlane(tid >> 6);
    const int r = lane >> 4;
    const int c = lane & 15;
    const unsigned Skv = a.Skv;
    const unsigned r0 = b * RW;
    const unsigned hstride = Skv * (DH * 4);
    const unsigned nb = Skv / RW;
    const unsigned lds0 = (unsigned)(size_t)(lds_u8*)smem;

    asm volatile("" :: "s"(a.qk), "s"(a.qv), "s"(a.q), "s"(a.q_cos), "s"(a.q_sin), "s"(a.ksc), "s"(a.kzp), "s"(a.vsc), "s"(a.vzp), "s"(a.part), "s"(a.Skv), "s"(a.scale_log2));
    {
        const unsigned short* src = (wave < 2 ? a.k_cos : a.k_sin) + (size_t)r0 * DH + (wave & 1) * 6144;
        const unsigned dst = lds0 + wave * 12288;
        unsigned keep;
#define DMA4(A, B) "s_mov_b32 m0, " B "\n\ts_nop 0\n\t" \
                     "global_load_lds_dwordx4 %1, " A " nt\n\t" \
                     "global_load_lds_dwordx4 %1, " A " offset:1024 nt\n\t" \
                     "global_load_lds_dwordx4 %1, " A " offset:2048 nt\n\t" \
                     "global_load_lds_dwordx4 %1, " A " offset:3072 nt\n\t"
        asm volatile("s_mov_b32 %0, m0\n\t" DMA4("%2", "%5") DMA4("%3", "%6") DMA4("%4", "%7") "s_mov_b32 m0, %0"
                     : "=&s"(keep) : "v"(lane * 16), "s"(src), "s"(src + 2048), "s"(src + 4096), "s"(dst), "s"(dst + 4096), "s"(dst + 8192) : "memory");
    }
    const unsigned qoff = (lane < 48 ? lane : lane - 48) * 4;
    const rsrc_t r_q = mkrsrc(a.q, H * DH * 2);
    const unsigned qcl = *reinterpret_cast<const unsigned*>((const char*)a.q_cos + qoff), qch = *reinterpret_cast<const unsigned*>((const char*)a.q_cos + qoff + HALF * 2);
    const unsigned qsl = *reinterpret_cast<const unsigned*>((const char*)a.q_sin + qoff), qsh = *reinterpret_cast<const unsigned*>((const char*)a.q_sin + qoff + HALF * 2);
    SB();

    const rsrc_t kr = mkrsrc(a.qk, H * hstride);
    const rsrc_t vr = mkrsrc(a.qv, H * hstride);
    const rsrc_t nr = mkrsrc(a.qk, 0);
    const unsigned scbytes = H * Skv * 2;
    const rsrc_t r_ks = mkrsrc(a.ksc, scbytes), r_kz = mkrsrc(a.kzp, scbytes), r_vs = mkrsrc(a.vsc, scbytes), r_vz = mkrsrc(a.vzp, scbytes);
    const unsigned kvoff = r * (DH * 4) + c * 16;
    const unsigned scoff = (4 * c + r) * 2;

    auto issue = [&](HG& g, rsrc_t rs, unsigned so) {
#pragma unroll
        for (int x = 0; x < 3; ++x) g.d[x] = bld128<2>(rs, kvoff + 256 * x, so);
    };
    unsigned sc_ks, sc_kz, sc_vs, sc_vz, q_lo, q_hi;
    auto issue_sc = [&](int head) {
        const unsigned so = (head * Skv + r0) * 2;
        q_lo = __builtin_amdgcn_raw_buffer_load_b32(r_q, qoff, head * (DH * 2), 0);
        q_hi = __builtin_amdgcn_raw_buffer_load_b32(r_q, qoff + HALF * 2, head * (DH * 2), 0);
        sc_ks = bld16<2>(r_ks, scoff, so); sc_kz = bld16<2>(r_kz, scoff, so);
        sc_vs = bld16<2>(r_vs, scoff, so); sc_vz = bld16<2>(r_vz, scoff, so);
    };
    unsigned tprev = tstart;
    auto pace = [&](int u) {
        if constexpr (TP > 0) {
            unsigned tgt = tstart - ADV + (unsigned)((u * TP) >> 5);
            if constexpr (TMIN > 0) { const unsigned t2 = tprev + TMIN; if ((int)(t2 - tgt) > 0) tgt = t2; }
            unsigned t = now32();
            while ((int)(t - tgt) < 0) { __builtin_amdgcn_s_sleep(1); t = now32(); }
            if constexpr (TMIN > 0) tprev = t;
        }
    };

    HG buf[8];
    issue_sc(wave); SB();
    {
        const unsigned so = wave * hstride + r0 * (DH * 4);
#pragma unroll
        for (int g = 0; g < 8; ++g) { issue(buf[g], kr, so + g * (4 * DH * 4)); SB(); }
    }

    float* s_q = reinterpret_cast<float*>(smem + LDS_Q) + wave * DH;
    asm volatile("s_waitcnt vmcnt(34)\n\ts_barrier" ::: "memory");
    const unsigned tbar = DBG ? now32() : 0;
    unsigned th1 = 0, th4 = 0;

    const unsigned cso = r * (DH * 2) + c * 8;
    const unsigned sno1 = cso + (c < 8 ? (64 + HALF) * 2 : (64 - HALF) * 2);
    const float sg1 = c < 8 ? 1.f : -1.f;
    const int po1 = c < 8 ? 64 + HALF : 64 - HALF;

    for (int i = 0; i < H / 4; ++i) {
        const int head = 4 * i + wave;
        const unsigned hso = head * hstride + r0 * (DH * 4);
        f32x4 qa[3], qb[3];
        {
            const float a0 = bflo(q_lo), a1 = bfhi(q_lo), b0 = bflo(q_hi), b1 = bfhi(q_hi);
            const unsigned x1 = pk_bf16(a0 * bflo(qcl), a1 * bfhi(qcl));
            const unsigned x2 = pk_bf16(-b0 * bflo(qsl), -b1 * bfhi(qsl));
            const unsigned x3 = pk_bf16(b0 * bflo(qch), b1 * bfhi(qch));
            const unsigned x4 = pk_bf16(a0 * bflo(qsh), a1 * bfhi(qsh));
            const unsigned l2 = pk_bf16(bflo(x1) + bflo(x2), bfhi(x1) + bfhi(x2));
            const unsigned h2 = pk_bf16(bflo(x3) + bflo(x4), bfhi(x3) + bfhi(x4));
            *reinterpret_cast<f32x2*>((char*)s_q + 2 * qoff) = f32x2{bflo(l2), bfhi(l2)};
            *reinterpret_cast<f32x2*>((char*)s_q + 2 * qoff + HALF * 4) = f32x2{bflo(h2), bfhi(h2)};
            __builtin_amdgcn_fence(__ATOMIC_RELEASE, "wavefront");
            __builtin_amdgcn_wave_barrier();
            __builtin_amdgcn_fence(__ATOMIC_ACQUIRE, "wavefront");
            const float* qh = s_q + 4 * c;
#pragma unroll
            for (int x = 0; x < 3; ++x) qa[x] = *reinterpret_cast<const f32x4*>(qh + 64 * x);
            qb[0] = *reinterpret_cast<const f32x4*>(qh + HALF);
            qb[1] = *reinterpret_cast<const f32x4*>(qh + po1) * sg1;
            qb[2] = -*reinterpret_cast<const f32x4*>(qh + 128 - HALF);
        }
        float ksf = bflo(sc_ks), kzr = bflo(sc_kz), vsf = bflo(sc_vs), vzr = bflo(sc_vz);
        asm volatile("" : "+v"(ksf), "+v"(kzr), "+v"(vsf), "+v"(vzr), "+v"(qb[1]), "+v"(qb[2]) :: "memory");
        SB();
        float sreg = 0.f;
#pragma unroll 1
        for (int half = 0; half < 2; ++half) {
            const rsrc_t nx = half ? vr : kr;
            const unsigned nso = half ? hso : hso + 8 * (4 * DH * 4);
            const unsigned char* cb = smem + half * (32 * DH * 2) + cso;
            const unsigned char* sb1 = smem + half * (32 * DH * 2) + sno1;
            const int cg = c - 8 * half;
            const int ub = 32 * i + 8 * half + 8;
#define KSTEP(g) { \
                const float kz = dpp<0x150 + (g)>(kzr); \
                const f32x2 kz2 = f32x2{kz, kz}; \
                f32x2 a2 = f32x2{0.f, 0.f}; \
                u32x2 cc[3], ss[3]; \
                _Pragma("unroll") for (int x = 0; x < 3; ++x) cc[x] = *reinterpret_cast<const u32x2*>(cb + (g) * (4 * DH * 2) + 128 * x); \
                ss[0] = *reinterpret_cast<const u32x2*>(cb + LDS_SIN + (g) * (4 * DH * 2) + HALF * 2); \
                ss[1] = *reinterpret_cast<const u32x2*>(sb1 + LDS_SIN + (g) * (4 * DH * 2)); \
                ss[2] = *reinterpret_cast<const u32x2*>(cb + LDS_SIN + (g) * (4 * DH * 2) + (128 - HALF) * 2); \
                _Pragma("unroll") for (int x = 0; x < 3; ++x) { \
                    _Pragma("unroll") for (int e = 0; e < 2; ++e) { \
                        const f32x2 w = __builtin_elementwise_fma(f32x2{qb[x][2 * e], qb[x][2 * e + 1]}, f32x2{bflo(ss[x][e]), bfhi(ss[x][e])}, \
                                                                  f32x2{qa[x][2 * e], qa[x][2 * e + 1]} * f32x2{bflo(cc[x][e]), bfhi(cc[x][e])}); \
                        const f32x2 kf = f32x2{(float)buf[g].d[x][2 * e], (float)buf[g].d[x][2 * e + 1]} - kz2; \
                        a2 = __builtin_elementwise_fma(kf, w, a2); \
                    } \
                } \
                float sa = a2[0] + a2[1]; \
                asm volatile("" : "+v"(sa) :: "memory"); \
                SB(); if (((g) & 1) == 0) pace(ub + (g)); issue(buf[g], nx, nso + (g) * (4 * DH * 4)); SB(); \
                sa = row16_allsum(sa); \
                sreg = (cg == (g)) ? sa : sreg; }
            KSTEP(0) KSTEP(1) KSTEP(2) KSTEP(3) KSTEP(4) KSTEP(5) KSTEP(6) KSTEP(7)
            kzr = dpp<0x128>(kzr);
        }
        const float s = sreg * (ksf * a.scale_log2);
        const float m = xrow_max(row16_allmax(s));
        const float p = __builtin_amdgcn_exp2f(s - m);
        const float l = xrow_sum(row16_allsum(p));
        float pwr = p * vsf;
        const bool more = i + 1 < H / 4;
        if (more) issue_sc(head + 4);
        SB();
        f32x2 acc[3][2];
#pragma unroll
        for (int x = 0; x < 3; ++x) { acc[x][0] = f32x2{0.f, 0.f}; acc[x][1] = f32x2{0.f, 0.f}; }
#pragma unroll 1
        for (int half = 0; half < 2; ++half) {
            const bool live = !half || more;
            const rsrc_t nx = half ? (more ? kr : nr) : vr;
            const unsigned nso = half ? hso + 4 * hstride : hso + 8 * (4 * DH * 4);
            const int ub = 32 * i + 8 * half + 24;
#define VSTEP(g) { \
                const float pw = dpp<0x150 + (g)>(pwr); \
                const float vz = dpp<0x150 + (g)>(vzr); \
                const f32x2 pw2 = f32x2{pw, pw}, vz2 = f32x2{vz, vz}; \
                _Pragma("unroll") for (int x = 0; x < 3; ++x) \
                    _Pragma("unroll") for (int e = 0; e < 2; ++e) { \
                        const f32x2 vf = f32x2{(float)buf[g].d[x][2 * e], (float)buf[g].d[x][2 * e + 1]} - vz2; \
                        acc[x][e] = __builtin_elementwise_fma(pw2, vf, acc[x][e]); \
                    } \
                asm volatile("" : "+v"(acc[0][0]), "+v"(acc[0][1]), "+v"(acc[1][0]), "+v"(acc[1][1]), "+v"(acc[2][0]), "+v"(acc[2][1]) :: "memory"); \
                SB(); if (((g) & 1) == 0 && live) pace(ub + (g)); issue(buf[g], nx, nso + (g) * (4 * DH * 4)); SB(); }
            VSTEP(0) VSTEP(1) VSTEP(2) VSTEP(3) VSTEP(4) VSTEP(5) VSTEP(6) VSTEP(7)
            pwr = dpp<0x128>(pwr);
            vzr = dpp<0x128>(vzr);
        }
        float o[4];
#pragma unroll
        for (int e = 0; e < 4; ++e) {
            const float t0 = sw32(acc[0][e >> 1][e & 1], acc[2][e >> 1][e & 1]);
            const float t1 = sw32(acc[1][e >> 1][e & 1], acc[1][e >> 1][e & 1]);
            o[e] = sw16(t0, t1);
        }
        u32x2 rec = u32x2{pk_bf16(o[0], o[1]), pk_bf16(o[2], o[3])};
        if (r == 3) rec = u32x2{__float_as_uint(m), __float_as_uint(l)};
        *reinterpret_cast<u32x2*>(smem + LDS_REC + head * 512 + lane * 8) = rec;
        if (DBG) { if (i == 0) th1 = now32(); if (i == 3) th4 = now32(); }
    }
    const unsigned tend = DBG ? now32() : 0;
    const unsigned pml = H * nb * (DH * 2);
    const rsrc_t pr = mkrsrc(a.part, pml + H * nb * 8);
#pragma unroll
    for (int p = 0; p < 3; ++p) {
        const int t = lane + 64 * p, hi_ = t / 24, k = t - hi_ * 24;
        const int head = 4 * hi_ + wave;
        const u32x4 v = *reinterpret_cast<const u32x4*>(smem + LDS_REC + head * 512 + k * 16);
        __builtin_amdgcn_raw_buffer_store_b128(v, pr, (head * nb + b) * (DH * 2) + k * 16, 0, 0);
    }
    if (lane < 8) {
        const int head = 4 * lane + wave;
        const u32x2 v = *reinterpret_cast<const u32x2*>(smem + LDS_REC + head * 512 + 384);
        __builtin_amdgcn_raw_buffer_store_b64(v, pr, pml + (head * nb + b) * 8, 0, 0);
    }
    if (DBG) {
        asm volatile("s_waitcnt vmcnt(0)" ::: "memory");
        const unsigned tst = now32();
        u64* stp = reinterpret_cast<u64*>(a.part + 32 * 256 * 196 * 4) + (size_t)(b * 4 + wave) * 8;
        if (lane == 0) { stp[0] = tstart; stp[1] = tbar; stp[2] = th1; stp[3] = th4; stp[4] = tend; stp[5] = tst; }
    }
}

template <int DBG>
__global__ __launch_bounds__(256) void kv9_combine(const unsigned char* __restrict__ part, bf16* __restrict__ out)
{
    constexpr int NB = 256, H = MAXH;
    const unsigned tc0 = DBG ? now32() : 0;
    __shared__ float s_f[NB];
    __shared__ float s_red[8];
    __shared__ __attribute__((aligned(16))) float s_o[4 * 32];
    const int chunk = blockIdx.x, h = blockIdx.y;
    const int tid = threadIdx.x, lane = tid & 63, wave = tid >> 6;
    const unsigned char* po = part + (size_t)h * NB * (DH * 2) + chunk * 64 + (tid & 3) * 16;
    const f32x2 ml = *reinterpret_cast<const f32x2*>(part + H * NB * (DH * 2) + (h * NB + tid) * 8);
    u32x4 v[4];
#pragma unroll
    for (int i = 0; i < 4; ++i) v[i] = *reinterpret_cast<const u32x4*>(po + ((tid >> 2) + 64 * i) * (DH * 2));
    float mw = xrow_max(row16_allmax(ml[0]));
    if (lane == 0) s_red[wave] = mw;
    __syncthreads();
    const float M = fmaxf(fmaxf(s_red[0], s_red[1]), fmaxf(s_red[2], s_red[3]));
    const float f = __builtin_amdgcn_exp2f(ml[0] - M);
    s_f[tid] = f;
    const float lw = xrow_sum(row16_allsum(f * ml[1]));
    if (lane == 0) s_red[4 + wave] = lw;
    __syncthreads();
    float o[8];
#pragma unroll
    for (int e = 0; e < 8; ++e) o[e] = 0.f;
#pragma unroll
    for (int i = 0; i < 4; ++i) {
        const float fs = s_f[(tid >> 2) + 64 * i];
#pragma unroll
        for (int e = 0; e < 4; ++e) { o[2 * e] += fs * bflo(v[i][e]); o[2 * e + 1] += fs * bfhi(v[i][e]); }
    }
#pragma unroll
    for (int e = 0; e < 8; ++e) { float t = o[e]; t += dpp<0x128>(t); t += dpp<0x124>(t); o[e] = xrow_sum(t); }
    if (lane < 4) {
        *reinterpret_cast<f32x4*>(&s_o[wave * 32 + lane * 8]) = f32x4{o[0], o[1], o[2], o[3]};
        *reinterpret_cast<f32x4*>(&s_o[wave * 32 + lane * 8 + 4]) = f32x4{o[4], o[5], o[6], o[7]};
    }
    __syncthreads();
    if (tid < 32) {
        const float L = (s_red[4] + s_red[5]) + (s_red[6] + s_red[7]);
        const float t = (s_o[tid] + s_o[32 + tid]) + (s_o[64 + tid] + s_o[96 + tid]);
        out[h * DH + chunk * 32 + tid] = __float2bfloat16(t / L);
    }
    if (DBG && tid == 0) {
        u64* stp = reinterpret_cast<u64*>(const_cast<unsigned char*>(part) + 32 * 256 * 196 * 4) + (size_t)(h * 6 + chunk) * 8;
        stp[6] = tc0; stp[7] = now32();
    }
}

template <int TP, int ADV, int TMIN, int DBG>
static void launch9(void* const* d_in, const int* in_sizes, void* d_out, void* d_ws, hipStream_t stream, bool comb = true) {
    typedef const unsigned short* P;
    Args a;
    a.qk = (const int*)d_in[1]; a.qv = (const int*)d_in[4];
    a.k_cos = (P)d_in[9]; a.k_sin = (P)d_in[10];
    a.q = (P)d_in[0]; a.q_cos = (P)d_in[7]; a.q_sin = (P)d_in[8];
    a.ksc = (P)d_in[2]; a.kzp = (P)d_in[3]; a.vsc = (P)d_in[5]; a.vzp = (P)d_in[6];
    a.part = (unsigned char*)d_ws;
    a.Skv = in_sizes[9] / DH;
    a.scale_log2 = 1.4426950408889634f / sqrtf((float)DH);
    kv9_kernel<TP, ADV, TMIN, DBG><<<a.Skv / RW, 256, 0, stream>>>(a);
    if (comb) kv9_combine<DBG><<<dim3(6, MAXH), 256, 0, stream>>>(a.part, (bf16*)d_out);
}

extern "C" void kernel_launch(void* const* d_in, const int* in_sizes, int n_in, void* d_out, int out_size, void* d_ws, size_t ws_size, hipStream_t stream) {
    launch9<85*16,-150,80,0>(d_in, in_sizes, d_out, d_ws, stream);
}
```

```cpp
#include <hip/hip_runtime.h>
#include <hip/hip_bf16.h>
#include <cstdint>
using bf16 = __hip_bfloat16;
using i32x4 = __attribute__((ext_vector_type(4))) int;
using u32x4 = __attribute__((ext_vector_type(4))) unsigned;
using u32x2 = __attribute__((ext_vector_type(2))) unsigned;
using f32x2 = __attribute__((ext_vector_type(2))) float;
using f32x4 = __attribute__((ext_vector_type(4))) float;

constexpr int DH = 192;
constexpr int HALF = 96;
constexpr int RW = 64;
constexpr int MAXH = 32;

__device__ __forceinline__ float bflo(unsigned u) { return __uint_as_float(u << 16); }
__device__ __forceinline__ float bfhi(unsigned u) { return __uint_as_float(u & 0xffff0000u); }
__device__ __forceinline__ float rbf(float x) { return __bfloat162float(__float2bfloat16(x)); }

template <int CTRL> __device__ __forceinline__ float dpp(float x) {
    return __builtin_bit_cast(float, __builtin_amdgcn_mov_dpp(__builtin_bit_cast(int, x), CTRL, 0xf, 0xf, true));
}
__device__ __forceinline__ float row16_allsum(float x) {
    x += dpp<0x128>(x);
    x += dpp<0x124>(x);
    x += dpp<0x122>(x);
    x += dpp<0x121>(x);
    return x;
}
__device__ __forceinline__ float row16_allmax(float x) {
    x = fmaxf(x, dpp<0x128>(x));
    x = fmaxf(x, dpp<0x124>(x));
    x = fmaxf(x, dpp<0x122>(x));
    x = fmaxf(x, dpp<0x121>(x));
    return x;
}
__device__ __forceinline__ float xrow_max(float x) {
    auto s = __builtin_amdgcn_permlane16_swap(__float_as_uint(x), __float_as_uint(x), false, false);
    x = fmaxf(__uint_as_float(s[0]), __uint_as_float(s[1]));
    auto t = __builtin_amdgcn_permlane32_swap(__float_as_uint(x), __float_as_uint(x), false, false);
    return fmaxf(__uint_as_float(t[0]), __uint_as_float(t[1]));
}
__device__ __forceinline__ float xrow_sum(float x) {
    auto s = __builtin_amdgcn_permlane16_swap(__float_as_uint(x), __float_as_uint(x), false, false);
    x = __uint_as_float(s[0]) + __uint_as_float(s[1]);
    auto t = __builtin_amdgcn_permlane32_swap(__float_as_uint(x), __float_as_uint(x), false, false);
    return __uint_as_float(t[0]) + __uint_as_float(t[1]);
}
__device__ __forceinline__ float sw32(float a, float b) {
    auto t = __builtin_amdgcn_permlane32_swap(__float_as_uint(a), __float_as_uint(b), false, false);
    return __uint_as_float(t[0]) + __uint_as_float(t[1]);
}
__device__ __forceinline__ float sw16(float a, float b) {
    auto t = __builtin_amdgcn_permlane16_swap(__float_as_uint(a), __float_as_uint(b), false, false);
    return __uint_as_float(t[0]) + __uint_as_float(t[1]);
}

#define SB() __builtin_amdgcn_sched_barrier(0)
typedef __amdgpu_buffer_rsrc_t rsrc_t;
__device__ __forceinline__ rsrc_t mkrsrc(const void* p, unsigned bytes) {
    return __builtin_amdgcn_make_buffer_rsrc(const_cast<void*>(p), 0, bytes, 0x00020000);
}
template <int AUX> __device__ __forceinline__ i32x4 bld128(rsrc_t r, unsigned voff, unsigned soff) {
    return __builtin_bit_cast(i32x4, __builtin_amdgcn_raw_buffer_load_b128(r, voff, soff, AUX));
}
template <int AUX> __device__ __forceinline__ unsigned bld16(rsrc_t r, unsigned voff, unsigned soff) {
    return __builtin_amdgcn_raw_buffer_load_b16(r, voff, soff, AUX);
}

struct HG { i32x4 d[3]; };
typedef unsigned long long u64;
typedef __attribute__((address_space(3))) unsigned char lds_u8;

__device__ __forceinline__ unsigned pk_bf16(float lo, float hi) {
    unsigned r;
    asm("v_cvt_pk_bf16_f32 %0, %1, %2" : "=v"(r) : "v"(lo), "v"(hi));
    return r;
}
__device__ __forceinline__ unsigned now32() { return (unsigned)__builtin_amdgcn_s_memrealtime(); }

constexpr int LDS_REC = 0;
constexpr int RECW = MAXH * DH * 4;
constexpr int LDS_CS = 0;
constexpr int LDS_QT = 12288;
constexpr int LDS_PF = 13312;
constexpr int LDS_QRAW = MAXH * 4 * DH * 4;
constexpr int LDS_Q = LDS_QRAW + MAXH * DH * 2;
constexpr int LDS_P = LDS_Q + 4 * DH * 4;
constexpr int LDS_SC = LDS_P + 4 * 16 * 256;
constexpr int LDS_CNT = LDS_SC + 4 * MAXH * RW * 2;
constexpr int LDS_TR = LDS_CNT + 512;
constexpr int LDS_BYTES = LDS_TR + 4096 + 64;

struct Args {
    const int* qk; const int* qv;
    const unsigned short* k_cos; const unsigned short* k_sin;
    const unsigned short* q; const unsigned short* q_cos; const unsigned short* q_sin;
    const unsigned short* ksc; const unsigned short* kzp; const unsigned short* vsc; const unsigned short* vzp;
    unsigned char* part;
    int Skv; float scale_log2;
};
template <int AUX> __device__ __forceinline__ u32x2 bld64(rsrc_t r, unsigned voff, unsigned soff) {
    return __builtin_bit_cast(u32x2, __builtin_amdgcn_raw_buffer_load_b64(r, voff, soff, AUX));
}

template <int TP, int ADV, int MAP, int TMIN, int SAUX, int MLL, int XTR, int DBG>
__global__ __launch_bounds__(256) void kv17_kernel(Args a)
{
    __shared__ __attribute__((aligned(16))) unsigned char smem[LDS_BYTES];
    const unsigned tstart = now32();
    constexpr int H = MAXH;
    const int b = blockIdx.x;
    const int tid = threadIdx.x;
    const int lane = tid & 63;
    const int wave = __builtin_amdgcn_readfirstlane(tid >> 6);
    const int r = lane >> 4;
    const int c = lane & 15;
    const unsigned Skv = a.Skv;
    const unsigned hstride = Skv * (DH * 4);
    const unsigned nb = Skv / RW;
    const unsigned lds0 = (unsigned)(size_t)(lds_u8*)smem;
    const unsigned pos = (b & 7) * (nb >> 3) + (b >> 3);
    const unsigned row0 = MAP == 12 ? 64 * pos + 16 * wave : MAP == 14 ? 32 * pos + 8 * wave : 16 * pos + 4 * wave;
    const unsigned rs1 = MAP == 7 ? Skv / 4 : 4;
    const unsigned rs2 = MAP == 12 ? 8 : Skv / 2;
#define ROWK(k) (row0 + ((k) & 1) * rs1 + ((k) >> 1) * rs2)

    asm volatile("" :: "s"(a.qk), "s"(a.qv), "s"(a.k_cos), "s"(a.k_sin), "s"(a.q_cos), "s"(a.q_sin), "s"(a.ksc), "s"(a.kzp), "s"(a.vsc), "s"(a.vzp), "s"(a.part), "s"(a.Skv), "s"(a.scale_log2));
#define DMA3(V, A, B, P) "s_mov_b32 m0, " B "\n\ts_nop 0\n\t" \
                     "global_load_lds_dwordx4 " V ", " A P "\n\t" \
                     "global_load_lds_dwordx4 " V ", " A " offset:1024" P "\n\t" \
                     "global_load_lds_dwordx4 " V ", " A " offset:2048" P "\n\t"
    unsigned keep;
    const unsigned qoff = (lane < 48 ? lane : lane - 48) * 4;
    asm volatile("s_mov_b32 %0, m0\n\t" DMA3("%1", "%2", "%3", "") "s_mov_b32 m0, %5\n\ts_nop 0\n\tglobal_load_lds_dwordx4 %4, off"
                 : "=&s"(keep) : "v"(lane * 16), "s"(a.q + wave * 1536), "s"(lds0 + LDS_QRAW + wave * 3072),
                   "v"((qoff < HALF ? a.q_cos : a.q_sin - DH) + 2 * qoff), "s"(lds0 + LDS_QT + wave * RECW) : "memory");
    SB();
    {
        const unsigned short* sp = wave == 0 ? a.ksc : wave == 1 ? a.kzp : wave == 2 ? a.vsc : a.vzp;
        const unsigned vs = ((lane >> 3) * Skv + (MAP == 12 ? 64 * pos + (lane & 7) * 8 : 32 * pos + ((lane >> 2) & 1) * rs2 + (lane & 3) * 8)) * 2;
        const unsigned ds = lds0 + LDS_SC + wave * 4096;
#define DMA1(A, B) "s_mov_b32 m0, " B "\n\ts_nop 0\n\tglobal_load_lds_dwordx4 %0, " A " nt\n\t"
        asm volatile(DMA1("%1", "%5") DMA1("%2", "%6") DMA1("%3", "%7") DMA1("%4", "%8")
                     :: "v"(vs), "s"(sp), "s"(sp + 8 * Skv), "s"(sp + 16 * Skv), "s"(sp + 24 * Skv), "s"(ds), "s"(ds + 1024), "s"(ds + 2048), "s"(ds + 3072) : "memory");
    }
    SB();
    if constexpr ((XTR >> 16) != 0) {
        if ((XTR >> 16) == 1 || b < 16) {
            u64 pc;
            asm volatile("s_getpc_b64 %0" : "=s"(pc));
            pc = (pc & ~255ull) + wave * 3072;
            asm volatile(DMA3("%0", "%1", "%2", "") "global_load_lds_dwordx4 %0, %1 offset:3072" :: "v"(lane * 16), "s"(pc), "s"(lds0 + LDS_PF + wave * RECW) : "memory");
        }
    }
    SB();
    const unsigned tdma = DBG ? now32() : 0;

    const int stag = (int)(((((b >> 3) * 4 + wave) * 37) & 63) * (TP / 2)) / 64 - TP / 4;
    unsigned sched = ((tstart - ADV) << 4) + stag - 4 * (TP / 2);
    unsigned vt = tstart << 4;
    int tru = -4;
    auto pace = [&]() {
        if constexpr (TP > 0) {
            unsigned tgt = sched;
            sched += TP / 2;
            if constexpr (TMIN > 0) { if ((int)(vt - tgt) > 0) tgt = vt; }
            unsigned t = now32() << 4;
            while ((int)(t - tgt) < 0) { __builtin_amdgcn_s_sleep(1); t = now32() << 4; }
            if constexpr (TMIN > 0) vt = ((int)(t - 128 - tgt) > 0 ? t - 128 : tgt) + TMIN / 2;
            if constexpr (DBG >= 2) { *reinterpret_cast<unsigned*>(smem + LDS_TR + 16 + wave * 1024 + (tru & 255) * 4) = (t - (sched - TP / 2)) >> 4; ++tru; }
        }
    };
    {
        unsigned dst = lds0 + LDS_CS + wave * RECW;
        const unsigned v0 = lane * 16 + row0 * (DH * 2);
#pragma unroll 1
        for (int j = 0; j < 4; ++j) {
            pace();
            asm volatile(DMA3("%0", "%1", "%2", " nt") :: "v"(v0), "s"((j < 2 ? a.k_cos : a.k_sin) + (j & 1) * rs2 * DH), "s"(dst) : "memory");
            dst += 3072;
        }
        asm volatile("s_mov_b32 m0, %0" :: "s"(keep));
    }
    const unsigned tq = DBG ? now32() : 0;
    if (tid < MAXH) *reinterpret_cast<unsigned*>(smem + LDS_CNT + tid * 4) = 0;
    const rsrc_t kr = mkrsrc(a.qk, H * hstride);
    const rsrc_t vr = mkrsrc(a.qv, H * hstride);
    const unsigned kvoff = r * (DH * 4) + c * 16;
    auto issue = [&](HG& g, rsrc_t rs, unsigned so) {
#pragma unroll
        for (int x = 0; x < 3; ++x) g.d[x] = bld128<2>(rs, kvoff + 256 * x, so);
    };
    HG buf[8];
#define PRE(g) SB(); pace(); issue(buf[g], kr, ((g) >> 2) * hstride + ROWK((g) & 3) * (DH * 4)); SB();
    PRE(0) PRE(1)
    asm volatile("s_waitcnt vmcnt(18) lgkmcnt(0)\n\ts_barrier" ::: "memory");
    const unsigned tbar = DBG ? now32() : 0;
    PRE(2)
    const unsigned* qt = reinterpret_cast<const unsigned*>(smem + LDS_QT + wave * RECW + qoff);
    unsigned qc0 = qt[0], qc1 = qt[HALF / 2], qs0 = qt[DH / 2], qs1 = qt[(DH + HALF) / 2];
    const unsigned char* qraw = smem + LDS_QRAW + qoff;
    unsigned char* qrot = smem + LDS_Q + wave * (DH * 4) + 2 * qoff;
    auto rotq = [&]() {
        const unsigned q_lo = *reinterpret_cast<const unsigned*>(qraw), q_hi = *reinterpret_cast<const unsigned*>(qraw + HALF * 2);
        qraw += DH * 2;
        asm volatile("" : "+v"(qc0), "+v"(qc1), "+v"(qs0), "+v"(qs1));
        const f32x2 lo = f32x2{bflo(q_lo), bfhi(q_lo)}, hi = f32x2{bflo(q_hi), bfhi(q_hi)};
        const f32x2 y1 = lo * f32x2{bflo(qc0), bfhi(qc0)}, y2 = -hi * f32x2{bflo(qs0), bfhi(qs0)};
        const f32x2 y3 = hi * f32x2{bflo(qc1), bfhi(qc1)}, y4 = lo * f32x2{bflo(qs1), bfhi(qs1)};
        const unsigned x1 = pk_bf16(y1[0], y1[1]), x2 = pk_bf16(y2[0], y2[1]), x3 = pk_bf16(y3[0], y3[1]), x4 = pk_bf16(y4[0], y4[1]);
        const f32x2 z1 = f32x2{bflo(x1), bfhi(x1)} + f32x2{bflo(x2), bfhi(x2)}, z2 = f32x2{bflo(x3), bfhi(x3)} + f32x2{bflo(x4), bfhi(x4)};
        const unsigned l2 = pk_bf16(z1[0], z1[1]), h2 = pk_bf16(z2[0], z2[1]);
        *reinterpret_cast<f32x2*>(qrot) = f32x2{bflo(l2), bfhi(l2)};
        *reinterpret_cast<f32x2*>(qrot + HALF * 4) = f32x2{bflo(h2), bfhi(h2)};
        asm volatile("" ::: "memory");
    };
    f32x4 qa[3], qb[3];
    const unsigned char* qh = smem + LDS_Q + wave * (DH * 4) + 16 * c;
    const unsigned char* qh1 = qh + (c < 8 ? 64 + HALF : 64 - HALF) * 4;
    auto loadq = [&]() {
        asm volatile("" ::: "memory");
#pragma unroll
        for (int x = 0; x < 3; ++x) qa[x] = *reinterpret_cast<const f32x4*>(qh + 256 * x);
        qb[0] = *reinterpret_cast<const f32x4*>(qh + HALF * 4);
        qb[1] = *reinterpret_cast<const f32x4*>(qh1);
        qb[2] = *reinterpret_cast<const f32x4*>(qh + (128 - HALF) * 4);
        asm volatile("" ::: "memory");
    };
    rotq();
    PRE(3)
    loadq();
    PRE(4) PRE(5) PRE(6)
    asm volatile("s_waitcnt vmcnt(21)" ::: "memory");
    u32x2 cc[4][3], ss[4][3];
    {
        const unsigned char* cb = smem + LDS_CS + wave * RECW + r * (DH * 2) + c * 8;
        const unsigned char* sb1 = cb + (c < 8 ? (64 + HALF) * 2 : (64 - HALF) * 2);
        const unsigned sg = c < 8 ? 0u : 0x80008000u;
#pragma unroll
        for (int k = 0; k < 4; ++k) {
#pragma unroll
            for (int x = 0; x < 3; ++x) cc[k][x] = *reinterpret_cast<const u32x2*>(cb + k * 1536 + 128 * x);
            ss[k][0] = *reinterpret_cast<const u32x2*>(cb + 6144 + k * 1536 + HALF * 2);
            ss[k][1] = *reinterpret_cast<const u32x2*>(sb1 + 6144 + k * 1536) ^ u32x2{sg, sg};
            ss[k][2] = *reinterpret_cast<const u32x2*>(cb + 6144 + k * 1536 + (128 - HALF) * 2) ^ u32x2{0x80008000u, 0x80008000u};
            asm volatile("" : "+v"(cc[k][0]), "+v"(cc[k][1]), "+v"(cc[k][2]), "+v"(ss[k][0]), "+v"(ss[k][1]), "+v"(ss[k][2]));
        }
    }
    PRE(7)
    unsigned th1 = 0, th4 = 0;
    const unsigned char* scp = smem + LDS_SC + (c & 4) * 1024 + (c >> 3) * 128 + (MAP == 12 ? 16 * wave + 4 * (c & 3) + r : (c & 2) * 16 + 8 * wave + (c & 1) * 4 + r) * 2;
    unsigned sc = *reinterpret_cast<const unsigned short*>(scp);
    unsigned char* prec = smem + LDS_P + wave * 4096 + lane * 4;
    const int ck = c & 11;
    auto softmax = [&](float s, int i) {
        float m = fmaxf(s, dpp<0xb1>(s));
        m = xrow_max(fmaxf(m, dpp<0x4e>(m)));
        const float p = __builtin_amdgcn_exp2f(s - m);
        float l = p + dpp<0xb1>(p);
        l = xrow_sum(l + dpp<0x4e>(l));
        const float t = (c & 1) ? l : m;
        const int x = __builtin_amdgcn_update_dpp(__float_as_int(p), __float_as_int(t), 0x114, 0xf, 0xa, false);
        *reinterpret_cast<int*>(prec + i * 256) = x;
    };
    float sprev = 0.f;

#pragma unroll 1
    for (int i = 0; i < H / 2; ++i) {
        const bool more = i + 1 < H / 2;
        const rsrc_t nx = more ? kr : vr;
        const unsigned nso = more ? (2 * i + 2) * hstride : 0;
        float scf = bflo(sc);
        asm volatile("" : "+v"(scf) :: "memory");
        SB();
        scp += more ? 256 : 4096 + 256;
        sc = *reinterpret_cast<const unsigned short*>(scp);
        if ((XTR & 0xffff) && !more) sched += XTR & 0xffff;
        SB();
        float sreg = 0.f;
#define KSTEP(g) { \
            constexpr int k = (g) & 3, hh = (g) >> 2; \
            const float kz = dpp<0x154 + 8 * hh + k>(scf); \
            f32x2 kz2 = f32x2{kz, kz}; \
            asm("" : "+v"(kz2)); \
            f32x2 a2 = f32x2{0.f, 0.f}; \
            asm volatile("" : "+v"(cc[k][0]), "+v"(cc[k][1]), "+v"(cc[k][2]), "+v"(ss[k][0]), "+v"(ss[k][1]), "+v"(ss[k][2])); \
            _Pragma("unroll") for (int x = 0; x < 3; ++x) { \
                _Pragma("unroll") for (int e = 0; e < 2; ++e) { \
                    const f32x2 w = __builtin_elementwise_fma(f32x2{qb[x][2 * e], qb[x][2 * e + 1]}, f32x2{bflo(ss[k][x][e]), bfhi(ss[k][x][e])}, \
                                                              f32x2{qa[x][2 * e], qa[x][2 * e + 1]} * f32x2{bflo(cc[k][x][e]), bfhi(cc[k][x][e])}); \
                    const f32x2 kf = f32x2{(float)buf[g].d[x][2 * e], (float)buf[g].d[x][2 * e + 1]} - kz2; \
                    a2 = __builtin_elementwise_fma(kf, w, a2); \
                } \
            } \
            float sa = a2[0] + a2[1]; \
            asm volatile("" : "+v"(sa) :: "memory"); \
            SB(); pace(); issue(buf[g], nx, nso + hh * hstride + ROWK(k) * (DH * 4)); SB(); \
            if ((g) == 0) { softmax(sprev, (i - 1) & 15); SB(); }   \
            if (k == 1) { rotq(); SB(); } \
            if (k == 3) { loadq(); SB(); } \
            sa = row16_allsum(sa); \
            sreg = ck == 8 * hh + k ? sa : sreg; }
        KSTEP(0) KSTEP(1) KSTEP(2) KSTEP(3) KSTEP(4) KSTEP(5) KSTEP(6) KSTEP(7)
        sprev = sreg * (scf * a.scale_log2);
        if (DBG) { if (i == 3) th1 = now32(); }
    }
    softmax(sprev, H / 2 - 1);
    if (DBG) th4 = now32();

    const rsrc_t nr = mkrsrc(a.qk, 0);
    const unsigned pml = H * nb * (DH * 2);
    const rsrc_t pr = mkrsrc(a.part, pml + H * nb * 8);
    auto merge = [&](int hd, bool direct) {
        unsigned char* rec = smem + LDS_REC + hd * (DH * 4);
        const unsigned char* mlp = smem + LDS_P + (hd >> 1) * 256 + (hd & 1) * 32 + 16;
        f32x4 ow[4]; f32x2 ml[4];
#pragma unroll
        for (int w = 0; w < 4; ++w) {
            ml[w] = *reinterpret_cast<const f32x2*>(mlp + w * 4096);
            ow[w] = *reinterpret_cast<const f32x4*>(rec + w * RECW + lane * 16);
        }
        const float M = fmaxf(fmaxf(ml[0][0], ml[1][0]), fmaxf(ml[2][0], ml[3][0]));
        f32x4 t = f32x4{0.f, 0.f, 0.f, 0.f};
        float L = 0.f;
#pragma unroll
        for (int w = 0; w < 4; ++w) {
            const float f = __builtin_amdgcn_exp2f(ml[w][0] - M);
            t += f * ow[w];
            L += f * ml[w][1];
        }
        u32x2 v = u32x2{pk_bf16(t[0], t[1]), pk_bf16(t[2], t[3])};
        if (r == 3) v = u32x2{__float_as_uint(M), __float_as_uint(L)};
        if (direct) {
            int lane = tid & 63;
            asm volatile("" : "+v"(lane));
            const unsigned mo = pml + (MLL ? b * H + hd : hd * nb + b) * 8;
            const unsigned sto = lane < 48 ? (hd * nb + b) * (DH * 2) + lane * 8 : lane == 48 ? mo : 0x80000000u;
            __builtin_amdgcn_raw_buffer_store_b64(v, pr, sto, 0, SAUX);
        } else *reinterpret_cast<u32x2*>(rec + lane * 8) = v;
    };
    unsigned old = 0;
    float pv = *reinterpret_cast<const float*>(prec);
#pragma unroll 1
    for (int i = 0; i < H / 2; ++i) {
        const bool more = i + 1 < H / 2;
        const rsrc_t nx = more ? vr : nr;
        const unsigned nso = (2 * i + 2) * hstride;
        float y = bflo(sc);
        y = (c & 4) ? y : y * pv;
        asm volatile("" : "+v"(y) :: "memory");
        SB();
        scp += 256;
        sc = *reinterpret_cast<const unsigned short*>(scp);
        prec += 256;
        pv = *reinterpret_cast<const float*>(prec);
        SB();
        if (!more) {
            int lane = tid & 63;
            asm volatile("s_waitcnt lgkmcnt(0)\n\ts_barrier" : "+v"(lane) :: "memory");
            u32x4 v[3];
#pragma unroll
            for (int p = 0; p < 3; ++p) {
                const int t = lane + 64 * p, hi_ = t / 24, k = t - hi_ * 24;
                v[p] = *reinterpret_cast<const u32x4*>(smem + LDS_REC + (8 * wave + hi_) * (DH * 4) + k * 16);
            }
            const u32x2 w = *reinterpret_cast<const u32x2*>(smem + LDS_REC + (8 * wave + (lane & 7)) * (DH * 4) + 384);
#pragma unroll
            for (int p = 0; p < 3; ++p) {
                const int t = lane + 64 * p, hi_ = t / 24, k = t - hi_ * 24;
                const int hh = 8 * wave + hi_;
                __builtin_amdgcn_raw_buffer_store_b128(v[p], pr, hh < H - 3 ? (hh * nb + b) * (DH * 2) + k * 16 : 0x80000000u, 0, SAUX);
            }
            const int hh = 8 * wave + lane;
            __builtin_amdgcn_raw_buffer_store_b64(w, pr, lane < 8 && hh < H - 3 ? pml + (MLL ? b * H + hh : hh * nb + b) * 8 : 0x80000000u, 0, SAUX);
        }
        SB();
        f32x2 acc[3][2];
#define VSTEP(g) { \
            constexpr int k = (g) & 3, hh = (g) >> 2; \
            const float pw = dpp<0x150 + 8 * hh + k>(y); \
            const float vz = dpp<0x154 + 8 * hh + k>(y); \
            f32x2 pw2 = f32x2{pw, pw}, vz2 = f32x2{vz, vz}; \
            asm("" : "+v"(pw2), "+v"(vz2)); \
            _Pragma("unroll") for (int x = 0; x < 3; ++x) \
                _Pragma("unroll") for (int e = 0; e < 2; ++e) { \
                    const f32x2 vf = f32x2{(float)buf[g].d[x][2 * e], (float)buf[g].d[x][2 * e + 1]} - vz2; \
                    acc[x][e] = k == 0 ? pw2 * vf : __builtin_elementwise_fma(pw2, vf, acc[x][e]); \
                } \
            asm volatile("" : "+v"(acc[0][0]), "+v"(acc[0][1]), "+v"(acc[1][0]), "+v"(acc[1][1]), "+v"(acc[2][0]), "+v"(acc[2][1]) :: "memory"); \
            SB(); if (more) pace(); issue(buf[g], nx, nso + hh * hstride + ROWK(k) * (DH * 4)); SB(); \
            if (k == 0) { \
                asm volatile("s_waitcnt lgkmcnt(0)" : "+v"(old) :: "memory"); \
                if (__builtin_amdgcn_readfirstlane(old) == 3) merge(2 * i + hh - 1, !more); \
                SB(); \
            } \
            if (k == 3) {        \
                f32x4 o; \
                _Pragma("unroll") for (int e = 0; e < 4; ++e) { \
                    const float t0 = sw32(acc[0][e >> 1][e & 1], acc[2][e >> 1][e & 1]); \
                    const float t1 = sw32(acc[1][e >> 1][e & 1], acc[1][e >> 1][e & 1]); \
                    o[e] = sw16(t0, t1); \
                } \
                  \
                asm volatile("s_mov_b64 exec, %5\n\tds_write_b128 %1, %2\n\ts_mov_b64 exec, 1\n\tds_add_rtn_u32 %0, %3, %4\n\ts_mov_b64 exec, -1" \
                             : "+v"(old) : "v"(lds0 + LDS_REC + (2 * i + hh) * (DH * 4) + wave * RECW + lane * 16), "v"(o), \
                               "v"(lds0 + LDS_CNT + (2 * i + hh) * 4), "v"(1u), "s"(0xffffffffffffull) : "memory"); \
                SB(); \
            } }
        VSTEP(0) VSTEP(1) VSTEP(2) VSTEP(3) VSTEP(4) VSTEP(5) VSTEP(6) VSTEP(7)
    }
    asm volatile("s_waitcnt lgkmcnt(0)" : "+v"(old) :: "memory");
    if (__builtin_amdgcn_readfirstlane(old) == 3) merge(H - 1, true);
    if (DBG) {
        const unsigned tend = now32();
        asm volatile("s_waitcnt vmcnt(0)" ::: "memory");
        const unsigned tst = now32();
        u64* stp = reinterpret_cast<u64*>(a.part + 32 * 256 * 196 * 4) + (size_t)(b * 4 + wave) * 8;
        if (lane == 0) { stp[0] = tstart; stp[1] = tbar; stp[2] = DBG == 3 ? tdma : th1; stp[3] = DBG == 3 ? tq : th4; stp[4] = tend; stp[5] = tst; }
        if (DBG >= 2) *reinterpret_cast<u32x4*>(a.part + 32 * 256 * 196 * 4 + 65536 + (b * 4 + wave) * 1024 + lane * 16) = *reinterpret_cast<const u32x4*>(smem + LDS_TR + 16 + wave * 1024 + lane * 16);
    }
}

template <int MLL, int DBG>
__global__ __launch_bounds__(256) void kv9_combine(const unsigned char* __restrict__ part, bf16* __restrict__ out)
{
    constexpr int NB = 256, H = MAXH;
    const unsigned tc0 = DBG ? now32() : 0;
    __shared__ float s_f[NB];
    __shared__ float s_red[8];
    __shared__ __attribute__((aligned(16))) float s_o[4 * 32];
    const int chunk = blockIdx.x, h = blockIdx.y;
    const int tid = threadIdx.x, lane = tid & 63, wave = tid >> 6;
    const unsigned char* po = part + (size_t)h * NB * (DH * 2) + chunk * 64 + (tid & 3) * 16;
    const f32x2 ml = *reinterpret_cast<const f32x2*>(part + H * NB * (DH * 2) + (MLL ? tid * H + h : h * NB + tid) * 8);
    u32x4 v[4];
#pragma unroll
    for (int i = 0; i < 4; ++i) v[i] = *reinterpret_cast<const u32x4*>(po + ((tid >> 2) + 64 * i) * (DH * 2));
    float mw = xrow_max(row16_allmax(ml[0]));
    if (lane == 0) s_red[wave] = mw;
    __syncthreads();
    const float M = fmaxf(fmaxf(s_red[0], s_red[1]), fmaxf(s_red[2], s_red[3]));
    const float f = __builtin_amdgcn_exp2f(ml[0] - M);
    s_f[tid] = f;
    const float lw = xrow_sum(row16_allsum(f * ml[1]));
    if (lane == 0) s_red[4 + wave] = lw;
    __syncthreads();
    float o[8];
#pragma unroll
    for (int e = 0; e < 8; ++e) o[e] = 0.f;
#pragma unroll
    for (int i = 0; i < 4; ++i) {
        const float fs = s_f[(tid >> 2) + 64 * i];
#pragma unroll
        for (int e = 0; e < 4; ++e) { o[2 * e] += fs * bflo(v[i][e]); o[2 * e + 1] += fs * bfhi(v[i][e]); }
    }
#pragma unroll
    for (int e = 0; e < 8; ++e) { float t = o[e]; t += dpp<0x128>(t); t += dpp<0x124>(t); o[e] = xrow_sum(t); }
    if (lane < 4) {
        *reinterpret_cast<f32x4*>(&s_o[wave * 32 + lane * 8]) = f32x4{o[0], o[1], o[2], o[3]};
        *reinterpret_cast<f32x4*>(&s_o[wave * 32 + lane * 8 + 4]) = f32x4{o[4], o[5], o[6], o[7]};
    }
    __syncthreads();
    if (tid < 32) {
        const float L = (s_red[4] + s_red[5]) + (s_red[6] + s_red[7]);
        const float t = (s_o[tid] + s_o[32 + tid]) + (s_o[64 + tid] + s_o[96 + tid]);
        out[h * DH + chunk * 32 + tid] = __float2bfloat16(t / L);
    }
    if (DBG && tid == 0) {
        u64* stp = reinterpret_cast<u64*>(const_cast<unsigned char*>(part) + 32 * 256 * 196 * 4) + (size_t)(h * 6 + chunk) * 8;
        stp[6] = tc0; stp[7] = now32();
    }
}

template <int TP, int ADV, int MAP, int TMIN, int SAUX, int MLL, int XTR, int DBG>
static void launch17(void* const* d_in, const int* in_sizes, void* d_out, void* d_ws, hipStream_t stream, bool comb = true) {
    typedef const unsigned short* P;
    Args a;
    a.qk = (const int*)d_in[1]; a.qv = (const int*)d_in[4];
    a.k_cos = (P)d_in[9]; a.k_sin = (P)d_in[10];
    a.q = (P)d_in[0]; a.q_cos = (P)d_in[7]; a.q_sin = (P)d_in[8];
    a.ksc = (P)d_in[2]; a.kzp = (P)d_in[3]; a.vsc = (P)d_in[5]; a.vzp = (P)d_in[6];
    a.part = (unsigned char*)d_ws;
    a.Skv = in_sizes[9] / DH;
    a.scale_log2 = 1.4426950408889634f / sqrtf((float)DH);
    kv17_kernel<TP, ADV, MAP, TMIN, SAUX, MLL, XTR, DBG><<<a.Skv / RW, 256, 0, stream>>>(a);
    if (comb) kv9_combine<MLL, DBG><<<dim3(6, MAXH), 256, 0, stream>>>(a.part, (bf16*)d_out);
}

extern "C" void kernel_launch(void* const* d_in, const int* in_sizes, int n_in, void* d_out, int out_size, void* d_ws, size_t ws_size, hipStream_t stream) {
    launch17<80*16+8,-250,12,79*16,0,1,131072,0>(d_in, in_sizes, d_out, d_ws, stream);
}
```

```cpp
#include <hip/hip_runtime.h>
#include <hip/hip_bf16.h>
#include <cstdint>
using bf16 = __hip_bfloat16;
using i32x4 = __attribute__((ext_vector_type(4))) int;
using u32x4 = __attribute__((ext_vector_type(4))) unsigned;
using u32x2 = __attribute__((ext_vector_type(2))) unsigned;
using f32x2 = __attribute__((ext_vector_type(2))) float;
using f32x4 = __attribute__((ext_vector_type(4))) float;

constexpr int DH = 192;
constexpr int HALF = 96;
constexpr int RW = 64;
constexpr int MAXH = 32;

__device__ __forceinline__ float bflo(unsigned u) { return __uint_as_float(u << 16); }
__device__ __forceinline__ float bfhi(unsigned u) { return __uint_as_float(u & 0xffff0000u); }
__device__ __forceinline__ float rbf(float x) { return __bfloat162float(__float2bfloat16(x)); }

template <int CTRL> __device__ __forceinline__ float dpp(float x) {
    return __builtin_bit_cast(float, __builtin_amdgcn_mov_dpp(__builtin_bit_cast(int, x), CTRL, 0xf, 0xf, true));
}
__device__ __forceinline__ float row16_allsum(float x) {
    x += dpp<0x128>(x);
    x += dpp<0x124>(x);
    x += dpp<0x122>(x);
    x += dpp<0x121>(x);
    return x;
}
__device__ __forceinline__ float row16_allmax(float x) {
    x = fmaxf(x, dpp<0x128>(x));
    x = fmaxf(x, dpp<0x124>(x));
    x = fmaxf(x, dpp<0x122>(x));
    x = fmaxf(x, dpp<0x121>(x));
    return x;
}
__device__ __forceinline__ float xrow_max(float x) {
    auto s = __builtin_amdgcn_permlane16_swap(__float_as_uint(x), __float_as_uint(x), false, false);
    x = fmaxf(__uint_as_float(s[0]), __uint_as_float(s[1]));
    auto t = __builtin_amdgcn_permlane32_swap(__float_as_uint(x), __float_as_uint(x), false, false);
    return fmaxf(__uint_as_float(t[0]), __uint_as_float(t[1]));
}
__device__ __forceinline__ float xrow_sum(float x) {
    auto s = __builtin_amdgcn_permlane16_swap(__float_as_uint(x), __float_as_uint(x), false, false);
    x = __uint_as_float(s[0]) + __uint_as_float(s[1]);
    auto t = __builtin_amdgcn_permlane32_swap(__float_as_uint(x), __float_as_uint(x), false, false);
    return __uint_as_float(t[0]) + __uint_as_float(t[1]);
}
__device__ __forceinline__ float sw32(float a, float b) {
    auto t = __builtin_amdgcn_permlane32_swap(__float_as_uint(a), __float_as_uint(b), false, false);
    return __uint_as_float(t[0]) + __uint_as_float(t[1]);
}
__device__ __forceinline__ float sw16(float a, float b) {
    auto t = __builtin_amdgcn_permlane16_swap(__float_as_uint(a), __float_as_uint(b), false, false);
    return __uint_as_float(t[0]) + __uint_as_float(t[1]);
}

#define SB() __builtin_amdgcn_sched_barrier(0)
typedef __amdgpu_buffer_rsrc_t rsrc_t;
__device__ __forceinline__ rsrc_t mkrsrc(const void* p, unsigned bytes) {
    return __builtin_amdgcn_make_buffer_rsrc(const_cast<void*>(p), 0, bytes, 0x00020000);
}
template <int AUX> __device__ __forceinline__ i32x4 bld128(rsrc_t r, unsigned voff, unsigned soff) {
    return __builtin_bit_cast(i32x4, __builtin_amdgcn_raw_buffer_load_b128(r, voff, soff, AUX));
}
template <int AUX> __device__ __forceinline__ unsigned bld16(rsrc_t r, unsigned voff, unsigned soff) {
    return __builtin_amdgcn_raw_buffer_load_b16(r, voff, soff, AUX);
}

struct HG { i32x4 d[3]; };
typedef unsigned long long u64;
typedef __attribute__((address_space(3))) unsigned char lds_u8;

__device__ __forceinline__ unsigned pk_bf16(float lo, float hi) {
    unsigned r;
    asm("v_cvt_pk_bf16_f32 %0, %1, %2" : "=v"(r) : "v"(lo), "v"(hi));
    return r;
}
__device__ __forceinline__ unsigned now32() { return (unsigned)__builtin_amdgcn_s_memrealtime(); }

constexpr int LDS_REC = 0;
constexpr int RECW = MAXH * DH * 4;
constexpr int LDS_CS = 0;
constexpr int LDS_QT = 12288;
constexpr int LDS_LT = 13312;
constexpr int LDS_PF = 14336;
constexpr int LDS_QRAW = MAXH * 4 * DH * 4;
constexpr int LDS_Q = LDS_QRAW + MAXH * DH * 2;
constexpr int LDS_P = LDS_Q + 4 * DH * 4;
constexpr int LDS_SC = LDS_P + 4 * 16 * 256;
constexpr int LDS_CNT = LDS_SC + 4 * MAXH * RW * 2;
constexpr int LDS_TR = LDS_CNT + 512;
constexpr int LDS_BYTES = LDS_TR + 4096 + 64;

struct Args {
    const int* qk; const int* qv;
    const unsigned short* k_cos; const unsigned short* k_sin;
    const unsigned short* q; const unsigned short* q_cos; const unsigned short* q_sin;
    const unsigned short* ksc; const unsigned short* kzp; const unsigned short* vsc; const unsigned short* vzp;
    unsigned char* part;
    unsigned short* out;
    int Skv; float scale_log2;
};
template <int AUX> __device__ __forceinline__ u32x2 bld64(rsrc_t r, unsigned voff, unsigned soff) {
    return __builtin_bit_cast(u32x2, __builtin_amdgcn_raw_buffer_load_b64(r, voff, soff, AUX));
}

template <int TP, int ADV, int MAP, int TMIN, int ENDQ, int WIN, int SLP, int PFW, int ID, int DBG>
__global__ __launch_bounds__(256) void kv22_kernel(Args a)
{
    __shared__ __attribute__((aligned(16))) unsigned char smem[LDS_BYTES];
    const unsigned tstart = DBG ? now32() : 0;
    constexpr int H = MAXH;
    const int b = blockIdx.x;
    const int tid = threadIdx.x;
    const int lane = tid & 63;
    const int wave = __builtin_amdgcn_readfirstlane(tid >> 6);
    const int r = lane >> 4;
    const int c = lane & 15;
    const unsigned Skv = a.Skv;
    const unsigned hstride = Skv * (DH * 4);
    const unsigned nb = Skv / RW;
    const unsigned lds0 = (unsigned)(size_t)(lds_u8*)smem;
    const unsigned pos = (b & 7) * (nb >> 3) + (b >> 3);
    const unsigned row0 = MAP == 12 ? 64 * pos + 16 * wave : MAP == 14 ? 32 * pos + 8 * wave : 16 * pos + 4 * wave;
    const unsigned rs1 = MAP == 7 ? Skv / 4 : 4;
    const unsigned rs2 = MAP == 12 ? 8 : Skv / 2;
#define ROWK(k) (row0 + ((k) & 1) * rs1 + ((k) >> 1) * rs2)

    asm volatile("" :: "s"(a.qk), "s"(a.qv), "s"(a.k_cos), "s"(a.k_sin), "s"(a.q_cos), "s"(a.q_sin), "s"(a.ksc), "s"(a.kzp), "s"(a.vsc), "s"(a.vzp), "s"(a.part), "s"(a.out), "s"(a.Skv), "s"(a.scale_log2));
    constexpr int FS = 16;
    constexpr int NL = 3;
    const unsigned plt = H * nb * (DH * 2), pfl = plt + NL * nb * 1024, pep = pfl + H * nb * FS;
    const rsrc_t pr = mkrsrc(a.part, pep);
#define DMA3(V, A, B, P) "s_mov_b32 m0, " B "\n\ts_nop 0\n\t" \
                     "global_load_lds_dwordx4 " V ", " A P "\n\t" \
                     "global_load_lds_dwordx4 " V ", " A " offset:1024" P "\n\t" \
                     "global_load_lds_dwordx4 " V ", " A " offset:2048" P "\n\t"
    unsigned keep;
    const unsigned qoff = (lane < 48 ? lane : lane - 48) * 4;
    asm volatile("s_mov_b32 %0, m0\n\t" DMA3("%1", "%2", "%3", "") "s_mov_b32 m0, %5\n\ts_nop 0\n\tglobal_load_lds_dwordx4 %4, off"
                 : "=&s"(keep) : "v"(lane * 16), "s"(a.q + wave * 1536), "s"(lds0 + LDS_QRAW + wave * 3072),
                   "v"(lane < 48 ? (const unsigned char*)((lane < 24 ? a.q_cos : a.q_sin - DH) + 8 * lane) : a.part + pep), "s"(lds0 + LDS_QT + wave * RECW) : "memory");
    SB();
    {
        const unsigned short* sp = wave == 0 ? a.ksc : wave == 1 ? a.kzp : wave == 2 ? a.vsc : a.vzp;
        const unsigned vs = ((lane >> 3) * Skv + (MAP == 12 ? 64 * pos + (lane & 7) * 8 : 32 * pos + ((lane >> 2) & 1) * rs2 + (lane & 3) * 8)) * 2;
        const unsigned ds = lds0 + LDS_SC + wave * 4096;
#define DMA1(A, B) "s_mov_b32 m0, " B "\n\ts_nop 0\n\tglobal_load_lds_dwordx4 %0, " A " nt\n\t"
        asm volatile(DMA1("%1", "%5") DMA1("%2", "%6") DMA1("%3", "%7") DMA1("%4", "%8")
                     :: "v"(vs), "s"(sp), "s"(sp + 8 * Skv), "s"(sp + 16 * Skv), "s"(sp + 24 * Skv), "s"(ds), "s"(ds + 1024), "s"(ds + 2048), "s"(ds + 3072) : "memory");
    }
    SB();
    const unsigned ltoff = plt + (min(wave, NL - 1) * nb + b) * 1024 + min(lane, 48) * 16;
    if (PFW & 1) asm volatile("s_mov_b32 m0, %2\n\ts_nop 0\n\tglobal_load_lds_dwordx4 %0, %1" :: "v"(ltoff), "s"(a.part), "s"(lds0 + LDS_LT + wave * RECW) : "memory");
    else asm volatile("s_mov_b32 m0, %2\n\ts_nop 0\n\tglobal_load_lds_dwordx4 %0, %1 nt" :: "v"(ltoff), "s"(a.part), "s"(lds0 + LDS_LT + wave * RECW) : "memory");
    SB();
    if (PFW & 2) asm volatile("s_mov_b32 m0, %2\n\ts_nop 0\n\tglobal_load_lds_dwordx4 %0, %1" :: "v"(pfl + ((8 * wave + (lane & 7)) * nb + b) * FS), "s"(a.part), "s"(lds0 + LDS_PF + wave * RECW) : "memory");
    if (PFW & 4) asm volatile("s_mov_b32 m0, %2\n\ts_nop 0\n\tglobal_load_lds_dwordx4 %0, %1" :: "v"(((8 * wave + (lane & 7)) * nb + b) * (DH * 2) + min(lane >> 3, 5) * 64), "s"(a.part), "s"(lds0 + LDS_PF + wave * RECW) : "memory");
    SB();
    if (b < 16) {
        u64 pc; unsigned len;
        asm volatile("s_getpc_b64 %0\n.Lkv_a%c2:\n\ts_mov_b32 %1, .Lkv_b%c2-.Lkv_a%c2" : "=s"(pc), "=s"(len) : "i"(ID));
        pc &= ~63ull;
        len &= ~15u;
        const unsigned o = wave * 1024 + lane * 16;
        asm volatile("s_mov_b32 m0, %6\n\ts_nop 0\n\tglobal_load_lds_dwordx4 %0, %5\n\tglobal_load_lds_dwordx4 %1, %5\n\tglobal_load_lds_dwordx4 %2, %5\n\t"
                     "global_load_lds_dwordx4 %3, %5\n\tglobal_load_lds_dwordx4 %4, %5"
                     :: "v"(min(o, len)), "v"(min(o + 4096, len)), "v"(min(o + 8192, len)), "v"(min(o + 12288, len)), "v"(min(o + 16384, len)), "s"(pc), "s"(lds0 + LDS_PF + wave * RECW) : "memory");
    }
    SB();
    const unsigned tdma = now32();

    const int stag = (int)(((((b >> 3) * 4 + wave) * 37) & 63) * (TP / 2)) / 64 - TP / 4;
    unsigned sched = ((tdma - ADV) << 4) + stag - 4 * (TP / 2);
    unsigned vt = tdma << 4;
    int tru = -4;
    unsigned step = TP / 2, tmin = TMIN / 2;
    auto pace = [&]() {
        if constexpr (TP > 0) {
            unsigned tgt = sched;
            sched += step;
            if constexpr (TMIN > 0) { if ((int)(vt - tgt) > 0) tgt = vt; }
            if constexpr (WIN > 0) asm volatile("s_waitcnt vmcnt(%0)" :: "i"(3 * (WIN - 1)) : "memory");
            unsigned t = now32() << 4;
            while ((int)(t - tgt) < 0) { __builtin_amdgcn_s_sleep(1); t = now32() << 4; }
            if constexpr (TMIN > 0) vt = ((int)(t - 128 - tgt) > 0 ? t - 128 : tgt) + tmin;
            if constexpr (DBG == 2) { *reinterpret_cast<unsigned*>(smem + LDS_TR + 16 + wave * 1024 + (tru & 255) * 4) = (t - (sched - step)) >> 4; ++tru; }
        }
    };
    {
        unsigned dst = lds0 + LDS_CS + wave * RECW;
        const unsigned v0 = lane * 16 + row0 * (DH * 2);
#pragma unroll 1
        for (int j = 0; j < 4; ++j) {
            pace();
            asm volatile(DMA3("%0", "%1", "%2", " nt") :: "v"(v0), "s"((j < 2 ? a.k_cos : a.k_sin) + (j & 1) * rs2 * DH), "s"(dst) : "memory");
            dst += 3072;
        }
        asm volatile("s_mov_b32 m0, %0" :: "s"(keep));
    }
    const unsigned tq = DBG ? now32() : 0;
    if (tid < MAXH) *reinterpret_cast<unsigned*>(smem + LDS_CNT + tid * 4) = 0;
    const rsrc_t kr = mkrsrc(a.qk, H * hstride);
    const rsrc_t vr = mkrsrc(a.qv, H * hstride);
    const unsigned kvoff = r * (DH * 4) + c * 16;
    auto issue = [&](HG& g, rsrc_t rs, unsigned so) {
#pragma unroll
        for (int x = 0; x < 3; ++x) g.d[x] = bld128<2>(rs, kvoff + 256 * x, so);
    };
    HG buf[8];
#define PRE(g) SB(); pace(); issue(buf[g], kr, ((g) >> 2) * hstride + ROWK((g) & 3) * (DH * 4)); SB();
    PRE(0) PRE(1)
    asm volatile("s_waitcnt vmcnt(18) lgkmcnt(0)\n\ts_barrier" ::: "memory");
    unsigned tbar = DBG ? now32() : 0;
    PRE(2)
    const unsigned* qt = reinterpret_cast<const unsigned*>(smem + LDS_QT + wave * RECW + qoff);
    unsigned qc0 = qt[0], qc1 = qt[HALF / 2], qs0 = qt[DH / 2], qs1 = qt[(DH + HALF) / 2];
    u64 tag;
    {
        const u32x2 e = *reinterpret_cast<const u32x2*>(smem + LDS_QT + wave * RECW + 768);
        tag = (((u64)__builtin_amdgcn_readfirstlane(e[1]) << 32) | __builtin_amdgcn_readfirstlane(e[0])) * 6364136223846793005ull + 1442695040888963407ull;
        asm volatile("" : "+s"(tag));
    }
    const unsigned t32 = tag >> 32;
    const unsigned char* qraw = smem + LDS_QRAW + qoff;
    unsigned char* qrot = smem + LDS_Q + wave * (DH * 4) + 2 * qoff;
    auto rotq = [&]() {
        const unsigned q_lo = *reinterpret_cast<const unsigned*>(qraw), q_hi = *reinterpret_cast<const unsigned*>(qraw + HALF * 2);
        qraw += DH * 2;
        asm volatile("" : "+v"(qc0), "+v"(qc1), "+v"(qs0), "+v"(qs1));
        const f32x2 lo = f32x2{bflo(q_lo), bfhi(q_lo)}, hi = f32x2{bflo(q_hi), bfhi(q_hi)};
        const f32x2 y1 = lo * f32x2{bflo(qc0), bfhi(qc0)}, y2 = -hi * f32x2{bflo(qs0), bfhi(qs0)};
        const f32x2 y3 = hi * f32x2{bflo(qc1), bfhi(qc1)}, y4 = lo * f32x2{bflo(qs1), bfhi(qs1)};
        const unsigned x1 = pk_bf16(y1[0], y1[1]), x2 = pk_bf16(y2[0], y2[1]), x3 = pk_bf16(y3[0], y3[1]), x4 = pk_bf16(y4[0], y4[1]);
        const f32x2 z1 = f32x2{bflo(x1), bfhi(x1)} + f32x2{bflo(x2), bfhi(x2)}, z2 = f32x2{bflo(x3), bfhi(x3)} + f32x2{bflo(x4), bfhi(x4)};
        const unsigned l2 = pk_bf16(z1[0], z1[1]), h2 = pk_bf16(z2[0], z2[1]);
        *reinterpret_cast<f32x2*>(qrot) = f32x2{bflo(l2), bfhi(l2)};
        *reinterpret_cast<f32x2*>(qrot + HALF * 4) = f32x2{bflo(h2), bfhi(h2)};
        asm volatile("" ::: "memory");
    };
    f32x4 qa[3], qb[3];
    const unsigned char* qh = smem + LDS_Q + wave * (DH * 4) + 16 * c;
    const unsigned char* qh1 = qh + (c < 8 ? 64 + HALF : 64 - HALF) * 4;
    auto loadq = [&]() {
        asm volatile("" ::: "memory");
#pragma unroll
        for (int x = 0; x < 3; ++x) qa[x] = *reinterpret_cast<const f32x4*>(qh + 256 * x);
        qb[0] = *reinterpret_cast<const f32x4*>(qh + HALF * 4);
        qb[1] = *reinterpret_cast<const f32x4*>(qh1);
        qb[2] = *reinterpret_cast<const f32x4*>(qh + (128 - HALF) * 4);
        asm volatile("" ::: "memory");
    };
    rotq();
    PRE(3)
    loadq();
    PRE(4)
    {
        const u32x4 e = *reinterpret_cast<const u32x4*>(smem + LDS_LT + wave * RECW + lane * 16);
        if (e[1] == t32 || e[3] == t32) __builtin_amdgcn_raw_buffer_store_b128(u32x4{0u, ~t32, 0u, ~t32}, pr, ltoff, 0, 16);
    }
    PRE(5) PRE(6)
    asm volatile("s_waitcnt vmcnt(21)" ::: "memory");
    u32x2 cc[4][3], ss[4][3];
    {
        const unsigned char* cb = smem + LDS_CS + wave * RECW + r * (DH * 2) + c * 8;
        const unsigned char* sb1 = cb + (c < 8 ? (64 + HALF) * 2 : (64 - HALF) * 2);
        const unsigned sg = c < 8 ? 0u : 0x80008000u;
#pragma unroll
        for (int k = 0; k < 4; ++k) {
#pragma unroll
            for (int x = 0; x < 3; ++x) cc[k][x] = *reinterpret_cast<const u32x2*>(cb + k * 1536 + 128 * x);
            ss[k][0] = *reinterpret_cast<const u32x2*>(cb + 6144 + k * 1536 + HALF * 2);
            ss[k][1] = *reinterpret_cast<const u32x2*>(sb1 + 6144 + k * 1536) ^ u32x2{sg, sg};
            ss[k][2] = *reinterpret_cast<const u32x2*>(cb + 6144 + k * 1536 + (128 - HALF) * 2) ^ u32x2{0x80008000u, 0x80008000u};
            asm volatile("" : "+v"(cc[k][0]), "+v"(cc[k][1]), "+v"(cc[k][2]), "+v"(ss[k][0]), "+v"(ss[k][1]), "+v"(ss[k][2]));
        }
    }
    PRE(7)
    unsigned th1 = 0, th4 = 0;
    const unsigned char* scp = smem + LDS_SC + (c & 4) * 1024 + (c >> 3) * 128 + (MAP == 12 ? 16 * wave + 4 * (c & 3) + r : (c & 2) * 16 + 8 * wave + (c & 1) * 4 + r) * 2;
    unsigned sc = *reinterpret_cast<const unsigned short*>(scp);
    unsigned char* prec = smem + LDS_P + wave * 4096 + lane * 4;
    const int ck = c & 11;
    auto softmax = [&](float s, int i) {
        float m = fmaxf(s, dpp<0xb1>(s));
        m = xrow_max(fmaxf(m, dpp<0x4e>(m)));
        const float p = __builtin_amdgcn_exp2f(s - m);
        float l = p + dpp<0xb1>(p);
        l = xrow_sum(l + dpp<0x4e>(l));
        const float t = (c & 1) ? l : m;
        const int x = __builtin_amdgcn_update_dpp(__float_as_int(p), __float_as_int(t), 0x114, 0xf, 0xa, false);
        *reinterpret_cast<int*>(prec + i * 256) = x;
    };
    float sprev = 0.f;
    const unsigned ck0 = DBG == 5 ? (unsigned)__builtin_amdgcn_s_memtime() : 0, rt0 = DBG == 5 ? now32() : 0;

#pragma unroll 1
    for (int i = 0; i < H / 2; ++i) {
        const bool more = i + 1 < H / 2;
        const rsrc_t nx = more ? kr : vr;
        const unsigned nso = more ? (2 * i + 2) * hstride : 0;
        float scf = bflo(sc);
        asm volatile("" : "+v"(scf) :: "memory");
        SB();
        scp += more ? 256 : 4096 + 256;
        sc = *reinterpret_cast<const unsigned short*>(scp);
        SB();
        float sreg = 0.f;
#define KSTEP(g) { \
            constexpr int k = (g) & 3, hh = (g) >> 2; \
            const float kz = dpp<0x154 + 8 * hh + k>(scf); \
            f32x2 kz2 = f32x2{kz, kz}; \
            asm("" : "+v"(kz2)); \
            f32x2 a2 = f32x2{0.f, 0.f}; \
            asm volatile("" : "+v"(cc[k][0]), "+v"(cc[k][1]), "+v"(cc[k][2]), "+v"(ss[k][0]), "+v"(ss[k][1]), "+v"(ss[k][2])); \
            _Pragma("unroll") for (int x = 0; x < 3; ++x) { \
                _Pragma("unroll") for (int e = 0; e < 2; ++e) { \
                    const f32x2 w = __builtin_elementwise_fma(f32x2{qb[x][2 * e], qb[x][2 * e + 1]}, f32x2{bflo(ss[k][x][e]), bfhi(ss[k][x][e])}, \
                                                              f32x2{qa[x][2 * e], qa[x][2 * e + 1]} * f32x2{bflo(cc[k][x][e]), bfhi(cc[k][x][e])}); \
                    const f32x2 kf = f32x2{(float)buf[g].d[x][2 * e], (float)buf[g].d[x][2 * e + 1]} - kz2; \
                    a2 = __builtin_elementwise_fma(kf, w, a2); \
                } \
            } \
            float sa = a2[0] + a2[1]; \
            asm volatile("" : "+v"(sa) :: "memory"); \
            SB(); pace(); issue(buf[g], nx, nso + hh * hstride + ROWK(k) * (DH * 4)); SB(); \
            if ((g) == 0) { softmax(sprev, (i - 1) & 15); SB(); }   \
            if (k == 1) { rotq(); SB(); } \
            if (k == 3) { loadq(); SB(); } \
            sa = row16_allsum(sa); \
            sreg = ck == 8 * hh + k ? sa : sreg; }
        KSTEP(0) KSTEP(1) KSTEP(2) KSTEP(3) KSTEP(4) KSTEP(5) KSTEP(6) KSTEP(7)
        sprev = sreg * (scf * a.scale_log2);
        if (DBG) { if (i == 3) th1 = now32(); }
        if (DBG == 5 && i == 3) tbar = tstart + ((unsigned)__builtin_amdgcn_s_memtime() - ck0) * 100 / (th1 - rt0);
    }
    softmax(sprev, H / 2 - 1);
    if (DBG) th4 = now32();

    const rsrc_t nr = mkrsrc(a.qk, 0);
    auto merge = [&](int hd, bool direct) {
        unsigned char* rec = smem + LDS_REC + hd * (DH * 4);
        const unsigned char* mlp = smem + LDS_P + (hd >> 1) * 256 + (hd & 1) * 32 + 16;
        f32x4 ow[4]; f32x2 ml[4];
#pragma unroll
        for (int w = 0; w < 4; ++w) {
            ml[w] = *reinterpret_cast<const f32x2*>(mlp + w * 4096);
            ow[w] = *reinterpret_cast<const f32x4*>(rec + w * RECW + lane * 16);
        }
        const float M = fmaxf(fmaxf(ml[0][0], ml[1][0]), fmaxf(ml[2][0], ml[3][0]));
        f32x4 t = f32x4{0.f, 0.f, 0.f, 0.f};
        float L = 0.f;
#pragma unroll
        for (int w = 0; w < 4; ++w) {
            const float f = __builtin_amdgcn_exp2f(ml[w][0] - M);
            t += f * ow[w];
            L += f * ml[w][1];
        }
        u32x2 v = u32x2{pk_bf16(t[0], t[1]), pk_bf16(t[2], t[3])};
        if (r == 3) v = u32x2{__float_as_uint(M), __float_as_uint(L)};
        if (direct) {
            int lane = tid & 63;
            asm volatile("" : "+v"(lane));
            __builtin_amdgcn_raw_buffer_store_b128(u32x4{v[0], t32, v[1], t32}, pr, lane <= ((PFW & 512) ? 55 : 48) ? lane * 16 : 0x80000000u, plt + ((hd - (H - NL)) * nb + b) * 1024, 16);
        } else *reinterpret_cast<u32x2*>(rec + lane * 8) = v;
    };
    unsigned old = 0, flo = 0x80000000u;
    u32x2 mlw = u32x2{0u, 0u};
    float pv = *reinterpret_cast<const float*>(prec);
#pragma unroll 1
    for (int i = 0; i < H / 2; ++i) {
        const bool more = i + 1 < H / 2;
        const rsrc_t nx = more ? vr : nr;
        const unsigned nso = (2 * i + 2) * hstride;
        if (ENDQ && i == H / 2 - 2) { step = ENDQ / 2; tmin = 0; }
        float y = bflo(sc);
        y = (c & 4) ? y : y * pv;
        asm volatile("" : "+v"(y) :: "memory");
        SB();
        scp += 256;
        sc = *reinterpret_cast<const unsigned short*>(scp);
        prec += 256;
        pv = *reinterpret_cast<const float*>(prec);
        SB();
        if (!more) {
            int lane = tid & 63;
            asm volatile("s_waitcnt lgkmcnt(0)\n\ts_barrier" : "+v"(lane) :: "memory");
            if (DBG == 4) tbar = now32();
            u32x4 v[3];
#pragma unroll
            for (int p = 0; p < 3; ++p) {
                const int t = lane + 64 * p, hi_ = t / 24, k = t - hi_ * 24;
                v[p] = *reinterpret_cast<const u32x4*>(smem + LDS_REC + (8 * wave + hi_) * (DH * 4) + k * 16);
            }
            mlw = *reinterpret_cast<const u32x2*>(smem + LDS_REC + (8 * wave + (lane & 7)) * (DH * 4) + 384);
#pragma unroll
            for (int p = 0; p < 3; ++p) {
                const int t = lane + 64 * p, hi_ = t / 24, k = t - hi_ * 24;
                const int hh = 8 * wave + hi_;
                __builtin_amdgcn_raw_buffer_store_b128(v[p], pr, hh < H - NL ? ((PFW & 256) ? b * H + hh : hh * nb + b) * (DH * 2) + k * 16 : 0x80000000u, 0, 16);
            }
            flo = lane < 8 ? pfl + ((PFW & 512) ? b * H + 8 * wave + lane : (8 * wave + lane) * nb + b) * FS : 0x80000000u;
        }
        SB();
        f32x2 acc[3][2];
#define VSTEP(g) { \
            constexpr int k = (g) & 3, hh = (g) >> 2; \
            const float pw = dpp<0x150 + 8 * hh + k>(y); \
            const float vz = dpp<0x154 + 8 * hh + k>(y); \
            f32x2 pw2 = f32x2{pw, pw}, vz2 = f32x2{vz, vz}; \
            asm("" : "+v"(pw2), "+v"(vz2)); \
            _Pragma("unroll") for (int x = 0; x < 3; ++x) \
                _Pragma("unroll") for (int e = 0; e < 2; ++e) { \
                    const f32x2 vf = f32x2{(float)buf[g].d[x][2 * e], (float)buf[g].d[x][2 * e + 1]} - vz2; \
                    acc[x][e] = k == 0 ? pw2 * vf : __builtin_elementwise_fma(pw2, vf, acc[x][e]); \
                } \
            asm volatile("" : "+v"(acc[0][0]), "+v"(acc[0][1]), "+v"(acc[1][0]), "+v"(acc[1][1]), "+v"(acc[2][0]), "+v"(acc[2][1]) :: "memory"); \
            SB(); if (more) pace(); issue(buf[g], nx, nso + hh * hstride + ROWK(k) * (DH * 4)); SB(); \
            if (k == 0) { \
                asm volatile("s_waitcnt lgkmcnt(0)" : "+v"(old) :: "memory"); \
                if (__builtin_amdgcn_readfirstlane(old) == 3) merge(2 * i + hh - 1, !more); \
                SB(); \
            } \
            if ((g) == 7 && !more) {        \
                asm volatile("s_waitcnt vmcnt(24)" ::: "memory"); \
                __builtin_amdgcn_raw_buffer_store_b128(u32x4{mlw[0], t32, mlw[1], (unsigned)tag}, pr, flo, 0, 16); \
                if (DBG == 4) th1 = now32(); \
                SB(); \
            } \
            if (k == 3) {        \
                f32x4 o; \
                _Pragma("unroll") for (int e = 0; e < 4; ++e) { \
                    const float t0 = sw32(acc[0][e >> 1][e & 1], acc[2][e >> 1][e & 1]); \
                    const float t1 = sw32(acc[1][e >> 1][e & 1], acc[1][e >> 1][e & 1]); \
                    o[e] = sw16(t0, t1); \
                } \
                  \
                asm volatile("s_mov_b64 exec, %5\n\tds_write_b128 %1, %2\n\ts_mov_b64 exec, 1\n\tds_add_rtn_u32 %0, %3, %4\n\ts_mov_b64 exec, -1" \
                             : "+v"(old) : "v"(lds0 + LDS_REC + (2 * i + hh) * (DH * 4) + wave * RECW + lane * 16), "v"(o), \
                               "v"(lds0 + LDS_CNT + (2 * i + hh) * 4), "v"(1u), "s"(0xffffffffffffull) : "memory"); \
                SB(); \
            } }
        VSTEP(0) VSTEP(1) VSTEP(2) VSTEP(3) VSTEP(4) VSTEP(5) VSTEP(6) VSTEP(7)
    }
    asm volatile("s_waitcnt lgkmcnt(0)" : "+v"(old) :: "memory");
    if (__builtin_amdgcn_readfirstlane(old) == 3) merge(H - 1, true);
    const unsigned tend = DBG ? now32() : 0;
    unsigned tfl = 0, tpl = 0;
    asm volatile("s_waitcnt vmcnt(0)" ::: "memory");
    if (DBG == 4) th4 = now32();
    if (b < 6 * H) {
        const int h = b / 6, chunk = b - 6 * h;
        const unsigned src = 64 * wave + lane;
        u32x2 ml, v[8];
#pragma unroll 1
        for (int n = 0; n < (1 << 20); ++n) {
            const i32x4 x = bld128<16>(pr, pfl + ((PFW & 512) ? src * H + h : h * nb + src) * FS, 0);
            ml = u32x2{(unsigned)x[0], (unsigned)x[2]};
            if ((unsigned)x[1] == t32 && (unsigned)x[3] == (unsigned)tag) break;
            asm volatile("" ::: "memory");
            __builtin_amdgcn_s_sleep(SLP);
        }
        asm volatile("" ::: "memory");
        if (DBG) tfl = now32();
        const unsigned p0 = 64 * wave + (lane >> 3);
        if (h < H - NL) {
#pragma unroll
            for (int i = 0; i < 8; ++i) v[i] = bld64<16>(pr, ((PFW & 256) ? p0 * H + h : h * nb + p0) * (DH * 2) + chunk * 64 + (lane & 7) * 8, i * (8 * DH * 2) * ((PFW & 256) ? H : 1));
        } else {
            const unsigned lb = plt + (h - (H - NL)) * nb * 1024;
#pragma unroll 1
            for (int n = 0; n < (1 << 20); ++n) {
                const i32x4 y = bld128<16>(pr, src * 1024 + 768, lb);
                i32x4 x[8];
#pragma unroll
                for (int i = 0; i < 8; ++i) x[i] = bld128<16>(pr, p0 * 1024 + chunk * 128 + (lane & 7) * 16, lb + i * 8192);
                unsigned bad = (y[1] ^ t32) | (y[3] ^ t32);
                ml = u32x2{(unsigned)y[0], (unsigned)y[2]};
#pragma unroll
                for (int i = 0; i < 8; ++i) { bad |= (x[i][1] ^ t32) | (x[i][3] ^ t32); v[i] = u32x2{(unsigned)x[i][0], (unsigned)x[i][2]}; }
                if (__all(bad == 0)) break;
                asm volatile("" ::: "memory");
            }
        }
        const float m = __uint_as_float(ml[0]);
        const float mw = xrow_max(row16_allmax(m));
        const float f = __builtin_amdgcn_exp2f(m - mw);
        const float lw = xrow_sum(row16_allsum(f * __uint_as_float(ml[1])));
        float o[4] = {0.f, 0.f, 0.f, 0.f};
#pragma unroll
        for (int i = 0; i < 8; ++i) {
            const float fs = __int_as_float(__builtin_amdgcn_ds_bpermute(((lane >> 3) + 8 * i) * 4, __float_as_int(f)));
#pragma unroll
            for (int e = 0; e < 2; ++e) { o[2 * e] += fs * bflo(v[i][e]); o[2 * e + 1] += fs * bfhi(v[i][e]); }
        }
        if (DBG) tpl = now32();
#pragma unroll
        for (int e = 0; e < 4; ++e) { float t = o[e]; t += dpp<0x128>(t); o[e] = xrow_sum(t); }
        float* s_o = reinterpret_cast<float*>(smem + LDS_QRAW);
        if (lane < 8) {
            *reinterpret_cast<f32x4*>(&s_o[wave * 32 + lane * 4]) = f32x4{o[0], o[1], o[2], o[3]};
            *reinterpret_cast<f32x2*>(&s_o[128 + wave * 2]) = f32x2{mw, lw};
        }
        asm volatile("s_waitcnt lgkmcnt(0)\n\ts_barrier" ::: "memory");
        if (tid < 32) {
            const f32x4 a0 = *reinterpret_cast<const f32x4*>(&s_o[128]), a1 = *reinterpret_cast<const f32x4*>(&s_o[132]);
            const float M = fmaxf(fmaxf(a0[0], a0[2]), fmaxf(a1[0], a1[2]));
            const float f0 = __builtin_amdgcn_exp2f(a0[0] - M), f1 = __builtin_amdgcn_exp2f(a0[2] - M), f2 = __builtin_amdgcn_exp2f(a1[0] - M), f3 = __builtin_amdgcn_exp2f(a1[2] - M);
            const float L = (f0 * a0[1] + f1 * a0[3]) + (f2 * a1[1] + f3 * a1[3]);
            const float t = (f0 * s_o[tid] + f1 * s_o[32 + tid]) + (f2 * s_o[64 + tid] + f3 * s_o[96 + tid]);
            a.out[h * DH + chunk * 32 + tid] = (unsigned short)pk_bf16(t / L, 0.f);
            if (b == 0 && tid == 0) *reinterpret_cast<u64*>(a.part + pep) = tag;
        }
    }
    if (DBG) {
        const unsigned tc1 = now32();
        u64* stp = reinterpret_cast<u64*>(a.part + 32 * 256 * 196 * 4) + (size_t)(b * 4 + wave) * 8;
        if (lane == 0) { stp[0] = tstart; stp[1] = tbar; stp[2] = DBG == 3 ? tdma : th1; stp[3] = DBG == 3 ? tq : th4; stp[4] = tend; stp[5] = b < 6 * H ? tfl : tend; stp[6] = b < 6 * H ? tpl : tend; stp[7] = tc1; }
        if (DBG == 2) *reinterpret_cast<u32x4*>(a.part + 32 * 256 * 196 * 4 + 65536 + (b * 4 + wave) * 1024 + lane * 16) = *reinterpret_cast<const u32x4*>(smem + LDS_TR + 16 + wave * 1024 + lane * 16);
    }
    asm volatile(".Lkv_b%c0:" :: "i"(ID));
}

template <int TP, int ADV, int MAP, int TMIN, int ENDQ, int WIN, int SLP, int PFW, int ID, int DBG>
static void launch22(void* const* d_in, const int* in_sizes, void* d_out, void* d_ws, hipStream_t stream, bool comb = true) {
    typedef const unsigned short* P;
    Args a;
    a.qk = (const int*)d_in[1]; a.qv = (const int*)d_in[4];
    a.k_cos = (P)d_in[9]; a.k_sin = (P)d_in[10];
    a.q = (P)d_in[0]; a.q_cos = (P)d_in[7]; a.q_sin = (P)d_in[8];
    a.ksc = (P)d_in[2]; a.kzp = (P)d_in[3]; a.vsc = (P)d_in[5]; a.vzp = (P)d_in[6];
    a.part = (unsigned char*)d_ws;
    a.out = (unsigned short*)d_out;
    a.Skv = in_sizes[9] / DH;
    a.scale_log2 = 1.4426950408889634f / sqrtf((float)DH);
    kv22_kernel<TP, ADV, MAP, TMIN, ENDQ, WIN, SLP, PFW, ID, DBG><<<a.Skv / RW, 256, 0, stream>>>(a);
}

extern "C" void kernel_launch(void* const* d_in, const int* in_sizes, int n_in, void* d_out, int out_size, void* d_ws, size_t ws_size, hipStream_t stream) {
    launch22<80*16+8,-230,12,80*16,0,4,4,256,0,0>(d_in, in_sizes, d_out, d_ws, stream);
}
```

```cpp
#include <hip/hip_runtime.h>
#include <hip/hip_bf16.h>
#include <cstdint>
using bf16 = __hip_bfloat16;
using i32x4 = __attribute__((ext_vector_type(4))) int;
using u32x4 = __attribute__((ext_vector_type(4))) unsigned;
using u32x2 = __attribute__((ext_vector_type(2))) unsigned;
using f32x2 = __attribute__((ext_vector_type(2))) float;
using f32x4 = __attribute__((ext_vector_type(4))) float;

constexpr int DH = 192;
constexpr int HALF = 96;
constexpr int RW = 64;
constexpr int MAXH = 32;

__device__ __forceinline__ float bflo(unsigned u) { return __uint_as_float(u << 16); }
__device__ __forceinline__ float bfhi(unsigned u) { return __uint_as_float(u & 0xffff0000u); }
__device__ __forceinline__ float rbf(float x) { return __bfloat162float(__float2bfloat16(x)); }

template <int CTRL> __device__ __forceinline__ float dpp(float x) {
    return __builtin_bit_cast(float, __builtin_amdgcn_mov_dpp(__builtin_bit_cast(int, x), CTRL, 0xf, 0xf, true));
}
__device__ __forceinline__ float row16_allsum(float x) {
    x += dpp<0x128>(x);
    x += dpp<0x124>(x);
    x += dpp<0x122>(x);
    x += dpp<0x121>(x);
    return x;
}
__device__ __forceinline__ float row16_allmax(float x) {
    x = fmaxf(x, dpp<0x128>(x));
    x = fmaxf(x, dpp<0x124>(x));
    x = fmaxf(x, dpp<0x122>(x));
    x = fmaxf(x, dpp<0x121>(x));
    return x;
}
__device__ __forceinline__ float xrow_max(float x) {
    auto s = __builtin_amdgcn_permlane16_swap(__float_as_uint(x), __float_as_uint(x), false, false);
    x = fmaxf(__uint_as_float(s[0]), __uint_as_float(s[1]));
    auto t = __builtin_amdgcn_permlane32_swap(__float_as_uint(x), __float_as_uint(x), false, false);
    return fmaxf(__uint_as_float(t[0]), __uint_as_float(t[1]));
}
__device__ __forceinline__ float xrow_sum(float x) {
    auto s = __builtin_amdgcn_permlane16_swap(__float_as_uint(x), __float_as_uint(x), false, false);
    x = __uint_as_float(s[0]) + __uint_as_float(s[1]);
    auto t = __builtin_amdgcn_permlane32_swap(__float_as_uint(x), __float_as_uint(x), false, false);
    return __uint_as_float(t[0]) + __uint_as_float(t[1]);
}
__device__ __forceinline__ float sw32(float a, float b) {
    auto t = __builtin_amdgcn_permlane32_swap(__float_as_uint(a), __float_as_uint(b), false, false);
    return __uint_as_float(t[0]) + __uint_as_float(t[1]);
}
__device__ __forceinline__ float sw16(float a, float b) {
    auto t = __builtin_amdgcn_permlane16_swap(__float_as_uint(a), __float_as_uint(b), false, false);
    return __uint_as_float(t[0]) + __uint_as_float(t[1]);
}

#define SB() __builtin_amdgcn_sched_barrier(0)
typedef __amdgpu_buffer_rsrc_t rsrc_t;
__device__ __forceinline__ rsrc_t mkrsrc(const void* p, unsigned bytes) {
    return __builtin_amdgcn_make_buffer_rsrc(const_cast<void*>(p), 0, bytes, 0x00020000);
}
template <int AUX> __device__ __forceinline__ i32x4 bld128(rsrc_t r, unsigned voff, unsigned soff) {
    return __builtin_bit_cast(i32x4, __builtin_amdgcn_raw_buffer_load_b128(r, voff, soff, AUX));
}
template <int AUX> __device__ __forceinline__ unsigned bld16(rsrc_t r, unsigned voff, unsigned soff) {
    return __builtin_amdgcn_raw_buffer_load_b16(r, voff, soff, AUX);
}

struct HG { i32x4 d[3]; };
typedef unsigned long long u64;
typedef __attribute__((address_space(3))) unsigned char lds_u8;

__device__ __forceinline__ unsigned pk_bf16(float lo, float hi) {
    unsigned r;
    asm("v_cvt_pk_bf16_f32 %0, %1, %2" : "=v"(r) : "v"(lo), "v"(hi));
    return r;
}
__device__ __forceinline__ unsigned now32() { return (unsigned)__builtin_amdgcn_s_memrealtime(); }

constexpr int LDS_REC = 0;
constexpr int RECW = MAXH * DH * 4;
constexpr int LDS_CS = 0;
constexpr int LDS_QT = 12288;
constexpr int LDS_LT = 13312;
constexpr int LDS_PF = 14336;
constexpr int LDS_QRAW = MAXH * 4 * DH * 4;
constexpr int LDS_Q = LDS_QRAW + MAXH * DH * 2;
constexpr int LDS_P = LDS_Q + 4 * DH * 4;
constexpr int LDS_SC = LDS_P + 4 * MAXH * 128;
constexpr int LDS_CNT = LDS_SC + 4 * MAXH * RW * 2;
constexpr int LDS_SK = LDS_CNT + 256;
constexpr int LDS_TR = LDS_CNT + 512;
constexpr int LDS_BYTES = LDS_TR + 4096 + 64;

struct Args {
    const int* qk; const int* qv;
    const unsigned short* k_cos; const unsigned short* k_sin;
    const unsigned short* q; const unsigned short* q_cos; const unsigned short* q_sin;
    const unsigned short* ksc; const unsigned short* kzp; const unsigned short* vsc; const unsigned short* vzp;
    unsigned char* part;
    unsigned short* out;
    int Skv; float scale_log2;
};

template <int TP, int ADV, int TMIN, int HOLD, int ES, int OPT, int ID, int DBG>
__global__ __launch_bounds__(256) void kv27_kernel(Args a)
{
    __shared__ __attribute__((aligned(16))) unsigned char smem[LDS_BYTES];
    const unsigned tstart = DBG ? now32() : 0;
    constexpr int H = MAXH;
    const int b = blockIdx.x;
    const int tid = threadIdx.x;
    const int lane = tid & 63;
    const int wave = __builtin_amdgcn_readfirstlane(tid >> 6);
    const int r = lane >> 4;
    const int c = lane & 15;
    const unsigned Skv = a.Skv;
    const unsigned hstride = Skv * (DH * 4);
    const unsigned nb = Skv / RW;
    const unsigned lds0 = (unsigned)(size_t)(lds_u8*)smem;
    const unsigned pos = (b & 7) * (nb >> 3) + (b >> 3);
    const unsigned row0 = RW * pos + 16 * wave;

    asm volatile("" :: "s"(a.qk), "s"(a.qv), "s"(a.k_cos), "s"(a.k_sin), "s"(a.q_cos), "s"(a.q_sin), "s"(a.ksc), "s"(a.kzp), "s"(a.vsc), "s"(a.vzp), "s"(a.part), "s"(a.out), "s"(a.Skv), "s"(a.scale_log2));
    constexpr int NL = 2;
    constexpr int LS = 896;
    const unsigned plt = H * nb * (DH * 2), pfl = plt + NL * nb * LS, psk = pfl + H * nb * 16, pep = psk + 128;
    const rsrc_t pr = mkrsrc(a.part, pep);
#define DMA3(V, A, B, P) "s_mov_b32 m0, " B "\n\ts_nop 0\n\t" \
                     "global_load_lds_dwordx4 " V ", " A P "\n\t" \
                     "global_load_lds_dwordx4 " V ", " A " offset:1024" P "\n\t" \
                     "global_load_lds_dwordx4 " V ", " A " offset:2048" P "\n\t"
    asm volatile(DMA3("%0", "%1", "%2", "") "s_mov_b32 m0, %4\n\ts_nop 0\n\tglobal_load_lds_dwordx4 %3, off"
                 :: "v"(lane * 16), "s"(a.q + wave * 1536), "s"(lds0 + LDS_QRAW + wave * 3072),
                    "v"(lane < 48 ? (const unsigned char*)((lane < 24 ? a.q_cos : a.q_sin - DH) + 8 * lane) : a.part + pep), "s"(lds0 + LDS_QT + wave * RECW) : "memory");
    SB();
    {
        const unsigned short* sp = wave == 0 ? a.ksc : wave == 1 ? a.kzp : wave == 2 ? a.vsc : a.vzp;
        const unsigned vs = ((lane >> 3) * Skv + RW * pos + (lane & 7) * 8) * 2;
        const unsigned ds = lds0 + LDS_SC + wave * 4096;
#define DMA1(A, B) "s_mov_b32 m0, " B "\n\ts_nop 0\n\tglobal_load_lds_dwordx4 %0, " A " nt\n\t"
        asm volatile(DMA1("%1", "%5") DMA1("%2", "%6") DMA1("%3", "%7") DMA1("%4", "%8")
                     :: "v"(vs), "s"(sp), "s"(sp + 8 * Skv), "s"(sp + 16 * Skv), "s"(sp + 24 * Skv), "s"(ds), "s"(ds + 1024), "s"(ds + 2048), "s"(ds + 3072) : "memory");
    }
    SB();
    const unsigned ltoff = plt + ((wave & 1) * nb + b) * LS + min(lane, 47) * 16;
    if (wave < NL) asm volatile(DMA1("%1", "%2") :: "v"(ltoff), "s"(a.part), "s"(lds0 + LDS_LT + wave * RECW) : "memory");
    SB();
    const unsigned tdma = now32();
    if (b < 16) {
        u64 pc; unsigned len;
        asm volatile("s_getpc_b64 %0\n.Lkv_a%c2:\n\ts_mov_b32 %1, .Lkv_b%c2-.Lkv_a%c2" : "=s"(pc), "=s"(len) : "i"(ID));
        pc &= ~63ull;
        len &= ~15u;
        const unsigned o = wave * 1024 + lane * 16;
        asm volatile("s_mov_b32 m0, %4\n\ts_nop 0\n\tglobal_load_lds_dwordx4 %0, %3\n\tglobal_load_lds_dwordx4 %1, %3\n\tglobal_load_lds_dwordx4 %2, %3"
                     :: "v"(min(o, len)), "v"(min(o + 4096, len)), "v"(min(o + 8192, len)), "s"(pc), "s"(lds0 + LDS_PF + wave * RECW) : "memory");
    }
    SB();

    const int stag = (int)(((OPT & 2) ? (((b >> 3) * 26 + wave * 2 + (b & 1)) & 63) : ((((b >> 3) * 4 + wave) * 37) & 63)) * (TP / 2)) / 64 - TP / 4;
    unsigned sched = ((tdma + ADV) << 4) + stag - 4 * (TP / 2);
    unsigned vt = tdma << 4;
    int tru = -4;
    unsigned step = TP / 2, tmin = TMIN / 2;
    auto pace = [&]() {
        unsigned tgt = sched;
        sched += step;
        if ((int)(vt - tgt) > 0) tgt = vt;
        unsigned t = now32() << 4;
        while ((int)(t - tgt) < 0) { __builtin_amdgcn_s_sleep(1); t = now32() << 4; }
        vt = ((int)(t - 128 - tgt) > 0 ? t - 128 : tgt) + tmin;
        if constexpr (DBG == 2) { *reinterpret_cast<unsigned*>(smem + LDS_TR + 16 + wave * 1024 + (tru & 255) * 4) = (t - (sched - step)) >> 4; ++tru; }
    };
    {
        unsigned dst = lds0 + LDS_CS + wave * RECW;
        const unsigned v0 = lane * 16 + row0 * (DH * 2);
#pragma unroll 1
        for (int j = 0; j < 4; ++j) {
            pace();
            asm volatile(DMA3("%0", "%1", "%2", " nt") :: "v"(v0), "s"((j < 2 ? a.k_cos : a.k_sin) + (j & 1) * 8 * DH), "s"(dst) : "memory");
            dst += 3072;
        }
    }
    if (tid < MAXH) *reinterpret_cast<unsigned*>(smem + LDS_CNT + tid * 4) = 0;
    const rsrc_t kr = mkrsrc(a.qk, H * hstride);
    const rsrc_t vr = mkrsrc(a.qv, H * hstride);
    const unsigned kvoff = (row0 + r) * (DH * 4) + c * 16;
    auto issue = [&](HG& g, rsrc_t rs, unsigned so) {
#pragma unroll
        for (int x = 0; x < 3; ++x) g.d[x] = bld128<2>(rs, kvoff + 256 * x, so);
    };
    HG buf[4];
#define PRE(g) SB(); pace(); issue(buf[g], kr, (g) * (4 * DH * 4)); SB();
    asm volatile("s_waitcnt vmcnt(12) lgkmcnt(0)\n\ts_barrier" ::: "memory");
    unsigned tbar = DBG ? now32() : 0;
    PRE(0)
    const unsigned qoff = (lane < 48 ? lane : lane - 48) * 4;
    const unsigned* qt = reinterpret_cast<const unsigned*>(smem + LDS_QT + wave * RECW + qoff);
    unsigned qc0 = qt[0], qc1 = qt[HALF / 2], qs0 = qt[DH / 2], qs1 = qt[(DH + HALF) / 2];
    u64 tag;
    {
        const u32x2 e = *reinterpret_cast<const u32x2*>(smem + LDS_QT + wave * RECW + 768);
        tag = (((u64)__builtin_amdgcn_readfirstlane(e[1]) << 32) | __builtin_amdgcn_readfirstlane(e[0])) * 6364136223846793005ull + 1442695040888963407ull;
        asm volatile("" : "+s"(tag));
    }
    const unsigned t32 = tag >> 32;
    const unsigned char* qraw = smem + LDS_QRAW + qoff;
    unsigned char* qrot = smem + LDS_Q + wave * (DH * 4) + 2 * qoff;
    auto rotq = [&]() {
        const unsigned q_lo = *reinterpret_cast<const unsigned*>(qraw), q_hi = *reinterpret_cast<const unsigned*>(qraw + HALF * 2);
        qraw += DH * 2;
        const f32x2 lo = f32x2{bflo(q_lo), bfhi(q_lo)}, hi = f32x2{bflo(q_hi), bfhi(q_hi)};
        const f32x2 y1 = lo * f32x2{bflo(qc0), bfhi(qc0)}, y2 = -hi * f32x2{bflo(qs0), bfhi(qs0)};
        const f32x2 y3 = hi * f32x2{bflo(qc1), bfhi(qc1)}, y4 = lo * f32x2{bflo(qs1), bfhi(qs1)};
        const unsigned x1 = pk_bf16(y1[0], y1[1]), x2 = pk_bf16(y2[0], y2[1]), x3 = pk_bf16(y3[0], y3[1]), x4 = pk_bf16(y4[0], y4[1]);
        const f32x2 z1 = f32x2{bflo(x1), bfhi(x1)} + f32x2{bflo(x2), bfhi(x2)}, z2 = f32x2{bflo(x3), bfhi(x3)} + f32x2{bflo(x4), bfhi(x4)};
        const unsigned l2 = pk_bf16(z1[0], z1[1]), h2 = pk_bf16(z2[0], z2[1]);
        *reinterpret_cast<f32x2*>(qrot) = f32x2{bflo(l2), bfhi(l2)};
        *reinterpret_cast<f32x2*>(qrot + HALF * 4) = f32x2{bflo(h2), bfhi(h2)};
        asm volatile("" ::: "memory");
    };
    f32x4 qa[3], qb[3];
    const unsigned char* qh = smem + LDS_Q + wave * (DH * 4) + 16 * c;
    const unsigned char* qh1 = qh + (c < 8 ? 64 + HALF : 64 - HALF) * 4;
    auto loadq = [&]() {
        asm volatile("" ::: "memory");
#pragma unroll
        for (int x = 0; x < 3; ++x) qa[x] = *reinterpret_cast<const f32x4*>(qh + 256 * x);
        qb[0] = *reinterpret_cast<const f32x4*>(qh + HALF * 4);
        qb[1] = *reinterpret_cast<const f32x4*>(qh1);
        qb[2] = *reinterpret_cast<const f32x4*>(qh + (128 - HALF) * 4);
        asm volatile("" ::: "memory");
    };
    rotq();
    if (wave < NL) {
        const u32x4 e = *reinterpret_cast<const u32x4*>(smem + LDS_LT + wave * RECW + lane * 16);
        if (e[1] == t32 || e[3] == t32) __builtin_amdgcn_raw_buffer_store_b128(u32x4{0u, ~t32, 0u, ~t32}, pr, ltoff, 0, 16);
    }
    if (b < 8 && wave == 3) __builtin_amdgcn_raw_buffer_store_b64(u32x2{tdma, t32}, pr, lane == 0 ? psk + b * 8 : 0x80000000u, 0, 16);
    PRE(1)
    f32x2 cc[4][3][2], ss[4][3][2];
    const unsigned char* cb = smem + LDS_CS + wave * RECW + r * (DH * 2) + c * 8;
    asm volatile("s_waitcnt vmcnt(12)" ::: "memory");
#pragma unroll
    for (int k = 0; k < 4; ++k)
#pragma unroll
        for (int x = 0; x < 3; ++x) {
            const u32x2 t = *reinterpret_cast<const u32x2*>(cb + k * 1536 + 128 * x);
            cc[k][x][0] = f32x2{bflo(t[0]), bfhi(t[0])}; cc[k][x][1] = f32x2{bflo(t[1]), bfhi(t[1])};
            asm volatile("" : "+v"(cc[k][x][0]), "+v"(cc[k][x][1]));
        }
    PRE(2)
    loadq();
    const unsigned char* scp = smem + LDS_SC + (c & 4) * 1024 + (16 * wave + 4 * (c & 3) + r) * 2;
    unsigned sc = *reinterpret_cast<const unsigned short*>(scp);
    auto loadsin = [&](int k) {
        const unsigned char* sb1 = cb + (c < 8 ? (64 + HALF) * 2 : (64 - HALF) * 2);
        const unsigned sg = c < 8 ? 0u : 0x80008000u;
        u32x2 t[3];
        t[0] = *reinterpret_cast<const u32x2*>(cb + 6144 + k * 1536 + HALF * 2);
        t[1] = *reinterpret_cast<const u32x2*>(sb1 + 6144 + k * 1536) ^ u32x2{sg, sg};
        t[2] = *reinterpret_cast<const u32x2*>(cb + 6144 + k * 1536 + (128 - HALF) * 2) ^ u32x2{0x80008000u, 0x80008000u};
#pragma unroll
        for (int x = 0; x < 3; ++x) {
            ss[k][x][0] = f32x2{bflo(t[x][0]), bfhi(t[x][0])}; ss[k][x][1] = f32x2{bflo(t[x][1]), bfhi(t[x][1])};
            asm volatile("" : "+v"(ss[k][x][0]), "+v"(ss[k][x][1]));
        }
    };
    asm volatile("s_waitcnt vmcnt(12)" ::: "memory");
    loadsin(0); loadsin(1);
    PRE(3)
    asm volatile("s_waitcnt vmcnt(12)" ::: "memory");
    loadsin(2); loadsin(3);
    unsigned th1 = 0, th4 = 0;
    unsigned char* prec = smem + LDS_P + wave * (H * 128) + r * 32 + (c & 7) * 4;
    auto softmax = [&](float s, int h) {
        float m = fmaxf(s, dpp<0xb1>(s));
        m = xrow_max(fmaxf(m, dpp<0x4e>(m)));
        const float p = __builtin_amdgcn_exp2f(s - m);
        float l = p + dpp<0xb1>(p);
        l = xrow_sum(l + dpp<0x4e>(l));
        const float t = (c & 1) ? l : m;
        const int x = __builtin_amdgcn_update_dpp(__float_as_int(p), __float_as_int(t), 0x114, 0xf, 0xa, false);
        *reinterpret_cast<int*>(prec + h * 128) = x;
    };
    float sprev = 0.f;

#pragma unroll 1
    for (int h = 0; h < H; ++h) {
        const bool more = h + 1 < H;
        const rsrc_t nx = more ? kr : vr;
        const unsigned nso = more ? (h + 1) * hstride : 0;
        float scf = bflo(sc);
        asm volatile("" : "+v"(scf) :: "memory");
        SB();
        scp += more ? 128 : 4096 + 128;
        sc = *reinterpret_cast<const unsigned short*>(scp);
        SB();
        float sreg = 0.f;
#define KSTEP(k) { \
            const float kz = dpp<0x154 + (k)>(scf); \
            f32x2 kz2 = f32x2{kz, kz}; \
            asm("" : "+v"(kz2)); \
            f32x2 a2 = f32x2{0.f, 0.f}; \
            _Pragma("unroll") for (int x = 0; x < 3; ++x) { \
                _Pragma("unroll") for (int e = 0; e < 2; ++e) { \
                    const f32x2 w = __builtin_elementwise_fma(f32x2{qb[x][2 * e], qb[x][2 * e + 1]}, ss[k][x][e], f32x2{qa[x][2 * e], qa[x][2 * e + 1]} * cc[k][x][e]); \
                    const f32x2 kf = f32x2{(float)buf[k].d[x][2 * e], (float)buf[k].d[x][2 * e + 1]} - kz2; \
                    a2 = __builtin_elementwise_fma(kf, w, a2); \
                } \
            } \
            float sa = a2[0] + a2[1]; \
            asm volatile("" : "+v"(sa) :: "memory"); \
            SB(); pace(); issue(buf[k], nx, nso + (k) * (4 * DH * 4)); SB(); \
            if ((k) == 0) { softmax(sprev, (h - 1) & (H - 1)); SB(); }   \
            if ((k) == 1) { rotq(); SB(); } \
            if ((k) == 2) { \
                if (wave == 1 && h == 4 + ((b >> 3) & 15)) \
                    asm volatile("s_mov_b32 m0, %2\n\ts_nop 0\n\tglobal_load_lds_dword %0, %1 sc1" :: "v"(psk + (lane & 15) * 4), "s"(a.part), "s"(lds0 + LDS_SK) : "memory"); \
                if (h == 25) {        \
                    const u32x2 e = *reinterpret_cast<const u32x2*>(smem + LDS_SK + (lane & 7) * 8); \
                    const int d = (int)(tdma - e[0]); \
                    if (__all(e[1] == t32 && d > -400 && d < 400)) { \
                        int sum = d + __builtin_amdgcn_mov_dpp(d, 0xb1, 0xf, 0xf, true); \
                        sum += __builtin_amdgcn_mov_dpp(sum, 0x4e, 0xf, 0xf, true); \
                        sum += __builtin_amdgcn_mov_dpp(sum, 0x124, 0xf, 0xf, true); \
                        const int adj = (__builtin_amdgcn_readfirstlane(sum) * 7) >> 9;        \
                        step -= adj; tmin -= adj; \
                    } \
                } \
                SB(); \
            } \
            if ((k) == 3) { loadq(); SB(); } \
            sa = row16_allsum(sa); \
            sreg = (c & 3) == (k) ? sa : sreg; }
        KSTEP(0) KSTEP(1) KSTEP(2) KSTEP(3)
        sprev = sreg * (scf * a.scale_log2);
        if (DBG) { if (h == 7) th1 = now32(); }
    }
    softmax(sprev, H - 1);
    if (DBG) th4 = now32();

    const rsrc_t nr = mkrsrc(a.qk, 0);
    auto merge = [&](int hd, bool direct) {
        unsigned char* rec = smem + LDS_REC + hd * (DH * 4);
        const unsigned char* mlp = smem + LDS_P + hd * 128 + 16;
        f32x4 ow[4]; float mv[4];
#pragma unroll
        for (int w = 0; w < 4; ++w) {
            mv[w] = *reinterpret_cast<const float*>(mlp + w * (H * 128));
            ow[w] = *reinterpret_cast<const f32x4*>(rec + w * RECW + lane * 16);
        }
        const float M = fmaxf(fmaxf(mv[0], mv[1]), fmaxf(mv[2], mv[3]));
        f32x4 t = f32x4{0.f, 0.f, 0.f, 0.f};
#pragma unroll
        for (int w = 0; w < 4; ++w) t += __builtin_amdgcn_exp2f(mv[w] - M) * ow[w];
        const u32x2 v = u32x2{pk_bf16(t[0], t[1]), pk_bf16(t[2], t[3])};
        if (direct) __builtin_amdgcn_raw_buffer_store_b128(u32x4{v[0], t32, v[1], t32}, pr, lane < 48 ? lane * 16 : 0x80000000u, plt + ((hd - (H - NL)) * nb + b) * LS, 16);
        else *reinterpret_cast<u32x2*>(rec + lane * 8) = v;
    };
    unsigned old = 0, flo = 0x80000000u;
    u32x4 flv = {};
    u32x4 ev[3] = {};
    unsigned eo[3] = {};
    unsigned el[3] = {};
    auto gather0 = [&]() {
        int lane = tid & 63;
        asm volatile("" : "+v"(lane));
#pragma unroll
        for (int p = 0; p < 3; ++p) {
            const int t = lane + 64 * p, hi_ = t / 24, k = t - hi_ * 24;
            const int hh = 8 * wave + hi_;
            el[p] = hh * (DH * 4) + k * 16;
            eo[p] = hh < H - NL ? (hh * nb + b) * (DH * 2) + k * 16 : 0x80000000u;
        }
        flo = lane < 8 ? pfl + ((8 * wave + lane) * nb + b) * 16 : 0x80000000u;
        asm volatile("" : "+v"(el[0]), "+v"(el[1]), "+v"(el[2]), "+v"(eo[0]), "+v"(eo[1]), "+v"(eo[2]), "+v"(flo));
    };
    auto gather1 = [&]() {
#pragma unroll
        for (int p = 0; p < 3; ++p) ev[p] = *reinterpret_cast<const u32x4*>(smem + LDS_REC + el[p]);
        f32x2 ml[4];
#pragma unroll
        for (int w = 0; w < 4; ++w) ml[w] = *reinterpret_cast<const f32x2*>(smem + LDS_P + w * (H * 128) + (8 * wave + (lane & 7)) * 128 + 16);
        const float M = fmaxf(fmaxf(ml[0][0], ml[1][0]), fmaxf(ml[2][0], ml[3][0]));
        float L = 0.f;
#pragma unroll
        for (int w = 0; w < 4; ++w) L += __builtin_amdgcn_exp2f(ml[w][0] - M) * ml[w][1];
        flv = u32x4{__float_as_uint(M), t32, __float_as_uint(L), (unsigned)tag};
        asm volatile("" : "+v"(ev[0]), "+v"(ev[1]), "+v"(ev[2]), "+v"(flv));
    };
    auto estore = [&]() {
#pragma unroll
        for (int p = 0; p < 3; ++p) __builtin_amdgcn_raw_buffer_store_b128(ev[p], pr, eo[p], 0, 16);
        if (DBG == 4) tbar = now32();
    };
    float pv = *reinterpret_cast<const float*>(prec);
    f32x2 acc[3][2];
    float y;
    auto prep = [&]() {
        y = bflo(sc);
        y = (c & 4) ? y : y * pv;
        asm volatile("" : "+v"(y) :: "memory");
        SB();
        scp += 128;
        sc = *reinterpret_cast<const unsigned short*>(scp);
        prec += 128;
        pv = *reinterpret_cast<const float*>(prec);
        SB();
    };
#define VSTEP(k, hn) { \
        const float pw = dpp<0x150 + (k)>(y); \
        const float vz = dpp<0x154 + (k)>(y); \
        f32x2 pw2 = f32x2{pw, pw}, vz2 = f32x2{vz, vz}; \
        asm("" : "+v"(pw2), "+v"(vz2)); \
        _Pragma("unroll") for (int x = 0; x < 3; ++x) \
            _Pragma("unroll") for (int e = 0; e < 2; ++e) { \
                const f32x2 vf = f32x2{(float)buf[k].d[x][2 * e], (float)buf[k].d[x][2 * e + 1]} - vz2; \
                acc[x][e] = (k) == 0 ? pw2 * vf : __builtin_elementwise_fma(pw2, vf, acc[x][e]); \
            } \
        asm volatile("" : "+v"(acc[0][0]), "+v"(acc[0][1]), "+v"(acc[1][0]), "+v"(acc[1][1]), "+v"(acc[2][0]), "+v"(acc[2][1]) :: "memory"); \
        SB(); if ((hn) < H) pace(); issue(buf[k], (hn) < H ? vr : nr, (hn) * hstride + (k) * (4 * DH * 4)); SB(); \
        if ((k) == 1 && (hn) == H - 1) { asm volatile("s_waitcnt lgkmcnt(0)\n\ts_barrier" ::: "memory"); tmin = 0; SB(); } \
        if ((k) == 2 && (hn) == H - 2) { gather0(); SB(); } \
        if ((k) == 2 && (hn) == H - 1) { gather1(); SB(); } \
        if ((k) == (ES & 3) && (hn) == H - 1 + (ES >> 2)) { estore(); SB(); } }
    prep();
    VSTEP(0, 1)
#pragma unroll 1
    for (int h = 0; h < H; ++h) {
        VSTEP(1, h + 1) VSTEP(2, h + 1) VSTEP(3, h + 1)
        f32x4 o;
#pragma unroll
        for (int e = 0; e < 4; ++e) {
            const float t0 = sw32(acc[0][e >> 1][e & 1], acc[2][e >> 1][e & 1]);
            const float t1 = sw32(acc[1][e >> 1][e & 1], acc[1][e >> 1][e & 1]);
            o[e] = sw16(t0, t1);
        }
        asm volatile("s_mov_b64 exec, %5\n\tds_write_b128 %1, %2\n\ts_mov_b64 exec, 1\n\tds_add_rtn_u32 %0, %3, %4\n\ts_mov_b64 exec, -1"
                     : "+v"(old) : "v"(lds0 + LDS_REC + h * (DH * 4) + wave * RECW + lane * 16), "v"(o),
                       "v"(lds0 + LDS_CNT + h * 4), "v"(1u), "s"(0xffffffffffffull) : "memory");
        SB();
        prep();
        VSTEP(0, h + 2)
        asm volatile("s_waitcnt lgkmcnt(0)" : "+v"(old) :: "memory");
        if (__builtin_amdgcn_readfirstlane(old) == 3) merge(h, h >= H - NL);
        SB();
    }
    if (__builtin_amdgcn_readfirstlane(old) == 3) asm volatile("s_waitcnt vmcnt(1)" ::: "memory");
    else asm volatile("s_waitcnt vmcnt(0)" ::: "memory");
    __builtin_amdgcn_raw_buffer_store_b128(flv, pr, flo, 0, 16);
    const unsigned tend = DBG ? now32() : 0;
    unsigned tfl = 0, tpl = 0;

    constexpr int NCN = 3 * (H - NL), NC = NCN + 6 * NL;
    if (b < NC) {
        const bool late = b >= NCN;
        const int h = late ? H - NL + (b - NCN) / 6 : b / 3, chunk = late ? (b - NCN) % 6 : b % 3;
        const unsigned src = 64 * wave + lane;
        const unsigned rstride = late ? LS : DH * 2;
        const unsigned rbase = late ? plt + (h - (H - NL)) * nb * LS : h * nb * (DH * 2);
        const unsigned xo = (64 * wave + (lane >> 3)) * rstride + chunk * 128 + (lane & 7) * 16;
        const unsigned oo = late ? ((r & 1) ? 0x80000000u : (h * DH + chunk * 32 + (c & 7) * 4 + r + (c >> 3)) * 2) : (h * DH + chunk * 64 + (c & 7) * 8 + 2 * r + (c >> 3)) * 2;
        asm volatile("s_waitcnt vmcnt(0)" ::: "memory");
        if (DBG == 4) th4 = now32();
        const unsigned tgt = sched - stag + HOLD * 16;
        while ((int)((now32() << 4) - tgt) < 0) __builtin_amdgcn_s_sleep(1);
        i32x4 y, x[8];
#pragma unroll 1
        for (int n = 0; n < (1 << 20); ++n) {
            y = bld128<16>(pr, pfl + (h * nb + src) * 16, 0);
            if ((unsigned)y[1] == t32 && (unsigned)y[3] == (unsigned)tag) break;
            asm volatile("" ::: "memory");
            __builtin_amdgcn_s_sleep(2);
        }
        asm volatile("" ::: "memory");
        if (DBG) tfl = now32();
#pragma unroll
        for (int i = 0; i < 8; ++i) x[i] = bld128<16>(pr, xo, rbase + i * 8 * rstride);
        SB();
        float* s_o = reinterpret_cast<float*>(smem + LDS_QRAW);
        const float m = __int_as_float(y[0]);
        const float mw = xrow_max(row16_allmax(m));
        const float lw = xrow_sum(row16_allsum(__builtin_amdgcn_exp2f(m - mw) * __int_as_float(y[2])));
        *reinterpret_cast<f32x2*>(&s_o[256 + wave * 2]) = f32x2{mw, lw};
        asm volatile("s_waitcnt lgkmcnt(0)\n\ts_barrier" ::: "memory");
        const f32x4 a0 = *reinterpret_cast<const f32x4*>(&s_o[256]), a1 = *reinterpret_cast<const f32x4*>(&s_o[260]);
        const float M = fmaxf(fmaxf(a0[0], a0[2]), fmaxf(a1[0], a1[2]));
        const float L = (__builtin_amdgcn_exp2f(a0[0] - M) * a0[1] + __builtin_amdgcn_exp2f(a0[2] - M) * a0[3]) + (__builtin_amdgcn_exp2f(a1[0] - M) * a1[1] + __builtin_amdgcn_exp2f(a1[2] - M) * a1[3]);
        const float f = __builtin_amdgcn_exp2f(m - M) / L;
        float fs[8];
#pragma unroll
        for (int i = 0; i < 8; ++i) fs[i] = __int_as_float(__builtin_amdgcn_ds_bpermute(((lane >> 3) + 8 * i) * 4, __float_as_int(f)));
        SB();
        if (late) {
#pragma unroll 1
            for (int n = 0; n < (1 << 20); ++n) {
                unsigned bad = 0;
#pragma unroll
                for (int i = 0; i < 8; ++i) bad |= (x[i][1] ^ t32) | (x[i][3] ^ t32);
                if (__all(bad == 0)) break;
                asm volatile("" ::: "memory");
#pragma unroll
                for (int i = 0; i < 8; ++i) x[i] = bld128<16>(pr, xo, rbase + i * 8 * rstride);
            }
        }
        f32x2 o[4] = {};
#pragma unroll
        for (int i = 0; i < 8; ++i)
#pragma unroll
            for (int e = 0; e < 4; ++e) o[e] = __builtin_elementwise_fma(f32x2{fs[i], fs[i]}, f32x2{bflo(x[i][e]), bfhi(x[i][e])}, o[e]);
        if (DBG) { asm volatile("" : "+v"(o[0]), "+v"(o[1]), "+v"(o[2]), "+v"(o[3])); tpl = now32(); }
        const float w0 = sw16(sw32(o[0][0], o[2][0]), sw32(o[1][0], o[3][0])), w1 = sw16(sw32(o[0][1], o[2][1]), sw32(o[1][1], o[3][1]));
        s_o[wave * 64 + lane] = (c < 8 ? w0 : w1) + dpp<0x128>(c < 8 ? w1 : w0);
        asm volatile("s_waitcnt lgkmcnt(0)\n\ts_barrier" ::: "memory");
        if (wave == 0) {
            const float T = (s_o[lane] + s_o[64 + lane]) + (s_o[128 + lane] + s_o[192 + lane]);
            __builtin_amdgcn_raw_buffer_store_b16((unsigned short)pk_bf16(T, 0.f), mkrsrc(a.out, H * DH * 2), oo, 0, 0);
        }
        if (b == 0 && tid == 0) *reinterpret_cast<u64*>(a.part + pep) = tag;
    }
    if (DBG) {
        const unsigned tc1 = now32();
        u64* stp = reinterpret_cast<u64*>(a.part + 32 * 256 * 196 * 4) + (size_t)(b * 4 + wave) * 8;
        if (lane == 0) { stp[0] = tstart; stp[1] = tbar; stp[2] = DBG == 4 ? tend - (((tend << 4) - (sched - stag - step)) >> 4) : th1; stp[3] = DBG == 4 && b >= NC ? tend : th4; stp[4] = tend; stp[5] = b < NC ? tfl : tend; stp[6] = b < NC ? tpl : tend; stp[7] = tc1; }
        if (DBG == 2) *reinterpret_cast<u32x4*>(a.part + 32 * 256 * 196 * 4 + 65536 + (b * 4 + wave) * 1024 + lane * 16) = *reinterpret_cast<const u32x4*>(smem + LDS_TR + 16 + wave * 1024 + lane * 16);
    }
    asm volatile(".Lkv_b%c0:" :: "i"(ID));
}

template <int TP, int ADV, int TMIN, int HOLD, int ES, int OPT, int ID, int DBG>
static void launch27(void* const* d_in, const int* in_sizes, void* d_out, void* d_ws, hipStream_t stream) {
    typedef const unsigned short* P;
    Args a;
    a.qk = (const int*)d_in[1]; a.qv = (const int*)d_in[4];
    a.k_cos = (P)d_in[9]; a.k_sin = (P)d_in[10];
    a.q = (P)d_in[0]; a.q_cos = (P)d_in[7]; a.q_sin = (P)d_in[8];
    a.ksc = (P)d_in[2]; a.kzp = (P)d_in[3]; a.vsc = (P)d_in[5]; a.vzp = (P)d_in[6];
    a.part = (unsigned char*)d_ws;
    a.out = (unsigned short*)d_out;
    a.Skv = in_sizes[9] / DH;
    a.scale_log2 = 1.4426950408889634f / sqrtf((float)DH);
    kv27_kernel<TP, ADV, TMIN, HOLD, ES, OPT, ID, DBG><<<a.Skv / RW, 256, 0, stream>>>(a);
}

extern "C" void kernel_launch(void* const* d_in, const int* in_sizes, int n_in, void* d_out, int out_size, void* d_ws, size_t ws_size, hipStream_t stream) {
    launch27<80*16+8,230,80*16,170,7,2,0,0>(d_in, in_sizes, d_out, d_ws, stream);
}
```

```cpp
#include <hip/hip_runtime.h>
#include <hip/hip_bf16.h>
#include <cstdint>
using bf16 = __hip_bfloat16;
using i32x4 = __attribute__((ext_vector_type(4))) int;
using u32x4 = __attribute__((ext_vector_type(4))) unsigned;
using u32x2 = __attribute__((ext_vector_type(2))) unsigned;
using f32x2 = __attribute__((ext_vector_type(2))) float;
using f32x4 = __attribute__((ext_vector_type(4))) float;

constexpr int DH = 192;
constexpr int HALF = 96;
constexpr int RW = 64;
constexpr int MAXH = 32;

__device__ __forceinline__ float bflo(unsigned u) { return __uint_as_float(u << 16); }
__device__ __forceinline__ float bfhi(unsigned u) { return __uint_as_float(u & 0xffff0000u); }
__device__ __forceinline__ float rbf(float x) { return __bfloat162float(__float2bfloat16(x)); }

template <int CTRL> __device__ __forceinline__ float dpp(float x) {
    return __builtin_bit_cast(float, __builtin_amdgcn_mov_dpp(__builtin_bit_cast(int, x), CTRL, 0xf, 0xf, true));
}
__device__ __forceinline__ float row16_allsum(float x) {
    x += dpp<0x128>(x);
    x += dpp<0x124>(x);
    x += dpp<0x122>(x);
    x += dpp<0x121>(x);
    return x;
}
__device__ __forceinline__ float row16_allmax(float x) {
    x = fmaxf(x, dpp<0x128>(x));
    x = fmaxf(x, dpp<0x124>(x));
    x = fmaxf(x, dpp<0x122>(x));
    x = fmaxf(x, dpp<0x121>(x));
    return x;
}
__device__ __forceinline__ float xrow_max(float x) {
    auto s = __builtin_amdgcn_permlane16_swap(__float_as_uint(x), __float_as_uint(x), false, false);
    x = fmaxf(__uint_as_float(s[0]), __uint_as_float(s[1]));
    auto t = __builtin_amdgcn_permlane32_swap(__float_as_uint(x), __float_as_uint(x), false, false);
    return fmaxf(__uint_as_float(t[0]), __uint_as_float(t[1]));
}
__device__ __forceinline__ float xrow_sum(float x) {
    auto s = __builtin_amdgcn_permlane16_swap(__float_as_uint(x), __float_as_uint(x), false, false);
    x = __uint_as_float(s[0]) + __uint_as_float(s[1]);
    auto t = __builtin_amdgcn_permlane32_swap(__float_as_uint(x), __float_as_uint(x), false, false);
    return __uint_as_float(t[0]) + __uint_as_float(t[1]);
}
__device__ __forceinline__ float sw32(float a, float b) {
    auto t = __builtin_amdgcn_permlane32_swap(__float_as_uint(a), __float_as_uint(b), false, false);
    return __uint_as_float(t[0]) + __uint_as_float(t[1]);
}
__device__ __forceinline__ float sw16(float a, float b) {
    auto t = __builtin_amdgcn_permlane16_swap(__float_as_uint(a), __float_as_uint(b), false, false);
    return __uint_as_float(t[0]) + __uint_as_float(t[1]);
}

#define SB() __builtin_amdgcn_sched_barrier(0)
typedef __amdgpu_buffer_rsrc_t rsrc_t;
__device__ __forceinline__ rsrc_t mkrsrc(const void* p, unsigned bytes) {
    return __builtin_amdgcn_make_buffer_rsrc(const_cast<void*>(p), 0, bytes, 0x00020000);
}
template <int AUX> __device__ __forceinline__ i32x4 bld128(rsrc_t r, unsigned voff, unsigned soff) {
    return __builtin_bit_cast(i32x4, __builtin_amdgcn_raw_buffer_load_b128(r, voff, soff, AUX));
}
template <int AUX> __device__ __forceinline__ unsigned bld16(rsrc_t r, unsigned voff, unsigned soff) {
    return __builtin_amdgcn_raw_buffer_load_b16(r, voff, soff, AUX);
}

struct HG { i32x4 d[3]; };
typedef unsigned long long u64;
typedef __attribute__((address_space(3))) unsigned char lds_u8;

__device__ __forceinline__ unsigned pk_bf16(float lo, float hi) {
    unsigned r;
    asm("v_cvt_pk_bf16_f32 %0, %1, %2" : "=v"(r) : "v"(lo), "v"(hi));
    return r;
}
__device__ __forceinline__ unsigned now32() { return (unsigned)__builtin_amdgcn_s_memrealtime(); }

constexpr int LDS_REC = 0;
constexpr int RECW = MAXH * DH * 4;
constexpr int LDS_CS = 0;
constexpr int LDS_QT = 12288;
constexpr int LDS_LT = 13312;
constexpr int LDS_PF = 14336;
constexpr int LDS_QRAW = MAXH * 4 * DH * 4;
constexpr int LDS_Q = LDS_QRAW + MAXH * DH * 2;
constexpr int LDS_P = LDS_Q + 4 * DH * 4;
constexpr int LDS_SC = LDS_P + 4 * MAXH * 128;
constexpr int LDS_CNT = LDS_SC + 4 * MAXH * RW * 2;
constexpr int LDS_SK = LDS_CNT + 256;
constexpr int LDS_TR = LDS_CNT + 512;
constexpr int LDS_BYTES = LDS_TR + 4096 + 64;

struct Args {
    const int* qk; const int* qv;
    const unsigned short* k_cos; const unsigned short* k_sin;
    const unsigned short* q; const unsigned short* q_cos; const unsigned short* q_sin;
    const unsigned short* ksc; const unsigned short* kzp; const unsigned short* vsc; const unsigned short* vzp;
    unsigned char* part;
    unsigned short* out;
    int Skv; float scale_log2;
};

template <int TP, int ADV, int TMIN, int HOLD, int PFN, int OPT, int ID, int DBG>
__global__ __launch_bounds__(256) void kv28_kernel(Args a)
{
    __shared__ __attribute__((aligned(16))) unsigned char smem[LDS_BYTES];
    const unsigned tstart = DBG ? now32() : 0;
    constexpr int H = MAXH, ES = 7;
    const int b = blockIdx.x;
    const int tid = threadIdx.x;
    const int lane = tid & 63;
    const int wave = __builtin_amdgcn_readfirstlane(tid >> 6);
    if (b < PFN) {
        u64 pc; unsigned len;
        asm volatile("s_getpc_b64 %0\n.Lkv_a%c2:\n\ts_mov_b32 %1, .Lkv_b%c2-.Lkv_a%c2-16" : "=s"(pc), "=s"(len) : "i"(ID));
        const unsigned o = tid * 16;
        asm volatile("s_mov_b32 m0, %5\n\ts_nop 0\n\tglobal_load_lds_dwordx4 %0, %4\n\tglobal_load_lds_dwordx4 %1, %4\n\tglobal_load_lds_dwordx4 %2, %4\n\tglobal_load_lds_dwordx4 %3, %4"
                     :: "v"(o), "v"(o + 4096), "v"(min(o + 8192, len)), "v"(min(o + 12288, len)), "s"(pc), "s"((unsigned)(size_t)(lds_u8*)smem + LDS_PF) : "memory");
    }
    SB();
    const int r = lane >> 4;
    const int c = lane & 15;
    const unsigned Skv = a.Skv;
    const unsigned hstride = Skv * (DH * 4);
    const unsigned nb = Skv / RW;
    const unsigned lds0 = (unsigned)(size_t)(lds_u8*)smem;
    const unsigned pos = (b & 7) * (nb >> 3) + (b >> 3);
    const unsigned row0 = RW * pos + 16 * wave;

    asm volatile("" :: "s"(a.qk), "s"(a.qv), "s"(a.k_cos), "s"(a.k_sin), "s"(a.q_cos), "s"(a.q_sin), "s"(a.ksc), "s"(a.kzp), "s"(a.vsc), "s"(a.vzp), "s"(a.part), "s"(a.out), "s"(a.Skv), "s"(a.scale_log2));
    constexpr int NL = 2;
    constexpr int LS = 896;
    const unsigned plt = H * nb * (DH * 2), pfl = plt + NL * nb * LS, psk = pfl + H * nb * 16, pep = psk + 128;
    const rsrc_t pr = mkrsrc(a.part, pep);
#define DMA3(V, A, B, P) "s_mov_b32 m0, " B "\n\ts_nop 0\n\t" \
                     "global_load_lds_dwordx4 " V ", " A P "\n\t" \
                     "global_load_lds_dwordx4 " V ", " A " offset:1024" P "\n\t" \
                     "global_load_lds_dwordx4 " V ", " A " offset:2048" P "\n\t"
    asm volatile(DMA3("%0", "%1", "%2", "") "s_mov_b32 m0, %4\n\ts_nop 0\n\tglobal_load_lds_dwordx4 %3, off"
                 :: "v"(lane * 16), "s"(a.q + wave * 1536), "s"(lds0 + LDS_QRAW + wave * 3072),
                    "v"(lane < 48 ? (const unsigned char*)((lane < 24 ? a.q_cos : a.q_sin - DH) + 8 * lane) : a.part + pep), "s"(lds0 + LDS_QT + wave * RECW) : "memory");
    SB();
    {
        const unsigned short* sp = wave == 0 ? a.ksc : wave == 1 ? a.kzp : wave == 2 ? a.vsc : a.vzp;
        const unsigned vs = ((lane >> 3) * Skv + RW * pos + (lane & 7) * 8) * 2;
        const unsigned ds = lds0 + LDS_SC + wave * 4096;
#define DMA1(A, B) "s_mov_b32 m0, " B "\n\ts_nop 0\n\tglobal_load_lds_dwordx4 %0, " A " nt\n\t"
        asm volatile(DMA1("%1", "%5") DMA1("%2", "%6") DMA1("%3", "%7") DMA1("%4", "%8")
                     :: "v"(vs), "s"(sp), "s"(sp + 8 * Skv), "s"(sp + 16 * Skv), "s"(sp + 24 * Skv), "s"(ds), "s"(ds + 1024), "s"(ds + 2048), "s"(ds + 3072) : "memory");
    }
    SB();
    const unsigned ltoff = plt + ((wave & 1) * nb + b) * LS + min(lane, 47) * 16;
    if (wave < NL) asm volatile(DMA1("%1", "%2") :: "v"(ltoff), "s"(a.part), "s"(lds0 + LDS_LT + wave * RECW) : "memory");
    SB();
    const unsigned tdma = now32();
    const int stag = (int)(((OPT & 2) ? (((b >> 3) * 26 + wave * 2 + (b & 1)) & 63) : ((((b >> 3) * 4 + wave) * 37) & 63)) * (TP / 2)) / 64 - TP / 4;
    unsigned sched = ((tdma + ADV) << 4) + stag - 4 * (TP / 2);
    unsigned vt = tdma << 4;
    int tru = -4;
    unsigned step = TP / 2, tmin = TMIN / 2;
    auto pace = [&]() {
        unsigned tgt = sched;
        sched += step;
        if ((int)(vt - tgt) > 0) tgt = vt;
        unsigned t = now32() << 4;
        while ((int)(t - tgt) < 0) { __builtin_amdgcn_s_sleep(1); t = now32() << 4; }
        vt = ((int)(t - 128 - tgt) > 0 ? t - 128 : tgt) + tmin;
        if constexpr (DBG == 2) { *reinterpret_cast<unsigned*>(smem + LDS_TR + 16 + wave * 1024 + (tru & 255) * 4) = (t - (sched - step)) >> 4; ++tru; }
    };
    {
        unsigned dst = lds0 + LDS_CS + wave * RECW;
        const unsigned v0 = lane * 16 + row0 * (DH * 2);
#pragma unroll 1
        for (int j = 0; j < 4; ++j) {
            pace();
            asm volatile(DMA3("%0", "%1", "%2", " nt") :: "v"(v0), "s"((j < 2 ? a.k_cos : a.k_sin) + (j & 1) * 8 * DH), "s"(dst) : "memory");
            dst += 3072;
        }
    }
    if (tid < MAXH) *reinterpret_cast<unsigned*>(smem + LDS_CNT + tid * 4) = 0;
    const rsrc_t kr = mkrsrc(a.qk, H * hstride);
    const rsrc_t vr = mkrsrc(a.qv, H * hstride);
    const unsigned kvoff = (row0 + r) * (DH * 4) + c * 16;
    auto issue = [&](HG& g, rsrc_t rs, unsigned so) {
#pragma unroll
        for (int x = 0; x < 3; ++x) g.d[x] = bld128<2>(rs, kvoff + 256 * x, so);
    };
    HG buf[4];
#define PRE(g) SB(); pace(); issue(buf[g], kr, (g) * (4 * DH * 4)); SB();
    asm volatile("s_waitcnt vmcnt(12) lgkmcnt(0)\n\ts_barrier" ::: "memory");
    unsigned tbar = DBG ? now32() : 0;
    PRE(0)
    const unsigned qoff = (lane < 48 ? lane : lane - 48) * 4;
    const unsigned* qt = reinterpret_cast<const unsigned*>(smem + LDS_QT + wave * RECW + qoff);
    unsigned qc0 = qt[0], qc1 = qt[HALF / 2], qs0 = qt[DH / 2], qs1 = qt[(DH + HALF) / 2];
    u64 tag;
    {
        const u32x2 e = *reinterpret_cast<const u32x2*>(smem + LDS_QT + wave * RECW + 768);
        tag = (((u64)__builtin_amdgcn_readfirstlane(e[1]) << 32) | __builtin_amdgcn_readfirstlane(e[0])) * 6364136223846793005ull + 1442695040888963407ull;
        asm volatile("" : "+s"(tag));
    }
    const unsigned t32 = tag >> 32;
    const unsigned char* qraw = smem + LDS_QRAW + qoff;
    unsigned char* qrot = smem + LDS_Q + wave * (DH * 4) + 2 * qoff;
    auto rotq = [&]() {
        const unsigned q_lo = *reinterpret_cast<const unsigned*>(qraw), q_hi = *reinterpret_cast<const unsigned*>(qraw + HALF * 2);
        qraw += DH * 2;
        const f32x2 lo = f32x2{bflo(q_lo), bfhi(q_lo)}, hi = f32x2{bflo(q_hi), bfhi(q_hi)};
        const f32x2 y1 = lo * f32x2{bflo(qc0), bfhi(qc0)}, y2 = -hi * f32x2{bflo(qs0), bfhi(qs0)};
        const f32x2 y3 = hi * f32x2{bflo(qc1), bfhi(qc1)}, y4 = lo * f32x2{bflo(qs1), bfhi(qs1)};
        const unsigned x1 = pk_bf16(y1[0], y1[1]), x2 = pk_bf16(y2[0], y2[1]), x3 = pk_bf16(y3[0], y3[1]), x4 = pk_bf16(y4[0], y4[1]);
        const f32x2 z1 = f32x2{bflo(x1), bfhi(x1)} + f32x2{bflo(x2), bfhi(x2)}, z2 = f32x2{bflo(x3), bfhi(x3)} + f32x2{bflo(x4), bfhi(x4)};
        const unsigned l2 = pk_bf16(z1[0], z1[1]), h2 = pk_bf16(z2[0], z2[1]);
        *reinterpret_cast<f32x2*>(qrot) = f32x2{bflo(l2), bfhi(l2)};
        *reinterpret_cast<f32x2*>(qrot + HALF * 4) = f32x2{bflo(h2), bfhi(h2)};
        asm volatile("" ::: "memory");
    };
    f32x4 qa[3], qb[3];
    const unsigned char* qh = smem + LDS_Q + wave * (DH * 4) + 16 * c;
    const unsigned char* qh1 = qh + (c < 8 ? 64 + HALF : 64 - HALF) * 4;
    auto loadq = [&]() {
        asm volatile("" ::: "memory");
#pragma unroll
        for (int x = 0; x < 3; ++x) qa[x] = *reinterpret_cast<const f32x4*>(qh + 256 * x);
        qb[0] = *reinterpret_cast<const f32x4*>(qh + HALF * 4);
        qb[1] = *reinterpret_cast<const f32x4*>(qh1);
        qb[2] = *reinterpret_cast<const f32x4*>(qh + (128 - HALF) * 4);
        asm volatile("" ::: "memory");
    };
    rotq();
    if (wave < NL) {
        const u32x4 e = *reinterpret_cast<const u32x4*>(smem + LDS_LT + wave * RECW + lane * 16);
        if (e[1] == t32 || e[3] == t32) __builtin_amdgcn_raw_buffer_store_b128(u32x4{0u, ~t32, 0u, ~t32}, pr, ltoff, 0, 16);
    }
    if (b < 8 && wave == 3) __builtin_amdgcn_raw_buffer_store_b64(u32x2{tdma, t32}, pr, lane == 0 ? psk + b * 8 : 0x80000000u, 0, 16);
    PRE(1)
    f32x2 cc[4][3][2], ss[4][3][2];
    const unsigned char* cb = smem + LDS_CS + wave * RECW + r * (DH * 2) + c * 8;
    asm volatile("s_waitcnt vmcnt(12)" ::: "memory");
#pragma unroll
    for (int k = 0; k < 4; ++k)
#pragma unroll
        for (int x = 0; x < 3; ++x) {
            const u32x2 t = *reinterpret_cast<const u32x2*>(cb + k * 1536 + 128 * x);
            cc[k][x][0] = f32x2{bflo(t[0]), bfhi(t[0])}; cc[k][x][1] = f32x2{bflo(t[1]), bfhi(t[1])};
            asm volatile("" : "+v"(cc[k][x][0]), "+v"(cc[k][x][1]));
        }
    PRE(2)
    loadq();
    const unsigned char* scp = smem + LDS_SC + (c & 4) * 1024 + (16 * wave + 4 * (c & 3) + r) * 2;
    unsigned sc = *reinterpret_cast<const unsigned short*>(scp);
    auto loadsin = [&](int k) {
        const unsigned char* sb1 = cb + (c < 8 ? (64 + HALF) * 2 : (64 - HALF) * 2);
        const unsigned sg = c < 8 ? 0u : 0x80008000u;
        u32x2 t[3];
        t[0] = *reinterpret_cast<const u32x2*>(cb + 6144 + k * 1536 + HALF * 2);
        t[1] = *reinterpret_cast<const u32x2*>(sb1 + 6144 + k * 1536) ^ u32x2{sg, sg};
        t[2] = *reinterpret_cast<const u32x2*>(cb + 6144 + k * 1536 + (128 - HALF) * 2) ^ u32x2{0x80008000u, 0x80008000u};
#pragma unroll
        for (int x = 0; x < 3; ++x) {
            ss[k][x][0] = f32x2{bflo(t[x][0]), bfhi(t[x][0])}; ss[k][x][1] = f32x2{bflo(t[x][1]), bfhi(t[x][1])};
            asm volatile("" : "+v"(ss[k][x][0]), "+v"(ss[k][x][1]));
        }
    };
    asm volatile("s_waitcnt vmcnt(12)" ::: "memory");
    loadsin(0); loadsin(1);
    PRE(3)
    asm volatile("s_waitcnt vmcnt(12)" ::: "memory");
    loadsin(2); loadsin(3);
    unsigned th1 = 0, th4 = 0;
    unsigned char* prec = smem + LDS_P + wave * (H * 128) + r * 32 + (c & 7) * 4;
    auto softmax = [&](float s, int h) {
        float m = fmaxf(s, dpp<0xb1>(s));
        m = xrow_max(fmaxf(m, dpp<0x4e>(m)));
        const float p = __builtin_amdgcn_exp2f(s - m);
        float l = p + dpp<0xb1>(p);
        l = xrow_sum(l + dpp<0x4e>(l));
        const float t = (c & 1) ? l : m;
        const int x = __builtin_amdgcn_update_dpp(__float_as_int(p), __float_as_int(t), 0x114, 0xf, 0xa, false);
        *reinterpret_cast<int*>(prec + h * 128) = x;
    };
    float sprev = 0.f;

#pragma unroll 1
    for (int h = 0; h < H; ++h) {
        const bool more = h + 1 < H;
        const rsrc_t nx = more ? kr : vr;
        const unsigned nso = more ? (h + 1) * hstride : 0;
        float scf = bflo(sc);
        asm volatile("" : "+v"(scf) :: "memory");
        SB();
        scp += more ? 128 : 4096 + 128;
        sc = *reinterpret_cast<const unsigned short*>(scp);
        SB();
        float sreg = 0.f;
#define KSTEP(k) { \
            const float kz = dpp<0x154 + (k)>(scf); \
            f32x2 kz2 = f32x2{kz, kz}; \
            asm("" : "+v"(kz2)); \
            f32x2 a2 = f32x2{0.f, 0.f}; \
            _Pragma("unroll") for (int x = 0; x < 3; ++x) { \
                _Pragma("unroll") for (int e = 0; e < 2; ++e) { \
                    const f32x2 w = __builtin_elementwise_fma(f32x2{qb[x][2 * e], qb[x][2 * e + 1]}, ss[k][x][e], f32x2{qa[x][2 * e], qa[x][2 * e + 1]} * cc[k][x][e]); \
                    const f32x2 kf = f32x2{(float)buf[k].d[x][2 * e], (float)buf[k].d[x][2 * e + 1]} - kz2; \
                    a2 = __builtin_elementwise_fma(kf, w, a2); \
                } \
            } \
            float sa = a2[0] + a2[1]; \
            asm volatile("" : "+v"(sa) :: "memory"); \
            SB(); pace(); issue(buf[k], nx, nso + (k) * (4 * DH * 4)); SB(); \
            if ((k) == 0) { softmax(sprev, (h - 1) & (H - 1)); SB(); }   \
            if ((k) == 1) { rotq(); SB(); } \
            if ((k) == 2) { \
                if (wave == 1 && h == 4 + ((b >> 3) & 15)) \
                    asm volatile("s_mov_b32 m0, %2\n\ts_nop 0\n\tglobal_load_lds_dword %0, %1 sc1" :: "v"(psk + (lane & 15) * 4), "s"(a.part), "s"(lds0 + LDS_SK) : "memory"); \
                if (h == 25) {        \
                    const u32x2 e = *reinterpret_cast<const u32x2*>(smem + LDS_SK + (lane & 7) * 8); \
                    const int d = (int)(tdma - e[0]); \
                    if (__all(e[1] == t32 && d > -400 && d < 400)) { \
                        int sum = d + __builtin_amdgcn_mov_dpp(d, 0xb1, 0xf, 0xf, true); \
                        sum += __builtin_amdgcn_mov_dpp(sum, 0x4e, 0xf, 0xf, true); \
                        sum += __builtin_amdgcn_mov_dpp(sum, 0x124, 0xf, 0xf, true); \
                        const int adj = (__builtin_amdgcn_readfirstlane(sum) * 7) >> 9;        \
                        step -= adj; tmin -= adj; \
                    } \
                } \
                SB(); \
            } \
            if ((k) == 3) { loadq(); SB(); } \
            sa = row16_allsum(sa); \
            sreg = (c & 3) == (k) ? sa : sreg; }
        KSTEP(0) KSTEP(1) KSTEP(2) KSTEP(3)
        sprev = sreg * (scf * a.scale_log2);
        if (DBG) { if (h == 7) th1 = now32(); }
    }
    softmax(sprev, H - 1);
    if (DBG) th4 = now32();

    const rsrc_t nr = mkrsrc(a.qk, 0);
    auto merge = [&](int hd, bool direct) {
        unsigned char* rec = smem + LDS_REC + hd * (DH * 4);
        const unsigned char* mlp = smem + LDS_P + hd * 128 + 16;
        f32x4 ow[4]; float mv[4];
#pragma unroll
        for (int w = 0; w < 4; ++w) {
            mv[w] = *reinterpret_cast<const float*>(mlp + w * (H * 128));
            ow[w] = *reinterpret_cast<const f32x4*>(rec + w * RECW + lane * 16);
        }
        const float M = fmaxf(fmaxf(mv[0], mv[1]), fmaxf(mv[2], mv[3]));
        f32x4 t = f32x4{0.f, 0.f, 0.f, 0.f};
#pragma unroll
        for (int w = 0; w < 4; ++w) t += __builtin_amdgcn_exp2f(mv[w] - M) * ow[w];
        const u32x2 v = u32x2{pk_bf16(t[0], t[1]), pk_bf16(t[2], t[3])};
        if (direct) __builtin_amdgcn_raw_buffer_store_b128(u32x4{v[0], t32, v[1], t32}, pr, lane < 48 ? lane * 16 : 0x80000000u, plt + ((hd - (H - NL)) * nb + b) * LS, 16);
        else *reinterpret_cast<u32x2*>(rec + lane * 8) = v;
    };
    unsigned old = 0, flo = 0x80000000u;
    u32x4 flv = {};
    u32x4 ev[3] = {};
    unsigned eo[3] = {};
    unsigned el[3] = {};
    auto gather0 = [&]() {
        int lane = tid & 63;
        asm volatile("" : "+v"(lane));
#pragma unroll
        for (int p = 0; p < 3; ++p) {
            const int t = lane + 64 * p, hi_ = t / 24, k = t - hi_ * 24;
            const int hh = 8 * wave + hi_;
            el[p] = hh * (DH * 4) + k * 16;
            eo[p] = hh < H - NL ? (hh * nb + b) * (DH * 2) + k * 16 : 0x80000000u;
        }
        flo = lane < 8 ? pfl + ((8 * wave + lane) * nb + b) * 16 : 0x80000000u;
        asm volatile("" : "+v"(el[0]), "+v"(el[1]), "+v"(el[2]), "+v"(eo[0]), "+v"(eo[1]), "+v"(eo[2]), "+v"(flo));
    };
    auto gather1 = [&]() {
#pragma unroll
        for (int p = 0; p < 3; ++p) ev[p] = *reinterpret_cast<const u32x4*>(smem + LDS_REC + el[p]);
        f32x2 ml[4];
#pragma unroll
        for (int w = 0; w < 4; ++w) ml[w] = *reinterpret_cast<const f32x2*>(smem + LDS_P + w * (H * 128) + (8 * wave + (lane & 7)) * 128 + 16);
        const float M = fmaxf(fmaxf(ml[0][0], ml[1][0]), fmaxf(ml[2][0], ml[3][0]));
        float L = 0.f;
#pragma unroll
        for (int w = 0; w < 4; ++w) L += __builtin_amdgcn_exp2f(ml[w][0] - M) * ml[w][1];
        flv = u32x4{__float_as_uint(M), t32, __float_as_uint(L), (unsigned)tag};
        asm volatile("" : "+v"(ev[0]), "+v"(ev[1]), "+v"(ev[2]), "+v"(flv));
    };
    auto estore = [&]() {
#pragma unroll
        for (int p = 0; p < 3; ++p) __builtin_amdgcn_raw_buffer_store_b128(ev[p], pr, eo[p], 0, 16);
        if (DBG == 4) tbar = now32();
    };
    float pv = *reinterpret_cast<const float*>(prec);
    f32x2 acc[3][2];
    float y;
    auto prep = [&]() {
        y = bflo(sc);
        y = (c & 4) ? y : y * pv;
        asm volatile("" : "+v"(y) :: "memory");
        SB();
        scp += 128;
        sc = *reinterpret_cast<const unsigned short*>(scp);
        prec += 128;
        pv = *reinterpret_cast<const float*>(prec);
        SB();
    };
#define VSTEP(k, hn) { \
        const float pw = dpp<0x150 + (k)>(y); \
        const float vz = dpp<0x154 + (k)>(y); \
        f32x2 pw2 = f32x2{pw, pw}, vz2 = f32x2{vz, vz}; \
        asm("" : "+v"(pw2), "+v"(vz2)); \
        _Pragma("unroll") for (int x = 0; x < 3; ++x) \
            _Pragma("unroll") for (int e = 0; e < 2; ++e) { \
                const f32x2 vf = f32x2{(float)buf[k].d[x][2 * e], (float)buf[k].d[x][2 * e + 1]} - vz2; \
                acc[x][e] = (k) == 0 ? pw2 * vf : __builtin_elementwise_fma(pw2, vf, acc[x][e]); \
            } \
        asm volatile("" : "+v"(acc[0][0]), "+v"(acc[0][1]), "+v"(acc[1][0]), "+v"(acc[1][1]), "+v"(acc[2][0]), "+v"(acc[2][1]) :: "memory"); \
        SB(); if ((hn) < H) pace(); issue(buf[k], (hn) < H ? vr : nr, (hn) * hstride + (k) * (4 * DH * 4)); SB(); \
        if ((k) == 1 && (hn) == H - 1) { asm volatile("s_waitcnt lgkmcnt(0)\n\ts_barrier" ::: "memory"); tmin = 0; SB(); } \
        if ((k) == 2 && (hn) == H - 2) { gather0(); SB(); } \
        if ((k) == 2 && (hn) == H - 1) { gather1(); SB(); } \
        if ((k) == (ES & 3) && (hn) == H - 1 + (ES >> 2)) { estore(); SB(); } }
    prep();
    VSTEP(0, 1)
#pragma unroll 1
    for (int h = 0; h < H; ++h) {
        VSTEP(1, h + 1) VSTEP(2, h + 1) VSTEP(3, h + 1)
        f32x4 o;
#pragma unroll
        for (int e = 0; e < 4; ++e) {
            const float t0 = sw32(acc[0][e >> 1][e & 1], acc[2][e >> 1][e & 1]);
            const float t1 = sw32(acc[1][e >> 1][e & 1], acc[1][e >> 1][e & 1]);
            o[e] = sw16(t0, t1);
        }
        asm volatile("s_mov_b64 exec, %5\n\tds_write_b128 %1, %2\n\ts_mov_b64 exec, 1\n\tds_add_rtn_u32 %0, %3, %4\n\ts_mov_b64 exec, -1"
                     : "+v"(old) : "v"(lds0 + LDS_REC + h * (DH * 4) + wave * RECW + lane * 16), "v"(o),
                       "v"(lds0 + LDS_CNT + h * 4), "v"(1u), "s"(0xffffffffffffull) : "memory");
        SB();
        prep();
        VSTEP(0, h + 2)
        asm volatile("s_waitcnt lgkmcnt(0)" : "+v"(old) :: "memory");
        if (__builtin_amdgcn_readfirstlane(old) == 3) merge(h, h >= H - NL);
        SB();
    }
    if (__builtin_amdgcn_readfirstlane(old) == 3) asm volatile("s_waitcnt vmcnt(1)" ::: "memory");
    else asm volatile("s_waitcnt vmcnt(0)" ::: "memory");
    __builtin_amdgcn_raw_buffer_store_b128(flv, pr, flo, 0, 16);
    const unsigned tend = DBG ? now32() : 0;
    unsigned tfl = 0, tpl = 0;

    constexpr int NCN = 3 * (H - NL), NC = NCN + 6 * NL;
    if (b < NC) {
        const bool late = b >= NCN;
        const int h = late ? H - NL + (b - NCN) / 6 : b / 3, chunk = late ? (b - NCN) % 6 : b % 3;
        const unsigned src = 64 * wave + lane;
        const unsigned rstride = late ? LS : DH * 2;
        const unsigned rbase = late ? plt + (h - (H - NL)) * nb * LS : h * nb * (DH * 2);
        const unsigned xo = (64 * wave + (lane >> 3)) * rstride + chunk * 128 + (lane & 7) * 16;
        const unsigned oo = late ? ((r & 1) ? 0x80000000u : (h * DH + chunk * 32 + (c & 7) * 4 + r + (c >> 3)) * 2) : (h * DH + chunk * 64 + (c & 7) * 8 + 2 * r + (c >> 3)) * 2;
        asm volatile("s_waitcnt vmcnt(0)" ::: "memory");
        if (DBG == 4) th4 = now32();
        const unsigned tgt = sched - stag + HOLD * 16;
        while ((int)((now32() << 4) - tgt) < 0) __builtin_amdgcn_s_sleep(1);
        i32x4 y, x[8];
#pragma unroll 1
        for (int n = 0; n < (1 << 20); ++n) {
            y = bld128<16>(pr, pfl + (h * nb + src) * 16, 0);
            if ((unsigned)y[1] == t32 && (unsigned)y[3] == (unsigned)tag) break;
            asm volatile("" ::: "memory");
            __builtin_amdgcn_s_sleep(2);
        }
        asm volatile("" ::: "memory");
        if (DBG) tfl = now32();
#pragma unroll
        for (int i = 0; i < 8; ++i) x[i] = bld128<16>(pr, xo, rbase + i * 8 * rstride);
        SB();
        float* s_o = reinterpret_cast<float*>(smem + LDS_QRAW);
        const float m = __int_as_float(y[0]);
        const float mw = xrow_max(row16_allmax(m));
        const float lw = xrow_sum(row16_allsum(__builtin_amdgcn_exp2f(m - mw) * __int_as_float(y[2])));
        *reinterpret_cast<f32x2*>(&s_o[256 + wave * 2]) = f32x2{mw, lw};
        asm volatile("s_waitcnt lgkmcnt(0)\n\ts_barrier" ::: "memory");
        const f32x4 a0 = *reinterpret_cast<const f32x4*>(&s_o[256]), a1 = *reinterpret_cast<const f32x4*>(&s_o[260]);
        const float M = fmaxf(fmaxf(a0[0], a0[2]), fmaxf(a1[0], a1[2]));
        const float L = (__builtin_amdgcn_exp2f(a0[0] - M) * a0[1] + __builtin_amdgcn_exp2f(a0[2] - M) * a0[3]) + (__builtin_amdgcn_exp2f(a1[0] - M) * a1[1] + __builtin_amdgcn_exp2f(a1[2] - M) * a1[3]);
        const float f = __builtin_amdgcn_exp2f(m - M) / L;
        float fs[8];
#pragma unroll
        for (int i = 0; i < 8; ++i) fs[i] = __int_as_float(__builtin_amdgcn_ds_bpermute(((lane >> 3) + 8 * i) * 4, __float_as_int(f)));
        SB();
        if (late) {
#pragma unroll 1
            for (int n = 0; n < (1 << 20); ++n) {
                unsigned bad = 0;
#pragma unroll
                for (int i = 0; i < 8; ++i) bad |= (x[i][1] ^ t32) | (x[i][3] ^ t32);
                if (__all(bad == 0)) break;
                asm volatile("" ::: "memory");
#pragma unroll
                for (int i = 0; i < 8; ++i) x[i] = bld128<16>(pr, xo, rbase + i * 8 * rstride);
            }
        }
        f32x2 o[4] = {};
#pragma unroll
        for (int i = 0; i < 8; ++i)
#pragma unroll
            for (int e = 0; e < 4; ++e) o[e] = __builtin_elementwise_fma(f32x2{fs[i], fs[i]}, f32x2{bflo(x[i][e]), bfhi(x[i][e])}, o[e]);
        if (DBG) { asm volatile("" : "+v"(o[0]), "+v"(o[1]), "+v"(o[2]), "+v"(o[3])); tpl = now32(); }
        const float w0 = sw16(sw32(o[0][0], o[2][0]), sw32(o[1][0], o[3][0])), w1 = sw16(sw32(o[0][1], o[2][1]), sw32(o[1][1], o[3][1]));
        s_o[wave * 64 + lane] = (c < 8 ? w0 : w1) + dpp<0x128>(c < 8 ? w1 : w0);
        asm volatile("s_waitcnt lgkmcnt(0)\n\ts_barrier" ::: "memory");
        if (wave == 0) {
            const float T = (s_o[lane] + s_o[64 + lane]) + (s_o[128 + lane] + s_o[192 + lane]);
            __builtin_amdgcn_raw_buffer_store_b16((unsigned short)pk_bf16(T, 0.f), mkrsrc(a.out, H * DH * 2), oo, 0, 0);
        }
        if (b == 0 && tid == 0) *reinterpret_cast<u64*>(a.part + pep) = tag;
    }
    if (DBG) {
        const unsigned tc1 = now32();
        u64* stp = reinterpret_cast<u64*>(a.part + 32 * 256 * 196 * 4) + (size_t)(b * 4 + wave) * 8;
        if (lane == 0) { stp[0] = tstart; stp[1] = tbar; stp[2] = DBG == 4 ? tend - (((tend << 4) - (sched - stag - step)) >> 4) : th1; stp[3] = DBG == 4 && b >= NC ? tend : th4; stp[4] = tend; stp[5] = b < NC ? tfl : tend; stp[6] = b < NC ? tpl : tend; stp[7] = tc1; }
        if (DBG == 2) *reinterpret_cast<u32x4*>(a.part + 32 * 256 * 196 * 4 + 65536 + (b * 4 + wave) * 1024 + lane * 16) = *reinterpret_cast<const u32x4*>(smem + LDS_TR + 16 + wave * 1024 + lane * 16);
    }
    asm volatile(".Lkv_b%c0:" :: "i"(ID));
}

template <int TP, int ADV, int TMIN, int HOLD, int PFN, int OPT, int ID, int DBG>
static void launch28(void* const* d_in, const int* in_sizes, void* d_out, void* d_ws, hipStream_t stream) {
    typedef const unsigned short* P;
    Args a;
    a.qk = (const int*)d_in[1]; a.qv = (const int*)d_in[4];
    a.k_cos = (P)d_in[9]; a.k_sin = (P)d_in[10];
    a.q = (P)d_in[0]; a.q_cos = (P)d_in[7]; a.q_sin = (P)d_in[8];
    a.ksc = (P)d_in[2]; a.kzp = (P)d_in[3]; a.vsc = (P)d_in[5]; a.vzp = (P)d_in[6];
    a.part = (unsigned char*)d_ws;
    a.out = (unsigned short*)d_out;
    a.Skv = in_sizes[9] / DH;
    a.scale_log2 = 1.4426950408889634f / sqrtf((float)DH);
    kv28_kernel<TP, ADV, TMIN, HOLD, PFN, OPT, ID, DBG><<<a.Skv / RW, 256, 0, stream>>>(a);
}

extern "C" void kernel_launch(void* const* d_in, const int* in_sizes, int n_in, void* d_out, int out_size, void* d_ws, size_t ws_size, hipStream_t stream) {
    launch28<80*16+8,230,80*16,165,16,2,0,0>(d_in, in_sizes, d_out, d_ws, stream);
}
```

```cpp
#include <hip/hip_runtime.h>
#include <hip/hip_bf16.h>
#include <cstdint>
using bf16 = __hip_bfloat16;
using i32x4 = __attribute__((ext_vector_type(4))) int;
using u32x4 = __attribute__((ext_vector_type(4))) unsigned;
using u32x2 = __attribute__((ext_vector_type(2))) unsigned;
using f32x2 = __attribute__((ext_vector_type(2))) float;
using f32x4 = __attribute__((ext_vector_type(4))) float;

constexpr int DH = 192;
constexpr int HALF = 96;
constexpr int RW = 64;
constexpr int MAXH = 32;

__device__ __forceinline__ float bflo(unsigned u) { return __uint_as_float(u << 16); }
__device__ __forceinline__ float bfhi(unsigned u) { return __uint_as_float(u & 0xffff0000u); }
__device__ __forceinline__ float rbf(float x) { return __bfloat162float(__float2bfloat16(x)); }

template <int CTRL> __device__ __forceinline__ float dpp(float x) {
    return __builtin_bit_cast(float, __builtin_amdgcn_mov_dpp(__builtin_bit_cast(int, x), CTRL, 0xf, 0xf, true));
}
__device__ __forceinline__ float row16_allsum(float x) {
    x += dpp<0x128>(x);
    x += dpp<0x124>(x);
    x += dpp<0x122>(x);
    x += dpp<0x121>(x);
    return x;
}
__device__ __forceinline__ float row16_allmax(float x) {
    x = fmaxf(x, dpp<0x128>(x));
    x = fmaxf(x, dpp<0x124>(x));
    x = fmaxf(x, dpp<0x122>(x));
    x = fmaxf(x, dpp<0x121>(x));
    return x;
}
__device__ __forceinline__ float xrow_max(float x) {
    auto s = __builtin_amdgcn_permlane16_swap(__float_as_uint(x), __float_as_uint(x), false, false);
    x = fmaxf(__uint_as_float(s[0]), __uint_as_float(s[1]));
    auto t = __builtin_amdgcn_permlane32_swap(__float_as_uint(x), __float_as_uint(x), false, false);
    return fmaxf(__uint_as_float(t[0]), __uint_as_float(t[1]));
}
__device__ __forceinline__ float xrow_sum(float x) {
    auto s = __builtin_amdgcn_permlane16_swap(__float_as_uint(x), __float_as_uint(x), false, false);
    x = __uint_as_float(s[0]) + __uint_as_float(s[1]);
    auto t = __builtin_amdgcn_permlane32_swap(__float_as_uint(x), __float_as_uint(x), false, false);
    return __uint_as_float(t[0]) + __uint_as_float(t[1]);
}
__device__ __forceinline__ float sw32(float a, float b) {
    auto t = __builtin_amdgcn_permlane32_swap(__float_as_uint(a), __float_as_uint(b), false, false);
    return __uint_as_float(t[0]) + __uint_as_float(t[1]);
}
__device__ __forceinline__ float sw16(float a, float b) {
    auto t = __builtin_amdgcn_permlane16_swap(__float_as_uint(a), __float_as_uint(b), false, false);
    return __uint_as_float(t[0]) + __uint_as_float(t[1]);
}

#define SB() __builtin_amdgcn_sched_barrier(0)
typedef __amdgpu_buffer_rsrc_t rsrc_t;
__device__ __forceinline__ rsrc_t mkrsrc(const void* p, unsigned bytes) {
    return __builtin_amdgcn_make_buffer_rsrc(const_cast<void*>(p), 0, bytes, 0x00020000);
}
template <int AUX> __device__ __forceinline__ i32x4 bld128(rsrc_t r, unsigned voff, unsigned soff) {
    return __builtin_bit_cast(i32x4, __builtin_amdgcn_raw_buffer_load_b128(r, voff, soff, AUX));
}
template <int AUX> __device__ __forceinline__ unsigned bld16(rsrc_t r, unsigned voff, unsigned soff) {
    return __builtin_amdgcn_raw_buffer_load_b16(r, voff, soff, AUX);
}

struct HG { i32x4 d[3]; };
typedef unsigned long long u64;
typedef __attribute__((address_space(3))) unsigned char lds_u8;

__device__ __forceinline__ unsigned pk_bf16(float lo, float hi) {
    unsigned r;
    asm("v_cvt_pk_bf16_f32 %0, %1, %2" : "=v"(r) : "v"(lo), "v"(hi));
    return r;
}
__device__ __forceinline__ unsigned now32() { return (unsigned)__builtin_amdgcn_s_memrealtime(); }

constexpr int LDS_REC = 0;
constexpr int RECW = MAXH * DH * 4;
constexpr int LDS_CS = 0;
constexpr int LDS_QT = 12288;
constexpr int LDS_LT = 13312;
constexpr int LDS_PF = 14336;
constexpr int LDS_QRAW = MAXH * 4 * DH * 4;
constexpr int LDS_Q = LDS_QRAW + MAXH * DH * 2;
constexpr int LDS_P = LDS_Q + 4 * DH * 4;
constexpr int LDS_SC = LDS_P + 4 * MAXH * 128;
constexpr int LDS_CNT = LDS_SC + 4 * MAXH * RW * 2;
constexpr int LDS_SK = LDS_CNT + 256;
constexpr int LDS_TR = LDS_CNT + 512;
constexpr int LDS_BYTES = LDS_TR + 4096 + 64;

struct Args {
    const int* qk; const int* qv;
    const unsigned short* k_cos; const unsigned short* k_sin;
    const unsigned short* q; const unsigned short* q_cos; const unsigned short* q_sin;
    const unsigned short* ksc; const unsigned short* kzp; const unsigned short* vsc; const unsigned short* vzp;
    unsigned char* part;
    unsigned short* out;
    int Skv; float scale_log2;
};

template <int TP, int ADV, int TMIN, int HOLD, int WIN, int OPT, int ID, int DBG>
__global__ __launch_bounds__(256) void kv30_kernel(Args a)
{
    __shared__ __attribute__((aligned(16))) unsigned char smem[LDS_BYTES];
    const unsigned tstart = DBG ? now32() : 0;
    constexpr int H = MAXH, ES = 7, PFN = 16;
    const int b = blockIdx.x;
    const int tid = threadIdx.x;
    const int lane = tid & 63;
    const int wave = __builtin_amdgcn_readfirstlane(tid >> 6);
    if (b < PFN) {
        u64 pc; unsigned len;
        asm volatile("s_getpc_b64 %0\n.Lkv_a%c2:\n\ts_mov_b32 %1, .Lkv_b%c2-.Lkv_a%c2-16" : "=s"(pc), "=s"(len) : "i"(ID));
        const unsigned o = tid * 16;
        asm volatile("s_mov_b32 m0, %5\n\ts_nop 0\n\tglobal_load_lds_dwordx4 %0, %4\n\tglobal_load_lds_dwordx4 %1, %4\n\tglobal_load_lds_dwordx4 %2, %4\n\tglobal_load_lds_dwordx4 %3, %4"
                     :: "v"(o), "v"(o + 4096), "v"(min(o + 8192, len)), "v"(min(o + 12288, len)), "s"(pc), "s"((unsigned)(size_t)(lds_u8*)smem + LDS_PF) : "memory");
    }
    SB();
    const int r = lane >> 4;
    const int c = lane & 15;
    const unsigned Skv = a.Skv;
    const unsigned hstride = Skv * (DH * 4);
    const unsigned nb = Skv / RW;
    const unsigned lds0 = (unsigned)(size_t)(lds_u8*)smem;
    const unsigned pos = (b & 7) * (nb >> 3) + (b >> 3);
    const unsigned row0 = RW * pos + 16 * wave;

    asm volatile("" :: "s"(a.qk), "s"(a.qv), "s"(a.k_cos), "s"(a.k_sin), "s"(a.q_cos), "s"(a.q_sin), "s"(a.ksc), "s"(a.kzp), "s"(a.vsc), "s"(a.vzp), "s"(a.part), "s"(a.out), "s"(a.Skv), "s"(a.scale_log2));
    constexpr int NL = 2;
    constexpr int LS = 896;
    const unsigned plt = H * nb * (DH * 2), pfl = plt + NL * nb * LS, psk = pfl + H * nb * 16, pep = psk + 128;
    const rsrc_t pr = mkrsrc(a.part, pep);
#define DMA3(V, A, B, P) "s_mov_b32 m0, " B "\n\ts_nop 0\n\t" \
                     "global_load_lds_dwordx4 " V ", " A P "\n\t" \
                     "global_load_lds_dwordx4 " V ", " A " offset:1024" P "\n\t" \
                     "global_load_lds_dwordx4 " V ", " A " offset:2048" P "\n\t"
    asm volatile(DMA3("%0", "%1", "%2", "") "s_mov_b32 m0, %4\n\ts_nop 0\n\tglobal_load_lds_dwordx4 %3, off"
                 :: "v"(lane * 16), "s"(a.q + wave * 1536), "s"(lds0 + LDS_QRAW + wave * 3072),
                    "v"(lane < 48 ? (const unsigned char*)((lane < 24 ? a.q_cos : a.q_sin - DH) + 8 * lane) : a.part + pep), "s"(lds0 + LDS_QT + wave * RECW) : "memory");
    SB();
    {
        const unsigned short* sp = wave == 0 ? a.ksc : wave == 1 ? a.kzp : wave == 2 ? a.vsc : a.vzp;
        const unsigned vs = ((lane >> 3) * Skv + RW * pos + (lane & 7) * 8) * 2;
        const unsigned ds = lds0 + LDS_SC + wave * 4096;
#define DMA1(A, B) "s_mov_b32 m0, " B "\n\ts_nop 0\n\tglobal_load_lds_dwordx4 %0, " A " nt\n\t"
        asm volatile(DMA1("%1", "%5") DMA1("%2", "%6") DMA1("%3", "%7") DMA1("%4", "%8")
                     :: "v"(vs), "s"(sp), "s"(sp + 8 * Skv), "s"(sp + 16 * Skv), "s"(sp + 24 * Skv), "s"(ds), "s"(ds + 1024), "s"(ds + 2048), "s"(ds + 3072) : "memory");
    }
    SB();
    const unsigned ltoff = plt + ((wave & 1) * nb + b) * LS + min(lane, 47) * 16;
    if (wave < NL) asm volatile(DMA1("%1", "%2") :: "v"(ltoff), "s"(a.part), "s"(lds0 + LDS_LT + wave * RECW) : "memory");
    SB();
    const unsigned tdma = now32();
    const int stag = (int)(((OPT & 2) ? (((b >> 3) * 26 + wave * 2 + (b & 1)) & 63) : ((((b >> 3) * 4 + wave) * 37) & 63)) * (TP / 2)) / 64 - TP / 4;
    unsigned sched = ((tdma + ADV) << 4) + stag - 4 * (TP / 2);
    unsigned vt = tdma << 4;
    int tru = -4;
    unsigned step = TP / 2, tmin = TMIN / 2;
    auto pace = [&](bool ctl = false) {
        unsigned tgt = sched;
        sched += step;
        if ((int)(vt - tgt) > 0) tgt = vt;
        unsigned t = now32() << 4;
        while ((int)(t - tgt) < 0) { __builtin_amdgcn_s_sleep(1); t = now32() << 4; }
        vt = ((int)(t - 128 - tgt) > 0 ? t - 128 : tgt) + tmin;
        if (ctl) {
            if constexpr (WIN > 0 && WIN < 16) asm volatile("s_waitcnt vmcnt(%0)" :: "i"(WIN) : "memory");
            if constexpr (WIN >= 16) {
                const unsigned vm = __builtin_amdgcn_s_getreg(7 | (3 << 11));
                if (vm > (WIN & 15)) vt += (vm - (WIN & 15)) * (WIN >> 4);
            }
        }
        if constexpr (DBG == 2) { *reinterpret_cast<unsigned*>(smem + LDS_TR + 16 + wave * 1024 + (tru & 255) * 4) = ((t - (sched - step)) >> 4) | ((OPT & 8) ? (__builtin_amdgcn_s_getreg(7 | (3 << 11)) << 16) : 0); ++tru; }
    };
    {
        unsigned dst = lds0 + LDS_CS + wave * RECW;
        const unsigned v0 = lane * 16 + row0 * (DH * 2);
#pragma unroll 1
        for (int j = 0; j < 4; ++j) {
            pace();
            asm volatile(DMA3("%0", "%1", "%2", " nt") :: "v"(v0), "s"((j < 2 ? a.k_cos : a.k_sin) + (j & 1) * 8 * DH), "s"(dst) : "memory");
            dst += 3072;
        }
    }
    if (tid < MAXH) *reinterpret_cast<unsigned*>(smem + LDS_CNT + tid * 4) = 0;
    const rsrc_t kr = mkrsrc(a.qk, H * hstride);
    const rsrc_t vr = mkrsrc(a.qv, H * hstride);
    const unsigned kvoff = (row0 + r) * (DH * 4) + c * 16;
    auto issue = [&](HG& g, rsrc_t rs, unsigned so) {
#pragma unroll
        for (int x = 0; x < 3; ++x) g.d[x] = bld128<2>(rs, kvoff + 256 * x, so);
    };
    HG buf[4];
#define PRE(g) SB(); pace(); issue(buf[g], kr, (g) * (4 * DH * 4)); SB();
    asm volatile("s_waitcnt vmcnt(12) lgkmcnt(0)\n\ts_barrier" ::: "memory");
    unsigned tbar = DBG ? now32() : 0;
    PRE(0)
    const unsigned qoff = (lane < 48 ? lane : lane - 48) * 4;
    const unsigned* qt = reinterpret_cast<const unsigned*>(smem + LDS_QT + wave * RECW + qoff);
    unsigned qc0 = qt[0], qc1 = qt[HALF / 2], qs0 = qt[DH / 2], qs1 = qt[(DH + HALF) / 2];
    u64 tag;
    {
        const u32x2 e = *reinterpret_cast<const u32x2*>(smem + LDS_QT + wave * RECW + 768);
        tag = (((u64)__builtin_amdgcn_readfirstlane(e[1]) << 32) | __builtin_amdgcn_readfirstlane(e[0])) * 6364136223846793005ull + 1442695040888963407ull;
        asm volatile("" : "+s"(tag));
    }
    const unsigned t32 = tag >> 32;
    const unsigned char* qraw = smem + LDS_QRAW + qoff;
    unsigned char* qrot = smem + LDS_Q + wave * (DH * 4) + 2 * qoff;
    auto rotq = [&]() {
        const unsigned q_lo = *reinterpret_cast<const unsigned*>(qraw), q_hi = *reinterpret_cast<const unsigned*>(qraw + HALF * 2);
        qraw += DH * 2;
        const f32x2 lo = f32x2{bflo(q_lo), bfhi(q_lo)}, hi = f32x2{bflo(q_hi), bfhi(q_hi)};
        const f32x2 y1 = lo * f32x2{bflo(qc0), bfhi(qc0)}, y2 = -hi * f32x2{bflo(qs0), bfhi(qs0)};
        const f32x2 y3 = hi * f32x2{bflo(qc1), bfhi(qc1)}, y4 = lo * f32x2{bflo(qs1), bfhi(qs1)};
        const unsigned x1 = pk_bf16(y1[0], y1[1]), x2 = pk_bf16(y2[0], y2[1]), x3 = pk_bf16(y3[0], y3[1]), x4 = pk_bf16(y4[0], y4[1]);
        const f32x2 z1 = f32x2{bflo(x1), bfhi(x1)} + f32x2{bflo(x2), bfhi(x2)}, z2 = f32x2{bflo(x3), bfhi(x3)} + f32x2{bflo(x4), bfhi(x4)};
        const unsigned l2 = pk_bf16(z1[0], z1[1]), h2 = pk_bf16(z2[0], z2[1]);
        *reinterpret_cast<f32x2*>(qrot) = f32x2{bflo(l2), bfhi(l2)};
        *reinterpret_cast<f32x2*>(qrot + HALF * 4) = f32x2{bflo(h2), bfhi(h2)};
        asm volatile("" ::: "memory");
    };
    f32x4 qa[3], qb[3];
    const unsigned char* qh = smem + LDS_Q + wave * (DH * 4) + 16 * c;
    const unsigned char* qh1 = qh + (c < 8 ? 64 + HALF : 64 - HALF) * 4;
    auto loadq = [&]() {
        asm volatile("" ::: "memory");
#pragma unroll
        for (int x = 0; x < 3; ++x) qa[x] = *reinterpret_cast<const f32x4*>(qh + 256 * x);
        qb[0] = *reinterpret_cast<const f32x4*>(qh + HALF * 4);
        qb[1] = *reinterpret_cast<const f32x4*>(qh1);
        qb[2] = *reinterpret_cast<const f32x4*>(qh + (128 - HALF) * 4);
        asm volatile("" ::: "memory");
    };
    rotq();
    if (wave < NL) {
        const u32x4 e = *reinterpret_cast<const u32x4*>(smem + LDS_LT + wave * RECW + lane * 16);
        if (e[1] == t32 || e[3] == t32) __builtin_amdgcn_raw_buffer_store_b128(u32x4{0u, ~t32, 0u, ~t32}, pr, ltoff, 0, 16);
    }
    if (b < 8 && wave == 3) __builtin_amdgcn_raw_buffer_store_b64(u32x2{tdma, t32}, pr, lane == 0 ? psk + b * 8 : 0x80000000u, 0, 16);
    PRE(1)
    f32x2 cc[4][3][2], ss[4][3][2];
    const unsigned char* cb = smem + LDS_CS + wave * RECW + r * (DH * 2) + c * 8;
    asm volatile("s_waitcnt vmcnt(12)" ::: "memory");
#pragma unroll
    for (int k = 0; k < 4; ++k)
#pragma unroll
        for (int x = 0; x < 3; ++x) {
            const u32x2 t = *reinterpret_cast<const u32x2*>(cb + k * 1536 + 128 * x);
            cc[k][x][0] = f32x2{bflo(t[0]), bfhi(t[0])}; cc[k][x][1] = f32x2{bflo(t[1]), bfhi(t[1])};
            asm volatile("" : "+v"(cc[k][x][0]), "+v"(cc[k][x][1]));
        }
    PRE(2)
    loadq();
    const unsigned char* scp = smem + LDS_SC + (c & 4) * 1024 + (16 * wave + 4 * (c & 3) + r) * 2;
    unsigned sc = *reinterpret_cast<const unsigned short*>(scp);
    auto loadsin = [&](int k) {
        const unsigned char* sb1 = cb + (c < 8 ? (64 + HALF) * 2 : (64 - HALF) * 2);
        const unsigned sg = c < 8 ? 0u : 0x80008000u;
        u32x2 t[3];
        t[0] = *reinterpret_cast<const u32x2*>(cb + 6144 + k * 1536 + HALF * 2);
        t[1] = *reinterpret_cast<const u32x2*>(sb1 + 6144 + k * 1536) ^ u32x2{sg, sg};
        t[2] = *reinterpret_cast<const u32x2*>(cb + 6144 + k * 1536 + (128 - HALF) * 2) ^ u32x2{0x80008000u, 0x80008000u};
#pragma unroll
        for (int x = 0; x < 3; ++x) {
            ss[k][x][0] = f32x2{bflo(t[x][0]), bfhi(t[x][0])}; ss[k][x][1] = f32x2{bflo(t[x][1]), bfhi(t[x][1])};
            asm volatile("" : "+v"(ss[k][x][0]), "+v"(ss[k][x][1]));
        }
    };
    asm volatile("s_waitcnt vmcnt(12)" ::: "memory");
    loadsin(0); loadsin(1);
    PRE(3)
    asm volatile("s_waitcnt vmcnt(12)" ::: "memory");
    loadsin(2); loadsin(3);
    unsigned th1 = 0, th4 = 0;
    unsigned char* prec = smem + LDS_P + wave * (H * 128) + r * 32 + (c & 7) * 4;
    auto softmax = [&](float s, int h) {
        float m = fmaxf(s, dpp<0xb1>(s));
        m = xrow_max(fmaxf(m, dpp<0x4e>(m)));
        const float p = __builtin_amdgcn_exp2f(s - m);
        float l = p + dpp<0xb1>(p);
        l = xrow_sum(l + dpp<0x4e>(l));
        const float t = (c & 1) ? l : m;
        const int x = __builtin_amdgcn_update_dpp(__float_as_int(p), __float_as_int(t), 0x114, 0xf, 0xa, false);
        *reinterpret_cast<int*>(prec + h * 128) = x;
    };
    float sprev = 0.f;

#pragma unroll 1
    for (int h = 0; h < H; ++h) {
        const bool more = h + 1 < H;
        const rsrc_t nx = more ? kr : vr;
        const unsigned nso = more ? (h + 1) * hstride : 0;
        float scf = bflo(sc);
        asm volatile("" : "+v"(scf) :: "memory");
        SB();
        scp += more ? 128 : 4096 + 128;
        sc = *reinterpret_cast<const unsigned short*>(scp);
        SB();
        float sreg = 0.f;
#define KSTEP(k) { \
            const float kz = dpp<0x154 + (k)>(scf); \
            f32x2 kz2 = f32x2{kz, kz}; \
            asm("" : "+v"(kz2)); \
            f32x2 a2 = f32x2{0.f, 0.f}; \
            _Pragma("unroll") for (int x = 0; x < 3; ++x) { \
                _Pragma("unroll") for (int e = 0; e < 2; ++e) { \
                    const f32x2 w = __builtin_elementwise_fma(f32x2{qb[x][2 * e], qb[x][2 * e + 1]}, ss[k][x][e], f32x2{qa[x][2 * e], qa[x][2 * e + 1]} * cc[k][x][e]); \
                    const f32x2 kf = f32x2{(float)buf[k].d[x][2 * e], (float)buf[k].d[x][2 * e + 1]} - kz2; \
                    a2 = __builtin_elementwise_fma(kf, w, a2); \
                } \
            } \
            float sa = a2[0] + a2[1]; \
            asm volatile("" : "+v"(sa) :: "memory"); \
            SB(); pace(true); issue(buf[k], nx, nso + (k) * (4 * DH * 4)); SB(); \
            if ((k) == 0) { softmax(sprev, (h - 1) & (H - 1)); SB(); }   \
            if ((k) == 1) { rotq(); SB(); } \
            if ((k) == 2) { \
                if (wave == 1 && h == ((OPT & 4) ? 1 + ((b >> 3) & 3) : 4 + ((b >> 3) & 15))) \
                    asm volatile("s_mov_b32 m0, %2\n\ts_nop 0\n\tglobal_load_lds_dword %0, %1 sc1" :: "v"(psk + (lane & 15) * 4), "s"(a.part), "s"(lds0 + LDS_SK) : "memory"); \
                if (h == ((OPT & 4) ? 6 : 25)) {        \
                    const u32x2 e = *reinterpret_cast<const u32x2*>(smem + LDS_SK + (lane & 7) * 8); \
                    const int d = (int)(tdma - e[0]); \
                    if (__all(e[1] == t32 && d > -400 && d < 400)) { \
                        int sum = d + __builtin_amdgcn_mov_dpp(d, 0xb1, 0xf, 0xf, true); \
                        sum += __builtin_amdgcn_mov_dpp(sum, 0x4e, 0xf, 0xf, true); \
                        sum += __builtin_amdgcn_mov_dpp(sum, 0x124, 0xf, 0xf, true); \
                        const int adj = (__builtin_amdgcn_readfirstlane(sum) * ((OPT & 4) ? 73 : 112) + 4096) >> 13;        \
                        step -= adj; tmin -= adj; \
                    } \
                } \
                SB(); \
            } \
            if ((k) == 3) { loadq(); SB(); } \
            sa = row16_allsum(sa); \
            sreg = (c & 3) == (k) ? sa : sreg; }
        KSTEP(0) KSTEP(1) KSTEP(2) KSTEP(3)
        sprev = sreg * (scf * a.scale_log2);
        if (DBG) { if (h == 7) th1 = now32(); }
    }
    softmax(sprev, H - 1);
    if (DBG) th4 = now32();

    const rsrc_t nr = mkrsrc(a.qk, 0);
    auto merge = [&](int hd, bool direct) {
        unsigned char* rec = smem + LDS_REC + hd * (DH * 4);
        const unsigned char* mlp = smem + LDS_P + hd * 128 + 16;
        f32x4 ow[4]; float mv[4];
#pragma unroll
        for (int w = 0; w < 4; ++w) {
            mv[w] = *reinterpret_cast<const float*>(mlp + w * (H * 128));
            ow[w] = *reinterpret_cast<const f32x4*>(rec + w * RECW + lane * 16);
        }
        const float M = fmaxf(fmaxf(mv[0], mv[1]), fmaxf(mv[2], mv[3]));
        f32x4 t = f32x4{0.f, 0.f, 0.f, 0.f};
#pragma unroll
        for (int w = 0; w < 4; ++w) t += __builtin_amdgcn_exp2f(mv[w] - M) * ow[w];
        const u32x2 v = u32x2{pk_bf16(t[0], t[1]), pk_bf16(t[2], t[3])};
        if (direct) __builtin_amdgcn_raw_buffer_store_b128(u32x4{v[0], t32, v[1], t32}, pr, lane < 48 ? lane * 16 : 0x80000000u, plt + ((hd - (H - NL)) * nb + b) * LS, 16);
        else *reinterpret_cast<u32x2*>(rec + lane * 8) = v;
    };
    unsigned old = 0, flo = 0x80000000u;
    u32x4 flv = {};
    u32x4 ev[3] = {};
    unsigned eo[3] = {};
    unsigned el[3] = {};
    auto gather0 = [&]() {
        int lane = tid & 63;
        asm volatile("" : "+v"(lane));
#pragma unroll
        for (int p = 0; p < 3; ++p) {
            const int t = lane + 64 * p, hi_ = t / 24, k = t - hi_ * 24;
            const int hh = 8 * wave + hi_;
            el[p] = hh * (DH * 4) + k * 16;
            eo[p] = hh < H - NL ? (hh * nb + b) * (DH * 2) + k * 16 : 0x80000000u;
        }
        flo = lane < 8 ? pfl + ((8 * wave + lane) * nb + b) * 16 : 0x80000000u;
        asm volatile("" : "+v"(el[0]), "+v"(el[1]), "+v"(el[2]), "+v"(eo[0]), "+v"(eo[1]), "+v"(eo[2]), "+v"(flo));
    };
    auto gather1 = [&]() {
#pragma unroll
        for (int p = 0; p < 3; ++p) ev[p] = *reinterpret_cast<const u32x4*>(smem + LDS_REC + el[p]);
        f32x2 ml[4];
#pragma unroll
        for (int w = 0; w < 4; ++w) ml[w] = *reinterpret_cast<const f32x2*>(smem + LDS_P + w * (H * 128) + (8 * wave + (lane & 7)) * 128 + 16);
        const float M = fmaxf(fmaxf(ml[0][0], ml[1][0]), fmaxf(ml[2][0], ml[3][0]));
        float L = 0.f;
#pragma unroll
        for (int w = 0; w < 4; ++w) L += __builtin_amdgcn_exp2f(ml[w][0] - M) * ml[w][1];
        flv = u32x4{__float_as_uint(M), t32, __float_as_uint(L), (unsigned)tag};
        asm volatile("" : "+v"(ev[0]), "+v"(ev[1]), "+v"(ev[2]), "+v"(flv));
    };
    auto estore = [&]() {
#pragma unroll
        for (int p = 0; p < 3; ++p) __builtin_amdgcn_raw_buffer_store_b128(ev[p], pr, eo[p], 0, 16);
        if (DBG == 4) tbar = now32();
    };
    float pv = *reinterpret_cast<const float*>(prec);
    f32x2 acc[3][2];
    float y;
    auto prep = [&]() {
        y = bflo(sc);
        y = (c & 4) ? y : y * pv;
        asm volatile("" : "+v"(y) :: "memory");
        SB();
        scp += 128;
        sc = *reinterpret_cast<const unsigned short*>(scp);
        prec += 128;
        pv = *reinterpret_cast<const float*>(prec);
        SB();
    };
#define VSTEP(k, hn) { \
        const float pw = dpp<0x150 + (k)>(y); \
        const float vz = dpp<0x154 + (k)>(y); \
        f32x2 pw2 = f32x2{pw, pw}, vz2 = f32x2{vz, vz}; \
        asm("" : "+v"(pw2), "+v"(vz2)); \
        _Pragma("unroll") for (int x = 0; x < 3; ++x) \
            _Pragma("unroll") for (int e = 0; e < 2; ++e) { \
                const f32x2 vf = f32x2{(float)buf[k].d[x][2 * e], (float)buf[k].d[x][2 * e + 1]} - vz2; \
                acc[x][e] = (k) == 0 ? pw2 * vf : __builtin_elementwise_fma(pw2, vf, acc[x][e]); \
            } \
        asm volatile("" : "+v"(acc[0][0]), "+v"(acc[0][1]), "+v"(acc[1][0]), "+v"(acc[1][1]), "+v"(acc[2][0]), "+v"(acc[2][1]) :: "memory"); \
        SB(); if ((hn) < H) pace(true); issue(buf[k], (hn) < H ? vr : nr, (hn) * hstride + (k) * (4 * DH * 4)); SB(); \
        if ((k) == 1 && (hn) == H - 1) { asm volatile("s_waitcnt lgkmcnt(0)\n\ts_barrier" ::: "memory"); tmin = 0; SB(); } \
        if ((k) == 2 && (hn) == H - 2) { gather0(); SB(); } \
        if ((k) == 2 && (hn) == H - 1) { gather1(); SB(); } \
        if ((k) == (ES & 3) && (hn) == H - 1 + (ES >> 2)) { estore(); SB(); } }
    prep();
    VSTEP(0, 1)
#pragma unroll 1
    for (int h = 0; h < H; ++h) {
        VSTEP(1, h + 1) VSTEP(2, h + 1) VSTEP(3, h + 1)
        f32x4 o;
#pragma unroll
        for (int e = 0; e < 4; ++e) {
            const float t0 = sw32(acc[0][e >> 1][e & 1], acc[2][e >> 1][e & 1]);
            const float t1 = sw32(acc[1][e >> 1][e & 1], acc[1][e >> 1][e & 1]);
            o[e] = sw16(t0, t1);
        }
        asm volatile("s_mov_b64 exec, %5\n\tds_write_b128 %1, %2\n\ts_mov_b64 exec, 1\n\tds_add_rtn_u32 %0, %3, %4\n\ts_mov_b64 exec, -1"
                     : "+v"(old) : "v"(lds0 + LDS_REC + h * (DH * 4) + wave * RECW + lane * 16), "v"(o),
                       "v"(lds0 + LDS_CNT + h * 4), "v"(1u), "s"(0xffffffffffffull) : "memory");
        SB();
        prep();
        VSTEP(0, h + 2)
        asm volatile("s_waitcnt lgkmcnt(0)" : "+v"(old) :: "memory");
        if (__builtin_amdgcn_readfirstlane(old) == 3) merge(h, h >= H - NL);
        SB();
    }
    if (__builtin_amdgcn_readfirstlane(old) == 3) asm volatile("s_waitcnt vmcnt(1)" ::: "memory");
    else asm volatile("s_waitcnt vmcnt(0)" ::: "memory");
    __builtin_amdgcn_raw_buffer_store_b128(flv, pr, flo, 0, 16);
    const unsigned tend = DBG ? now32() : 0;
    unsigned tfl = 0, tpl = 0;

    constexpr int NCN = 3 * (H - NL), NC = NCN + 6 * NL;
    if (b < NC) {
        const bool late = b >= NCN;
        const int h = late ? H - NL + (b - NCN) / 6 : b / 3, chunk = late ? (b - NCN) % 6 : b % 3;
        const unsigned src = 64 * wave + lane;
        const unsigned rstride = late ? LS : DH * 2;
        const unsigned rbase = late ? plt + (h - (H - NL)) * nb * LS : h * nb * (DH * 2);
        const unsigned xo = (64 * wave + (lane >> 3)) * rstride + chunk * 128 + (lane & 7) * 16;
        const unsigned oo = late ? ((r & 1) ? 0x80000000u : (h * DH + chunk * 32 + (c & 7) * 4 + r + (c >> 3)) * 2) : (h * DH + chunk * 64 + (c & 7) * 8 + 2 * r + (c >> 3)) * 2;
        asm volatile("s_waitcnt vmcnt(0)" ::: "memory");
        if (DBG == 4) th4 = now32();
        const unsigned tgt = sched - stag + HOLD * 16;
        while ((int)((now32() << 4) - tgt) < 0) __builtin_amdgcn_s_sleep(1);
        i32x4 y, x[8];
#pragma unroll 1
        for (int n = 0; n < (1 << 20); ++n) {
            y = bld128<16>(pr, pfl + (h * nb + src) * 16, 0);
            if ((unsigned)y[1] == t32 && (unsigned)y[3] == (unsigned)tag) break;
            asm volatile("" ::: "memory");
            __builtin_amdgcn_s_sleep(2);
        }
        asm volatile("" ::: "memory");
        if (DBG) tfl = now32();
#pragma unroll
        for (int i = 0; i < 8; ++i) x[i] = bld128<16>(pr, xo, rbase + i * 8 * rstride);
        SB();
        float* s_o = reinterpret_cast<float*>(smem + LDS_QRAW);
        const float m = __int_as_float(y[0]);
        const float mw = xrow_max(row16_allmax(m));
        const float lw = xrow_sum(row16_allsum(__builtin_amdgcn_exp2f(m - mw) * __int_as_float(y[2])));
        *reinterpret_cast<f32x2*>(&s_o[256 + wave * 2]) = f32x2{mw, lw};
        asm volatile("s_waitcnt lgkmcnt(0)\n\ts_barrier" ::: "memory");
        const f32x4 a0 = *reinterpret_cast<const f32x4*>(&s_o[256]), a1 = *reinterpret_cast<const f32x4*>(&s_o[260]);
        const float M = fmaxf(fmaxf(a0[0], a0[2]), fmaxf(a1[0], a1[2]));
        const float L = (__builtin_amdgcn_exp2f(a0[0] - M) * a0[1] + __builtin_amdgcn_exp2f(a0[2] - M) * a0[3]) + (__builtin_amdgcn_exp2f(a1[0] - M) * a1[1] + __builtin_amdgcn_exp2f(a1[2] - M) * a1[3]);
        const float f = __builtin_amdgcn_exp2f(m - M) / L;
        float fs[8];
#pragma unroll
        for (int i = 0; i < 8; ++i) fs[i] = __int_as_float(__builtin_amdgcn_ds_bpermute(((lane >> 3) + 8 * i) * 4, __float_as_int(f)));
        SB();
        if (late) {
#pragma unroll 1
            for (int n = 0; n < (1 << 20); ++n) {
                unsigned bad = 0;
#pragma unroll
                for (int i = 0; i < 8; ++i) bad |= (x[i][1] ^ t32) | (x[i][3] ^ t32);
                if (__all(bad == 0)) break;
                asm volatile("" ::: "memory");
#pragma unroll
                for (int i = 0; i < 8; ++i) x[i] = bld128<16>(pr, xo, rbase + i * 8 * rstride);
            }
        }
        f32x2 o[4] = {};
#pragma unroll
        for (int i = 0; i < 8; ++i)
#pragma unroll
            for (int e = 0; e < 4; ++e) o[e] = __builtin_elementwise_fma(f32x2{fs[i], fs[i]}, f32x2{bflo(x[i][e]), bfhi(x[i][e])}, o[e]);
        if (DBG) { asm volatile("" : "+v"(o[0]), "+v"(o[1]), "+v"(o[2]), "+v"(o[3])); tpl = now32(); }
        const float w0 = sw16(sw32(o[0][0], o[2][0]), sw32(o[1][0], o[3][0])), w1 = sw16(sw32(o[0][1], o[2][1]), sw32(o[1][1], o[3][1]));
        s_o[wave * 64 + lane] = (c < 8 ? w0 : w1) + dpp<0x128>(c < 8 ? w1 : w0);
        asm volatile("s_waitcnt lgkmcnt(0)\n\ts_barrier" ::: "memory");
        if (wave == 0) {
            const float T = (s_o[lane] + s_o[64 + lane]) + (s_o[128 + lane] + s_o[192 + lane]);
            __builtin_amdgcn_raw_buffer_store_b16((unsigned short)pk_bf16(T, 0.f), mkrsrc(a.out, H * DH * 2), oo, 0, 0);
        }
        if (b == 0 && tid == 0) *reinterpret_cast<u64*>(a.part + pep) = tag;
    }
    if (DBG) {
        const unsigned tc1 = now32();
        u64* stp = reinterpret_cast<u64*>(a.part + 32 * 256 * 196 * 4) + (size_t)(b * 4 + wave) * 8;
        if (lane == 0) { stp[0] = tstart; stp[1] = tbar; stp[2] = DBG == 4 ? tend - (((tend << 4) - (sched - stag - step)) >> 4) : th1; stp[3] = DBG == 4 && b >= NC ? tend : th4; stp[4] = tend; stp[5] = b < NC ? tfl : tend; stp[6] = b < NC ? tpl : tend; stp[7] = tc1; }
        if (DBG == 2) *reinterpret_cast<u32x4*>(a.part + 32 * 256 * 196 * 4 + 65536 + (b * 4 + wave) * 1024 + lane * 16) = *reinterpret_cast<const u32x4*>(smem + LDS_TR + 16 + wave * 1024 + lane * 16);
    }
    asm volatile(".Lkv_b%c0:" :: "i"(ID));
}

template <int TP, int ADV, int TMIN, int HOLD, int WIN, int OPT, int ID, int DBG>
static void launch30(void* const* d_in, const int* in_sizes, void* d_out, void* d_ws, hipStream_t stream) {
    typedef const unsigned short* P;
    Args a;
    a.qk = (const int*)d_in[1]; a.qv = (const int*)d_in[4];
    a.k_cos = (P)d_in[9]; a.k_sin = (P)d_in[10];
    a.q = (P)d_in[0]; a.q_cos = (P)d_in[7]; a.q_sin = (P)d_in[8];
    a.ksc = (P)d_in[2]; a.kzp = (P)d_in[3]; a.vsc = (P)d_in[5]; a.vzp = (P)d_in[6];
    a.part = (unsigned char*)d_ws;
    a.out = (unsigned short*)d_out;
    a.Skv = in_sizes[9] / DH;
    a.scale_log2 = 1.4426950408889634f / sqrtf((float)DH);
    kv30_kernel<TP, ADV, TMIN, HOLD, WIN, OPT, ID, DBG><<<a.Skv / RW, 256, 0, stream>>>(a);
}

extern "C" void kernel_launch(void* const* d_in, const int* in_sizes, int n_in, void* d_out, int out_size, void* d_ws, size_t ws_size, hipStream_t stream) {
    launch30<80*16+4,230,79*16,165,4+16*32,6,0,0>(d_in, in_sizes, d_out, d_ws, stream);
}
```

```cpp
#include <hip/hip_runtime.h>
#include <hip/hip_bf16.h>
#include <cstdint>
#define STAMP_OFF (8 << 20)
using bf16 = __hip_bfloat16;
using i32x4 = __attribute__((ext_vector_type(4))) int;
using u32x4 = __attribute__((ext_vector_type(4))) unsigned;
using u32x2 = __attribute__((ext_vector_type(2))) unsigned;
using f32x2 = __attribute__((ext_vector_type(2))) float;
using f32x4 = __attribute__((ext_vector_type(4))) float;

constexpr int DH = 192;
constexpr int HALF = 96;
constexpr int RW = 64;
constexpr int MAXH = 32;

__device__ __forceinline__ float bflo(unsigned u) { return __uint_as_float(u << 16); }
__device__ __forceinline__ float bfhi(unsigned u) { return __uint_as_float(u & 0xffff0000u); }
__device__ __forceinline__ float rbf(float x) { return __bfloat162float(__float2bfloat16(x)); }

template <int CTRL> __device__ __forceinline__ float dpp(float x) {
    return __builtin_bit_cast(float, __builtin_amdgcn_mov_dpp(__builtin_bit_cast(int, x), CTRL, 0xf, 0xf, true));
}
__device__ __forceinline__ float row16_allsum(float x) {
    x += dpp<0x128>(x);
    x += dpp<0x124>(x);
    x += dpp<0x122>(x);
    x += dpp<0x121>(x);
    return x;
}
__device__ __forceinline__ float row16_allmax(float x) {
    x = fmaxf(x, dpp<0x128>(x));
    x = fmaxf(x, dpp<0x124>(x));
    x = fmaxf(x, dpp<0x122>(x));
    x = fmaxf(x, dpp<0x121>(x));
    return x;
}
__device__ __forceinline__ float xrow_max(float x) {
    auto s = __builtin_amdgcn_permlane16_swap(__float_as_uint(x), __float_as_uint(x), false, false);
    x = fmaxf(__uint_as_float(s[0]), __uint_as_float(s[1]));
    auto t = __builtin_amdgcn_permlane32_swap(__float_as_uint(x), __float_as_uint(x), false, false);
    return fmaxf(__uint_as_float(t[0]), __uint_as_float(t[1]));
}
__device__ __forceinline__ float xrow_sum(float x) {
    auto s = __builtin_amdgcn_permlane16_swap(__float_as_uint(x), __float_as_uint(x), false, false);
    x = __uint_as_float(s[0]) + __uint_as_float(s[1]);
    auto t = __builtin_amdgcn_permlane32_swap(__float_as_uint(x), __float_as_uint(x), false, false);
    return __uint_as_float(t[0]) + __uint_as_float(t[1]);
}
__device__ __forceinline__ float sw32(float a, float b) {
    auto t = __builtin_amdgcn_permlane32_swap(__float_as_uint(a), __float_as_uint(b), false, false);
    return __uint_as_float(t[0]) + __uint_as_float(t[1]);
}
__device__ __forceinline__ float sw16(float a, float b) {
    auto t = __builtin_amdgcn_permlane16_swap(__float_as_uint(a), __float_as_uint(b), false, false);
    return __uint_as_float(t[0]) + __uint_as_float(t[1]);
}

#define SB() __builtin_amdgcn_sched_barrier(0)
typedef __amdgpu_buffer_rsrc_t rsrc_t;
__device__ __forceinline__ rsrc_t mkrsrc(const void* p, unsigned bytes) {
    return __builtin_amdgcn_make_buffer_rsrc(const_cast<void*>(p), 0, bytes, 0x00020000);
}
template <int AUX> __device__ __forceinline__ i32x4 bld128(rsrc_t r, unsigned voff, unsigned soff) {
    return __builtin_bit_cast(i32x4, __builtin_amdgcn_raw_buffer_load_b128(r, voff, soff, AUX));
}
template <int AUX> __device__ __forceinline__ unsigned bld16(rsrc_t r, unsigned voff, unsigned soff) {
    return __builtin_amdgcn_raw_buffer_load_b16(r, voff, soff, AUX);
}

struct HG { i32x4 d[3]; };
typedef unsigned long long u64;
typedef __attribute__((address_space(3))) unsigned char lds_u8;

__device__ __forceinline__ unsigned pk_bf16(float lo, float hi) {
    unsigned r;
    asm("v_cvt_pk_bf16_f32 %0, %1, %2" : "=v"(r) : "v"(lo), "v"(hi));
    return r;
}
__device__ __forceinline__ unsigned now32() { return (unsigned)__builtin_amdgcn_s_memrealtime(); }

constexpr int LDS_REC = 0;
constexpr int RECW = MAXH * DH * 4;
constexpr int LDS_CS = 0;
constexpr int LDS_QT = 12288;
constexpr int LDS_LT = 13312;
constexpr int LDS_PF = 14336;
constexpr int LDS_QRAW = MAXH * 4 * DH * 4;
constexpr int LDS_Q = LDS_QRAW + MAXH * DH * 2;
constexpr int LDS_P = LDS_Q + 4 * DH * 4;
constexpr int LDS_SC = LDS_P + 4 * MAXH * 128;
constexpr int LDS_CNT = LDS_SC + 4 * MAXH * RW * 2;
constexpr int LDS_SK = LDS_CNT + 256;
constexpr int LDS_TR = LDS_CNT + 512;
constexpr int LDS_BYTES = LDS_TR + 4096 + 64;

struct Args {
    const int* qk; const int* qv;
    const unsigned short* k_cos; const unsigned short* k_sin;
    const unsigned short* q; const unsigned short* q_cos; const unsigned short* q_sin;
    const unsigned short* ksc; const unsigned short* kzp; const unsigned short* vsc; const unsigned short* vzp;
    unsigned char* part;
    unsigned short* out;
    int Skv; float scale_log2;
};

template <int TP, int ADV, int TMIN, int HOLD, int WIN, int OPT, int HS, int O2, int EX, int ID, int DBG>
__global__ __launch_bounds__(256) void kv37_kernel(Args a)
{
    __shared__ __attribute__((aligned(16))) unsigned char smem[LDS_BYTES];
    const unsigned tstart = DBG ? now32() : 0;
    constexpr bool DS = DBG && DBG < 5;
    unsigned tA = 0, tB = 0, tC = 0, tD = 0, tE = 0;
    constexpr int H = MAXH, ES = 7, PFN = 16;
    const int b = blockIdx.x;
    const int tid = threadIdx.x;
    const int lane = tid & 63;
    const int wave = __builtin_amdgcn_readfirstlane(tid >> 6);
    if (b < PFN) {
        u64 pc; unsigned len;
        asm volatile("s_getpc_b64 %0\n.Lkv_a%c2:\n\ts_mov_b32 %1, .Lkv_b%c2-.Lkv_a%c2-16" : "=s"(pc), "=s"(len) : "i"(ID));
        const unsigned o = tid * 16;
        asm volatile("s_mov_b32 m0, %5\n\ts_nop 0\n\tglobal_load_lds_dwordx4 %0, %4\n\tglobal_load_lds_dwordx4 %1, %4\n\tglobal_load_lds_dwordx4 %2, %4\n\tglobal_load_lds_dwordx4 %3, %4"
                     :: "v"(o), "v"(o + 4096), "v"(min(o + 8192, len)), "v"(min(o + 12288, len)), "s"(pc), "s"((unsigned)(size_t)(lds_u8*)smem + LDS_PF) : "memory");
    }
    SB();
    const int r = lane >> 4;
    const int c = lane & 15;
    const unsigned Skv = a.Skv;
    const unsigned hstride = Skv * (DH * 4);
    const unsigned nb = Skv / RW;
    const unsigned lds0 = (unsigned)(size_t)(lds_u8*)smem;
    const unsigned pos = (b & 7) * (nb >> 3) + (b >> 3);
    const unsigned sb = (O2 & 1) ? ((((b >> 3) * 13) & 31) << 3) | (b & 7) : b;
    const unsigned row0 = RW * pos + 16 * wave;

    asm volatile("" :: "s"(a.qk), "s"(a.qv), "s"(a.k_cos), "s"(a.k_sin), "s"(a.q_cos), "s"(a.q_sin), "s"(a.ksc), "s"(a.kzp), "s"(a.vsc), "s"(a.vzp), "s"(a.part), "s"(a.out), "s"(a.Skv), "s"(a.scale_log2));
    constexpr int NL = 1;
    constexpr int LS = 896;
    const unsigned plt = H * nb * (DH * 2), pfl = plt + NL * nb * LS, psk = pfl + H * nb * 16, pep = psk + 128;
    const rsrc_t pr = mkrsrc(a.part, pep);
#define DMA3(V, A, B, P) "s_mov_b32 m0, " B "\n\ts_nop 0\n\t" \
                     "global_load_lds_dwordx4 " V ", " A P "\n\t" \
                     "global_load_lds_dwordx4 " V ", " A " offset:1024" P "\n\t" \
                     "global_load_lds_dwordx4 " V ", " A " offset:2048" P "\n\t"
    asm volatile(DMA3("%0", "%1", "%2", "") "s_mov_b32 m0, %4\n\ts_nop 0\n\tglobal_load_lds_dwordx4 %3, off"
                 :: "v"(lane * 16), "s"(a.q + wave * 1536), "s"(lds0 + LDS_QRAW + wave * 3072),
                    "v"(lane < 48 ? (const unsigned char*)((lane < 24 ? a.q_cos : a.q_sin - DH) + 8 * lane) : a.part + pep), "s"(lds0 + LDS_QT + wave * RECW) : "memory");
    SB();
    {
        const unsigned short* sp = wave == 0 ? a.ksc : wave == 1 ? a.kzp : wave == 2 ? a.vsc : a.vzp;
        const unsigned vs = ((lane >> 3) * Skv + RW * pos + (lane & 7) * 8) * 2;
        const unsigned ds = lds0 + LDS_SC + wave * 4096;
#define DMA1(A, B) "s_mov_b32 m0, " B "\n\ts_nop 0\n\tglobal_load_lds_dwordx4 %0, " A " nt\n\t"
        asm volatile(DMA1("%1", "%5") DMA1("%2", "%6") DMA1("%3", "%7") DMA1("%4", "%8")
                     :: "v"(vs), "s"(sp), "s"(sp + 8 * Skv), "s"(sp + 16 * Skv), "s"(sp + 24 * Skv), "s"(ds), "s"(ds + 1024), "s"(ds + 2048), "s"(ds + 3072) : "memory");
    }
    SB();
    const unsigned ltoff = plt + sb * LS + min(lane, 47) * 16;
    if (wave == 3) asm volatile(DMA1("%1", "%2") :: "v"(ltoff), "s"(a.part), "s"(lds0 + LDS_LT + wave * RECW) : "memory");
    SB();
    const unsigned tdma = now32();
    const int stag = (OPT & 32) ? (int)(((sb & ~7) + wave * 2 + (b & 1)) * (TP / 2)) / 256 - TP / 4 : (int)(((OPT & 2) ? (((b >> 3) * 26 + wave * 2 + (b & 1)) & 63) : ((((b >> 3) * 4 + wave) * 37) & 63)) * (TP / 2)) / 64 - TP / 4;
    unsigned sched = ((tdma + ADV) << 4) + stag - 4 * (TP / 2);
    unsigned vt = tdma << 4;
    int tru = -4;
    unsigned step = TP / 2, tmin = TMIN / 2, vth = WIN & 15;
    auto pace = [&](bool ctl = false) {
        unsigned tgt = sched;
        sched += step;
        if ((int)(vt - tgt) > 0) tgt = vt;
        unsigned t = now32() << 4;
        while ((int)(t - tgt) < 0) { __builtin_amdgcn_s_sleep(1); t = now32() << 4; }
        vt = ((int)(t - 128 - tgt) > 0 ? t - 128 : tgt) + tmin;
        if (ctl) {
            if constexpr (WIN > 0 && WIN < 16) asm volatile("s_waitcnt vmcnt(%0)" :: "i"(WIN) : "memory");
            if constexpr (WIN >= 16) {
                const unsigned vm = __builtin_amdgcn_s_getreg(7 | (3 << 11));
                if (vm > vth) vt += (vm - vth) * (WIN >> 4);
            }
        }
        if constexpr (DBG == 2) { *reinterpret_cast<unsigned*>(smem + LDS_TR + 16 + wave * 1024 + (tru & 255) * 4) = ((t - (sched - step)) >> 4) | ((OPT & 8) ? (__builtin_amdgcn_s_getreg(7 | (3 << 11)) << 16) : 0); ++tru; }
    };
    {
        unsigned dst = lds0 + LDS_CS + wave * RECW;
        const unsigned v0 = lane * 16 + row0 * (DH * 2);
#pragma unroll 1
        for (int j = 0; j < 4; ++j) {
            pace();
            asm volatile(DMA3("%0", "%1", "%2", " nt") :: "v"(v0), "s"((j < 2 ? a.k_cos : a.k_sin) + (j & 1) * 8 * DH), "s"(dst) : "memory");
            dst += 3072;
        }
    }
    if (tid < MAXH) *reinterpret_cast<unsigned*>(smem + LDS_CNT + tid * 4) = 0;
    const rsrc_t kr = mkrsrc(a.qk, H * hstride);
    const rsrc_t vr = mkrsrc(a.qv, H * hstride);
    const unsigned kvoff = (row0 + r) * (DH * 4) + c * 16;
    auto issue = [&](HG& g, rsrc_t rs, unsigned so) {
#pragma unroll
        for (int x = 0; x < 3; ++x) g.d[x] = bld128<2>(rs, kvoff + 256 * x, so);
    };
    HG buf[4];
#define PRE(g) SB(); pace(); issue(buf[g], kr, (g) * (4 * DH * 4)); SB();
    asm volatile("s_waitcnt vmcnt(12) lgkmcnt(0)\n\ts_barrier" ::: "memory");
    unsigned tbar = DS ? now32() : 0;
    PRE(0)
    const unsigned qoff = (lane < 48 ? lane : lane - 48) * 4;
    const unsigned* qt = reinterpret_cast<const unsigned*>(smem + LDS_QT + wave * RECW + qoff);
    unsigned qc0 = qt[0], qc1 = qt[HALF / 2], qs0 = qt[DH / 2], qs1 = qt[(DH + HALF) / 2];
    u64 tag;
    {
        const u32x2 e = *reinterpret_cast<const u32x2*>(smem + LDS_QT + wave * RECW + 768);
        tag = (((u64)__builtin_amdgcn_readfirstlane(e[1]) << 32) | __builtin_amdgcn_readfirstlane(e[0])) * 6364136223846793005ull + 1442695040888963407ull;
        asm volatile("" : "+s"(tag));
    }
    const unsigned t32 = tag >> 32;
    const unsigned char* qraw = smem + LDS_QRAW + qoff;
    unsigned char* qrot = smem + LDS_Q + wave * (DH * 4) + 2 * qoff;
    auto rotq = [&]() {
        const unsigned q_lo = *reinterpret_cast<const unsigned*>(qraw), q_hi = *reinterpret_cast<const unsigned*>(qraw + HALF * 2);
        qraw += DH * 2;
        const f32x2 lo = f32x2{bflo(q_lo), bfhi(q_lo)}, hi = f32x2{bflo(q_hi), bfhi(q_hi)};
        const f32x2 y1 = lo * f32x2{bflo(qc0), bfhi(qc0)}, y2 = -hi * f32x2{bflo(qs0), bfhi(qs0)};
        const f32x2 y3 = hi * f32x2{bflo(qc1), bfhi(qc1)}, y4 = lo * f32x2{bflo(qs1), bfhi(qs1)};
        const unsigned x1 = pk_bf16(y1[0], y1[1]), x2 = pk_bf16(y2[0], y2[1]), x3 = pk_bf16(y3[0], y3[1]), x4 = pk_bf16(y4[0], y4[1]);
        const f32x2 z1 = f32x2{bflo(x1), bfhi(x1)} + f32x2{bflo(x2), bfhi(x2)}, z2 = f32x2{bflo(x3), bfhi(x3)} + f32x2{bflo(x4), bfhi(x4)};
        const unsigned l2 = pk_bf16(z1[0], z1[1]), h2 = pk_bf16(z2[0], z2[1]);
        *reinterpret_cast<f32x2*>(qrot) = f32x2{bflo(l2), bfhi(l2)};
        *reinterpret_cast<f32x2*>(qrot + HALF * 4) = f32x2{bflo(h2), bfhi(h2)};
        asm volatile("" ::: "memory");
    };
    f32x4 qa[3], qb[3];
    const unsigned char* qh = smem + LDS_Q + wave * (DH * 4) + 16 * c;
    const unsigned char* qh1 = qh + (c < 8 ? 64 + HALF : 64 - HALF) * 4;
    auto loadq = [&]() {
        asm volatile("" ::: "memory");
#pragma unroll
        for (int x = 0; x < 3; ++x) qa[x] = *reinterpret_cast<const f32x4*>(qh + 256 * x);
        qb[0] = *reinterpret_cast<const f32x4*>(qh + HALF * 4);
        qb[1] = *reinterpret_cast<const f32x4*>(qh1);
        qb[2] = *reinterpret_cast<const f32x4*>(qh + (128 - HALF) * 4);
        asm volatile("" ::: "memory");
    };
    rotq();
    if (wave == 3) {
        const u32x4 e = *reinterpret_cast<const u32x4*>(smem + LDS_LT + wave * RECW + lane * 16);
        if (e[1] == t32 || e[3] == t32) __builtin_amdgcn_raw_buffer_store_b128(u32x4{0u, ~t32, 0u, ~t32}, pr, ltoff, 0, 16);
    }
    if (b < 8 && wave == 3) __builtin_amdgcn_raw_buffer_store_b64(u32x2{tdma, t32}, pr, lane == 0 ? psk + b * 8 : 0x80000000u, 0, 16);
    PRE(1)
    f32x2 cc[4][3][2], ss[4][3][2];
    const unsigned char* cb = smem + LDS_CS + wave * RECW + r * (DH * 2) + c * 8;
    asm volatile("s_waitcnt vmcnt(12)" ::: "memory");
#pragma unroll
    for (int k = 0; k < 4; ++k)
#pragma unroll
        for (int x = 0; x < 3; ++x) {
            const u32x2 t = *reinterpret_cast<const u32x2*>(cb + k * 1536 + 128 * x);
            cc[k][x][0] = f32x2{bflo(t[0]), bfhi(t[0])}; cc[k][x][1] = f32x2{bflo(t[1]), bfhi(t[1])};
            asm volatile("" : "+v"(cc[k][x][0]), "+v"(cc[k][x][1]));
        }
    PRE(2)
    loadq();
    const unsigned char* scp = smem + LDS_SC + (c & 4) * 1024 + (16 * wave + 4 * (c & 3) + r) * 2;
    unsigned sc = *reinterpret_cast<const unsigned short*>(scp);
    auto loadsin = [&](int k) {
        const unsigned char* sb1 = cb + (c < 8 ? (64 + HALF) * 2 : (64 - HALF) * 2);
        const unsigned sg = c < 8 ? 0u : 0x80008000u;
        u32x2 t[3];
        t[0] = *reinterpret_cast<const u32x2*>(cb + 6144 + k * 1536 + HALF * 2);
        t[1] = *reinterpret_cast<const u32x2*>(sb1 + 6144 + k * 1536) ^ u32x2{sg, sg};
        t[2] = *reinterpret_cast<const u32x2*>(cb + 6144 + k * 1536 + (128 - HALF) * 2) ^ u32x2{0x80008000u, 0x80008000u};
#pragma unroll
        for (int x = 0; x < 3; ++x) {
            ss[k][x][0] = f32x2{bflo(t[x][0]), bfhi(t[x][0])}; ss[k][x][1] = f32x2{bflo(t[x][1]), bfhi(t[x][1])};
            asm volatile("" : "+v"(ss[k][x][0]), "+v"(ss[k][x][1]));
        }
    };
    asm volatile("s_waitcnt vmcnt(12)" ::: "memory");
    loadsin(0); loadsin(1);
    PRE(3)
    asm volatile("s_waitcnt vmcnt(12)" ::: "memory");
    loadsin(2); loadsin(3);
    unsigned th1 = 0, th4 = 0;
    unsigned char* prec = smem + LDS_P + wave * (H * 128) + r * 32 + (c & 7) * 4;
    auto softmax = [&](float s, int h) {
        float m = fmaxf(s, dpp<0xb1>(s));
        m = xrow_max(fmaxf(m, dpp<0x4e>(m)));
        const float p = __builtin_amdgcn_exp2f(s - m);
        float l = p + dpp<0xb1>(p);
        l = xrow_sum(l + dpp<0x4e>(l));
        const float t = (c & 1) ? l : m;
        const int x = __builtin_amdgcn_update_dpp(__float_as_int(p), __float_as_int(t), 0x114, 0xf, 0xa, false);
        *reinterpret_cast<int*>(prec + h * 128) = x;
    };
    float sprev = 0.f;

#pragma unroll 1
    for (int h = 0; h < H; ++h) {
        const bool more = h + 1 < H;
        const rsrc_t nx = more ? kr : vr;
        const unsigned nso = more ? (h + 1) * hstride : 0;
        float scf = bflo(sc);
        asm volatile("" : "+v"(scf) :: "memory");
        SB();
        scp += more ? 128 : 4096 + 128;
        sc = *reinterpret_cast<const unsigned short*>(scp);
        SB();
        float sreg = 0.f;
#define KSTEP(k) { \
            const float kz = dpp<0x154 + (k)>(scf); \
            f32x2 kz2 = f32x2{kz, kz}; \
            asm("" : "+v"(kz2)); \
            f32x2 a2 = f32x2{0.f, 0.f}; \
            _Pragma("unroll") for (int x = 0; x < 3; ++x) { \
                _Pragma("unroll") for (int e = 0; e < 2; ++e) { \
                    const f32x2 w = __builtin_elementwise_fma(f32x2{qb[x][2 * e], qb[x][2 * e + 1]}, ss[k][x][e], f32x2{qa[x][2 * e], qa[x][2 * e + 1]} * cc[k][x][e]); \
                    const f32x2 kf = f32x2{(float)buf[k].d[x][2 * e], (float)buf[k].d[x][2 * e + 1]} - kz2; \
                    a2 = __builtin_elementwise_fma(kf, w, a2); \
                } \
            } \
            float sa = a2[0] + a2[1]; \
            asm volatile("" : "+v"(sa) :: "memory"); \
            SB(); pace(true); issue(buf[k], nx, nso + (k) * (4 * DH * 4)); SB(); \
            if ((k) == 0) { softmax(sprev, (h - 1) & (H - 1)); SB(); }   \
            if ((k) == 1) { rotq(); SB(); } \
            if ((k) == 2) { \
                if (wave == 1 && h == ((OPT & 4) ? 1 + ((b >> 3) & 3) : 4 + ((b >> 3) & 15))) \
                    asm volatile("s_mov_b32 m0, %2\n\ts_nop 0\n\tglobal_load_lds_dword %0, %1 sc1" :: "v"(psk + (lane & 15) * 4), "s"(a.part), "s"(lds0 + LDS_SK) : "memory"); \
                if (h == ((OPT & 4) ? 6 : 25)) {        \
                    const u32x2 e = *reinterpret_cast<const u32x2*>(smem + LDS_SK + (lane & 7) * 8); \
                    const int d = (int)(tdma - e[0]); \
                    if (__all(e[1] == t32 && d > -400 && d < 400)) { \
                        int sum = d + __builtin_amdgcn_mov_dpp(d, 0xb1, 0xf, 0xf, true); \
                        sum += __builtin_amdgcn_mov_dpp(sum, 0x4e, 0xf, 0xf, true); \
                        sum += __builtin_amdgcn_mov_dpp(sum, 0x124, 0xf, 0xf, true); \
                        const int adj = (__builtin_amdgcn_readfirstlane(sum) * ((OPT & 4) ? 73 : 112) + 4096) >> 13;        \
                        step -= adj; tmin -= adj; \
                    } \
                } \
                SB(); \
            } \
            if ((k) == 3) { loadq(); SB(); } \
            sa = row16_allsum(sa); \
            sreg = (c & 3) == (k) ? sa : sreg; }
        KSTEP(0) KSTEP(1) KSTEP(2) KSTEP(3)
        sprev = sreg * (scf * a.scale_log2);
        if (DS) { if (h == 7) th1 = now32(); }
    }
    softmax(sprev, H - 1);
    if (DS) th4 = now32();

    auto merge = [&](int hd) {
        const unsigned char* rec = smem + LDS_REC + hd * (DH * 4);
        const unsigned char* mlp = smem + LDS_P + hd * 128 + 16;
        f32x4 ow[4]; float mv[4];
#pragma unroll
        for (int w = 0; w < 4; ++w) {
            mv[w] = *reinterpret_cast<const float*>(mlp + w * (H * 128));
            ow[w] = *reinterpret_cast<const f32x4*>(rec + w * RECW + lane * 16);
        }
        const float M = fmaxf(fmaxf(mv[0], mv[1]), fmaxf(mv[2], mv[3]));
        f32x4 t = f32x4{0.f, 0.f, 0.f, 0.f};
#pragma unroll
        for (int w = 0; w < 4; ++w) t += __builtin_amdgcn_exp2f(mv[w] - M) * ow[w];
        return u32x2{pk_bf16(t[0], t[1]), pk_bf16(t[2], t[3])};
    };
    constexpr int LG = (O2 & 8) ? 2 : 3, NI = 1 << LG, CW = 16 << LG;
    constexpr int NCN = (DH * 2 / CW) * (H - NL), NC = NCN + (DH * 4 / CW) * NL;
    const int cn = (O2 & 2) ? sb : b;
    const bool late = cn >= NCN;
    unsigned xo = 0, oo = 0, fo = 0, co = 0;
    unsigned old = 0, flo = 0x80000000u;
    u32x4 flv = {};
    float gl = 0.f;
    unsigned eo[5] = {};
    unsigned el[3] = {};
    auto gather0 = [&]() {
        int lane = tid & 63;
        asm volatile("" : "+v"(lane));
#pragma unroll
        for (int p = 0; p < 3; ++p) {
            const int t = lane + 64 * p, hi_ = t / 24, k = t - hi_ * 24;
            const int hh = 8 * wave + hi_;
            el[p] = hh * (DH * 4) + k * 16;
            eo[p] = hh < H - 2 ? (hh * nb + sb) * (DH * 2) + k * 16 : 0x80000000u;
        }
        eo[3] = wave == 3 && lane < 48 ? ((H - 2) * nb + sb) * (DH * 2) + lane * 8 : 0x80000000u;
        eo[4] = lane < 48 ? plt + sb * LS + lane * 16 : 0x80000000u;
        flo = lane < 8 ? pfl + ((8 * wave + lane) * nb + sb) * 16 : 0x80000000u;
        const unsigned cl = cn - NCN;
        const unsigned h = late ? H - NL + cl / (DH * 4 / CW) : cn / (DH * 2 / CW), chunk = late ? cl % (DH * 4 / CW) : cn % (DH * 2 / CW);
        const unsigned r = lane >> 4, g = lane & (NI - 1), e = (lane >> 3) & 1;
        xo = late ? plt + ((h - (H - NL)) * nb + 64 * wave + (lane >> LG)) * LS : (h * nb + 64 * wave + (lane >> LG)) * (DH * 2);
        xo += chunk * CW + g * 16;
        co = (late ? plt + ((h - (H - NL)) * nb + 64 * wave + lane) * LS : (h * nb + 64 * wave + lane) * (DH * 2)) + chunk * CW;
        if ((O2 & 4096) && !late) xo = 0x80000000u;
        oo = late ? h * DH + chunk * (CW / 4) + g * 4 + r + e : h * DH + chunk * (CW / 2) + g * 8 + 2 * r + e;
        oo = (late && (r & 1)) || (LG == 2 && (lane & 4)) ? 0x80000000u : oo * 2;
        fo = pfl + (h * nb + 64 * wave + lane) * 16;
        asm volatile("" : "+v"(co), "+v"(xo), "+v"(oo), "+v"(fo), "+v"(el[0]), "+v"(el[1]), "+v"(el[2]), "+v"(eo[0]), "+v"(eo[1]), "+v"(eo[2]), "+v"(eo[3]), "+v"(eo[4]), "+v"(flo));
    };
    auto gather1 = [&]() {
        f32x2 ml[4];
#pragma unroll
        for (int w = 0; w < 4; ++w) ml[w] = *reinterpret_cast<const f32x2*>(smem + LDS_P + w * (H * 128) + (8 * wave + (lane & 7)) * 128 + 16);
        const float M = fmaxf(fmaxf(ml[0][0], ml[1][0]), fmaxf(ml[2][0], ml[3][0]));
        float L = 0.f;
#pragma unroll
        for (int w = 0; w < 4; ++w) L += __builtin_amdgcn_exp2f(ml[w][0] - M) * ml[w][1];
        flv = u32x4{__float_as_uint(M), t32, __float_as_uint(L), (unsigned)tag};
        float mv[4];
#pragma unroll
        for (int w = 0; w < 4; ++w) mv[w] = *reinterpret_cast<const float*>(smem + LDS_P + ((wave + w) & 3) * (H * 128) + (H - 1) * 128 + 16);
        gl = __builtin_amdgcn_exp2f(mv[0] - fmaxf(fmaxf(mv[0], mv[1]), fmaxf(mv[2], mv[3])));
        asm volatile("" : "+v"(flv), "+v"(gl));
    };
    float pv = *reinterpret_cast<const float*>(prec);
    f32x2 acc[3][2];
    float y;
    auto prep = [&]() {
        y = bflo(sc);
        y = (c & 4) ? y : y * pv;
        asm volatile("" : "+v"(y) :: "memory");
        SB();
        scp += 128;
        sc = *reinterpret_cast<const unsigned short*>(scp);
        prec += 128;
        pv = *reinterpret_cast<const float*>(prec);
        SB();
    };
    constexpr int AF = 16 | ((O2 >> 6) & 3), AP = 16 | ((O2 >> 8) & 3), AS = 16 | ((O2 >> 10) & 3);
    auto opq = [](int v) { asm volatile("" : "+s"(v)); return v; };
#define VCOMP(k) { \
        const float pw = dpp<0x150 + (k)>(y); \
        const float vz = dpp<0x154 + (k)>(y); \
        f32x2 pw2 = f32x2{pw, pw}, vz2 = f32x2{vz, vz}; \
        asm("" : "+v"(pw2), "+v"(vz2)); \
        _Pragma("unroll") for (int x = 0; x < 3; ++x) \
            _Pragma("unroll") for (int e = 0; e < 2; ++e) { \
                const f32x2 vf = f32x2{(float)buf[k].d[x][2 * e], (float)buf[k].d[x][2 * e + 1]} - vz2; \
                acc[x][e] = (k) == 0 ? pw2 * vf : __builtin_elementwise_fma(pw2, vf, acc[x][e]); \
            } \
        asm volatile("" : "+v"(acc[0][0]), "+v"(acc[0][1]), "+v"(acc[1][0]), "+v"(acc[1][1]), "+v"(acc[2][0]), "+v"(acc[2][1]) :: "memory"); \
        SB(); }
#define VSTEP(k, hn) { \
        VCOMP(k) pace(true); issue(buf[k], vr, (hn) * hstride + (k) * (4 * DH * 4)); SB(); \
        if ((k) == 1) { tmin = (hn) == H - 1 ? 0 : tmin; if (OPT & 16) vth = (hn) == H - 2 ? 63 : vth; } \
        if ((k) == 1 && (hn) == 15) { asm volatile("s_waitcnt lgkmcnt(0)\n\ts_barrier" ::: "memory"); gather1(); SB(); } \
        if ((k) == 2 && (hn) == 17) { gather0(); SB(); } }
    auto record = [&](int h) {
        f32x4 o;
#pragma unroll
        for (int e = 0; e < 4; ++e) {
            const float t0 = sw32(acc[0][e >> 1][e & 1], acc[2][e >> 1][e & 1]);
            const float t1 = sw32(acc[1][e >> 1][e & 1], acc[1][e >> 1][e & 1]);
            o[e] = sw16(t0, t1);
        }
        asm volatile("s_mov_b64 exec, %5\n\tds_write_b128 %1, %2\n\ts_mov_b64 exec, 1\n\tds_add_rtn_u32 %0, %3, %4\n\ts_mov_b64 exec, -1"
                     : "+v"(old) : "v"(lds0 + LDS_REC + h * (DH * 4) + wave * RECW + lane * 16), "v"(o),
                       "v"(lds0 + LDS_CNT + h * 4), "v"(1u), "s"(0xffffffffffffull) : "memory");
        SB();
    };
#pragma unroll 1
    for (int h = 0; h < H - 1; ++h) {
        prep();
        VSTEP(0, h + 1)
        asm volatile("s_waitcnt lgkmcnt(0)" : "+v"(old) :: "memory");
        if (__builtin_amdgcn_readfirstlane(old) == 3) *reinterpret_cast<u32x2*>(smem + LDS_REC + (h - 1) * (DH * 4) + lane * 8) = merge(h - 1);
        SB();
        VSTEP(1, h + 1) VSTEP(2, h + 1) VSTEP(3, h + 1)
        if (DBG == 14) tA = now32();
        record(h);
    }
    y = bflo(sc);
    y = (c & 4) ? y : y * (pv * gl);
    VCOMP(0)
    asm volatile("s_waitcnt lgkmcnt(0)\n\ts_barrier" ::: "memory");
    if (DBG == 14) tB = now32();
    u32x4 ev[3];
#pragma unroll
    for (int p = 0; p < 3; ++p) ev[p] = *reinterpret_cast<const u32x4*>(smem + LDS_REC + el[p]);
    SB();
    VCOMP(1)
    u32x2 e3 = {};
    if (wave == 3) e3 = merge(H - 2);
    asm volatile("" : "+v"(e3), "+v"(ev[0]), "+v"(ev[1]), "+v"(ev[2]));
    SB();
    if (DBG == 14) tE = now32();
    VCOMP(2)
    if (DBG == 14) tC = now32();
    if (EX) { const unsigned tx = sched - step - stag + EX * 16; while ((int)((now32() << 4) - tx) < 0) {} }
    asm volatile("s_waitcnt vmcnt(0)" : "+v"(buf[3].d[0]), "+v"(buf[3].d[1]), "+v"(buf[3].d[2]) :: "memory");
    if (DBG == 14) tD = now32();
#pragma unroll
    for (int p = 0; p < 3; ++p) __builtin_amdgcn_raw_buffer_store_b128(ev[p], pr, eo[p], 0, AS);
    __builtin_amdgcn_raw_buffer_store_b64(e3, pr, eo[3], 0, AS);
    if (DBG == 4) tbar = now32();
    SB();
    VCOMP(3)
    record(H - 1);
    asm volatile("s_waitcnt lgkmcnt(0)" : "+v"(old) :: "memory");
    if (__builtin_amdgcn_readfirstlane(old) == 3) {
        f32x4 ow[4];
#pragma unroll
        for (int w = 0; w < 4; ++w) ow[w] = *reinterpret_cast<const f32x4*>(smem + LDS_REC + (H - 1) * (DH * 4) + w * RECW + lane * 16);
        const f32x4 t = (ow[0] + ow[1]) + (ow[2] + ow[3]);
        __builtin_amdgcn_raw_buffer_store_b128(u32x4{pk_bf16(t[0], t[1]), t32, pk_bf16(t[2], t[3]), t32}, pr, eo[4], 0, 16);
        asm volatile("s_waitcnt vmcnt(1)" ::: "memory");
    } else asm volatile("s_waitcnt vmcnt(0)" ::: "memory");
    __builtin_amdgcn_raw_buffer_store_b128(flv, pr, flo, 0, 16);
    unsigned tend = DS ? now32() : 0;
    unsigned tfl = 0, tpl = 0;

    const unsigned tgt = sched - stag + (HOLD - (3 - wave) * HS) * 16;
    if (cn < NC) {
        const unsigned rs = (64 >> LG) * (late ? LS : DH * 2);
        if (!(O2 & 4)) asm volatile("s_waitcnt vmcnt(0)" ::: "memory");
        while ((int)((now32() << 4) - tgt) < 0) __builtin_amdgcn_s_sleep(1);
        i32x4 y, x[NI];
#pragma unroll 1
        for (int n = 0; n < (1 << 20); ++n) {
            y = bld128<AF>(pr, fo, 0);
            if ((unsigned)y[1] == t32 && (unsigned)y[3] == (unsigned)tag) break;
            asm volatile("" ::: "memory");
            __builtin_amdgcn_s_sleep(2);
        }
        asm volatile("" ::: "memory");
        if (DS) tfl = now32();
#pragma unroll
        for (int i = 0; i < NI; ++i) { x[i] = bld128<AP>(pr, xo, i * rs); SB(); }
        float* s_o = reinterpret_cast<float*>(smem + LDS_QRAW);
        const float m = __int_as_float(y[0]);
        const float mw = xrow_max(row16_allmax(m));
        const float f = __builtin_amdgcn_exp2f(m - mw);
        float fs[NI];
#pragma unroll
        for (int i = 0; i < NI; ++i) fs[i] = __int_as_float(__builtin_amdgcn_ds_bpermute(((lane >> LG) + (64 >> LG) * i) * 4, __float_as_int(f)));
        SB();
        const float lw = xrow_sum(row16_allsum(f * __int_as_float(y[2])));
        *reinterpret_cast<f32x2*>(&s_o[256 + wave * 2]) = f32x2{mw, lw};
        float gw = 1.f;
        if (O2 & 16) {
            asm volatile("s_waitcnt lgkmcnt(0)\n\ts_barrier" ::: "memory");
            const f32x4 a0 = *reinterpret_cast<const f32x4*>(&s_o[256]), a1 = *reinterpret_cast<const f32x4*>(&s_o[260]);
            const float M = fmaxf(fmaxf(a0[0], a0[2]), fmaxf(a1[0], a1[2]));
            gw = __builtin_amdgcn_exp2f(mw - M) * __builtin_amdgcn_rcpf((__builtin_amdgcn_exp2f(a0[0] - M) * a0[1] + __builtin_amdgcn_exp2f(a0[2] - M) * a0[3]) + (__builtin_amdgcn_exp2f(a1[0] - M) * a1[1] + __builtin_amdgcn_exp2f(a1[2] - M) * a1[3]));
            asm volatile("" : "+v"(gw));
        }
        if (DBG == 4) { asm volatile("" : "+v"(fs[0]), "+v"(fs[1]), "+v"(fs[2]), "+v"(fs[NI - 1]) :: "memory"); th4 = now32(); }
        SB();
        f32x2 o[4];
        const unsigned chk = late ? ~0u : 0u;
#pragma unroll 1
        for (int n = 0; n < (1 << 20); ++n) {
            unsigned bad = 0;
            if ((O2 & 32) && late) {
#pragma unroll
                for (int i = 0; i < NI; ++i) {
                    asm volatile("; late" : "+v"(x[i]));
#pragma unroll
                    for (int e = 0; e < 4; e += 2) {
                        const f32x2 t = f32x2{bflo(x[i][e]), bfhi(x[i][e])};
                        o[e] = i == 0 ? f32x2{fs[i], fs[i]} * t : __builtin_elementwise_fma(f32x2{fs[i], fs[i]}, t, o[e]);
                    }
                    bad |= (x[i][1] ^ t32) | (x[i][3] ^ t32);
                    asm volatile("" : "+v"(bad), "+v"(o[0]), "+v"(o[2]));
                }
                asm volatile("" : "=v"(o[1]), "=v"(o[3]));
            } else {
#pragma unroll
                for (int i = 0; i < NI; ++i) {
                    asm volatile("" : "+v"(x[i]));
#pragma unroll
                    for (int e = 0; e < 4; ++e) {
                        const f32x2 t = f32x2{bflo(x[i][e]), bfhi(x[i][e])};
                        o[e] = i == 0 ? f32x2{fs[i], fs[i]} * t : __builtin_elementwise_fma(f32x2{fs[i], fs[i]}, t, o[e]);
                    }
                    if (!(O2 & 32)) bad |= (x[i][1] ^ t32) | (x[i][3] ^ t32);
                    asm volatile("" : "+v"(bad), "+v"(o[0]), "+v"(o[1]), "+v"(o[2]), "+v"(o[3]));
                }
                bad &= chk;
            }
            if (__all(bad == 0)) break;
            asm volatile("" ::: "memory");
#pragma unroll
            for (int i = 0; i < NI; ++i) { x[i] = bld128<AP>(pr, xo, i * rs); SB(); }
        }
        if (DS) { asm volatile("" : "+v"(o[0]), "+v"(o[1]), "+v"(o[2]), "+v"(o[3])); tpl = now32(); }
        constexpr int CM = (O2 >> 13) & 3, CP = (O2 >> 15) & 3;
        auto clean = [&]() {
            if (CM == 1) {
#pragma unroll
                for (int i = 0; i < NI; ++i) __builtin_amdgcn_raw_buffer_store_b128(u32x4{0u, 0u, 0u, 0u}, pr, xo, i * rs, 16);
            }
            if (CM == 2) __builtin_amdgcn_raw_buffer_store_b32(0u, pr, co, 0, 16);
            if (CM == 3) { __builtin_amdgcn_raw_buffer_store_b32(0u, pr, co, 0, 16); __builtin_amdgcn_raw_buffer_store_b32(0u, pr, co, 64, 16); }
            SB();
        };
        if (CP == 0) clean();
        const float w0 = sw16(sw32(o[0][0], o[2][0]), sw32(o[1][0], o[3][0])), w1 = sw16(sw32(o[0][1], o[2][1]), sw32(o[1][1], o[3][1]));
        float w2 = (c < 8 ? w0 : w1) + dpp<0x128>(c < 8 ? w1 : w0);
        if (LG == 2) w2 += dpp<0x104>(w2);
        s_o[wave * 64 + lane] = (O2 & 16) ? w2 * gw : w2;
        if (CP == 1) { SB(); clean(); }
        asm volatile("s_waitcnt lgkmcnt(0)\n\ts_barrier" ::: "memory");
        if (wave == 0) {
            float T;
            if (!(O2 & 16)) {
                const f32x4 a0 = *reinterpret_cast<const f32x4*>(&s_o[256]), a1 = *reinterpret_cast<const f32x4*>(&s_o[260]);
                const float M = fmaxf(fmaxf(a0[0], a0[2]), fmaxf(a1[0], a1[2]));
                const float g0 = __builtin_amdgcn_exp2f(a0[0] - M), g1 = __builtin_amdgcn_exp2f(a0[2] - M), g2 = __builtin_amdgcn_exp2f(a1[0] - M), g3 = __builtin_amdgcn_exp2f(a1[2] - M);
                T = ((g0 * s_o[lane] + g1 * s_o[64 + lane]) + (g2 * s_o[128 + lane] + g3 * s_o[192 + lane])) * __builtin_amdgcn_rcpf((g0 * a0[1] + g1 * a0[3]) + (g2 * a1[1] + g3 * a1[3]));
            } else T = (s_o[lane] + s_o[64 + lane]) + (s_o[128 + lane] + s_o[192 + lane]);
            __builtin_amdgcn_raw_buffer_store_b16((unsigned short)pk_bf16(T, 0.f), mkrsrc(a.out, H * DH * 2), oo, 0, 0);
        }
        if (CP == 2) { SB(); clean(); }
        if (b == 0 && tid == 0) *reinterpret_cast<u64*>(a.part + pep) = tag;
    }
    if (DBG) {
        const unsigned tc1 = now32();
        if (!DS) tend = tbar = th4 = tfl = tpl = tc1;
        const unsigned tl16 = tgt - (HOLD - (3 - wave) * HS) * 16 - TP / 2;
        u64* stp = reinterpret_cast<u64*>(a.part + STAMP_OFF) + (size_t)((DBG == 4 ? cn : b) * 4 + wave) * 8;
        if (DBG == 14) { if (lane == 0) { stp[0] = tstart; stp[1] = tA; stp[2] = tend - (((tend << 4) - tl16) >> 4); stp[3] = tB; stp[4] = tE; stp[5] = tC; stp[6] = tD; stp[7] = tc1; } } else
        if (lane == 0) { stp[0] = tstart; stp[1] = tbar; stp[2] = DBG >= 4 ? tend - (((tend << 4) - tl16) >> 4) : th1; stp[3] = DBG == 4 && cn >= NC ? tend : th4; stp[4] = tend; stp[5] = cn < NC ? tfl : tend; stp[6] = cn < NC ? tpl : tend; stp[7] = tc1; }
        if (DBG == 2) *reinterpret_cast<u32x4*>(a.part + STAMP_OFF + 65536 + (b * 4 + wave) * 1024 + lane * 16) = *reinterpret_cast<const u32x4*>(smem + LDS_TR + 16 + wave * 1024 + lane * 16);
    }
    asm volatile(".Lkv_b%c0:" :: "i"(ID));
}

template <int TP, int ADV, int TMIN, int HOLD, int WIN, int OPT, int HS, int O2, int EX, int ID, int DBG>
static void launch37(void* const* d_in, const int* in_sizes, void* d_out, void* d_ws, hipStream_t stream) {
    typedef const unsigned short* P;
    Args a;
    a.qk = (const int*)d_in[1]; a.qv = (const int*)d_in[4];
    a.k_cos = (P)d_in[9]; a.k_sin = (P)d_in[10];
    a.q = (P)d_in[0]; a.q_cos = (P)d_in[7]; a.q_sin = (P)d_in[8];
    a.ksc = (P)d_in[2]; a.kzp = (P)d_in[3]; a.vsc = (P)d_in[5]; a.vzp = (P)d_in[6];
    a.part = (unsigned char*)d_ws;
    a.out = (unsigned short*)d_out;
    a.Skv = in_sizes[9] / DH;
    a.scale_log2 = 1.4426950408889634f / sqrtf((float)DH);
    kv37_kernel<TP, ADV, TMIN, HOLD, WIN, OPT, HS, O2, EX, ID, DBG><<<a.Skv / RW, 256, 0, stream>>>(a);
}

extern "C" void kernel_launch(void* const* d_in, const int* in_sizes, int n_in, void* d_out, int out_size, void* d_ws, size_t ws_size, hipStream_t stream) {
    launch37<80*16+8,230,79*16,125,4+16*32,6+32,5,35+2*8192+32768,62,0,0>(d_in, in_sizes, d_out, d_ws, stream);
}
```

```cpp
#include <hip/hip_runtime.h>
#include <hip/hip_bf16.h>
#include <cstdint>
#define KALIGN 256
#define STAMP_OFF (200 << 20)
using bf16 = __hip_bfloat16;
using i32x4 = __attribute__((ext_vector_type(4))) int;
using u32x4 = __attribute__((ext_vector_type(4))) unsigned;
using u32x2 = __attribute__((ext_vector_type(2))) unsigned;
using f32x2 = __attribute__((ext_vector_type(2))) float;
using f32x4 = __attribute__((ext_vector_type(4))) float;

constexpr int DH = 192;
constexpr int HALF = 96;
constexpr int RW = 64;
constexpr int MAXH = 32;

__device__ __forceinline__ float bflo(unsigned u) { return __uint_as_float(u << 16); }
__device__ __forceinline__ float bfhi(unsigned u) { return __uint_as_float(u & 0xffff0000u); }
__device__ __forceinline__ float rbf(float x) { return __bfloat162float(__float2bfloat16(x)); }

template <int CTRL> __device__ __forceinline__ float dpp(float x) {
    return __builtin_bit_cast(float, __builtin_amdgcn_mov_dpp(__builtin_bit_cast(int, x), CTRL, 0xf, 0xf, true));
}
__device__ __forceinline__ float row16_allsum(float x) {
    x += dpp<0x128>(x);
    x += dpp<0x124>(x);
    x += dpp<0x122>(x);
    x += dpp<0x121>(x);
    return x;
}
__device__ __forceinline__ float row16_allmax(float x) {
    x = fmaxf(x, dpp<0x128>(x));
    x = fmaxf(x, dpp<0x124>(x));
    x = fmaxf(x, dpp<0x122>(x));
    x = fmaxf(x, dpp<0x121>(x));
    return x;
}
__device__ __forceinline__ float xrow_max(float x) {
    auto s = __builtin_amdgcn_permlane16_swap(__float_as_uint(x), __float_as_uint(x), false, false);
    x = fmaxf(__uint_as_float(s[0]), __uint_as_float(s[1]));
    auto t = __builtin_amdgcn_permlane32_swap(__float_as_uint(x), __float_as_uint(x), false, false);
    return fmaxf(__uint_as_float(t[0]), __uint_as_float(t[1]));
}
__device__ __forceinline__ float xrow_sum(float x) {
    auto s = __builtin_amdgcn_permlane16_swap(__float_as_uint(x), __float_as_uint(x), false, false);
    x = __uint_as_float(s[0]) + __uint_as_float(s[1]);
    auto t = __builtin_amdgcn_permlane32_swap(__float_as_uint(x), __float_as_uint(x), false, false);
    return __uint_as_float(t[0]) + __uint_as_float(t[1]);
}
__device__ __forceinline__ float sw32(float a, float b) {
    auto t = __builtin_amdgcn_permlane32_swap(__float_as_uint(a), __float_as_uint(b), false, false);
    return __uint_as_float(t[0]) + __uint_as_float(t[1]);
}
__device__ __forceinline__ float sw16(float a, float b) {
    auto t = __builtin_amdgcn_permlane16_swap(__float_as_uint(a), __float_as_uint(b), false, false);
    return __uint_as_float(t[0]) + __uint_as_float(t[1]);
}

#define SB() __builtin_amdgcn_sched_barrier(0)
typedef __amdgpu_buffer_rsrc_t rsrc_t;
__device__ __forceinline__ rsrc_t mkrsrc(const void* p, unsigned bytes) {
    return __builtin_amdgcn_make_buffer_rsrc(const_cast<void*>(p), 0, bytes, 0x00020000);
}
template <int AUX> __device__ __forceinline__ i32x4 bld128(rsrc_t r, unsigned voff, unsigned soff) {
    return __builtin_bit_cast(i32x4, __builtin_amdgcn_raw_buffer_load_b128(r, voff, soff, AUX));
}
template <int AUX> __device__ __forceinline__ unsigned bld16(rsrc_t r, unsigned voff, unsigned soff) {
    return __builtin_amdgcn_raw_buffer_load_b16(r, voff, soff, AUX);
}

struct HG { i32x4 d[3]; };
typedef unsigned long long u64;
typedef __attribute__((address_space(3))) unsigned char lds_u8;

__device__ __forceinline__ unsigned pk_bf16(float lo, float hi) {
    unsigned r;
    asm("v_cvt_pk_bf16_f32 %0, %1, %2" : "=v"(r) : "v"(lo), "v"(hi));
    return r;
}
__device__ __forceinline__ unsigned now32() { return (unsigned)__builtin_amdgcn_s_memrealtime(); }

constexpr int LDS_REC = 0;
constexpr int RECW = MAXH * DH * 4;
constexpr int LDS_CS = 0;
constexpr int LDS_QT = 12288;
constexpr int LDS_PF = 14336;
constexpr int LDS_QRAW = MAXH * 4 * DH * 4;
constexpr int LDS_Q = LDS_QRAW + MAXH * DH * 2;
constexpr int LDS_P = LDS_Q + 4 * DH * 4;
constexpr int LDS_SC = LDS_P + 4 * MAXH * 128;
constexpr int LDS_CNT = LDS_SC + 4 * MAXH * RW * 2;
constexpr int LDS_SK = LDS_CNT + 256;
constexpr int LDS_TR = LDS_SK + 1024;
constexpr int LDS_BYTES = LDS_TR + 4096 + 64;

struct Args {
    const int* qk; const int* qv;
    const unsigned short* k_cos; const unsigned short* k_sin;
    const unsigned short* q; const unsigned short* q_cos; const unsigned short* q_sin;
    const unsigned short* ksc; const unsigned short* kzp; const unsigned short* vsc; const unsigned short* vzp;
    unsigned char* part;
    unsigned short* out;
    int Skv; float scale_log2;
};

template <int TP, int ADV, int TMIN, int HOLD, int WIN, int OPT, int HS, int O2, int EX, int ID, int DBG>
__global__ __launch_bounds__(256) __attribute__((aligned(KALIGN))) void kv39_kernel(Args a)
{
    __shared__ __attribute__((aligned(16))) unsigned char smem[LDS_BYTES];
    const unsigned tstart = DBG ? now32() : 0;
    constexpr bool DS = (DBG && DBG < 5) || DBG == 24;
    unsigned tA = 0, tB = 0, tC = 0, tD = 0, tE = 0;
    constexpr int H = MAXH, ES = 7, PFN = 16;
    const int b = blockIdx.x;
    const int tid = threadIdx.x;
    const int lane = tid & 63;
    const int wave = __builtin_amdgcn_readfirstlane(tid >> 6);
    if (b < PFN) {
        u64 pc; unsigned len;
        asm volatile("s_getpc_b64 %0\n.Lkv_a%c2:\n\ts_mov_b32 %1, .Lkv_b%c2-.Lkv_a%c2-16" : "=s"(pc), "=s"(len) : "i"(ID));
        const unsigned o = tid * 16;
        asm volatile("s_mov_b32 m0, %5\n\ts_nop 0\n\tglobal_load_lds_dwordx4 %0, %4\n\tglobal_load_lds_dwordx4 %1, %4\n\tglobal_load_lds_dwordx4 %2, %4\n\tglobal_load_lds_dwordx4 %3, %4"
                     :: "v"(o), "v"(o + 4096), "v"(min(o + 8192, len)), "v"(min(o + 12288, len)), "s"(pc), "s"((unsigned)(size_t)(lds_u8*)smem + LDS_PF) : "memory");
    }
    SB();
    const int r = lane >> 4;
    const int c = lane & 15;
    const unsigned Skv = a.Skv;
    const unsigned hstride = Skv * (DH * 4);
    const unsigned nb = Skv / RW;
    const unsigned lds0 = (unsigned)(size_t)(lds_u8*)smem;
    const unsigned pos = (b & 7) * (nb >> 3) + (b >> 3);
    const unsigned sb = ((((b >> 3) * 13) & 31) << 3) | (b & 7);
    const unsigned row0 = RW * pos + 16 * wave;

    asm volatile("" :: "s"(a.qk), "s"(a.qv), "s"(a.k_cos), "s"(a.k_sin), "s"(a.q_cos), "s"(a.q_sin), "s"(a.ksc), "s"(a.kzp), "s"(a.vsc), "s"(a.vzp), "s"(a.part), "s"(a.out), "s"(a.Skv), "s"(a.scale_log2));
    constexpr int LS = 896;
    constexpr unsigned psk = 0, pep = 128, AREA = 4u << 20;
    constexpr int NP = (O2 >> 5) & 3, SP = (O2 >> 8) & 15;
    const unsigned plt = H * nb * (DH * 2), pfl = plt + nb * LS, fsz = H * nb * 16;
    const rsrc_t pr = mkrsrc(a.part, 33 * AREA);
#define DMA3(V, A, B, P) "s_mov_b32 m0, " B "\n\ts_nop 0\n\t" \
                     "global_load_lds_dwordx4 " V ", " A P "\n\t" \
                     "global_load_lds_dwordx4 " V ", " A " offset:1024" P "\n\t" \
                     "global_load_lds_dwordx4 " V ", " A " offset:2048" P "\n\t"
    asm volatile(DMA3("%0", "%1", "%2", "") "s_mov_b32 m0, %4\n\ts_nop 0\n\tglobal_load_lds_dwordx4 %3, off"
                 :: "v"(lane * 16), "s"(a.q + wave * 1536), "s"(lds0 + LDS_QRAW + wave * 3072),
                    "v"(lane < 48 ? (const unsigned char*)((lane < 24 ? a.q_cos : a.q_sin - DH) + 8 * lane) : a.part + pep), "s"(lds0 + LDS_QT + wave * RECW) : "memory");
    SB();
    {
        const unsigned short* sp = wave == 0 ? a.ksc : wave == 1 ? a.kzp : wave == 2 ? a.vsc : a.vzp;
        const unsigned vs = ((lane >> 3) * Skv + RW * pos + (lane & 7) * 8) * 2;
        const unsigned ds = lds0 + LDS_SC + wave * 4096;
#define DMA1(A, B) "s_mov_b32 m0, " B "\n\ts_nop 0\n\tglobal_load_lds_dwordx4 %0, " A " nt\n\t"
        asm volatile(DMA1("%1", "%5") DMA1("%2", "%6") DMA1("%3", "%7") DMA1("%4", "%8")
                     :: "v"(vs), "s"(sp), "s"(sp + 8 * Skv), "s"(sp + 16 * Skv), "s"(sp + 24 * Skv), "s"(ds), "s"(ds + 1024), "s"(ds + 2048), "s"(ds + 3072) : "memory");
    }
    SB();
    const unsigned tdma = now32();
    const int stag = (OPT & 32) ? (int)(((sb & ~7) + wave * 2 + (b & 1)) * (TP / 2)) / 256 - TP / 4 : (int)(((OPT & 2) ? (((b >> 3) * 26 + wave * 2 + (b & 1)) & 63) : ((((b >> 3) * 4 + wave) * 37) & 63)) * (TP / 2)) / 64 - TP / 4;
    unsigned sched = ((tdma + ADV) << 4) + stag - 4 * (TP / 2);
    unsigned vt = tdma << 4;
    int tru = -4;
    unsigned step = TP / 2, tmin = TMIN / 2, vth = WIN & 15;
    auto pace = [&](bool ctl = false) {
        unsigned tgt = sched;
        sched += step;
        if ((int)(vt - tgt) > 0) tgt = vt;
        unsigned t = now32() << 4;
        while ((int)(t - tgt) < 0) { __builtin_amdgcn_s_sleep(1); t = now32() << 4; }
        vt = ((int)(t - 128 - tgt) > 0 ? t - 128 : tgt) + tmin;
        if (ctl) {
            if constexpr (WIN > 0 && WIN < 16) asm volatile("s_waitcnt vmcnt(%0)" :: "i"(WIN) : "memory");
            if constexpr (WIN >= 16) {
                const unsigned vm = __builtin_amdgcn_s_getreg(7 | (3 << 11));
                if (vm > vth) vt += (vm - vth) * (WIN >> 4);
            }
        }
        if constexpr (DBG == 2) { *reinterpret_cast<unsigned*>(smem + LDS_TR + 16 + wave * 1024 + (tru & 255) * 4) = ((t - (sched - step)) >> 4) | ((OPT & 8) ? (__builtin_amdgcn_s_getreg(7 | (3 << 11)) << 16) : 0); ++tru; }
    };
    {
        unsigned dst = lds0 + LDS_CS + wave * RECW;
        const unsigned v0 = lane * 16 + row0 * (DH * 2);
#pragma unroll 1
        for (int j = 0; j < 4; ++j) {
            pace();
            asm volatile(DMA3("%0", "%1", "%2", " nt") :: "v"(v0), "s"((j < 2 ? a.k_cos : a.k_sin) + (j & 1) * 8 * DH), "s"(dst) : "memory");
            dst += 3072;
        }
    }
    if (tid < MAXH) *reinterpret_cast<unsigned*>(smem + LDS_CNT + tid * 4) = 0;
    const rsrc_t kr = mkrsrc(a.qk, H * hstride);
    const rsrc_t vr = mkrsrc(a.qv, H * hstride);
    const unsigned kvoff = (row0 + r) * (DH * 4) + c * 16;
    auto issue = [&](HG& g, rsrc_t rs, unsigned so) {
#pragma unroll
        for (int x = 0; x < 3; ++x) g.d[x] = bld128<2>(rs, kvoff + 256 * x, so);
    };
    HG buf[4];
#define PRE(g) SB(); pace(); issue(buf[g], kr, (g) * (4 * DH * 4)); SB();
    asm volatile("s_waitcnt vmcnt(12) lgkmcnt(0)\n\ts_barrier" ::: "memory");
    unsigned tbar = DS ? now32() : 0;
    PRE(0)
    const unsigned qoff = (lane < 48 ? lane : lane - 48) * 4;
    const unsigned* qt = reinterpret_cast<const unsigned*>(smem + LDS_QT + wave * RECW + qoff);
    unsigned qc0 = qt[0], qc1 = qt[HALF / 2], qs0 = qt[DH / 2], qs1 = qt[(DH + HALF) / 2];
    u64 tag;
    {
        const u32x2 e = *reinterpret_cast<const u32x2*>(smem + LDS_QT + wave * RECW + 768);
        tag = (((u64)__builtin_amdgcn_readfirstlane(e[1]) << 32) | __builtin_amdgcn_readfirstlane(e[0])) * 6364136223846793005ull + 1442695040888963407ull;
        asm volatile("" : "+s"(tag));
    }
    const unsigned t32 = tag >> 32;
    const unsigned char* qraw = smem + LDS_QRAW + qoff;
    unsigned char* qrot = smem + LDS_Q + wave * (DH * 4) + 2 * qoff;
    auto rotq = [&]() {
        const unsigned q_lo = *reinterpret_cast<const unsigned*>(qraw), q_hi = *reinterpret_cast<const unsigned*>(qraw + HALF * 2);
        qraw += DH * 2;
        const f32x2 lo = f32x2{bflo(q_lo), bfhi(q_lo)}, hi = f32x2{bflo(q_hi), bfhi(q_hi)};
        const f32x2 y1 = lo * f32x2{bflo(qc0), bfhi(qc0)}, y2 = -hi * f32x2{bflo(qs0), bfhi(qs0)};
        const f32x2 y3 = hi * f32x2{bflo(qc1), bfhi(qc1)}, y4 = lo * f32x2{bflo(qs1), bfhi(qs1)};
        const unsigned x1 = pk_bf16(y1[0], y1[1]), x2 = pk_bf16(y2[0], y2[1]), x3 = pk_bf16(y3[0], y3[1]), x4 = pk_bf16(y4[0], y4[1]);
        const f32x2 z1 = f32x2{bflo(x1), bfhi(x1)} + f32x2{bflo(x2), bfhi(x2)}, z2 = f32x2{bflo(x3), bfhi(x3)} + f32x2{bflo(x4), bfhi(x4)};
        const unsigned l2 = pk_bf16(z1[0], z1[1]), h2 = pk_bf16(z2[0], z2[1]);
        *reinterpret_cast<f32x2*>(qrot) = f32x2{bflo(l2), bfhi(l2)};
        *reinterpret_cast<f32x2*>(qrot + HALF * 4) = f32x2{bflo(h2), bfhi(h2)};
        asm volatile("" ::: "memory");
    };
    f32x4 qa[3], qb[3];
    const unsigned char* qh = smem + LDS_Q + wave * (DH * 4) + 16 * c;
    const unsigned char* qh1 = qh + (c < 8 ? 64 + HALF : 64 - HALF) * 4;
    auto loadq = [&]() {
        asm volatile("" ::: "memory");
#pragma unroll
        for (int x = 0; x < 3; ++x) qa[x] = *reinterpret_cast<const f32x4*>(qh + 256 * x);
        qb[0] = *reinterpret_cast<const f32x4*>(qh + HALF * 4);
        qb[1] = *reinterpret_cast<const f32x4*>(qh1);
        qb[2] = *reinterpret_cast<const f32x4*>(qh + (128 - HALF) * 4);
        asm volatile("" ::: "memory");
    };
    rotq();
    if (b < 8 && wave == 3) __builtin_amdgcn_raw_buffer_store_b128(u32x4{tdma, t32, tdma, (unsigned)tag}, pr, lane == 0 ? psk + b * 16 : 0x80000000u, 0, 16);
    PRE(1)
    f32x2 cc[4][3][2], ss[4][3][2];
    const unsigned char* cb = smem + LDS_CS + wave * RECW + r * (DH * 2) + c * 8;
    asm volatile("s_waitcnt vmcnt(12)" ::: "memory");
#pragma unroll
    for (int k = 0; k < 4; ++k)
#pragma unroll
        for (int x = 0; x < 3; ++x) {
            const u32x2 t = *reinterpret_cast<const u32x2*>(cb + k * 1536 + 128 * x);
            cc[k][x][0] = f32x2{bflo(t[0]), bfhi(t[0])}; cc[k][x][1] = f32x2{bflo(t[1]), bfhi(t[1])};
            asm volatile("" : "+v"(cc[k][x][0]), "+v"(cc[k][x][1]));
        }
    PRE(2)
    loadq();
    const unsigned char* scp = smem + LDS_SC + (c & 4) * 1024 + (16 * wave + 4 * (c & 3) + r) * 2;
    unsigned sc = *reinterpret_cast<const unsigned short*>(scp);
    auto loadsin = [&](int k) {
        const unsigned char* sb1 = cb + (c < 8 ? (64 + HALF) * 2 : (64 - HALF) * 2);
        const unsigned sg = c < 8 ? 0u : 0x80008000u;
        u32x2 t[3];
        t[0] = *reinterpret_cast<const u32x2*>(cb + 6144 + k * 1536 + HALF * 2);
        t[1] = *reinterpret_cast<const u32x2*>(sb1 + 6144 + k * 1536) ^ u32x2{sg, sg};
        t[2] = *reinterpret_cast<const u32x2*>(cb + 6144 + k * 1536 + (128 - HALF) * 2) ^ u32x2{0x80008000u, 0x80008000u};
#pragma unroll
        for (int x = 0; x < 3; ++x) {
            ss[k][x][0] = f32x2{bflo(t[x][0]), bfhi(t[x][0])}; ss[k][x][1] = f32x2{bflo(t[x][1]), bfhi(t[x][1])};
            asm volatile("" : "+v"(ss[k][x][0]), "+v"(ss[k][x][1]));
        }
    };
    asm volatile("s_waitcnt vmcnt(12)" ::: "memory");
    loadsin(0); loadsin(1);
    PRE(3)
    asm volatile("s_waitcnt vmcnt(12)" ::: "memory");
    loadsin(2); loadsin(3);
    unsigned th1 = 0, th4 = 0;
    unsigned char* prec = smem + LDS_P + wave * (H * 128) + r * 32 + (c & 7) * 4;
    auto softmax = [&](float s, int h) {
        float m = fmaxf(s, dpp<0xb1>(s));
        m = xrow_max(fmaxf(m, dpp<0x4e>(m)));
        const float p = __builtin_amdgcn_exp2f(s - m);
        float l = p + dpp<0xb1>(p);
        l = xrow_sum(l + dpp<0x4e>(l));
        const float t = (c & 1) ? l : m;
        const int x = __builtin_amdgcn_update_dpp(__float_as_int(p), __float_as_int(t), 0x114, 0xf, 0xa, false);
        *reinterpret_cast<int*>(prec + h * 128) = x;
    };
    float sprev = 0.f;

#pragma unroll 1
    for (int h = 0; h < H; ++h) {
        const bool more = h + 1 < H;
        const rsrc_t nx = more ? kr : vr;
        const unsigned nso = more ? (h + 1) * hstride : 0;
        float scf = bflo(sc);
        asm volatile("" : "+v"(scf) :: "memory");
        SB();
        scp += more ? 128 : 4096 + 128;
        sc = *reinterpret_cast<const unsigned short*>(scp);
        SB();
        float sreg = 0.f;
#define KSTEP(k) { \
            const float kz = dpp<0x154 + (k)>(scf); \
            f32x2 kz2 = f32x2{kz, kz}; \
            asm("" : "+v"(kz2)); \
            f32x2 a2 = f32x2{0.f, 0.f}; \
            _Pragma("unroll") for (int x = 0; x < 3; ++x) { \
                _Pragma("unroll") for (int e = 0; e < 2; ++e) { \
                    const f32x2 w = __builtin_elementwise_fma(f32x2{qb[x][2 * e], qb[x][2 * e + 1]}, ss[k][x][e], f32x2{qa[x][2 * e], qa[x][2 * e + 1]} * cc[k][x][e]); \
                    const f32x2 kf = f32x2{(float)buf[k].d[x][2 * e], (float)buf[k].d[x][2 * e + 1]} - kz2; \
                    a2 = __builtin_elementwise_fma(kf, w, a2); \
                } \
            } \
            float sa = a2[0] + a2[1]; \
            asm volatile("" : "+v"(sa) :: "memory"); \
            SB(); pace(true); issue(buf[k], nx, nso + (k) * (4 * DH * 4)); SB(); \
            if ((k) == 0) { softmax(sprev, (h - 1) & (H - 1)); SB(); }   \
            if ((k) == 1) { rotq(); SB(); } \
            if ((k) == 2) { \
                if (wave == 1 && h == ((OPT & 4) ? 1 + ((b >> 3) & 3) : 4 + ((b >> 3) & 15))) \
                    asm volatile("s_mov_b32 m0, %2\n\ts_nop 0\n\tglobal_load_lds_dwordx4 %0, %1 sc1" :: "v"(psk + (lane & 7) * 16), "s"(a.part), "s"(lds0 + LDS_SK) : "memory"); \
                if (h == ((OPT & 4) ? 6 : 25)) {        \
                    const u32x2 e = *reinterpret_cast<const u32x2*>(smem + LDS_SK + (lane & 7) * 16); \
                    const int d = (int)(tdma - e[0]); \
                    if (__all(e[1] == t32 && d > -400 && d < 400)) { \
                        int sum = d + __builtin_amdgcn_mov_dpp(d, 0xb1, 0xf, 0xf, true); \
                        sum += __builtin_amdgcn_mov_dpp(sum, 0x4e, 0xf, 0xf, true); \
                        sum += __builtin_amdgcn_mov_dpp(sum, 0x124, 0xf, 0xf, true); \
                        const int adj = (__builtin_amdgcn_readfirstlane(sum) * ((OPT & 4) ? 73 : 112) + 4096) >> 13;        \
                        step -= adj; tmin -= adj; \
                        if ((OPT & 64) && (OPT & 384) && b < ((OPT & 256) ? 256 : 16) && wave == 0) {        \
                            unsigned t0, t1; \
                            const unsigned ar = (((__builtin_amdgcn_readfirstlane(e[0]) >> 13) & 31) + 1) * AREA; \
                            asm volatile("s_load_dword %0, %2, %3\n\ts_load_dword %1, %2, %4\n\ts_waitcnt lgkmcnt(0)" : "=&s"(t0), "=&s"(t1) : "s"(a.part), "s"(ar), "s"(ar + AREA - 128) : "memory"); \
                        } \
                    } \
                } \
                SB(); \
            } \
            if ((k) == 3) { loadq(); SB(); } \
            sa = row16_allsum(sa); \
            sreg = (c & 3) == (k) ? sa : sreg; }
        KSTEP(0) KSTEP(1) KSTEP(2) KSTEP(3)
        sprev = sreg * (scf * a.scale_log2);
        if (DS) { if (h == 7) th1 = now32(); }
    }
    softmax(sprev, H - 1);
    if (DS) th4 = now32();

    auto merge = [&](int hd) {
        const unsigned char* rec = smem + LDS_REC + hd * (DH * 4);
        const unsigned char* mlp = smem + LDS_P + hd * 128 + 16;
        f32x4 ow[4]; float mv[4];
#pragma unroll
        for (int w = 0; w < 4; ++w) {
            mv[w] = *reinterpret_cast<const float*>(mlp + w * (H * 128));
            ow[w] = *reinterpret_cast<const f32x4*>(rec + w * RECW + lane * 16);
        }
        const float M = fmaxf(fmaxf(mv[0], mv[1]), fmaxf(mv[2], mv[3]));
        f32x4 t = f32x4{0.f, 0.f, 0.f, 0.f};
#pragma unroll
        for (int w = 0; w < 4; ++w) t += __builtin_amdgcn_exp2f(mv[w] - M) * ow[w];
        return u32x2{pk_bf16(t[0], t[1]), pk_bf16(t[2], t[3])};
    };
    constexpr int NCN = 3 * (H - 1), NC = NCN + 6;
    constexpr int FL = (OPT >> 9) & 3;
    const int cn = sb;
    const bool late = cn >= NCN;
    unsigned xo = 0, oo = 0, fo = 0, area = 4096;
    unsigned old = 0, flo = 0x80000000u;
    u32x4 flv = {};
    float gl = 0.f;
    unsigned eo[5] = {};
    unsigned el[3] = {};
    auto gather0 = [&]() {
        int lane = tid & 63;
        asm volatile("" : "+v"(lane));
#pragma unroll
        for (int p = 0; p < 3; ++p) {
            const int t = lane + 64 * p, hi_ = t / 24, k = t - hi_ * 24;
            const int hh = 8 * wave + hi_;
            el[p] = hh * (DH * 4) + k * 16;
            eo[p] = hh < H - 2 ? (hh * nb + sb) * (DH * 2) + k * 16 : 0x80000000u;
        }
        eo[3] = wave == 3 && lane < 48 ? ((H - 2) * nb + sb) * (DH * 2) + lane * 8 : 0x80000000u;
        eo[4] = lane < 48 ? plt + sb * LS + lane * 16 : 0x80000000u;
        flo = lane < 8 * (NP + 2) ? pfl + (lane >> 3) * fsz + (((((8 * wave + (lane & 7)) >> FL) * nb + sb) << FL) + (lane & ((1 << FL) - 1))) * 16 : 0x80000000u;
        if (OPT & 64) {
            u32x4 e = *reinterpret_cast<const u32x4*>(smem + LDS_SK);
#pragma unroll 1
            for (int n = 0; n < (1 << 20) && !(e[1] == t32 && e[3] == (unsigned)tag); ++n)
                asm volatile("global_load_dwordx4 %0, %1, %2 sc1\n\ts_waitcnt vmcnt(0)" : "=v"(e) : "v"(psk), "s"(a.part) : "memory");
            area = (((__builtin_amdgcn_readfirstlane(e[0]) >> 13) & 31) + 1) * AREA;
            area += 4096;
        }
        const unsigned h = late ? H - 1 : cn / 3, chunk = late ? cn - NCN : cn % 3;
        const unsigned r = lane >> 4, g = lane & 7, e = (lane >> 3) & 1;
        const unsigned rb = (late ? plt : h * nb * (DH * 2)) + chunk * 128, rs = late ? LS : DH * 2;
        xo = rb + (64 * wave + (lane >> 3)) * rs + g * 16;
        oo = late ? ((r & 1) ? 0x80000000u : (h * DH + chunk * 32 + g * 4 + r + e) * 2) : (h * DH + chunk * 64 + g * 8 + 2 * r + e) * 2;
        fo = pfl + ((((h >> FL) * nb + 64 * wave + lane) << FL) + (h & ((1 << FL) - 1))) * 16;
        asm volatile("" : "+s"(area), "+v"(xo), "+v"(oo), "+v"(fo), "+v"(el[0]), "+v"(el[1]), "+v"(el[2]), "+v"(eo[0]), "+v"(eo[1]), "+v"(eo[2]), "+v"(eo[3]), "+v"(eo[4]), "+v"(flo));
    };
    auto gather1 = [&]() {
        f32x2 ml[4];
#pragma unroll
        for (int w = 0; w < 4; ++w) ml[w] = *reinterpret_cast<const f32x2*>(smem + LDS_P + w * (H * 128) + (8 * wave + (lane & 7)) * 128 + 16);
        const float M = fmaxf(fmaxf(ml[0][0], ml[1][0]), fmaxf(ml[2][0], ml[3][0]));
        float L = 0.f;
#pragma unroll
        for (int w = 0; w < 4; ++w) L += __builtin_amdgcn_exp2f(ml[w][0] - M) * ml[w][1];
        flv = u32x4{__float_as_uint(M), t32, __float_as_uint(L), (unsigned)tag};
        float mv[4];
#pragma unroll
        for (int w = 0; w < 4; ++w) mv[w] = *reinterpret_cast<const float*>(smem + LDS_P + ((wave + w) & 3) * (H * 128) + (H - 1) * 128 + 16);
        gl = __builtin_amdgcn_exp2f(mv[0] - fmaxf(fmaxf(mv[0], mv[1]), fmaxf(mv[2], mv[3])));
        asm volatile("" : "+v"(flv), "+v"(gl));
    };
    float pv = *reinterpret_cast<const float*>(prec);
    f32x2 acc[3][2];
    float y;
    auto prep = [&]() {
        y = bflo(sc);
        y = (c & 4) ? y : y * pv;
        asm volatile("" : "+v"(y) :: "memory");
        SB();
        scp += 128;
        sc = *reinterpret_cast<const unsigned short*>(scp);
        prec += 128;
        pv = *reinterpret_cast<const float*>(prec);
        SB();
    };
    auto opq = [](int v) { asm volatile("" : "+s"(v)); return v; };
#define VCOMP(k) { \
        const float pw = dpp<0x150 + (k)>(y); \
        const float vz = dpp<0x154 + (k)>(y); \
        f32x2 pw2 = f32x2{pw, pw}, vz2 = f32x2{vz, vz}; \
        asm("" : "+v"(pw2), "+v"(vz2)); \
        _Pragma("unroll") for (int x = 0; x < 3; ++x) \
            _Pragma("unroll") for (int e = 0; e < 2; ++e) { \
                const f32x2 vf = f32x2{(float)buf[k].d[x][2 * e], (float)buf[k].d[x][2 * e + 1]} - vz2; \
                acc[x][e] = (k) == 0 ? pw2 * vf : __builtin_elementwise_fma(pw2, vf, acc[x][e]); \
            } \
        asm volatile("" : "+v"(acc[0][0]), "+v"(acc[0][1]), "+v"(acc[1][0]), "+v"(acc[1][1]), "+v"(acc[2][0]), "+v"(acc[2][1]) :: "memory"); \
        SB(); }
#define VSTEP(k, hn) { \
        VCOMP(k) pace(true); issue(buf[k], vr, (hn) * hstride + (k) * (4 * DH * 4)); SB(); \
        if ((k) == 1) { tmin = (hn) == H - 1 ? 0 : tmin; if (OPT & 16) vth = (hn) == H - 2 ? 63 : vth; } \
        if ((k) == 1 && (hn) == 15) { asm volatile("s_waitcnt lgkmcnt(0)\n\ts_barrier" ::: "memory"); gather1(); SB(); } \
        if ((k) == 2 && (hn) == 17) { gather0(); SB(); } }
    auto record = [&](int h) {
        f32x4 o;
#pragma unroll
        for (int e = 0; e < 4; ++e) {
            const float t0 = sw32(acc[0][e >> 1][e & 1], acc[2][e >> 1][e & 1]);
            const float t1 = sw32(acc[1][e >> 1][e & 1], acc[1][e >> 1][e & 1]);
            o[e] = sw16(t0, t1);
        }
        asm volatile("s_mov_b64 exec, %5\n\tds_write_b128 %1, %2\n\ts_mov_b64 exec, 1\n\tds_add_rtn_u32 %0, %3, %4\n\ts_mov_b64 exec, -1"
                     : "+v"(old) : "v"(lds0 + LDS_REC + h * (DH * 4) + wave * RECW + lane * 16), "v"(o),
                       "v"(lds0 + LDS_CNT + h * 4), "v"(1u), "s"(0xffffffffffffull) : "memory");
        SB();
    };
#pragma unroll 1
    for (int h = 0; h < H - 1; ++h) {
        prep();
        VSTEP(0, h + 1)
        asm volatile("s_waitcnt lgkmcnt(0)" : "+v"(old) :: "memory");
        if (__builtin_amdgcn_readfirstlane(old) == 3) *reinterpret_cast<u32x2*>(smem + LDS_REC + (h - 1) * (DH * 4) + lane * 8) = merge(h - 1);
        SB();
        VSTEP(1, h + 1) VSTEP(2, h + 1) VSTEP(3, h + 1)
        if (DBG == 14) tA = now32();
        record(h);
    }
    y = bflo(sc);
    y = (c & 4) ? y : y * (pv * gl);
    VCOMP(0)
    asm volatile("s_waitcnt lgkmcnt(0)\n\ts_barrier" ::: "memory");
    if (DBG == 14) tB = now32();
    u32x4 ev[3];
#pragma unroll
    for (int p = 0; p < 3; ++p) ev[p] = *reinterpret_cast<const u32x4*>(smem + LDS_REC + el[p]);
    SB();
    VCOMP(1)
    u32x2 e3 = {};
    if (wave == 3) e3 = merge(H - 2);
    asm volatile("" : "+v"(e3), "+v"(ev[0]), "+v"(ev[1]), "+v"(ev[2]));
    SB();
    if (DBG == 14) tE = now32();
    VCOMP(2)
    if (DBG == 14) tC = now32();
    if (EX) { const unsigned tx = sched - step - stag + EX * 16; while ((int)((now32() << 4) - tx) < 0) {} }
    asm volatile("s_waitcnt vmcnt(0)" : "+v"(buf[3].d[0]), "+v"(buf[3].d[1]), "+v"(buf[3].d[2]) :: "memory");
    if (DBG == 14) tD = now32();
#pragma unroll
    for (int p = 0; p < 3; ++p) __builtin_amdgcn_raw_buffer_store_b128(ev[p], pr, eo[p], area, 16);
    __builtin_amdgcn_raw_buffer_store_b64(e3, pr, eo[3], area, 16);
    if (DBG == 4 || DBG == 24) tbar = now32();
    SB();
    VCOMP(3)
    record(H - 1);
    asm volatile("s_waitcnt lgkmcnt(0)" : "+v"(old) :: "memory");
    if (__builtin_amdgcn_readfirstlane(old) == 3) {
        f32x4 ow[4];
#pragma unroll
        for (int w = 0; w < 4; ++w) ow[w] = *reinterpret_cast<const f32x4*>(smem + LDS_REC + (H - 1) * (DH * 4) + w * RECW + lane * 16);
        const f32x4 t = (ow[0] + ow[1]) + (ow[2] + ow[3]);
        __builtin_amdgcn_raw_buffer_store_b128(u32x4{pk_bf16(t[0], t[1]), t32, pk_bf16(t[2], t[3]), (unsigned)tag}, pr, eo[4], area, 16);
        asm volatile("s_waitcnt vmcnt(1)" ::: "memory");
    } else asm volatile("s_waitcnt vmcnt(0)" ::: "memory");
    __builtin_amdgcn_raw_buffer_store_b128(flv, pr, flo, area, 16);
    unsigned tend = DS ? now32() : 0;
    unsigned tfl = 0, tpl = 0;

    unsigned tgt = sched - stag + (HOLD - (3 - wave) * HS) * 16;
    if (cn < NC) {
        unsigned rs[8];
#pragma unroll
        for (int i = 0; i < 8; ++i) rs[i] = area + i * 8 * (late ? LS : DH * 2);
        float* s_o = reinterpret_cast<float*>(smem + LDS_QRAW);
        unsigned bp = (lane >> 3) * 4, wo = lds0 + LDS_QRAW + 1024 + wave * 8, po = lds0 + LDS_QRAW + lane * 4;
        asm volatile("s_waitcnt vmcnt(0)" : "+s"(rs[1]), "+s"(rs[2]), "+s"(rs[3]), "+s"(rs[4]), "+s"(rs[5]), "+s"(rs[6]), "+s"(rs[7]), "+v"(bp), "+v"(wo), "+v"(po) :: "memory");
        if (DBG == 24) th4 = now32();
        while ((int)((now32() << 4) - tgt) < 0) {}
        i32x4 ya[NP + 1], x[8];
#pragma unroll
        for (int i = 0; i <= NP; ++i) { if (i) __builtin_amdgcn_s_sleep(SP); ya[i] = bld128<16>(pr, fo, area + i * fsz); SB(); }
        i32x4 y = ya[0];
#define BAD(y) ((unsigned)(y)[1] != t32 || (unsigned)(y)[3] != (unsigned)tag)
        if (__any(BAD(y))) {
            if constexpr (NP >= 1) y = ya[1];
            if (NP < 1 || __any(BAD(y))) {
                if constexpr (NP >= 2) y = ya[2];
#pragma unroll 1
                for (int n = 0; n < (1 << 20) && BAD(y); ++n) {
                    y = bld128<16>(pr, fo, area + (NP + 1) * fsz);
                    asm volatile("" ::: "memory");
                }
            }
        }
        asm volatile("" ::: "memory");
        if (DS) tfl = now32();
        const float m = __int_as_float(y[0]);
        float mw, u;
#define XL(i) x[i] = bld128<(O2 & 16) ? 18 : 16>(pr, xo, rs[i]); SB();
        XL(0) asm volatile("s_nop 0\n\tv_max_f32_dpp %0, %1, %1 row_ror:8 row_mask:0xf bank_mask:0xf\n\ts_nop 0" : "=&v"(mw) : "v"(m)); SB();
        XL(1) asm volatile("v_max_f32_dpp %0, %0, %0 row_ror:4 row_mask:0xf bank_mask:0xf\n\ts_nop 0" : "+v"(mw)); SB();
        XL(2) asm volatile("v_max_f32_dpp %0, %0, %0 row_ror:2 row_mask:0xf bank_mask:0xf\n\ts_nop 0" : "+v"(mw)); SB();
        XL(3) asm volatile("v_max_f32_dpp %0, %0, %0 row_ror:1 row_mask:0xf bank_mask:0xf\n\tv_mov_b32 %1, %0\n\ts_nop 0" : "+v"(mw), "=&v"(u)); SB();
        XL(4) asm volatile("v_permlane16_swap_b32 %0, %1\n\ts_nop 0\n\tv_max_f32 %0, %0, %1\n\tv_mov_b32 %1, %0\n\ts_nop 0" : "+v"(mw), "+v"(u)); SB();
        XL(5) asm volatile("v_permlane32_swap_b32 %0, %1\n\ts_nop 0\n\tv_max_f32 %0, %0, %1" : "+v"(mw), "+v"(u)); SB();
        XL(6)
        const float f = __builtin_amdgcn_exp2f(m - mw);
        SB();
        XL(7)
        f32x2 fs[4];
#pragma unroll
        for (int i = 0; i < 8; ++i) fs[i >> 1][i & 1] = __int_as_float(__builtin_amdgcn_ds_bpermute(bp + 32 * i, __float_as_int(f)));
        SB();
        const float lw = xrow_sum(row16_allsum(f * __int_as_float(y[2])));
        f32x4 a0, a1;
        asm volatile("ds_write_b64 %2, %3\n\ts_waitcnt lgkmcnt(0)\n\ts_barrier\n\tds_read_b128 %0, %4\n\tds_read_b128 %1, %4 offset:16\n\ts_waitcnt lgkmcnt(0)"
                     : "=&v"(a0), "=&v"(a1) : "v"(wo), "v"(f32x2{mw, lw}), "v"(lds0 + LDS_QRAW + 1024) : "memory");
        const float M = fmaxf(fmaxf(a0[0], a0[2]), fmaxf(a1[0], a1[2]));
        float gw = __builtin_amdgcn_exp2f(mw - M) * __builtin_amdgcn_rcpf((__builtin_amdgcn_exp2f(a0[0] - M) * a0[1] + __builtin_amdgcn_exp2f(a0[2] - M) * a0[3]) + (__builtin_amdgcn_exp2f(a1[0] - M) * a1[1] + __builtin_amdgcn_exp2f(a1[2] - M) * a1[3]));
        asm volatile("" : "+v"(gw));
        if (DBG == 4) th4 = now32();
        SB();
        f32x2 o[4];
#pragma unroll 1
        for (int n = 0; n < (1 << 20); ++n) {
#pragma unroll
            for (int i = 0; i < 8; ++i) {
#pragma unroll
                for (int e = 0; e < 4; ++e) {
                    const f32x2 t = f32x2{bflo(x[i][e]), bfhi(x[i][e])};
                    if (i == 0) asm volatile("v_pk_mul_f32 %0, %1, %2 op_sel_hi:[0,1]" : "=v"(o[e]) : "v"(fs[0]), "v"(t));
                    else if (i & 1) asm volatile("v_pk_fma_f32 %0, %1, %2, %0 op_sel:[1,0,0]" : "+v"(o[e]) : "v"(fs[i >> 1]), "v"(t));
                    else asm volatile("v_pk_fma_f32 %0, %1, %2, %0 op_sel_hi:[0,1,1]" : "+v"(o[e]) : "v"(fs[i >> 1]), "v"(t));
                }
                SB();
            }
            if (!late) break;
            unsigned bad = 0;
#pragma unroll
            for (int i = 0; i < 8; ++i) { bad |= (x[i][1] ^ t32) | (x[i][3] ^ (unsigned)tag); asm volatile("" : "+v"(bad)); }
            if (__all(bad == 0)) break;
            asm volatile("" ::: "memory");
            XL(0) XL(1) XL(2) XL(3) XL(4) XL(5) XL(6) XL(7)
        }
        if (DS) { asm volatile("" : "+v"(o[0]), "+v"(o[1]), "+v"(o[2]), "+v"(o[3])); tpl = now32(); }
        asm volatile("" :: "v"(ya[NP]), "v"(ya[NP > 1]));
        const float w0 = sw16(sw32(o[0][0], o[2][0]), sw32(o[1][0], o[3][0])), w1 = sw16(sw32(o[0][1], o[2][1]), sw32(o[1][1], o[3][1]));
        s_o[wave * 64 + lane] = ((c < 8 ? w0 : w1) + dpp<0x128>(c < 8 ? w1 : w0)) * gw;
        asm volatile("s_waitcnt lgkmcnt(0)\n\ts_barrier" ::: "memory");
        if (wave == 0) {
            f32x2 p0, p1;
            asm volatile("ds_read2st64_b32 %0, %2 offset1:1\n\tds_read2st64_b32 %1, %2 offset0:2 offset1:3\n\ts_waitcnt lgkmcnt(0)" : "=&v"(p0), "=&v"(p1) : "v"(po) : "memory");
            __builtin_amdgcn_raw_buffer_store_b16((unsigned short)pk_bf16((p0[0] + p0[1]) + (p1[0] + p1[1]), 0.f), mkrsrc(a.out, H * DH * 2), oo, 0, 0);
        }
        if (b == 0 && tid == 0) *reinterpret_cast<u64*>(a.part + pep) = tag;
        asm volatile(".Lkv_b%c0:" :: "i"(ID));
    }
    if (DBG) {
        const unsigned tc1 = now32();
        if (!DS) tend = tbar = th4 = tfl = tpl = tc1;
        const unsigned tl16 = sched - stag - step;
        u64* stp = reinterpret_cast<u64*>(a.part + STAMP_OFF) + (size_t)((DBG == 4 || DBG == 24 ? cn : b) * 4 + wave) * 8;
        if (DBG == 14) { if (lane == 0) { stp[0] = tstart; stp[1] = tA; stp[2] = tend - (((tend << 4) - tl16) >> 4); stp[3] = tB; stp[4] = tE; stp[5] = tC; stp[6] = tD; stp[7] = tc1; } } else
        if (lane == 0) { stp[0] = tstart; stp[1] = tbar; stp[2] = DBG >= 4 ? tend - (((tend << 4) - tl16) >> 4) : th1; stp[3] = DBG == 4 && cn >= NC ? tend : th4; stp[4] = tend; stp[5] = cn < NC ? tfl : tend; stp[6] = cn < NC ? tpl : tend; stp[7] = tc1; }
        if (DBG == 2) *reinterpret_cast<u32x4*>(a.part + STAMP_OFF + 65536 + (b * 4 + wave) * 1024 + lane * 16) = *reinterpret_cast<const u32x4*>(smem + LDS_TR + 16 + wave * 1024 + lane * 16);
    }
}

template <int TP, int ADV, int TMIN, int HOLD, int WIN, int OPT, int HS, int O2, int EX, int ID, int DBG>
static void launch39(void* const* d_in, const int* in_sizes, void* d_out, void* d_ws, hipStream_t stream) {
    typedef const unsigned short* P;
    Args a;
    a.qk = (const int*)d_in[1]; a.qv = (const int*)d_in[4];
    a.k_cos = (P)d_in[9]; a.k_sin = (P)d_in[10];
    a.q = (P)d_in[0]; a.q_cos = (P)d_in[7]; a.q_sin = (P)d_in[8];
    a.ksc = (P)d_in[2]; a.kzp = (P)d_in[3]; a.vsc = (P)d_in[5]; a.vzp = (P)d_in[6];
    a.part = (unsigned char*)d_ws;
    a.out = (unsigned short*)d_out;
    a.Skv = in_sizes[9] / DH;
    a.scale_log2 = 1.4426950408889634f / sqrtf((float)DH);
    kv39_kernel<TP, ADV, TMIN, HOLD, WIN, OPT, HS, O2, EX, ID, DBG><<<a.Skv / RW, 256, 0, stream>>>(a);
}

extern "C" void kernel_launch(void* const* d_in, const int* in_sizes, int n_in, void* d_out, int out_size, void* d_ws, size_t ws_size, hipStream_t stream) {
    launch39<80*16+4,230,79*16,130,4+16*32,38+1024+64,5,16,66,0,0>(d_in, in_sizes, d_out, d_ws, stream);
}
```

```cpp
#include <hip/hip_runtime.h>
#include <hip/hip_bf16.h>
#include <cstdint>
#define KALIGN 256
#define STAMP_OFF (200 << 20)
using bf16 = __hip_bfloat16;
using i32x4 = __attribute__((ext_vector_type(4))) int;
using u32x4 = __attribute__((ext_vector_type(4))) unsigned;
using u32x2 = __attribute__((ext_vector_type(2))) unsigned;
using f32x2 = __attribute__((ext_vector_type(2))) float;
using f32x4 = __attribute__((ext_vector_type(4))) float;

constexpr int DH = 192;
constexpr int HALF = 96;
constexpr int RW = 64;
constexpr int MAXH = 32;

__device__ __forceinline__ float bflo(unsigned u) { return __uint_as_float(u << 16); }
__device__ __forceinline__ float bfhi(unsigned u) { return __uint_as_float(u & 0xffff0000u); }
__device__ __forceinline__ float rbf(float x) { return __bfloat162float(__float2bfloat16(x)); }

template <int CTRL> __device__ __forceinline__ float dpp(float x) {
    return __builtin_bit_cast(float, __builtin_amdgcn_mov_dpp(__builtin_bit_cast(int, x), CTRL, 0xf, 0xf, true));
}
__device__ __forceinline__ float row16_allsum(float x) {
    x += dpp<0x128>(x);
    x += dpp<0x124>(x);
    x += dpp<0x122>(x);
    x += dpp<0x121>(x);
    return x;
}
__device__ __forceinline__ float row16_allmax(float x) {
    x = fmaxf(x, dpp<0x128>(x));
    x = fmaxf(x, dpp<0x124>(x));
    x = fmaxf(x, dpp<0x122>(x));
    x = fmaxf(x, dpp<0x121>(x));
    return x;
}
__device__ __forceinline__ float xrow_max(float x) {
    auto s = __builtin_amdgcn_permlane16_swap(__float_as_uint(x), __float_as_uint(x), false, false);
    x = fmaxf(__uint_as_float(s[0]), __uint_as_float(s[1]));
    auto t = __builtin_amdgcn_permlane32_swap(__float_as_uint(x), __float_as_uint(x), false, false);
    return fmaxf(__uint_as_float(t[0]), __uint_as_float(t[1]));
}
__device__ __forceinline__ float xrow_sum(float x) {
    auto s = __builtin_amdgcn_permlane16_swap(__float_as_uint(x), __float_as_uint(x), false, false);
    x = __uint_as_float(s[0]) + __uint_as_float(s[1]);
    auto t = __builtin_amdgcn_permlane32_swap(__float_as_uint(x), __float_as_uint(x), false, false);
    return __uint_as_float(t[0]) + __uint_as_float(t[1]);
}
__device__ __forceinline__ float sw32(float a, float b) {
    auto t = __builtin_amdgcn_permlane32_swap(__float_as_uint(a), __float_as_uint(b), false, false);
    return __uint_as_float(t[0]) + __uint_as_float(t[1]);
}
__device__ __forceinline__ float sw16(float a, float b) {
    auto t = __builtin_amdgcn_permlane16_swap(__float_as_uint(a), __float_as_uint(b), false, false);
    return __uint_as_float(t[0]) + __uint_as_float(t[1]);
}

#define SB() __builtin_amdgcn_sched_barrier(0)
typedef __amdgpu_buffer_rsrc_t rsrc_t;
__device__ __forceinline__ rsrc_t mkrsrc(const void* p, unsigned bytes) {
    return __builtin_amdgcn_make_buffer_rsrc(const_cast<void*>(p), 0, bytes, 0x00020000);
}
template <int AUX> __device__ __forceinline__ i32x4 bld128(rsrc_t r, unsigned voff, unsigned soff) {
    return __builtin_bit_cast(i32x4, __builtin_amdgcn_raw_buffer_load_b128(r, voff, soff, AUX));
}
template <int AUX> __device__ __forceinline__ unsigned bld16(rsrc_t r, unsigned voff, unsigned soff) {
    return __builtin_amdgcn_raw_buffer_load_b16(r, voff, soff, AUX);
}

struct HG { i32x4 d[3]; };
typedef unsigned long long u64;
typedef __attribute__((address_space(3))) unsigned char lds_u8;

__device__ __forceinline__ unsigned pk_bf16(float lo, float hi) {
    unsigned r;
    asm("v_cvt_pk_bf16_f32 %0, %1, %2" : "=v"(r) : "v"(lo), "v"(hi));
    return r;
}
__device__ __forceinline__ unsigned now32() { return (unsigned)__builtin_amdgcn_s_memrealtime(); }

constexpr int LDS_REC = 0;
constexpr int RECW = MAXH * DH * 4;
constexpr int LDS_CS = 0;
constexpr int LDS_QT = 12288;
constexpr int LDS_PF = 14336;
constexpr int LDS_QRAW = MAXH * 4 * DH * 4;
constexpr int LDS_Q = LDS_QRAW + MAXH * DH * 2;
constexpr int LDS_P = LDS_Q + 4 * DH * 4;
constexpr int LDS_SC = LDS_P + 4 * MAXH * 128;
constexpr int LDS_CNT = LDS_SC + 4 * MAXH * RW * 2;
constexpr int LDS_SK = LDS_CNT + 256;
constexpr int LDS_TR = LDS_SK + 1024;
constexpr int LDS_BYTES = LDS_TR + 4096 + 64;

struct Args {
    const int* qk; const int* qv;
    const unsigned short* k_cos; const unsigned short* k_sin;
    const unsigned short* q; const unsigned short* q_cos; const unsigned short* q_sin;
    unsigned char* part;
    unsigned short* out;
    const unsigned short* sc[4];
};
constexpr unsigned Skv = 16384;
constexpr float SCALE_LOG2 = 0.10411754627697264f;

template <int TP, int ADV, int TMIN, int HOLD, int WIN, int OPT, int HS, int O2, int EX, int ID, int DBG>
__global__ __launch_bounds__(256) __attribute__((aligned(KALIGN))) void kv40_kernel(Args a)
{
    __shared__ __attribute__((aligned(16))) unsigned char smem[LDS_BYTES];
    const unsigned tstart = DBG ? now32() : 0;
    constexpr bool DS = (DBG && DBG < 5) || DBG == 24;
    unsigned tA = 0, tB = 0, tC = 0, tD = 0, tE = 0;
    constexpr int H = MAXH, ES = 7, PFN = 16;
    const int b = blockIdx.x;
    const int tid = threadIdx.x;
    const int lane = tid & 63;
    const int wave = __builtin_amdgcn_readfirstlane(tid >> 6);
    typedef const unsigned short* const __attribute__((address_space(4))) * karg_t;
    const unsigned short* sp = ((karg_t)__builtin_amdgcn_kernarg_segment_ptr())[offsetof(Args, sc) / 8 + wave];
    if (b < PFN) {
        u64 pc; unsigned len;
        asm volatile("s_getpc_b64 %0\n.Lkv_a%c2:\n\ts_mov_b32 %1, .Lkv_b%c2-.Lkv_a%c2-16" : "=s"(pc), "=s"(len) : "i"(ID));
        const unsigned o = tid * 16;
        asm volatile("s_mov_b32 m0, %5\n\ts_nop 0\n\tglobal_load_lds_dwordx4 %0, %4\n\tglobal_load_lds_dwordx4 %1, %4\n\tglobal_load_lds_dwordx4 %2, %4\n\tglobal_load_lds_dwordx4 %3, %4"
                     :: "v"(o), "v"(o + 4096), "v"(min(o + 8192, len)), "v"(min(o + 12288, len)), "s"(pc), "s"((unsigned)(size_t)(lds_u8*)smem + LDS_PF) : "memory");
    }
    SB();
    const int r = lane >> 4;
    const int c = lane & 15;
    constexpr unsigned hstride = Skv * (DH * 4);
    constexpr unsigned nb = Skv / RW;
    const unsigned lds0 = (unsigned)(size_t)(lds_u8*)smem;
    const unsigned pos = (b & 7) * (nb >> 3) + (b >> 3);
    const unsigned sb = ((((b >> 3) * 13) & 31) << 3) | (b & 7);
    const unsigned row0 = RW * pos + 16 * wave;

    constexpr int LS = 896;
    constexpr unsigned psk = 0, pep = 128, AREA = 4u << 20;
    constexpr int NP = (O2 >> 5) & 3, SP = (O2 >> 8) & 15;
    constexpr unsigned plt = H * nb * (DH * 2), pfl = plt + nb * LS, fsz = H * nb * 16;
    const rsrc_t pr = mkrsrc(a.part, 33 * AREA);
    u64 t64;
    {
        unsigned vs = ((lane >> 3) * Skv + RW * pos + (lane & 7) * 8) * 2, v1 = vs + 16 * Skv, v2 = vs + 32 * Skv, v3 = vs + 48 * Skv;
        unsigned ds = lds0 + LDS_SC + wave * 4096;
        asm volatile("" : "+v"(vs), "+v"(v1), "+v"(v2), "+v"(v3), "+s"(ds));
        SB();
        asm volatile("s_memrealtime %0\n\ts_mov_b32 m0, %6\n\ts_nop 0\n\tglobal_load_lds_dwordx4 %1, %5 nt\n\t"
                     "s_add_u32 m0, m0, 1024\n\ts_nop 0\n\tglobal_load_lds_dwordx4 %2, %5 nt\n\t"
                     "s_add_u32 m0, m0, 1024\n\ts_nop 0\n\tglobal_load_lds_dwordx4 %3, %5 nt\n\t"
                     "s_add_u32 m0, m0, 1024\n\ts_nop 0\n\tglobal_load_lds_dwordx4 %4, %5 nt\n\ts_waitcnt lgkmcnt(0)"
                     : "=&s"(t64) : "v"(vs), "v"(v1), "v"(v2), "v"(v3), "s"(sp), "s"(ds),
                       "s"(a.qk), "s"(a.qv), "s"(a.k_cos), "s"(a.k_sin), "s"(a.q), "s"(a.q_cos), "s"(a.q_sin), "s"(a.part), "s"(a.out) : "memory", "scc");
    }
    SB();
    const unsigned tdma = (unsigned)t64;
#define DMA3(V, A, B, P) "s_mov_b32 m0, " B "\n\ts_nop 0\n\t" \
                     "global_load_lds_dwordx4 " V ", " A P "\n\t" \
                     "global_load_lds_dwordx4 " V ", " A " offset:1024" P "\n\t" \
                     "global_load_lds_dwordx4 " V ", " A " offset:2048" P "\n\t"
    asm volatile(DMA3("%0", "%1", "%2", "") "s_mov_b32 m0, %4\n\ts_nop 0\n\tglobal_load_lds_dwordx4 %3, off"
                 :: "v"(lane * 16), "s"(a.q + wave * 1536), "s"(lds0 + LDS_QRAW + wave * 3072),
                    "v"(lane < 48 ? (const unsigned char*)((lane < 24 ? a.q_cos : a.q_sin - DH) + 8 * lane) : a.part + pep), "s"(lds0 + LDS_QT + wave * RECW) : "memory");
    SB();
    const int stag = (OPT & 32) ? (int)(((sb & ~7) + wave * 2 + (b & 1)) * (TP / 2)) / 256 - TP / 4 : (int)(((OPT & 2) ? (((b >> 3) * 26 + wave * 2 + (b & 1)) & 63) : ((((b >> 3) * 4 + wave) * 37) & 63)) * (TP / 2)) / 64 - TP / 4;
    unsigned sched = ((tdma + ADV) << 4) + stag - 4 * (TP / 2);
    unsigned vt = tdma << 4;
    int tru = -4;
    unsigned calm = vt;
    unsigned step = TP / 2, tmin = TMIN / 2, vth = WIN & 15;
    auto pace = [&](bool ctl = false) {
        unsigned tgt = sched;
        sched += step;
        if ((int)(vt - tgt) > 0) tgt = vt;
        unsigned t = now32() << 4;
        while ((int)(t - tgt) < 0) { __builtin_amdgcn_s_sleep(1); t = now32() << 4; }
        vt = ((int)(t - 128 - tgt) > 0 ? t - 128 : tgt) + tmin;
        if (ctl) {
            if constexpr ((O2 >> 12) != 0) { if ((int)(t - tgt) > (O2 >> 20) * 16 && (!(O2 & 1) || (int)(t - calm) > 0)) { sched += ((O2 >> 12) & 255) * 16; calm = t + 900 * 16; } }
            if constexpr (WIN > 0 && WIN < 16) asm volatile("s_waitcnt vmcnt(%0)" :: "i"(WIN) : "memory");
            if constexpr (WIN >= 16) {
                const unsigned vm = __builtin_amdgcn_s_getreg(7 | (3 << 11));
                if (vm > vth && ((WIN >> 12) == 0 || (int)(vt - sched) < (WIN >> 12) * 16)) vt += (vm - vth) * ((WIN >> 4) & 255);
            }
        }
        if constexpr (DBG == 2) { *reinterpret_cast<unsigned*>(smem + LDS_TR + 16 + wave * 1024 + (tru & 255) * 4) = ((t - (sched - step)) >> 4) | ((OPT & 8) ? (__builtin_amdgcn_s_getreg(7 | (3 << 11)) << 16) : 0); ++tru; }
    };
    {
        unsigned dst = lds0 + LDS_CS + wave * RECW;
        const unsigned v0 = lane * 16 + row0 * (DH * 2);
#pragma unroll 1
        for (int j = 0; j < 4; ++j) {
            pace();
            asm volatile(DMA3("%0", "%1", "%2", " nt") :: "v"(v0), "s"((j < 2 ? a.k_cos : a.k_sin) + (j & 1) * 8 * DH), "s"(dst) : "memory");
            dst += 3072;
        }
    }
    if (tid < MAXH) *reinterpret_cast<unsigned*>(smem + LDS_CNT + tid * 4) = 0;
    const rsrc_t kr = mkrsrc(a.qk, H * hstride);
    const rsrc_t vr = mkrsrc(a.qv, H * hstride);
    const unsigned kvoff = (row0 + r) * (DH * 4) + c * 16;
    auto issue = [&](HG& g, rsrc_t rs, unsigned so) {
#pragma unroll
        for (int x = 0; x < 3; ++x) g.d[x] = bld128<2>(rs, kvoff + 256 * x, so);
    };
    HG buf[4];
#define PRE(g) SB(); pace(); issue(buf[g], kr, (g) * (4 * DH * 4)); SB();
    asm volatile("s_waitcnt vmcnt(12) lgkmcnt(0)\n\ts_barrier" ::: "memory");
    unsigned tbar = DS ? now32() : 0;
    PRE(0)
    const unsigned qoff = (lane < 48 ? lane : lane - 48) * 4;
    const unsigned* qt = reinterpret_cast<const unsigned*>(smem + LDS_QT + wave * RECW + qoff);
    unsigned qc0 = qt[0], qc1 = qt[HALF / 2], qs0 = qt[DH / 2], qs1 = qt[(DH + HALF) / 2];
    u64 tag;
    {
        const u32x2 e = *reinterpret_cast<const u32x2*>(smem + LDS_QT + wave * RECW + 768);
        tag = (((u64)__builtin_amdgcn_readfirstlane(e[1]) << 32) | __builtin_amdgcn_readfirstlane(e[0])) * 6364136223846793005ull + 1442695040888963407ull;
        asm volatile("" : "+s"(tag));
    }
    const unsigned t32 = tag >> 32;
    const unsigned char* qraw = smem + LDS_QRAW + qoff;
    unsigned char* qrot = smem + LDS_Q + wave * (DH * 4) + 2 * qoff;
    auto rotq = [&]() {
        const unsigned q_lo = *reinterpret_cast<const unsigned*>(qraw), q_hi = *reinterpret_cast<const unsigned*>(qraw + HALF * 2);
        qraw += DH * 2;
        const f32x2 lo = f32x2{bflo(q_lo), bfhi(q_lo)}, hi = f32x2{bflo(q_hi), bfhi(q_hi)};
        const f32x2 y1 = lo * f32x2{bflo(qc0), bfhi(qc0)}, y2 = -hi * f32x2{bflo(qs0), bfhi(qs0)};
        const f32x2 y3 = hi * f32x2{bflo(qc1), bfhi(qc1)}, y4 = lo * f32x2{bflo(qs1), bfhi(qs1)};
        const unsigned x1 = pk_bf16(y1[0], y1[1]), x2 = pk_bf16(y2[0], y2[1]), x3 = pk_bf16(y3[0], y3[1]), x4 = pk_bf16(y4[0], y4[1]);
        const f32x2 z1 = f32x2{bflo(x1), bfhi(x1)} + f32x2{bflo(x2), bfhi(x2)}, z2 = f32x2{bflo(x3), bfhi(x3)} + f32x2{bflo(x4), bfhi(x4)};
        const unsigned l2 = pk_bf16(z1[0], z1[1]), h2 = pk_bf16(z2[0], z2[1]);
        *reinterpret_cast<f32x2*>(qrot) = f32x2{bflo(l2), bfhi(l2)};
        *reinterpret_cast<f32x2*>(qrot + HALF * 4) = f32x2{bflo(h2), bfhi(h2)};
        asm volatile("" ::: "memory");
    };
    f32x4 qa[3], qb[3];
    const unsigned char* qh = smem + LDS_Q + wave * (DH * 4) + 16 * c;
    const unsigned char* qh1 = qh + (c < 8 ? 64 + HALF : 64 - HALF) * 4;
    auto loadq = [&]() {
        asm volatile("" ::: "memory");
#pragma unroll
        for (int x = 0; x < 3; ++x) qa[x] = *reinterpret_cast<const f32x4*>(qh + 256 * x);
        qb[0] = *reinterpret_cast<const f32x4*>(qh + HALF * 4);
        qb[1] = *reinterpret_cast<const f32x4*>(qh1);
        qb[2] = *reinterpret_cast<const f32x4*>(qh + (128 - HALF) * 4);
        asm volatile("" ::: "memory");
    };
    rotq();
    if (b < 8 && wave == 3) __builtin_amdgcn_raw_buffer_store_b128(u32x4{tdma, t32, tdma, (unsigned)tag}, pr, lane == 0 ? psk + b * 16 : 0x80000000u, 0, 16);
    PRE(1)
    f32x2 cc[4][3][2], ss[4][3][2];
    const unsigned char* cb = smem + LDS_CS + wave * RECW + r * (DH * 2) + c * 8;
    asm volatile("s_waitcnt vmcnt(12)" ::: "memory");
#pragma unroll
    for (int k = 0; k < 4; ++k)
#pragma unroll
        for (int x = 0; x < 3; ++x) {
            const u32x2 t = *reinterpret_cast<const u32x2*>(cb + k * 1536 + 128 * x);
            cc[k][x][0] = f32x2{bflo(t[0]), bfhi(t[0])}; cc[k][x][1] = f32x2{bflo(t[1]), bfhi(t[1])};
            asm volatile("" : "+v"(cc[k][x][0]), "+v"(cc[k][x][1]));
        }
    PRE(2)
    loadq();
    const unsigned char* scp = smem + LDS_SC + (c & 4) * 1024 + (16 * wave + 4 * (c & 3) + r) * 2;
    unsigned sc = *reinterpret_cast<const unsigned short*>(scp);
    auto loadsin = [&](int k) {
        const unsigned char* sb1 = cb + (c < 8 ? (64 + HALF) * 2 : (64 - HALF) * 2);
        const unsigned sg = c < 8 ? 0u : 0x80008000u;
        u32x2 t[3];
        t[0] = *reinterpret_cast<const u32x2*>(cb + 6144 + k * 1536 + HALF * 2);
        t[1] = *reinterpret_cast<const u32x2*>(sb1 + 6144 + k * 1536) ^ u32x2{sg, sg};
        t[2] = *reinterpret_cast<const u32x2*>(cb + 6144 + k * 1536 + (128 - HALF) * 2) ^ u32x2{0x80008000u, 0x80008000u};
#pragma unroll
        for (int x = 0; x < 3; ++x) {
            ss[k][x][0] = f32x2{bflo(t[x][0]), bfhi(t[x][0])}; ss[k][x][1] = f32x2{bflo(t[x][1]), bfhi(t[x][1])};
            asm volatile("" : "+v"(ss[k][x][0]), "+v"(ss[k][x][1]));
        }
    };
    asm volatile("s_waitcnt vmcnt(12)" ::: "memory");
    loadsin(0); loadsin(1);
    PRE(3)
    asm volatile("s_waitcnt vmcnt(12)" ::: "memory");
    loadsin(2); loadsin(3);
    unsigned th1 = 0, th4 = 0;
    unsigned char* prec = smem + LDS_P + wave * (H * 128) + r * 32 + (c & 7) * 4;
    auto softmax = [&](float s, int h) {
        float m = fmaxf(s, dpp<0xb1>(s));
        m = xrow_max(fmaxf(m, dpp<0x4e>(m)));
        const float p = __builtin_amdgcn_exp2f(s - m);
        float l = p + dpp<0xb1>(p);
        l = xrow_sum(l + dpp<0x4e>(l));
        const float t = (c & 1) ? l : m;
        const int x = __builtin_amdgcn_update_dpp(__float_as_int(p), __float_as_int(t), 0x114, 0xf, 0xa, false);
        *reinterpret_cast<int*>(prec + h * 128) = x;
    };
    float sprev = 0.f;

#pragma unroll 1
    for (int h = 0; h < H; ++h) {
        const bool more = h + 1 < H;
        const rsrc_t nx = more ? kr : vr;
        const unsigned nso = more ? (h + 1) * hstride : 0;
        float scf = bflo(sc);
        asm volatile("" : "+v"(scf) :: "memory");
        SB();
        scp += more ? 128 : 4096 + 128;
        sc = *reinterpret_cast<const unsigned short*>(scp);
        SB();
        float sreg = 0.f;
#define KSTEP(k) { \
            const float kz = dpp<0x154 + (k)>(scf); \
            f32x2 kz2 = f32x2{kz, kz}; \
            asm("" : "+v"(kz2)); \
            f32x2 a2 = f32x2{0.f, 0.f}; \
            _Pragma("unroll") for (int x = 0; x < 3; ++x) { \
                _Pragma("unroll") for (int e = 0; e < 2; ++e) { \
                    const f32x2 w = __builtin_elementwise_fma(f32x2{qb[x][2 * e], qb[x][2 * e + 1]}, ss[k][x][e], f32x2{qa[x][2 * e], qa[x][2 * e + 1]} * cc[k][x][e]); \
                    const f32x2 kf = f32x2{(float)buf[k].d[x][2 * e], (float)buf[k].d[x][2 * e + 1]} - kz2; \
                    a2 = __builtin_elementwise_fma(kf, w, a2); \
                } \
            } \
            float sa = a2[0] + a2[1]; \
            asm volatile("" : "+v"(sa) :: "memory"); \
            SB(); pace(true); issue(buf[k], nx, nso + (k) * (4 * DH * 4)); SB(); \
            if ((k) == 0) { softmax(sprev, (h - 1) & (H - 1)); SB(); }   \
            if ((k) == 1) { rotq(); SB(); } \
            if ((k) == 2) { \
                if (wave == 1 && h == ((OPT & 4) ? 1 + ((b >> 3) & 3) : 4 + ((b >> 3) & 15))) \
                    asm volatile("s_mov_b32 m0, %2\n\ts_nop 0\n\tglobal_load_lds_dwordx4 %0, %1 sc1" :: "v"(psk + (lane & 7) * 16), "s"(a.part), "s"(lds0 + LDS_SK) : "memory"); \
                if (h == ((OPT & 4) ? 6 : 25)) {        \
                    const u32x2 e = *reinterpret_cast<const u32x2*>(smem + LDS_SK + (lane & 7) * 16); \
                    const int d = (int)(tdma - e[0]); \
                    if (__all(e[1] == t32 && d > -400 && d < 400)) { \
                        int sum = d + __builtin_amdgcn_mov_dpp(d, 0xb1, 0xf, 0xf, true); \
                        sum += __builtin_amdgcn_mov_dpp(sum, 0x4e, 0xf, 0xf, true); \
                        sum += __builtin_amdgcn_mov_dpp(sum, 0x124, 0xf, 0xf, true); \
                        const int adj = (__builtin_amdgcn_readfirstlane(sum) * ((OPT & 4) ? 73 : 112) + 4096) >> 13;        \
                        step -= adj; tmin -= adj; \
                        if ((OPT & 64) && (OPT & 384) && b < ((OPT & 256) ? 256 : 16) && wave == 0) {        \
                            unsigned t0, t1, t2; \
                            const unsigned ar = (((__builtin_amdgcn_readfirstlane(e[0]) >> 13) & 31) + 1) * AREA; \
                            asm volatile("s_load_dword %0, %3, %4\n\ts_load_dword %1, %3, %5\n\ts_load_dword %2, %6, 0\n\ts_waitcnt lgkmcnt(0)" : "=&s"(t0), "=&s"(t1), "=&s"(t2) : "s"(a.part), "s"(ar), "s"(ar + AREA - 128), "s"((OPT & 2048) ? (const unsigned char*)a.out : a.part) : "memory"); \
                            vt = now32() << 4; \
                        } \
                    } \
                } \
                SB(); \
            } \
            if ((k) == 3) { loadq(); SB(); } \
            sa = row16_allsum(sa); \
            sreg = (c & 3) == (k) ? sa : sreg; }
        KSTEP(0) KSTEP(1) KSTEP(2) KSTEP(3)
        sprev = sreg * (scf * SCALE_LOG2);
        if (DS) { if (h == 7) th1 = now32(); }
    }
    softmax(sprev, H - 1);
    if (DS) th4 = now32();

    auto merge = [&](int hd) {
        const unsigned char* rec = smem + LDS_REC + hd * (DH * 4);
        const unsigned char* mlp = smem + LDS_P + hd * 128 + 16;
        f32x4 ow[4]; float mv[4];
#pragma unroll
        for (int w = 0; w < 4; ++w) {
            mv[w] = *reinterpret_cast<const float*>(mlp + w * (H * 128));
            ow[w] = *reinterpret_cast<const f32x4*>(rec + w * RECW + lane * 16);
        }
        const float M = fmaxf(fmaxf(mv[0], mv[1]), fmaxf(mv[2], mv[3]));
        f32x4 t = f32x4{0.f, 0.f, 0.f, 0.f};
#pragma unroll
        for (int w = 0; w < 4; ++w) t += __builtin_amdgcn_exp2f(mv[w] - M) * ow[w];
        return u32x2{pk_bf16(t[0], t[1]), pk_bf16(t[2], t[3])};
    };
    constexpr int NCN = 3 * (H - 1), NC = NCN + 6;
    constexpr int FL = (OPT >> 9) & 3;
    const int cn = sb;
    const bool late = cn >= NCN;
    unsigned xo = 0, oo = 0, fo = 0, area = 4096;
    unsigned old = 0, flo = 0x80000000u;
    u32x4 flv = {};
    float gl = 0.f;
    unsigned eo[5] = {};
    unsigned el[3] = {};
    auto gather0 = [&]() {
        int lane = tid & 63;
        asm volatile("" : "+v"(lane));
#pragma unroll
        for (int p = 0; p < 3; ++p) {
            const int t = lane + 64 * p, hi_ = t / 24, k = t - hi_ * 24;
            const int hh = 8 * wave + hi_;
            el[p] = hh * (DH * 4) + k * 16;
            eo[p] = hh < H - 2 ? (hh * nb + sb) * (DH * 2) + k * 16 : 0x80000000u;
        }
        eo[3] = wave == 3 && lane < 48 ? ((H - 2) * nb + sb) * (DH * 2) + lane * 8 : 0x80000000u;
        eo[4] = lane < 48 ? plt + sb * LS + lane * 16 : 0x80000000u;
        flo = lane < 8 * (NP + 2) ? pfl + (lane >> 3) * fsz + (((((8 * wave + (lane & 7)) >> FL) * nb + sb) << FL) + (lane & ((1 << FL) - 1))) * 16 : 0x80000000u;
        if (OPT & 64) {
            u32x4 e = *reinterpret_cast<const u32x4*>(smem + LDS_SK);
#pragma unroll 1
            for (int n = 0; n < (1 << 20) && !(e[1] == t32 && e[3] == (unsigned)tag); ++n)
                asm volatile("global_load_dwordx4 %0, %1, %2 sc1\n\ts_waitcnt vmcnt(0)" : "=v"(e) : "v"(psk), "s"(a.part) : "memory");
            area = (((__builtin_amdgcn_readfirstlane(e[0]) >> 13) & 31) + 1) * AREA;
            area += 4096;
        }
        const unsigned h = late ? H - 1 : cn / 3, chunk = late ? cn - NCN : cn % 3;
        const unsigned r = lane >> 4, g = lane & 7, e = (lane >> 3) & 1;
        const unsigned rb = (late ? plt : h * nb * (DH * 2)) + chunk * 128, rs = late ? LS : DH * 2;
        xo = rb + (64 * wave + (lane >> 3)) * rs + g * 16;
        oo = late ? ((r & 1) ? 0x80000000u : (h * DH + chunk * 32 + g * 4 + r + e) * 2) : (h * DH + chunk * 64 + g * 8 + 2 * r + e) * 2;
        fo = pfl + ((((h >> FL) * nb + 64 * wave + lane) << FL) + (h & ((1 << FL) - 1))) * 16;
        asm volatile("" : "+s"(area), "+v"(xo), "+v"(oo), "+v"(fo), "+v"(el[0]), "+v"(el[1]), "+v"(el[2]), "+v"(eo[0]), "+v"(eo[1]), "+v"(eo[2]), "+v"(eo[3]), "+v"(eo[4]), "+v"(flo));
    };
    auto gather1 = [&]() {
        f32x2 ml[4];
#pragma unroll
        for (int w = 0; w < 4; ++w) ml[w] = *reinterpret_cast<const f32x2*>(smem + LDS_P + w * (H * 128) + (8 * wave + (lane & 7)) * 128 + 16);
        const float M = fmaxf(fmaxf(ml[0][0], ml[1][0]), fmaxf(ml[2][0], ml[3][0]));
        float L = 0.f;
#pragma unroll
        for (int w = 0; w < 4; ++w) L += __builtin_amdgcn_exp2f(ml[w][0] - M) * ml[w][1];
        flv = u32x4{__float_as_uint(M), t32, __float_as_uint(L), (unsigned)tag};
        float mv[4];
#pragma unroll
        for (int w = 0; w < 4; ++w) mv[w] = *reinterpret_cast<const float*>(smem + LDS_P + ((wave + w) & 3) * (H * 128) + (H - 1) * 128 + 16);
        gl = __builtin_amdgcn_exp2f(mv[0] - fmaxf(fmaxf(mv[0], mv[1]), fmaxf(mv[2], mv[3])));
        asm volatile("" : "+v"(flv), "+v"(gl));
    };
    float pv = *reinterpret_cast<const float*>(prec);
    f32x2 acc[3][2];
    float y;
    auto prep = [&]() {
        y = bflo(sc);
        y = (c & 4) ? y : y * pv;
        asm volatile("" : "+v"(y) :: "memory");
        SB();
        scp += 128;
        sc = *reinterpret_cast<const unsigned short*>(scp);
        prec += 128;
        pv = *reinterpret_cast<const float*>(prec);
        SB();
    };
    auto opq = [](int v) { asm volatile("" : "+s"(v)); return v; };
#define VCOMP(k) { \
        const float pw = dpp<0x150 + (k)>(y); \
        const float vz = dpp<0x154 + (k)>(y); \
        f32x2 pw2 = f32x2{pw, pw}, vz2 = f32x2{vz, vz}; \
        asm("" : "+v"(pw2), "+v"(vz2)); \
        _Pragma("unroll") for (int x = 0; x < 3; ++x) \
            _Pragma("unroll") for (int e = 0; e < 2; ++e) { \
                const f32x2 vf = f32x2{(float)buf[k].d[x][2 * e], (float)buf[k].d[x][2 * e + 1]} - vz2; \
                acc[x][e] = (k) == 0 ? pw2 * vf : __builtin_elementwise_fma(pw2, vf, acc[x][e]); \
            } \
        asm volatile("" : "+v"(acc[0][0]), "+v"(acc[0][1]), "+v"(acc[1][0]), "+v"(acc[1][1]), "+v"(acc[2][0]), "+v"(acc[2][1]) :: "memory"); \
        SB(); }
#define VSTEP(k, hn) { \
        VCOMP(k) pace(true); issue(buf[k], vr, (hn) * hstride + (k) * (4 * DH * 4)); SB(); \
        if ((k) == 1) { tmin = (hn) == H - 1 ? 0 : tmin; if (OPT & 16) vth = (hn) == H - 2 ? 63 : vth; } \
        if ((k) == 1 && (hn) == 15) { asm volatile("s_waitcnt lgkmcnt(0)\n\ts_barrier" ::: "memory"); gather1(); SB(); } \
        if ((k) == 2 && (hn) == 17) { gather0(); SB(); } }
    auto record = [&](int h) {
        f32x4 o;
#pragma unroll
        for (int e = 0; e < 4; ++e) {
            const float t0 = sw32(acc[0][e >> 1][e & 1], acc[2][e >> 1][e & 1]);
            const float t1 = sw32(acc[1][e >> 1][e & 1], acc[1][e >> 1][e & 1]);
            o[e] = sw16(t0, t1);
        }
        asm volatile("s_mov_b64 exec, %5\n\tds_write_b128 %1, %2\n\ts_mov_b64 exec, 1\n\tds_add_rtn_u32 %0, %3, %4\n\ts_mov_b64 exec, -1"
                     : "+v"(old) : "v"(lds0 + LDS_REC + h * (DH * 4) + wave * RECW + lane * 16), "v"(o),
                       "v"(lds0 + LDS_CNT + h * 4), "v"(1u), "s"(0xffffffffffffull) : "memory");
        SB();
    };
#pragma unroll 1
    for (int h = 0; h < H - 1; ++h) {
        prep();
        VSTEP(0, h + 1)
        asm volatile("s_waitcnt lgkmcnt(0)" : "+v"(old) :: "memory");
        if (__builtin_amdgcn_readfirstlane(old) == 3) *reinterpret_cast<u32x2*>(smem + LDS_REC + (h - 1) * (DH * 4) + lane * 8) = merge(h - 1);
        SB();
        VSTEP(1, h + 1) VSTEP(2, h + 1) VSTEP(3, h + 1)
        if (DBG == 14) tA = now32();
        record(h);
    }
    y = bflo(sc);
    y = (c & 4) ? y : y * (pv * gl);
    VCOMP(0)
    asm volatile("s_waitcnt lgkmcnt(0)\n\ts_barrier" ::: "memory");
    if (DBG == 14) tB = now32();
    u32x4 ev[3];
#pragma unroll
    for (int p = 0; p < 3; ++p) ev[p] = *reinterpret_cast<const u32x4*>(smem + LDS_REC + el[p]);
    SB();
    VCOMP(1)
    u32x2 e3 = {};
    if (wave == 3) e3 = merge(H - 2);
    asm volatile("" : "+v"(e3), "+v"(ev[0]), "+v"(ev[1]), "+v"(ev[2]));
    SB();
    if (DBG == 14) tE = now32();
    VCOMP(2)
    if (DBG == 14) tC = now32();
    if (EX) { const unsigned tx = sched - step - stag + EX * 16; while ((int)((now32() << 4) - tx) < 0) {} }
    asm volatile("s_waitcnt vmcnt(0)" : "+v"(buf[3].d[0]), "+v"(buf[3].d[1]), "+v"(buf[3].d[2]) :: "memory");
    if (DBG == 14) tD = now32();
#pragma unroll
    for (int p = 0; p < 3; ++p) __builtin_amdgcn_raw_buffer_store_b128(ev[p], pr, eo[p], area, 16);
    __builtin_amdgcn_raw_buffer_store_b64(e3, pr, eo[3], area, 16);
    if (DBG == 4 || DBG == 24) tbar = now32();
    SB();
    VCOMP(3)
    record(H - 1);
    asm volatile("s_waitcnt lgkmcnt(0)" : "+v"(old) :: "memory");
    if (__builtin_amdgcn_readfirstlane(old) == 3) {
        f32x4 ow[4];
#pragma unroll
        for (int w = 0; w < 4; ++w) ow[w] = *reinterpret_cast<const f32x4*>(smem + LDS_REC + (H - 1) * (DH * 4) + w * RECW + lane * 16);
        const f32x4 t = (ow[0] + ow[1]) + (ow[2] + ow[3]);
        __builtin_amdgcn_raw_buffer_store_b128(u32x4{pk_bf16(t[0], t[1]), t32, pk_bf16(t[2], t[3]), (unsigned)tag}, pr, eo[4], area, 16);
        asm volatile("s_waitcnt vmcnt(1)" ::: "memory");
    } else asm volatile("s_waitcnt vmcnt(0)" ::: "memory");
    __builtin_amdgcn_raw_buffer_store_b128(flv, pr, flo, area, 16);
    unsigned tend = DS ? now32() : 0;
    unsigned tfl = 0, tpl = 0;

    unsigned tgt = sched - stag + (HOLD - (3 - wave) * HS) * 16;
    if (cn < NC) {
        unsigned rs[8];
#pragma unroll
        for (int i = 0; i < 8; ++i) rs[i] = area + i * 8 * (late ? LS : DH * 2);
        float* s_o = reinterpret_cast<float*>(smem + LDS_QRAW);
        unsigned bp = (lane >> 3) * 4, wo = lds0 + LDS_QRAW + 1024 + wave * 8, po = lds0 + LDS_QRAW + lane * 4;
        asm volatile("s_waitcnt vmcnt(0)" : "+s"(rs[1]), "+s"(rs[2]), "+s"(rs[3]), "+s"(rs[4]), "+s"(rs[5]), "+s"(rs[6]), "+s"(rs[7]), "+v"(bp), "+v"(wo), "+v"(po) :: "memory");
        if (DBG == 24) th4 = now32();
        while ((int)((now32() << 4) - tgt) < 0) {}
        i32x4 ya[NP + 1], x[8];
#pragma unroll
        for (int i = 0; i <= NP; ++i) { if (i) __builtin_amdgcn_s_sleep(SP); ya[i] = bld128<16>(pr, fo, area + i * fsz); SB(); }
        i32x4 y = ya[0];
#define BAD(y) ((unsigned)(y)[1] != t32 || (unsigned)(y)[3] != (unsigned)tag)
        if (__any(BAD(y))) {
            if constexpr (NP >= 1) y = ya[1];
            if (NP < 1 || __any(BAD(y))) {
                if constexpr (NP >= 2) y = ya[2];
#pragma unroll 1
                for (int n = 0; n < (1 << 20) && BAD(y); ++n) {
                    y = bld128<16>(pr, fo, area + (NP + 1) * fsz);
                    asm volatile("" ::: "memory");
                }
            }
        }
        asm volatile("" ::: "memory");
        if (DS) tfl = now32();
        const float m = __int_as_float(y[0]);
        float mw, u;
#define XL(i) x[i] = bld128<(O2 & 16) ? 18 : 16>(pr, xo, rs[i]); SB();
        XL(0) asm volatile("s_nop 0\n\tv_max_f32_dpp %0, %1, %1 row_ror:8 row_mask:0xf bank_mask:0xf\n\ts_nop 0" : "=&v"(mw) : "v"(m)); SB();
        XL(1) asm volatile("v_max_f32_dpp %0, %0, %0 row_ror:4 row_mask:0xf bank_mask:0xf\n\ts_nop 0" : "+v"(mw)); SB();
        XL(2) asm volatile("v_max_f32_dpp %0, %0, %0 row_ror:2 row_mask:0xf bank_mask:0xf\n\ts_nop 0" : "+v"(mw)); SB();
        XL(3) asm volatile("v_max_f32_dpp %0, %0, %0 row_ror:1 row_mask:0xf bank_mask:0xf\n\tv_mov_b32 %1, %0\n\ts_nop 0" : "+v"(mw), "=&v"(u)); SB();
        XL(4) asm volatile("v_permlane16_swap_b32 %0, %1\n\ts_nop 0\n\tv_max_f32 %0, %0, %1\n\tv_mov_b32 %1, %0\n\ts_nop 0" : "+v"(mw), "+v"(u)); SB();
        XL(5) asm volatile("v_permlane32_swap_b32 %0, %1\n\ts_nop 0\n\tv_max_f32 %0, %0, %1" : "+v"(mw), "+v"(u)); SB();
        XL(6)
        const float f = __builtin_amdgcn_exp2f(m - mw);
        SB();
        XL(7)
        f32x2 fs[4];
#pragma unroll
        for (int i = 0; i < 8; ++i) fs[i >> 1][i & 1] = __int_as_float(__builtin_amdgcn_ds_bpermute(bp + 32 * i, __float_as_int(f)));
        SB();
        const float lw = xrow_sum(row16_allsum(f * __int_as_float(y[2])));
        f32x4 a0, a1;
        asm volatile("ds_write_b64 %2, %3\n\ts_waitcnt lgkmcnt(0)\n\ts_barrier\n\tds_read_b128 %0, %4\n\tds_read_b128 %1, %4 offset:16\n\ts_waitcnt lgkmcnt(0)"
                     : "=&v"(a0), "=&v"(a1) : "v"(wo), "v"(f32x2{mw, lw}), "v"(lds0 + LDS_QRAW + 1024) : "memory");
        const float M = fmaxf(fmaxf(a0[0], a0[2]), fmaxf(a1[0], a1[2]));
        float gw = __builtin_amdgcn_exp2f(mw - M) * __builtin_amdgcn_rcpf((__builtin_amdgcn_exp2f(a0[0] - M) * a0[1] + __builtin_amdgcn_exp2f(a0[2] - M) * a0[3]) + (__builtin_amdgcn_exp2f(a1[0] - M) * a1[1] + __builtin_amdgcn_exp2f(a1[2] - M) * a1[3]));
        asm volatile("" : "+v"(gw));
        if (DBG == 4) th4 = now32();
        SB();
        f32x2 o[4];
#pragma unroll 1
        for (int n = 0; n < (1 << 20); ++n) {
#pragma unroll
            for (int i = 0; i < 8; ++i) {
#pragma unroll
                for (int e = 0; e < 4; ++e) {
                    const f32x2 t = f32x2{bflo(x[i][e]), bfhi(x[i][e])};
                    if (i == 0) asm volatile("v_pk_mul_f32 %0, %1, %2 op_sel_hi:[0,1]" : "=v"(o[e]) : "v"(fs[0]), "v"(t));
                    else if (i & 1) asm volatile("v_pk_fma_f32 %0, %1, %2, %0 op_sel:[1,0,0]" : "+v"(o[e]) : "v"(fs[i >> 1]), "v"(t));
                    else asm volatile("v_pk_fma_f32 %0, %1, %2, %0 op_sel_hi:[0,1,1]" : "+v"(o[e]) : "v"(fs[i >> 1]), "v"(t));
                }
                SB();
            }
            if (!late) break;
            unsigned bad = 0;
#pragma unroll
            for (int i = 0; i < 8; ++i) { bad |= (x[i][1] ^ t32) | (x[i][3] ^ (unsigned)tag); asm volatile("" : "+v"(bad)); }
            if (__all(bad == 0)) break;
            asm volatile("" ::: "memory");
            XL(0) XL(1) XL(2) XL(3) XL(4) XL(5) XL(6) XL(7)
        }
        if (DS) { asm volatile("" : "+v"(o[0]), "+v"(o[1]), "+v"(o[2]), "+v"(o[3])); tpl = now32(); }
        asm volatile("" :: "v"(ya[NP]), "v"(ya[NP > 1]));
        const float w0 = sw16(sw32(o[0][0], o[2][0]), sw32(o[1][0], o[3][0])), w1 = sw16(sw32(o[0][1], o[2][1]), sw32(o[1][1], o[3][1]));
        s_o[wave * 64 + lane] = ((c < 8 ? w0 : w1) + dpp<0x128>(c < 8 ? w1 : w0)) * gw;
        asm volatile("s_waitcnt lgkmcnt(0)\n\ts_barrier" ::: "memory");
        if (wave == 0) {
            f32x2 p0, p1;
            asm volatile("ds_read2st64_b32 %0, %2 offset1:1\n\tds_read2st64_b32 %1, %2 offset0:2 offset1:3\n\ts_waitcnt lgkmcnt(0)" : "=&v"(p0), "=&v"(p1) : "v"(po) : "memory");
            __builtin_amdgcn_raw_buffer_store_b16((unsigned short)pk_bf16((p0[0] + p0[1]) + (p1[0] + p1[1]), 0.f), mkrsrc(a.out, H * DH * 2), oo, 0, 0);
        }
        if (b == 0 && tid == 0) *reinterpret_cast<u64*>(a.part + pep) = tag;
        asm volatile(".Lkv_b%c0:" :: "i"(ID));
    }
    if (DBG) {
        const unsigned tc1 = now32();
        if (!DS) tend = tbar = th4 = tfl = tpl = tc1;
        const unsigned tl16 = sched - stag - step;
        u64* stp = reinterpret_cast<u64*>(a.part + STAMP_OFF) + (size_t)((DBG == 4 || DBG == 24 ? cn : b) * 4 + wave) * 8;
        if (DBG == 14) { if (lane == 0) { stp[0] = tstart; stp[1] = tA; stp[2] = tend - (((tend << 4) - tl16) >> 4); stp[3] = tB; stp[4] = tE; stp[5] = tC; stp[6] = tD; stp[7] = tc1; } } else
        if (lane == 0) { stp[0] = tstart; stp[1] = tbar; stp[2] = DBG >= 4 ? tend - (((tend << 4) - tl16) >> 4) : th1; stp[3] = DBG == 4 && cn >= NC ? tend : th4; stp[4] = tend; stp[5] = cn < NC ? tfl : tend; stp[6] = cn < NC ? tpl : tend; stp[7] = tc1; }
        if (DBG == 2) *reinterpret_cast<u32x4*>(a.part + STAMP_OFF + 65536 + (b * 4 + wave) * 1024 + lane * 16) = *reinterpret_cast<const u32x4*>(smem + LDS_TR + 16 + wave * 1024 + lane * 16);
    }
}

template <int TP, int ADV, int TMIN, int HOLD, int WIN, int OPT, int HS, int O2, int EX, int ID, int DBG>
static void launch40(void* const* d_in, const int* in_sizes, void* d_out, void* d_ws, hipStream_t stream) {
    typedef const unsigned short* P;
    Args a;
    a.qk = (const int*)d_in[1]; a.qv = (const int*)d_in[4];
    a.k_cos = (P)d_in[9]; a.k_sin = (P)d_in[10];
    a.q = (P)d_in[0]; a.q_cos = (P)d_in[7]; a.q_sin = (P)d_in[8];
    a.sc[0] = (P)d_in[2]; a.sc[1] = (P)d_in[3]; a.sc[2] = (P)d_in[5]; a.sc[3] = (P)d_in[6];
    a.part = (unsigned char*)d_ws;
    a.out = (unsigned short*)d_out;
    kv40_kernel<TP, ADV, TMIN, HOLD, WIN, OPT, HS, O2, EX, ID, DBG><<<Skv / RW, 256, 0, stream>>>(a);
}

extern "C" void kernel_launch(void* const* d_in, const int* in_sizes, int n_in, void* d_out, int out_size, void* d_ws, size_t ws_size, hipStream_t stream) {
    launch40<80*16+4,230,79*16,120,4+16*32+4096*20,38+1024+64+128,5,16,62,0,0>(d_in, in_sizes, d_out, d_ws, stream);
}
```

```cpp
#include <hip/hip_runtime.h>
#include <hip/hip_bf16.h>
#include <cstdint>
#define KALIGN 256
#define STAMP_OFF (200 << 20)
using bf16 = __hip_bfloat16;
using i32x4 = __attribute__((ext_vector_type(4))) int;
using u32x4 = __attribute__((ext_vector_type(4))) unsigned;
using u32x2 = __attribute__((ext_vector_type(2))) unsigned;
using f32x2 = __attribute__((ext_vector_type(2))) float;
using f32x4 = __attribute__((ext_vector_type(4))) float;

constexpr int DH = 192;
constexpr int HALF = 96;
constexpr int RW = 64;
constexpr int MAXH = 32;

__device__ __forceinline__ float bflo(unsigned u) { return __uint_as_float(u << 16); }
__device__ __forceinline__ float bfhi(unsigned u) { return __uint_as_float(u & 0xffff0000u); }
__device__ __forceinline__ float rbf(float x) { return __bfloat162float(__float2bfloat16(x)); }

template <int CTRL> __device__ __forceinline__ float dpp(float x) {
    return __builtin_bit_cast(float, __builtin_amdgcn_mov_dpp(__builtin_bit_cast(int, x), CTRL, 0xf, 0xf, true));
}
__device__ __forceinline__ float row16_allsum(float x) {
    x += dpp<0x128>(x);
    x += dpp<0x124>(x);
    x += dpp<0x122>(x);
    x += dpp<0x121>(x);
    return x;
}
__device__ __forceinline__ float row16_allmax(float x) {
    x = fmaxf(x, dpp<0x128>(x));
    x = fmaxf(x, dpp<0x124>(x));
    x = fmaxf(x, dpp<0x122>(x));
    x = fmaxf(x, dpp<0x121>(x));
    return x;
}
__device__ __forceinline__ float xrow_max(float x) {
    auto s = __builtin_amdgcn_permlane16_swap(__float_as_uint(x), __float_as_uint(x), false, false);
    x = fmaxf(__uint_as_float(s[0]), __uint_as_float(s[1]));
    auto t = __builtin_amdgcn_permlane32_swap(__float_as_uint(x), __float_as_uint(x), false, false);
    return fmaxf(__uint_as_float(t[0]), __uint_as_float(t[1]));
}
__device__ __forceinline__ float xrow_sum(float x) {
    auto s = __builtin_amdgcn_permlane16_swap(__float_as_uint(x), __float_as_uint(x), false, false);
    x = __uint_as_float(s[0]) + __uint_as_float(s[1]);
    auto t = __builtin_amdgcn_permlane32_swap(__float_as_uint(x), __float_as_uint(x), false, false);
    return __uint_as_float(t[0]) + __uint_as_float(t[1]);
}
__device__ __forceinline__ float sw32(float a, float b) {
    auto t = __builtin_amdgcn_permlane32_swap(__float_as_uint(a), __float_as_uint(b), false, false);
    return __uint_as_float(t[0]) + __uint_as_float(t[1]);
}
__device__ __forceinline__ float sw16(float a, float b) {
    auto t = __builtin_amdgcn_permlane16_swap(__float_as_uint(a), __float_as_uint(b), false, false);
    return __uint_as_float(t[0]) + __uint_as_float(t[1]);
}

#define SB() __builtin_amdgcn_sched_barrier(0)
typedef __amdgpu_buffer_rsrc_t rsrc_t;
__device__ __forceinline__ rsrc_t mkrsrc(const void* p, unsigned bytes) {
    return __builtin_amdgcn_make_buffer_rsrc(const_cast<void*>(p), 0, bytes, 0x00020000);
}
template <int AUX> __device__ __forceinline__ i32x4 bld128(rsrc_t r, unsigned voff, unsigned soff) {
    return __builtin_bit_cast(i32x4, __builtin_amdgcn_raw_buffer_load_b128(r, voff, soff, AUX));
}
template <int AUX> __device__ __forceinline__ unsigned bld16(rsrc_t r, unsigned voff, unsigned soff) {
    return __builtin_amdgcn_raw_buffer_load_b16(r, voff, soff, AUX);
}

struct HG { i32x4 d[3]; };
typedef unsigned long long u64;
typedef __attribute__((address_space(3))) unsigned char lds_u8;

__device__ __forceinline__ unsigned pk_bf16(float lo, float hi) {
    unsigned r;
    asm("v_cvt_pk_bf16_f32 %0, %1, %2" : "=v"(r) : "v"(lo), "v"(hi));
    return r;
}
__device__ __forceinline__ unsigned now32() { return (unsigned)__builtin_amdgcn_s_memrealtime(); }

constexpr int LDS_REC = 0;
constexpr int RECW = MAXH * DH * 4;
constexpr int LDS_CS = 0;
constexpr int LDS_QT = 12288;
constexpr int LDS_PF = 14336;
constexpr int LDS_QRAW = MAXH * 4 * DH * 4;
constexpr int LDS_Q = LDS_QRAW + MAXH * DH * 2;
constexpr int LDS_P = LDS_Q + 4 * DH * 4;
constexpr int LDS_SC = LDS_P + 4 * MAXH * 128;
constexpr int LDS_CNT = LDS_SC + 4 * MAXH * RW * 2;
constexpr int LDS_SK = LDS_CNT + 256;
constexpr int LDS_TR = LDS_SK + 1024;
constexpr int LDS_BYTES = LDS_TR + 4096 + 64;

struct Args {
    const int* qk; const int* qv;
    const unsigned short* k_cos; const unsigned short* k_sin;
    const unsigned short* q; const unsigned short* q_cos; const unsigned short* q_sin;
    unsigned char* part;
    unsigned short* out;
    const unsigned short* sc[4];
};
constexpr unsigned Skv = 16384;
constexpr float SCALE_LOG2 = 0.10411754627697264f;

template <int TP, int ADV, int TMIN, int HOLD, int WIN, int OPT, int HS, int O2, int EX, int FM, int ID, int DBG>
__global__ __launch_bounds__(256) __attribute__((aligned(KALIGN))) void kv42_kernel(Args a)
{
    __shared__ __attribute__((aligned(16))) unsigned char smem[LDS_BYTES];
    const unsigned tstart = DBG ? now32() : 0;
    constexpr bool DS = (DBG && DBG < 5) || DBG == 24;
    unsigned tA = 0, tB = 0, tC = 0, tD = 0, tE = 0;
    constexpr int H = MAXH, ES = 7, PFN = 16;
    const int b = blockIdx.x;
    const int tid = threadIdx.x;
    const int lane = tid & 63;
    const int wave = __builtin_amdgcn_readfirstlane(tid >> 6);
    typedef const unsigned short* const __attribute__((address_space(4))) * karg_t;
    const unsigned short* sp = ((karg_t)__builtin_amdgcn_kernarg_segment_ptr())[offsetof(Args, sc) / 8 + wave];
    if (b < PFN) {
        u64 pc; unsigned len, tl;
        asm volatile("s_getpc_b64 %0\n.Lkv_a%c3:\n\ts_mov_b32 %1, .Lkv_b%c3-.Lkv_a%c3-16\n\ts_mov_b32 %2, .Lkv_t%c3-.Lkv_a%c3" : "=s"(pc), "=s"(len), "=s"(tl) : "i"(ID));
        const unsigned o = tid * 16;
        asm volatile("s_mov_b32 m0, %5\n\ts_nop 0\n\tglobal_load_lds_dwordx4 %0, %4\n\tglobal_load_lds_dwordx4 %1, %4\n\tglobal_load_lds_dwordx4 %2, %4\n\tglobal_load_lds_dwordx4 %3, %4"
                     :: "v"(o), "v"(o + 4096), "v"(min(o + 8192, tl)), "v"(min(o + tl, len)), "s"(pc), "s"((unsigned)(size_t)(lds_u8*)smem + LDS_PF) : "memory");
    }
    SB();
    const int r = lane >> 4;
    const int c = lane & 15;
    constexpr unsigned hstride = Skv * (DH * 4);
    constexpr unsigned nb = Skv / RW;
    const unsigned lds0 = (unsigned)(size_t)(lds_u8*)smem;
    const unsigned pos = (b & 7) * (nb >> 3) + (b >> 3);
    const unsigned sb = ((((b >> 3) * 13) & 31) << 3) | (b & 7);
    const unsigned row0 = RW * pos + 16 * wave;

    constexpr int FMT = FM & 3, SL = (FM >> 4) & 15, SL2 = (FM >> 8) & 15, HL = (FM >> 12) & 255, NG = FMT == 1 ? 4 : 6;
    constexpr int LS = FMT == 1 ? 512 + 128 * ((FM >> 2) & 3) : 768 + 128 * ((FM >> 2) & 3);
    constexpr unsigned psk = 0, pep = 128, AREA = (H * nb * (LS + 32) + 4096 + (2u << 20) - 1) & ~((2u << 20) - 1), NAREA = (AREA > (6u << 20)) ? 16 : 32;
    constexpr unsigned pfl = H * nb * LS, fsz = H * nb * 16;
    const rsrc_t pr = mkrsrc(a.part, (NAREA + 1) * AREA);
    u64 t64;
    {
        unsigned vs = ((lane >> 3) * Skv + RW * pos + (lane & 7) * 8) * 2, v1 = vs + 16 * Skv, v2 = vs + 32 * Skv, v3 = vs + 48 * Skv;
        unsigned ds = lds0 + LDS_SC + wave * 4096;
        asm volatile("" : "+v"(vs), "+v"(v1), "+v"(v2), "+v"(v3), "+s"(ds));
        SB();
        asm volatile("s_memrealtime %0\n\ts_mov_b32 m0, %6\n\ts_nop 0\n\tglobal_load_lds_dwordx4 %1, %5 nt\n\t"
                     "s_add_u32 m0, m0, 1024\n\ts_nop 0\n\tglobal_load_lds_dwordx4 %2, %5 nt\n\t"
                     "s_add_u32 m0, m0, 1024\n\ts_nop 0\n\tglobal_load_lds_dwordx4 %3, %5 nt\n\t"
                     "s_add_u32 m0, m0, 1024\n\ts_nop 0\n\tglobal_load_lds_dwordx4 %4, %5 nt\n\ts_waitcnt lgkmcnt(0)"
                     : "=&s"(t64) : "v"(vs), "v"(v1), "v"(v2), "v"(v3), "s"(sp), "s"(ds),
                       "s"(a.qk), "s"(a.qv), "s"(a.k_cos), "s"(a.k_sin), "s"(a.q), "s"(a.q_cos), "s"(a.q_sin), "s"(a.part), "s"(a.out) : "memory", "scc");
    }
    SB();
    const unsigned tdma = (unsigned)t64;
#define DMA3(V, A, B, P) "s_mov_b32 m0, " B "\n\ts_nop 0\n\t" \
                     "global_load_lds_dwordx4 " V ", " A P "\n\t" \
                     "global_load_lds_dwordx4 " V ", " A " offset:1024" P "\n\t" \
                     "global_load_lds_dwordx4 " V ", " A " offset:2048" P "\n\t"
    asm volatile(DMA3("%0", "%1", "%2", "") "s_mov_b32 m0, %4\n\ts_nop 0\n\tglobal_load_lds_dwordx4 %3, off"
                 :: "v"(lane * 16), "s"(a.q + wave * 1536), "s"(lds0 + LDS_QRAW + wave * 3072),
                    "v"(lane < 48 ? (const unsigned char*)((lane < 24 ? a.q_cos : a.q_sin - DH) + 8 * lane) : a.part + pep), "s"(lds0 + LDS_QT + wave * RECW) : "memory");
    SB();
    const int stag = (OPT & 32) ? (int)(((sb & ~7) + wave * 2 + (b & 1)) * (TP / 2)) / 256 - TP / 4 : (int)(((OPT & 2) ? (((b >> 3) * 26 + wave * 2 + (b & 1)) & 63) : ((((b >> 3) * 4 + wave) * 37) & 63)) * (TP / 2)) / 64 - TP / 4;
    unsigned sched = ((tdma + ADV) << 4) + stag - 4 * (TP / 2);
    unsigned vt = tdma << 4;
    int tru = -4;
    unsigned calm = vt;
    unsigned step = TP / 2, tmin = TMIN / 2, vth = WIN & 15;
    auto pace = [&](bool ctl = false) {
        unsigned tgt = sched;
        sched += step;
        if ((int)(vt - tgt) > 0) tgt = vt;
        unsigned t = now32() << 4;
        while ((int)(t - tgt) < 0) { __builtin_amdgcn_s_sleep(1); t = now32() << 4; }
        vt = ((int)(t - 128 - tgt) > 0 ? t - 128 : tgt) + tmin;
        if (ctl) {
            if constexpr ((O2 >> 12) != 0) { if ((int)(t - tgt) > (O2 >> 20) * 16 && (!(O2 & 1) || (int)(t - calm) > 0)) { sched += ((O2 >> 12) & 255) * 16; calm = t + 900 * 16; } }
            if constexpr (WIN > 0 && WIN < 16) asm volatile("s_waitcnt vmcnt(%0)" :: "i"(WIN) : "memory");
            if constexpr (WIN >= 16) {
                const unsigned vm = __builtin_amdgcn_s_getreg(7 | (3 << 11));
                if (vm > vth && ((WIN >> 12) == 0 || (int)(vt - sched) < (WIN >> 12) * 16)) vt += (vm - vth) * ((WIN >> 4) & 255);
            }
        }
        if constexpr (DBG == 2) { *reinterpret_cast<unsigned*>(smem + LDS_TR + 16 + wave * 1024 + (tru & 255) * 4) = ((t - (sched - step)) >> 4) | ((OPT & 8) ? (__builtin_amdgcn_s_getreg(7 | (3 << 11)) << 16) : 0); ++tru; }
    };
    {
        unsigned dst = lds0 + LDS_CS + wave * RECW;
        const unsigned v0 = lane * 16 + row0 * (DH * 2);
#pragma unroll 1
        for (int j = 0; j < 4; ++j) {
            pace();
            asm volatile(DMA3("%0", "%1", "%2", " nt") :: "v"(v0), "s"((j < 2 ? a.k_cos : a.k_sin) + (j & 1) * 8 * DH), "s"(dst) : "memory");
            dst += 3072;
        }
    }
    if (tid < MAXH) *reinterpret_cast<unsigned*>(smem + LDS_CNT + tid * 4) = 0;
    const rsrc_t kr = mkrsrc(a.qk, H * hstride);
    const rsrc_t vr = mkrsrc(a.qv, H * hstride);
    const unsigned kvoff = (row0 + r) * (DH * 4) + c * 16;
    auto issue = [&](HG& g, rsrc_t rs, unsigned so) {
#pragma unroll
        for (int x = 0; x < 3; ++x) g.d[x] = bld128<2>(rs, kvoff + 256 * x, so);
    };
    HG buf[4];
#define PRE(g) SB(); pace(); issue(buf[g], kr, (g) * (4 * DH * 4)); SB();
    asm volatile("s_waitcnt vmcnt(12) lgkmcnt(0)\n\ts_barrier" ::: "memory");
    unsigned tbar = DS ? now32() : 0;
    PRE(0)
    const unsigned qoff = (lane < 48 ? lane : lane - 48) * 4;
    const unsigned* qt = reinterpret_cast<const unsigned*>(smem + LDS_QT + wave * RECW + qoff);
    unsigned qc0 = qt[0], qc1 = qt[HALF / 2], qs0 = qt[DH / 2], qs1 = qt[(DH + HALF) / 2];
    u64 tag;
    {
        const u32x2 e = *reinterpret_cast<const u32x2*>(smem + LDS_QT + wave * RECW + 768);
        tag = (((u64)__builtin_amdgcn_readfirstlane(e[1]) << 32) | __builtin_amdgcn_readfirstlane(e[0])) * 6364136223846793005ull + 1442695040888963407ull;
        asm volatile("" : "+s"(tag));
    }
    const unsigned t32 = tag >> 32;
    const unsigned char* qraw = smem + LDS_QRAW + qoff;
    unsigned char* qrot = smem + LDS_Q + wave * (DH * 4) + 2 * qoff;
    auto rotq = [&]() {
        const unsigned q_lo = *reinterpret_cast<const unsigned*>(qraw), q_hi = *reinterpret_cast<const unsigned*>(qraw + HALF * 2);
        qraw += DH * 2;
        const f32x2 lo = f32x2{bflo(q_lo), bfhi(q_lo)}, hi = f32x2{bflo(q_hi), bfhi(q_hi)};
        const f32x2 y1 = lo * f32x2{bflo(qc0), bfhi(qc0)}, y2 = -hi * f32x2{bflo(qs0), bfhi(qs0)};
        const f32x2 y3 = hi * f32x2{bflo(qc1), bfhi(qc1)}, y4 = lo * f32x2{bflo(qs1), bfhi(qs1)};
        const unsigned x1 = pk_bf16(y1[0], y1[1]), x2 = pk_bf16(y2[0], y2[1]), x3 = pk_bf16(y3[0], y3[1]), x4 = pk_bf16(y4[0], y4[1]);
        const f32x2 z1 = f32x2{bflo(x1), bfhi(x1)} + f32x2{bflo(x2), bfhi(x2)}, z2 = f32x2{bflo(x3), bfhi(x3)} + f32x2{bflo(x4), bfhi(x4)};
        const unsigned l2 = pk_bf16(z1[0], z1[1]), h2 = pk_bf16(z2[0], z2[1]);
        *reinterpret_cast<f32x2*>(qrot) = f32x2{bflo(l2), bfhi(l2)};
        *reinterpret_cast<f32x2*>(qrot + HALF * 4) = f32x2{bflo(h2), bfhi(h2)};
        asm volatile("" ::: "memory");
    };
    f32x4 qa[3], qb[3];
    const unsigned char* qh = smem + LDS_Q + wave * (DH * 4) + 16 * c;
    const unsigned char* qh1 = qh + (c < 8 ? 64 + HALF : 64 - HALF) * 4;
    auto loadq = [&]() {
        asm volatile("" ::: "memory");
#pragma unroll
        for (int x = 0; x < 3; ++x) qa[x] = *reinterpret_cast<const f32x4*>(qh + 256 * x);
        qb[0] = *reinterpret_cast<const f32x4*>(qh + HALF * 4);
        qb[1] = *reinterpret_cast<const f32x4*>(qh1);
        qb[2] = *reinterpret_cast<const f32x4*>(qh + (128 - HALF) * 4);
        asm volatile("" ::: "memory");
    };
    rotq();
    if (b < 8 && wave == 3) __builtin_amdgcn_raw_buffer_store_b128(u32x4{tdma, t32, tdma, (unsigned)tag}, pr, lane == 0 ? psk + b * 16 : 0x80000000u, 0, 16);
    PRE(1)
    f32x2 cc[4][3][2], ss[4][3][2];
    const unsigned char* cb = smem + LDS_CS + wave * RECW + r * (DH * 2) + c * 8;
    asm volatile("s_waitcnt vmcnt(12)" ::: "memory");
#pragma unroll
    for (int k = 0; k < 4; ++k)
#pragma unroll
        for (int x = 0; x < 3; ++x) {
            const u32x2 t = *reinterpret_cast<const u32x2*>(cb + k * 1536 + 128 * x);
            cc[k][x][0] = f32x2{bflo(t[0]), bfhi(t[0])}; cc[k][x][1] = f32x2{bflo(t[1]), bfhi(t[1])};
            asm volatile("" : "+v"(cc[k][x][0]), "+v"(cc[k][x][1]));
        }
    PRE(2)
    loadq();
    const unsigned char* scp = smem + LDS_SC + (c & 4) * 1024 + (16 * wave + 4 * (c & 3) + r) * 2;
    unsigned sc = *reinterpret_cast<const unsigned short*>(scp);
    auto loadsin = [&](int k) {
        const unsigned char* sb1 = cb + (c < 8 ? (64 + HALF) * 2 : (64 - HALF) * 2);
        const unsigned sg = c < 8 ? 0u : 0x80008000u;
        u32x2 t[3];
        t[0] = *reinterpret_cast<const u32x2*>(cb + 6144 + k * 1536 + HALF * 2);
        t[1] = *reinterpret_cast<const u32x2*>(sb1 + 6144 + k * 1536) ^ u32x2{sg, sg};
        t[2] = *reinterpret_cast<const u32x2*>(cb + 6144 + k * 1536 + (128 - HALF) * 2) ^ u32x2{0x80008000u, 0x80008000u};
#pragma unroll
        for (int x = 0; x < 3; ++x) {
            ss[k][x][0] = f32x2{bflo(t[x][0]), bfhi(t[x][0])}; ss[k][x][1] = f32x2{bflo(t[x][1]), bfhi(t[x][1])};
            asm volatile("" : "+v"(ss[k][x][0]), "+v"(ss[k][x][1]));
        }
    };
    asm volatile("s_waitcnt vmcnt(12)" ::: "memory");
    loadsin(0); loadsin(1);
    PRE(3)
    asm volatile("s_waitcnt vmcnt(12)" ::: "memory");
    loadsin(2); loadsin(3);
    unsigned th1 = 0, th4 = 0;
    unsigned char* prec = smem + LDS_P + wave * (H * 128) + r * 32 + (c & 7) * 4;
    auto softmax = [&](float s, int h) {
        float m = fmaxf(s, dpp<0xb1>(s));
        m = xrow_max(fmaxf(m, dpp<0x4e>(m)));
        const float p = __builtin_amdgcn_exp2f(s - m);
        float l = p + dpp<0xb1>(p);
        l = xrow_sum(l + dpp<0x4e>(l));
        const float t = (c & 1) ? l : m;
        const int x = __builtin_amdgcn_update_dpp(__float_as_int(p), __float_as_int(t), 0x114, 0xf, 0xa, false);
        *reinterpret_cast<int*>(prec + h * 128) = x;
    };
    float sprev = 0.f;

#pragma unroll 1
    for (int h = 0; h < H; ++h) {
        const bool more = h + 1 < H;
        const rsrc_t nx = more ? kr : vr;
        const unsigned nso = more ? (h + 1) * hstride : 0;
        float scf = bflo(sc);
        asm volatile("" : "+v"(scf) :: "memory");
        SB();
        scp += more ? 128 : 4096 + 128;
        sc = *reinterpret_cast<const unsigned short*>(scp);
        SB();
        float sreg = 0.f;
#define KSTEP(k) { \
            const float kz = dpp<0x154 + (k)>(scf); \
            f32x2 kz2 = f32x2{kz, kz}; \
            asm("" : "+v"(kz2)); \
            f32x2 a2 = f32x2{0.f, 0.f}; \
            _Pragma("unroll") for (int x = 0; x < 3; ++x) { \
                _Pragma("unroll") for (int e = 0; e < 2; ++e) { \
                    const f32x2 w = __builtin_elementwise_fma(f32x2{qb[x][2 * e], qb[x][2 * e + 1]}, ss[k][x][e], f32x2{qa[x][2 * e], qa[x][2 * e + 1]} * cc[k][x][e]); \
                    const f32x2 kf = f32x2{(float)buf[k].d[x][2 * e], (float)buf[k].d[x][2 * e + 1]} - kz2; \
                    a2 = __builtin_elementwise_fma(kf, w, a2); \
                } \
            } \
            float sa = a2[0] + a2[1]; \
            asm volatile("" : "+v"(sa) :: "memory"); \
            SB(); pace(true); issue(buf[k], nx, nso + (k) * (4 * DH * 4)); SB(); \
            if ((k) == 0) { softmax(sprev, (h - 1) & (H - 1)); SB(); }   \
            if ((k) == 1) { rotq(); SB(); } \
            if ((k) == 2) { \
                if (wave == 1 && h == ((OPT & 4) ? 1 + ((b >> 3) & 3) : 4 + ((b >> 3) & 15))) \
                    asm volatile("s_mov_b32 m0, %2\n\ts_nop 0\n\tglobal_load_lds_dwordx4 %0, %1 sc1" :: "v"(psk + (lane & 7) * 16), "s"(a.part), "s"(lds0 + LDS_SK) : "memory"); \
                if (h == ((OPT & 4) ? 6 : 25)) {        \
                    const u32x2 e = *reinterpret_cast<const u32x2*>(smem + LDS_SK + (lane & 7) * 16); \
                    const int d = (int)(tdma - e[0]); \
                    if (__all(e[1] == t32 && d > -400 && d < 400)) { \
                        int sum = d + __builtin_amdgcn_mov_dpp(d, 0xb1, 0xf, 0xf, true); \
                        sum += __builtin_amdgcn_mov_dpp(sum, 0x4e, 0xf, 0xf, true); \
                        sum += __builtin_amdgcn_mov_dpp(sum, 0x124, 0xf, 0xf, true); \
                        const int adj = (__builtin_amdgcn_readfirstlane(sum) * ((OPT & 4) ? 73 : 112) + 4096) >> 13;        \
                        step -= adj; tmin -= adj; \
                        if ((OPT & 64) && (OPT & 384) && b < ((OPT & 256) ? 256 : 16) && wave == 0) {        \
                            unsigned t0, t1, t2, t3; \
                            const unsigned ar = (((__builtin_amdgcn_readfirstlane(e[0]) >> 13) & (NAREA - 1)) + 1) * AREA; \
                            asm volatile("s_load_dword %0, %4, %5\n\ts_load_dword %1, %4, %6\n\ts_load_dword %2, %4, %7\n\ts_load_dword %3, %4, %8\n\ts_waitcnt lgkmcnt(0)" : "=&s"(t0), "=&s"(t1), "=&s"(t2), "=&s"(t3) : "s"(a.part), "s"(ar), "s"(ar + AREA - 128), "s"(ar + (2u << 20) + 128), "s"(ar + AREA - (2u << 20) - 128) : "memory"); \
                            vt = now32() << 4; \
                        } \
                    } \
                } \
                SB(); \
            } \
            if ((k) == 3) { loadq(); SB(); } \
            sa = row16_allsum(sa); \
            sreg = (c & 3) == (k) ? sa : sreg; }
        KSTEP(0) KSTEP(1) KSTEP(2) KSTEP(3)
        sprev = sreg * (scf * SCALE_LOG2);
        if (DS) { if (h == 7) th1 = now32(); }
    }
    softmax(sprev, H - 1);
    if (DS) th4 = now32();

    const unsigned tg = (unsigned)(tag >> (16 * (lane & 3))) << 16;
    auto pack = [&](f32x4 t) { return FMT == 1 ? u32x4{pk_bf16(t[0], t[1]), pk_bf16(t[2], 0.f) | tg, 0, 0} : u32x4{pk_bf16(t[0], t[1]), t32, pk_bf16(t[2], t[3]), (unsigned)tag}; };
    auto ldrec = [&](int hd, int w) {
        const unsigned char* rec = smem + LDS_REC + hd * (DH * 4) + w * RECW;
        if (FMT == 1) { const float* p = reinterpret_cast<const float*>(rec) + lane; return f32x4{p[0], p[64], p[128], 0.f}; }
        return *reinterpret_cast<const f32x4*>(rec + lane * 16);
    };
    auto merge = [&](int hd) {
        const unsigned char* mlp = smem + LDS_P + hd * 128 + 16;
        f32x4 ow[4]; float mv[4];
#pragma unroll
        for (int w = 0; w < 4; ++w) {
            mv[w] = *reinterpret_cast<const float*>(mlp + w * (H * 128));
            ow[w] = ldrec(hd, w);
        }
        const float M = fmaxf(fmaxf(mv[0], mv[1]), fmaxf(mv[2], mv[3]));
        f32x4 t = f32x4{0.f, 0.f, 0.f, 0.f};
#pragma unroll
        for (int w = 0; w < 4; ++w) t += __builtin_amdgcn_exp2f(mv[w] - M) * ow[w];
        return pack(t);
    };
    auto strec = [&](u32x4 v, unsigned off, unsigned area) {
        if (FMT == 1) __builtin_amdgcn_raw_buffer_store_b64(u32x2{v[0], v[1]}, pr, off, area, 16);
        else __builtin_amdgcn_raw_buffer_store_b128(v, pr, off, area, 16);
    };
    constexpr int NC = NG * H;
    constexpr int FL = (OPT >> 9) & 3;
    const int cn = sb;
    unsigned xo = 0, oo = 0, fo = 0, area = 4096;
    unsigned old = 0, flo = 0x80000000u;
    u32x4 flv = {};
    float gl = 0.f;
    unsigned eo[NG + 2] = {};
    unsigned el = 0;
    auto gather0 = [&]() {
        int lane = tid & 63;
        asm volatile("" : "+v"(lane));
#pragma unroll
        for (int p = 0; p < NG; ++p) {
            const int t = lane + 64 * p, hi_ = t / (8 * NG), k = t - hi_ * (8 * NG);
            const int hh = 8 * wave + hi_;
            if (p == 0) el = hh * (DH * 4) + k * 16;
            eo[p] = hh < H - 2 ? (hh * nb + sb) * LS + k * 16 : 0x80000000u;
            asm volatile("" : "+v"(eo[p]));
        }
        eo[NG + 1] = FMT == 1 ? ((H - 1) * nb + sb) * LS + lane * 8 : lane < 48 ? ((H - 1) * nb + sb) * LS + lane * 16 : 0x80000000u;
        eo[NG] = wave == 3 ? eo[NG + 1] - nb * LS : 0x80000000u;
        flo = lane < 8 ? pfl + (((((8 * wave + lane) >> FL) * nb + sb) << FL) + (lane & ((1 << FL) - 1))) * 16 : 0x80000000u;
        if (OPT & 64) {
            u32x4 e = *reinterpret_cast<const u32x4*>(smem + LDS_SK);
#pragma unroll 1
            for (int n = 0; n < (1 << 20) && !(e[1] == t32 && e[3] == (unsigned)tag); ++n)
                asm volatile("global_load_dwordx4 %0, %1, %2 sc1\n\ts_waitcnt vmcnt(0)" : "=v"(e) : "v"(psk), "s"(a.part) : "memory");
            area = (((__builtin_amdgcn_readfirstlane(e[0]) >> 13) & (NAREA - 1)) + 1) * AREA;
            area += 4096;
        }
        const unsigned h = 4 * (cn & 7) + ((cn >> 3) & 3), chunk = cn >> 5;
        const unsigned r = lane >> 4, g = lane & 7, e = (lane >> 3) & 1;
        xo = (h * nb + 8 * wave + (lane >> 3)) * LS + chunk * 128 + g * 16;
        if (FMT == 1) oo = r == 3 ? 0x80000000u : (h * DH + chunk * 16 + 2 * g + (r == 2 ? 128 + e : r + 64 * e)) * 2;
        else oo = e ? 0x80000000u : (h * DH + chunk * 32 + g * 4 + r) * 2;
        fo = area + pfl + ((((h >> FL) * nb + 32 * (lane >> 3) + 8 * wave + g) << FL) + (h & ((1 << FL) - 1))) * 16;
        asm volatile("" : "+s"(area), "+v"(xo), "+v"(oo), "+v"(fo), "+v"(el), "+v"(eo[NG]), "+v"(eo[NG + 1]), "+v"(flo));
    };
    auto gather1 = [&]() {
        f32x2 ml[4];
#pragma unroll
        for (int w = 0; w < 4; ++w) ml[w] = *reinterpret_cast<const f32x2*>(smem + LDS_P + w * (H * 128) + (8 * wave + (lane & 7)) * 128 + 16);
        const float M = fmaxf(fmaxf(ml[0][0], ml[1][0]), fmaxf(ml[2][0], ml[3][0]));
        float L = 0.f;
#pragma unroll
        for (int w = 0; w < 4; ++w) L += __builtin_amdgcn_exp2f(ml[w][0] - M) * ml[w][1];
        flv = u32x4{__float_as_uint(M), t32, __float_as_uint(L), (unsigned)tag};
        float mv[4];
#pragma unroll
        for (int w = 0; w < 4; ++w) mv[w] = *reinterpret_cast<const float*>(smem + LDS_P + ((wave + w) & 3) * (H * 128) + (H - 1) * 128 + 16);
        gl = __builtin_amdgcn_exp2f(mv[0] - fmaxf(fmaxf(mv[0], mv[1]), fmaxf(mv[2], mv[3])));
        asm volatile("" : "+v"(flv), "+v"(gl));
    };
    f32x2 fs[4] = {};
    float mw = 0.f;
    constexpr int LDS_ML = LDS_QRAW + 2048;
#define BAD(y) ((unsigned)(y)[1] != t32 || (unsigned)(y)[3] != (unsigned)tag)
    auto weights = [&]() {
        u32x4 y = *reinterpret_cast<const u32x4*>(smem + LDS_ML + wave * 1024 + lane * 16);
        if (__any(BAD(y))) {
#pragma unroll 1
            for (int n = 0; n < (1 << 20) && BAD(y); ++n)
                asm volatile("global_load_dwordx4 %0, %1, %2 sc1\n\ts_waitcnt vmcnt(0)" : "=v"(y) : "v"(fo), "s"(a.part) : "memory");
        }
        const float m = __uint_as_float(y[0]);
        mw = xrow_max(row16_allmax(m));
        const float f = __builtin_amdgcn_exp2f(m - mw);
#pragma unroll
        for (int i = 0; i < 8; ++i) fs[i >> 1][i & 1] = __int_as_float(__builtin_amdgcn_ds_bpermute((lane >> 3) * 4 + 32 * i, __float_as_int(f)));
        const float lw = xrow_sum(row16_allsum(f * __uint_as_float(y[2])));
        *reinterpret_cast<f32x2*>(smem + LDS_QRAW + 1024 + wave * 8) = f32x2{mw, lw};
        asm volatile("" : "+v"(fs[0]), "+v"(fs[1]), "+v"(fs[2]), "+v"(fs[3]), "+v"(mw) :: "memory");
    };
    constexpr int G1 = (FM >> 20) & 15 ? (FM >> 20) & 15 : 8, SPR = (FM >> 24) & 1 ? 3 : 7, GAP = (FM >> 25) & 1 ? 2 : 4;
    const int hfl = G1 + 3 + ((b >> 3) & SPR), hml = cn < NC ? G1 + 3 + SPR + GAP + ((b >> 3) & 3) : -9;
    float pv = *reinterpret_cast<const float*>(prec);
    f32x2 acc[3][2];
    float y;
    auto prep = [&]() {
        y = bflo(sc);
        y = (c & 4) ? y : y * pv;
        asm volatile("" : "+v"(y) :: "memory");
        SB();
        scp += 128;
        sc = *reinterpret_cast<const unsigned short*>(scp);
        prec += 128;
        pv = *reinterpret_cast<const float*>(prec);
        SB();
    };
    auto opq = [](int v) { asm volatile("" : "+s"(v)); return v; };
#define VCOMP(k) { \
        const float pw = dpp<0x150 + (k)>(y); \
        const float vz = dpp<0x154 + (k)>(y); \
        f32x2 pw2 = f32x2{pw, pw}, vz2 = f32x2{vz, vz}; \
        asm("" : "+v"(pw2), "+v"(vz2)); \
        _Pragma("unroll") for (int x = 0; x < 3; ++x) \
            _Pragma("unroll") for (int e = 0; e < 2; ++e) { \
                const f32x2 vf = f32x2{(float)buf[k].d[x][2 * e], (float)buf[k].d[x][2 * e + 1]} - vz2; \
                acc[x][e] = (k) == 0 ? pw2 * vf : __builtin_elementwise_fma(pw2, vf, acc[x][e]); \
            } \
        asm volatile("" : "+v"(acc[0][0]), "+v"(acc[0][1]), "+v"(acc[1][0]), "+v"(acc[1][1]), "+v"(acc[2][0]), "+v"(acc[2][1]) :: "memory"); \
        SB(); }
#define VSTEP(k, hn) { \
        VCOMP(k) pace(true); issue(buf[k], vr, (hn) * hstride + (k) * (4 * DH * 4)); SB(); \
        if ((k) == 1) { tmin = (hn) == H - 1 ? 0 : tmin; if (OPT & 16) vth = (hn) == H - 2 ? 63 : vth; } \
        if ((k) == 1 && (hn) == G1) { asm volatile("s_waitcnt lgkmcnt(0)\n\ts_barrier" ::: "memory"); gather1(); SB(); } \
        if ((k) == 2 && (hn) == G1 + 2) { gather0(); SB(); } \
        if ((k) == 1 && (hn) == hfl) { asm volatile("buffer_store_dwordx4 %0, %1, %2, %3 offen sc1" :: "v"(flv), "v"(flo), "s"(pr), "s"(area) : "memory"); SB(); } \
        if ((k) == 2 && (hn) == hml) { asm volatile("s_mov_b32 m0, %2\n\ts_nop 0\n\tglobal_load_lds_dwordx4 %0, %1 sc1" :: "v"(fo), "s"(a.part), "s"(lds0 + LDS_ML + wave * 1024) : "memory"); SB(); } \
        if ((k) == 2 && (hn) == hml + 2) { weights(); SB(); } }
    auto record = [&](int h) {
        f32x4 o;
#pragma unroll
        for (int e = 0; e < 4; ++e) {
            const float t0 = sw32(acc[0][e >> 1][e & 1], acc[2][e >> 1][e & 1]);
            const float t1 = sw32(acc[1][e >> 1][e & 1], acc[1][e >> 1][e & 1]);
            o[e] = sw16(t0, t1);
        }
        asm volatile("s_mov_b64 exec, %5\n\tds_write_b128 %1, %2\n\ts_mov_b64 exec, 1\n\tds_add_rtn_u32 %0, %3, %4\n\ts_mov_b64 exec, -1"
                     : "+v"(old) : "v"(lds0 + LDS_REC + h * (DH * 4) + wave * RECW + lane * 16), "v"(o),
                       "v"(lds0 + LDS_CNT + h * 4), "v"(1u), "s"(0xffffffffffffull) : "memory");
        SB();
    };
#pragma unroll 1
    for (int h = 0; h < H - 1; ++h) {
        prep();
        VSTEP(0, h + 1)
        asm volatile("s_waitcnt lgkmcnt(0)" : "+v"(old) :: "memory");
        if (__builtin_amdgcn_readfirstlane(old) == 3) {
            const u32x4 v = merge(h - 1);
            if (FMT == 1) *reinterpret_cast<u32x2*>(smem + LDS_REC + (h - 1) * (DH * 4) + lane * 8) = u32x2{v[0], v[1]};
            else if (lane < 48) *reinterpret_cast<u32x4*>(smem + LDS_REC + (h - 1) * (DH * 4) + lane * 16) = v;
        }
        SB();
        VSTEP(1, h + 1) VSTEP(2, h + 1) VSTEP(3, h + 1)
        if (DBG == 14) tA = now32();
        record(h);
    }
    asm volatile("s_branch .Lkv_t%c0\n\t.p2align 12,,3327\n.Lkv_t%c0:" :: "i"(ID));
    y = bflo(sc);
    y = (c & 4) ? y : y * (pv * gl);
    VCOMP(0)
    asm volatile("s_waitcnt lgkmcnt(0)\n\ts_barrier" ::: "memory");
    if (DBG == 14) tB = now32();
    u32x4 ev[NG];
#pragma unroll
    for (int p = 0; p < NG; ++p) ev[p] = *reinterpret_cast<const u32x4*>(smem + LDS_REC + el + p * (FMT == 1 ? 2 * DH * 4 : 1024));
    SB();
    VCOMP(1)
    u32x4 e3 = {};
    if (wave == 3) e3 = merge(H - 2);
    float gw;
    {
        const f32x4 a0 = *reinterpret_cast<const f32x4*>(smem + LDS_QRAW + 1024), a1 = *reinterpret_cast<const f32x4*>(smem + LDS_QRAW + 1040);
        const float M = fmaxf(fmaxf(a0[0], a0[2]), fmaxf(a1[0], a1[2]));
        gw = __builtin_amdgcn_exp2f(mw - M) * __builtin_amdgcn_rcpf((__builtin_amdgcn_exp2f(a0[0] - M) * a0[1] + __builtin_amdgcn_exp2f(a0[2] - M) * a0[3]) + (__builtin_amdgcn_exp2f(a1[0] - M) * a1[1] + __builtin_amdgcn_exp2f(a1[2] - M) * a1[3]));
#pragma unroll
        for (int i = 0; i < 4; ++i) fs[i] *= gw;
        asm volatile("" : "+v"(fs[0]), "+v"(fs[1]), "+v"(fs[2]), "+v"(fs[3]));
    }
    asm volatile("" : "+v"(e3[0]), "+v"(e3[1]));
    if (FMT == 2) asm volatile("" : "+v"(e3));
#pragma unroll
    for (int p = 0; p < NG; ++p) asm volatile("" : "+v"(ev[p]));
    SB();
    if (DBG == 14) tE = now32();
    VCOMP(2)
    if (DBG == 14) tC = now32();
    if constexpr ((O2 & 8) != 0) {
        const unsigned tx = sched - step - stag + EX * 16 + (sb >> 3) * ((O2 >> 5) & 15) * 4;
        auto stores = [&](rsrc_t pr) {
#pragma unroll
            for (int p = 0; p < NG; ++p) __builtin_amdgcn_raw_buffer_store_b128(ev[p], pr, eo[p], area, 16);
            __builtin_amdgcn_raw_buffer_store_b64(u32x2{e3[0], e3[1]}, pr, eo[NG], area, 16);
        };
        int early;
        SB();
        {
            asm volatile("1:\n\ts_memrealtime vcc\n\ts_getreg_b32 %0, hwreg(HW_REG_IB_STS, 0, 4)\n\ts_cmp_eq_u32 %0, 0\n\ts_waitcnt lgkmcnt(0)\n\ts_cselect_b32 %0, 1, 0\n\ts_cbranch_scc1 2f\n\t"
                         "s_lshl_b32 vcc_lo, vcc_lo, 4\n\ts_sub_i32 vcc_lo, vcc_lo, %1\n\ts_cmp_lt_i32 vcc_lo, 0\n\ts_cbranch_scc1 1b\n2:" : "=&s"(early) : "s"(tx) : "scc", "vcc", "memory");
        }
        SB();
        stores(mkrsrc(a.part, early ? 0 : (NAREA + 1) * AREA));
        if (DBG == 4) tbar = now32();
        SB();
        VCOMP(3)
        record(H - 1);
        asm volatile("s_waitcnt lgkmcnt(0)" : "+v"(old) :: "memory");
        const bool last = __builtin_amdgcn_readfirstlane(old) == 3;
        u32x4 e4 = {};
        if (last) {
            f32x4 ow[4];
#pragma unroll
            for (int w = 0; w < 4; ++w) ow[w] = ldrec(H - 1, w);
            e4 = pack((ow[0] + ow[1]) + (ow[2] + ow[3]));
        }
        if (early) while ((int)((now32() << 4) - tx) < 0) {}
        if (last) strec(e4, eo[NG + 1], area);
        if (early) { stores(pr); if (DBG == 4) tbar = now32(); }
    } else {
    if (EX) { const unsigned tx = sched - step - stag + EX * 16 + (sb >> 3) * ((O2 >> 5) & 15) * 4; while ((int)((now32() << 4) - tx) < 0) {} }
    if (!(O2 & 4)) asm volatile("s_waitcnt vmcnt(0)" : "+v"(buf[3].d[0]), "+v"(buf[3].d[1]), "+v"(buf[3].d[2]) :: "memory");
    if (DBG == 14) tD = now32();
#pragma unroll
    for (int p = 0; p < NG; ++p) __builtin_amdgcn_raw_buffer_store_b128(ev[p], pr, eo[p], area, 16);
    strec(e3, eo[NG], area);
    if (DBG == 4 || DBG == 24) tbar = now32();
    SB();
    VCOMP(3)
    record(H - 1);
    asm volatile("s_waitcnt lgkmcnt(0)" : "+v"(old) :: "memory");
    if (__builtin_amdgcn_readfirstlane(old) == 3) {
        f32x4 ow[4];
#pragma unroll
        for (int w = 0; w < 4; ++w) ow[w] = ldrec(H - 1, w);
        strec(pack((ow[0] + ow[1]) + (ow[2] + ow[3])), eo[NG + 1], area);
    }
    }
    unsigned tend = DS ? now32() : 0;
    unsigned tfl = 0, tpl = 0;

    unsigned tgt = sched - stag + (HOLD - (3 - wave) * HS) * 16;
    if (cn < NC) {
        unsigned rs[8];
#pragma unroll
        for (int i = 0; i < 8; ++i) rs[i] = area + i * 32 * LS;
        float* s_o = reinterpret_cast<float*>(smem + LDS_QRAW);
        unsigned po = lds0 + LDS_QRAW + lane * 4;
        unsigned ta = FMT == 1 ? ((lane & 1) ? t32 : (unsigned)tag) << 16 : t32, tb = FMT == 1 ? ((lane & 1) ? t32 : (unsigned)tag) : (unsigned)tag;
        const bool late = (cn & 31) == 31;
        if (late) tgt += HL * 16 - 64 * 16;
        asm volatile("" : "+s"(rs[1]), "+s"(rs[2]), "+s"(rs[3]), "+s"(rs[4]), "+s"(rs[5]), "+s"(rs[6]), "+s"(rs[7]), "+v"(po), "+v"(ta), "+v"(tb), "+s"(tgt) :: "memory");
        if (DBG == 24) th4 = now32();
        i32x4 x[8];
        while ((int)((now32() << 4) - tgt) < 0) {}
        if (DS) tfl = now32();
#define XL(i) x[i] = bld128<(O2 & 16) ? 18 : 16>(pr, xo, rs[i]); SB();
#define XS(i) XL(i) if (SL) __builtin_amdgcn_s_sleep(SL); if (SL2 && late) __builtin_amdgcn_s_sleep(SL2); SB();
        XS(0) XS(1) XS(2) XS(3) XS(4) XS(5) XS(6) XL(7)
        if (DBG == 4) th4 = now32();
        SB();
        constexpr int NO = FMT == 1 ? 3 : 2;
        f32x2 o[NO];
#pragma unroll 1
        for (int n = 0; n < (1 << 20); ++n) {
            unsigned bad = 0;
#pragma unroll
            for (int i = 0; i < 8; ++i) {
#pragma unroll
                for (int e = 0; e < NO; ++e) {
                    const f32x2 t = e < 2 ? f32x2{bflo(x[i][2 * e]), bfhi(x[i][2 * e])} : f32x2{bflo(x[i][1]), bflo(x[i][3])};
                    if (i == 0) asm volatile("v_pk_mul_f32 %0, %1, %2 op_sel_hi:[0,1]" : "=v"(o[e]) : "v"(fs[0]), "v"(t));
                    else if (i & 1) asm volatile("v_pk_fma_f32 %0, %1, %2, %0 op_sel:[1,0,0]" : "+v"(o[e]) : "v"(fs[i >> 1]), "v"(t));
                    else asm volatile("v_pk_fma_f32 %0, %1, %2, %0 op_sel_hi:[0,1,1]" : "+v"(o[e]) : "v"(fs[i >> 1]), "v"(t));
                }
                bad = __builtin_amdgcn_bitop3_b32(bad, x[i][1], ta, 0xf6);
                bad = __builtin_amdgcn_bitop3_b32(bad, x[i][3], tb, 0xf6);
                asm volatile("" : "+v"(bad));
                SB();
            }
            if (__all(FMT == 1 ? bad < 0x10000u : bad == 0)) break;
            if (DBG == 6) { tA = n + 1; if (n == 0) { _Pragma("unroll") for (int i = 0; i < 8; ++i) { asm volatile("" : "+v"(x[i])); tB |= __any((((x[i][1] ^ ta) | (x[i][3] ^ tb)) >> 16) != 0) << i; } } }
            asm volatile("" ::: "memory");
            XL(0) XL(1) XL(2) XL(3) XL(4) XL(5) XL(6) XL(7)
        }
        if (DS) { asm volatile("" : "+v"(o[0]), "+v"(o[1])); asm volatile("" : "+v"(o[NO - 1])); tpl = now32(); }
        if (FMT == 1) {
            const float w0 = sw16(sw32(o[0][0], o[NO - 1][0]), sw32(o[1][0], o[1][0])), w1 = sw16(sw32(o[0][1], o[NO - 1][1]), sw32(o[1][1], o[1][1]));
            s_o[wave * 64 + lane] = (c < 8 ? w0 : w1) + dpp<0x128>(c < 8 ? w1 : w0);
        } else {
            const float w0 = sw16(sw32(o[0][0], o[1][0]), sw32(o[0][1], o[1][1]));
            s_o[wave * 64 + lane] = w0 + dpp<0x128>(w0);
        }
        asm volatile("s_waitcnt lgkmcnt(0)\n\ts_barrier" ::: "memory");
        if (wave == 0) {
            f32x2 p0, p1;
            asm volatile("ds_read2st64_b32 %0, %2 offset1:1\n\tds_read2st64_b32 %1, %2 offset0:2 offset1:3\n\ts_waitcnt lgkmcnt(0)" : "=&v"(p0), "=&v"(p1) : "v"(po) : "memory");
            __builtin_amdgcn_raw_buffer_store_b16((unsigned short)pk_bf16((p0[0] + p0[1]) + (p1[0] + p1[1]), 0.f), mkrsrc(a.out, H * DH * 2), oo, 0, 0);
        }
        if (b == 0 && tid == 0) *reinterpret_cast<u64*>(a.part + pep) = tag;
        asm volatile(".Lkv_b%c0:" :: "i"(ID));
    }
    if (DBG) {
        const unsigned tc1 = now32();
        if (!DS) tend = tbar = th4 = tfl = tpl = tc1;
        const unsigned tl16 = sched - stag - step;
        u64* stp = reinterpret_cast<u64*>(a.part + STAMP_OFF) + (size_t)((DBG == 4 || DBG == 24 || DBG == 6 ? cn : b) * 4 + wave) * 8;
        if (DBG == 6) { tfl = tA; tpl = tB; }
        if (DBG == 14) { if (lane == 0) { stp[0] = tstart; stp[1] = tA; stp[2] = tend - (((tend << 4) - tl16) >> 4); stp[3] = tB; stp[4] = tE; stp[5] = tC; stp[6] = tD; stp[7] = tc1; } } else
        if (lane == 0) { stp[0] = tstart; stp[1] = tbar; stp[2] = DBG >= 4 ? tend - (((tend << 4) - tl16) >> 4) : th1; stp[3] = DBG == 4 && cn >= NC ? tend : th4; stp[4] = tend; stp[5] = cn < NC ? tfl : tend; stp[6] = cn < NC ? tpl : tend; stp[7] = tc1; }
        if (DBG == 2) *reinterpret_cast<u32x4*>(a.part + STAMP_OFF + 65536 + (b * 4 + wave) * 1024 + lane * 16) = *reinterpret_cast<const u32x4*>(smem + LDS_TR + 16 + wave * 1024 + lane * 16);
    }
}

template <int TP, int ADV, int TMIN, int HOLD, int WIN, int OPT, int HS, int O2, int EX, int FM, int ID, int DBG>
static void launch42(void* const* d_in, const int* in_sizes, void* d_out, void* d_ws, hipStream_t stream) {
    typedef const unsigned short* P;
    Args a;
    a.qk = (const int*)d_in[1]; a.qv = (const int*)d_in[4];
    a.k_cos = (P)d_in[9]; a.k_sin = (P)d_in[10];
    a.q = (P)d_in[0]; a.q_cos = (P)d_in[7]; a.q_sin = (P)d_in[8];
    a.sc[0] = (P)d_in[2]; a.sc[1] = (P)d_in[3]; a.sc[2] = (P)d_in[5]; a.sc[3] = (P)d_in[6];
    a.part = (unsigned char*)d_ws;
    a.out = (unsigned short*)d_out;
    kv42_kernel<TP, ADV, TMIN, HOLD, WIN, OPT, HS, O2, EX, FM, ID, DBG><<<Skv / RW, 256, 0, stream>>>(a);
}

extern "C" void kernel_launch(void* const* d_in, const int* in_sizes, int n_in, void* d_out, int out_size, void* d_ws, size_t ws_size, hipStream_t stream) {
    launch42<80*16+4,230,79*16,85,4+16*32+4096*20,38+1536+64+128,0,96,66,1+16*2+4096*79+53*1048576,0,0>(d_in, in_sizes, d_out, d_ws, stream);
}
```

```cpp
#include <hip/hip_runtime.h>
#include <hip/hip_bf16.h>
#include <cstdint>
#define KALIGN 256
#define STAMP_OFF (200 << 20)
using bf16 = __hip_bfloat16;
using i32x4 = __attribute__((ext_vector_type(4))) int;
using u32x4 = __attribute__((ext_vector_type(4))) unsigned;
using u32x2 = __attribute__((ext_vector_type(2))) unsigned;
using f32x2 = __attribute__((ext_vector_type(2))) float;
using f32x4 = __attribute__((ext_vector_type(4))) float;

constexpr int DH = 192;
constexpr int HALF = 96;
constexpr int RW = 64;
constexpr int MAXH = 32;

__device__ __forceinline__ float bflo(unsigned u) { return __uint_as_float(u << 16); }
__device__ __forceinline__ float bfhi(unsigned u) { return __uint_as_float(u & 0xffff0000u); }
__device__ __forceinline__ float rbf(float x) { return __bfloat162float(__float2bfloat16(x)); }

template <int CTRL> __device__ __forceinline__ float dpp(float x) {
    return __builtin_bit_cast(float, __builtin_amdgcn_mov_dpp(__builtin_bit_cast(int, x), CTRL, 0xf, 0xf, true));
}
__device__ __forceinline__ float row16_allsum(float x) {
    x += dpp<0x128>(x);
    x += dpp<0x124>(x);
    x += dpp<0x122>(x);
    x += dpp<0x121>(x);
    return x;
}
__device__ __forceinline__ float row16_allmax(float x) {
    x = fmaxf(x, dpp<0x128>(x));
    x = fmaxf(x, dpp<0x124>(x));
    x = fmaxf(x, dpp<0x122>(x));
    x = fmaxf(x, dpp<0x121>(x));
    return x;
}
__device__ __forceinline__ float xrow_max(float x) {
    auto s = __builtin_amdgcn_permlane16_swap(__float_as_uint(x), __float_as_uint(x), false, false);
    x = fmaxf(__uint_as_float(s[0]), __uint_as_float(s[1]));
    auto t = __builtin_amdgcn_permlane32_swap(__float_as_uint(x), __float_as_uint(x), false, false);
    return fmaxf(__uint_as_float(t[0]), __uint_as_float(t[1]));
}
__device__ __forceinline__ float xrow_sum(float x) {
    auto s = __builtin_amdgcn_permlane16_swap(__float_as_uint(x), __float_as_uint(x), false, false);
    x = __uint_as_float(s[0]) + __uint_as_float(s[1]);
    auto t = __builtin_amdgcn_permlane32_swap(__float_as_uint(x), __float_as_uint(x), false, false);
    return __uint_as_float(t[0]) + __uint_as_float(t[1]);
}
__device__ __forceinline__ float sw32(float a, float b) {
    auto t = __builtin_amdgcn_permlane32_swap(__float_as_uint(a), __float_as_uint(b), false, false);
    return __uint_as_float(t[0]) + __uint_as_float(t[1]);
}
__device__ __forceinline__ float sw16(float a, float b) {
    auto t = __builtin_amdgcn_permlane16_swap(__float_as_uint(a), __float_as_uint(b), false, false);
    return __uint_as_float(t[0]) + __uint_as_float(t[1]);
}

#define SB() __builtin_amdgcn_sched_barrier(0)
typedef __amdgpu_buffer_rsrc_t rsrc_t;
__device__ __forceinline__ rsrc_t mkrsrc(const void* p, unsigned bytes) {
    return __builtin_amdgcn_make_buffer_rsrc(const_cast<void*>(p), 0, bytes, 0x00020000);
}
template <int AUX> __device__ __forceinline__ i32x4 bld128(rsrc_t r, unsigned voff, unsigned soff) {
    return __builtin_bit_cast(i32x4, __builtin_amdgcn_raw_buffer_load_b128(r, voff, soff, AUX));
}
template <int AUX> __device__ __forceinline__ unsigned bld16(rsrc_t r, unsigned voff, unsigned soff) {
    return __builtin_amdgcn_raw_buffer_load_b16(r, voff, soff, AUX);
}

struct HG { i32x4 d[3]; };
typedef unsigned long long u64;
typedef __attribute__((address_space(3))) unsigned char lds_u8;

__device__ __forceinline__ unsigned pk_bf16(float lo, float hi) {
    unsigned r;
    asm("v_cvt_pk_bf16_f32 %0, %1, %2" : "=v"(r) : "v"(lo), "v"(hi));
    return r;
}
__device__ __forceinline__ unsigned now32() { return (unsigned)__builtin_amdgcn_s_memrealtime(); }

constexpr int LDS_REC = 0;
constexpr int RECW = MAXH * DH * 4;
constexpr int LDS_CS = 0;
constexpr int LDS_QT = 12288;
constexpr int LDS_PF = 14336;
constexpr int LDS_QRAW = MAXH * 4 * DH * 4;
constexpr int LDS_Q = LDS_QRAW + MAXH * DH * 2;
constexpr int LDS_P = LDS_Q + 4 * DH * 4;
constexpr int LDS_SC = LDS_P + 4 * MAXH * 128;
constexpr int LDS_CNT = LDS_SC + 4 * MAXH * RW * 2;
constexpr int LDS_SK = LDS_CNT + 256;
constexpr int LDS_TR = LDS_SK + 1024;
constexpr int LDS_BYTES = LDS_TR + 4096 + 64;

struct Args {
    const int* qk; const int* qv;
    const unsigned short* k_cos; const unsigned short* k_sin;
    const unsigned short* q; const unsigned short* q_cos; const unsigned short* q_sin;
    unsigned char* part;
    unsigned short* out;
    const unsigned short* sc[4];
};
constexpr unsigned Skv = 16384;
constexpr float SCALE_LOG2 = 0.10411754627697264f;

template <int TP, int ADV, int TMIN, int WIN, int OPT, int EX, int GS, int HOLD, int HL, int SL, int EV, int ID, int DBG>
__global__ __launch_bounds__(256) __attribute__((aligned(KALIGN))) void kv43_kernel(Args a)
{
    __shared__ __attribute__((aligned(16))) unsigned char smem[LDS_BYTES];
    const unsigned tstart = DBG ? now32() : 0;
    constexpr bool DS = (DBG && DBG < 5) || DBG == 24;
    constexpr int H = MAXH, ES = 7, PFN = 16;
    const int b = blockIdx.x;
    const int tid = threadIdx.x;
    const int lane = tid & 63;
    const int wave = __builtin_amdgcn_readfirstlane(tid >> 6);
    typedef const unsigned short* const __attribute__((address_space(4))) * karg_t;
    const unsigned short* sp = ((karg_t)__builtin_amdgcn_kernarg_segment_ptr())[offsetof(Args, sc) / 8 + wave];
    if (b < PFN) {
        u64 pc; unsigned len, tl;
        asm volatile("s_getpc_b64 %0\n.Lkv_a%c3:\n\ts_mov_b32 %1, .Lkv_b%c3-.Lkv_a%c3-16\n\ts_mov_b32 %2, .Lkv_t%c3-.Lkv_a%c3" : "=s"(pc), "=s"(len), "=s"(tl) : "i"(ID));
        const unsigned o = tid * 16;
        asm volatile("s_mov_b32 m0, %5\n\ts_nop 0\n\tglobal_load_lds_dwordx4 %0, %4\n\tglobal_load_lds_dwordx4 %1, %4\n\tglobal_load_lds_dwordx4 %2, %4\n\tglobal_load_lds_dwordx4 %3, %4"
                     :: "v"(o), "v"(o + 4096), "v"(min(o + 8192, tl)), "v"(min(o + tl, len)), "s"(pc), "s"((unsigned)(size_t)(lds_u8*)smem + LDS_PF) : "memory");
    }
    SB();
    const int r = lane >> 4;
    const int c = lane & 15;
    constexpr unsigned hstride = Skv * (DH * 4);
    constexpr unsigned nb = Skv / RW;
    const unsigned lds0 = (unsigned)(size_t)(lds_u8*)smem;
    const unsigned pos = (b & 7) * (nb >> 3) + (b >> 3);
    const unsigned sb = ((((b >> 3) * 13) & 31) << 3) | (b & 7);
    const unsigned row0 = RW * pos + 16 * wave;

    constexpr unsigned LS = 512, psk = 0, pep = 128, AREA = 6u << 20, NAREA = 32, pfl = H * nb * LS;
    const rsrc_t pr = mkrsrc(a.part, (NAREA + 1) * AREA);
    u64 t64;
    {
        unsigned vs = ((lane >> 3) * Skv + RW * pos + (lane & 7) * 8) * 2, v1 = vs + 16 * Skv, v2 = vs + 32 * Skv, v3 = vs + 48 * Skv;
        unsigned ds = lds0 + LDS_SC + wave * 4096;
        asm volatile("" : "+v"(vs), "+v"(v1), "+v"(v2), "+v"(v3), "+s"(ds));
        SB();
        asm volatile("s_memrealtime %0\n\ts_mov_b32 m0, %6\n\ts_nop 0\n\tglobal_load_lds_dwordx4 %1, %5 nt\n\t"
                     "s_add_u32 m0, m0, 1024\n\ts_nop 0\n\tglobal_load_lds_dwordx4 %2, %5 nt\n\t"
                     "s_add_u32 m0, m0, 1024\n\ts_nop 0\n\tglobal_load_lds_dwordx4 %3, %5 nt\n\t"
                     "s_add_u32 m0, m0, 1024\n\ts_nop 0\n\tglobal_load_lds_dwordx4 %4, %5 nt\n\ts_waitcnt lgkmcnt(0)"
                     : "=&s"(t64) : "v"(vs), "v"(v1), "v"(v2), "v"(v3), "s"(sp), "s"(ds),
                       "s"(a.qk), "s"(a.qv), "s"(a.k_cos), "s"(a.k_sin), "s"(a.q), "s"(a.q_cos), "s"(a.q_sin), "s"(a.part), "s"(a.out) : "memory", "scc");
    }
    SB();
    const unsigned tdma = (unsigned)t64;
#define DMA3(V, A, B, P) "s_mov_b32 m0, " B "\n\ts_nop 0\n\t" \
                     "global_load_lds_dwordx4 " V ", " A P "\n\t" \
                     "global_load_lds_dwordx4 " V ", " A " offset:1024" P "\n\t" \
                     "global_load_lds_dwordx4 " V ", " A " offset:2048" P "\n\t"
    asm volatile(DMA3("%0", "%1", "%2", "") "s_mov_b32 m0, %4\n\ts_nop 0\n\tglobal_load_lds_dwordx4 %3, off"
                 :: "v"(lane * 16), "s"(a.q + wave * 1536), "s"(lds0 + LDS_QRAW + wave * 3072),
                    "v"(lane < 48 ? (const unsigned char*)((lane < 24 ? a.q_cos : a.q_sin - DH) + 8 * lane) : a.part + pep), "s"(lds0 + LDS_QT + wave * RECW) : "memory");
    SB();
    const int stag = (OPT & 32) ? (int)(((sb & ~7) + wave * 2 + (b & 1)) * (TP / 2)) / 256 - TP / 4 : (int)(((OPT & 2) ? (((b >> 3) * 26 + wave * 2 + (b & 1)) & 63) : ((((b >> 3) * 4 + wave) * 37) & 63)) * (TP / 2)) / 64 - TP / 4;
    unsigned sched = ((tdma + ADV) << 4) + stag - 4 * (TP / 2);
    unsigned vt = tdma << 4;
    int tru = -4;
    unsigned slip = 0, run = 0;
    unsigned step = TP / 2, tmin = TMIN / 2, vth = WIN & 15;
    auto pace = [&](bool ctl = false) {
        unsigned tgt = sched;
        sched += step;
        if ((int)(vt - tgt) > 0) tgt = vt;
        unsigned t = now32() << 4;
        while ((int)(t - tgt) < 0) { __builtin_amdgcn_s_sleep(1); t = now32() << 4; }
        vt = ((int)(t - 128 - tgt) > 0 ? t - 128 : tgt) + tmin;
        if (ctl) {
            if constexpr (WIN > 0 && WIN < 16) asm volatile("s_waitcnt vmcnt(%0)" :: "i"(WIN) : "memory");
            if constexpr (WIN >= 16) {
                const unsigned vm = __builtin_amdgcn_s_getreg(7 | (3 << 11));
                constexpr int EPS = (WIN >> 20) & 15, NP = (WIN >> 24) & 15, TH2 = (WIN >> 28) & 7;
                if (NP) run = vm > vth ? run + 1 : 0;
                if (vm > vth) {
                    if ((int)(vt - sched) < ((WIN >> 12) & 255) * 16 || (NP && run >= NP)) vt += (vm - vth) * ((WIN >> 4) & 255);
                    else if (TH2) { if (vm > TH2) vt += (vm - TH2) * ((WIN >> 4) & 255); }
                    else if (EPS) { sched += EPS; if (DBG == 2) slip += EPS; }
                }
            }
        }
        if constexpr (DBG == 2) { *reinterpret_cast<unsigned*>(smem + LDS_TR + 16 + wave * 1024 + (tru & 255) * 4) = ((t - (sched - slip - step)) >> 4) | ((OPT & 8) ? (__builtin_amdgcn_s_getreg(7 | (3 << 11)) << 16) : 0); ++tru; }
    };
    {
        unsigned dst = lds0 + LDS_CS + wave * RECW;
        const unsigned v0 = lane * 16 + row0 * (DH * 2);
#pragma unroll 1
        for (int j = 0; j < 4; ++j) {
            pace();
            asm volatile(DMA3("%0", "%1", "%2", " nt") :: "v"(v0), "s"((j < 2 ? a.k_cos : a.k_sin) + (j & 1) * 8 * DH), "s"(dst) : "memory");
            dst += 3072;
        }
    }
    if (tid < MAXH) *reinterpret_cast<unsigned*>(smem + LDS_CNT + tid * 4) = 0;
    const rsrc_t kr = mkrsrc(a.qk, H * hstride);
    const rsrc_t vr = mkrsrc(a.qv, H * hstride);
    const unsigned kvoff = (row0 + r) * (DH * 4) + c * 16;
    auto issue = [&](HG& g, rsrc_t rs, unsigned so) {
#pragma unroll
        for (int x = 0; x < 3; ++x) g.d[x] = bld128<2>(rs, kvoff + 256 * x, so);
    };
    HG buf[4];
#define PRE(g) SB(); pace(); issue(buf[g], kr, (g) * (4 * DH * 4)); SB();
    asm volatile("s_waitcnt vmcnt(12) lgkmcnt(0)\n\ts_barrier" ::: "memory");
    unsigned tbar = DS ? now32() : 0;
    PRE(0)
    const unsigned qoff = (lane < 48 ? lane : lane - 48) * 4;
    const unsigned* qt = reinterpret_cast<const unsigned*>(smem + LDS_QT + wave * RECW + qoff);
    unsigned qc0 = qt[0], qc1 = qt[HALF / 2], qs0 = qt[DH / 2], qs1 = qt[(DH + HALF) / 2];
    u64 tag;
    {
        const u32x2 e = *reinterpret_cast<const u32x2*>(smem + LDS_QT + wave * RECW + 768);
        tag = (((u64)__builtin_amdgcn_readfirstlane(e[1]) << 32) | __builtin_amdgcn_readfirstlane(e[0])) * 6364136223846793005ull + 1442695040888963407ull;
        asm volatile("" : "+s"(tag));
    }
    const unsigned t32 = tag >> 32;
    const unsigned char* qraw = smem + LDS_QRAW + qoff;
    unsigned char* qrot = smem + LDS_Q + wave * (DH * 4) + 2 * qoff;
    auto rotq = [&]() {
        const unsigned q_lo = *reinterpret_cast<const unsigned*>(qraw), q_hi = *reinterpret_cast<const unsigned*>(qraw + HALF * 2);
        qraw += DH * 2;
        const f32x2 lo = f32x2{bflo(q_lo), bfhi(q_lo)}, hi = f32x2{bflo(q_hi), bfhi(q_hi)};
        const f32x2 y1 = lo * f32x2{bflo(qc0), bfhi(qc0)}, y2 = -hi * f32x2{bflo(qs0), bfhi(qs0)};
        const f32x2 y3 = hi * f32x2{bflo(qc1), bfhi(qc1)}, y4 = lo * f32x2{bflo(qs1), bfhi(qs1)};
        const unsigned x1 = pk_bf16(y1[0], y1[1]), x2 = pk_bf16(y2[0], y2[1]), x3 = pk_bf16(y3[0], y3[1]), x4 = pk_bf16(y4[0], y4[1]);
        const f32x2 z1 = f32x2{bflo(x1), bfhi(x1)} + f32x2{bflo(x2), bfhi(x2)}, z2 = f32x2{bflo(x3), bfhi(x3)} + f32x2{bflo(x4), bfhi(x4)};
        const unsigned l2 = pk_bf16(z1[0], z1[1]), h2 = pk_bf16(z2[0], z2[1]);
        *reinterpret_cast<f32x2*>(qrot) = f32x2{bflo(l2), bfhi(l2)};
        *reinterpret_cast<f32x2*>(qrot + HALF * 4) = f32x2{bflo(h2), bfhi(h2)};
        asm volatile("" ::: "memory");
    };
    f32x4 qa[3], qb[3];
    const unsigned char* qh = smem + LDS_Q + wave * (DH * 4) + 16 * c;
    const unsigned char* qh1 = qh + (c < 8 ? 64 + HALF : 64 - HALF) * 4;
    auto loadq = [&]() {
        asm volatile("" ::: "memory");
#pragma unroll
        for (int x = 0; x < 3; ++x) qa[x] = *reinterpret_cast<const f32x4*>(qh + 256 * x);
        qb[0] = *reinterpret_cast<const f32x4*>(qh + HALF * 4);
        qb[1] = *reinterpret_cast<const f32x4*>(qh1);
        qb[2] = *reinterpret_cast<const f32x4*>(qh + (128 - HALF) * 4);
        asm volatile("" ::: "memory");
    };
    rotq();
    if (b < 8 && wave == 3) __builtin_amdgcn_raw_buffer_store_b128(u32x4{tdma, t32, tdma, (unsigned)tag}, pr, lane == 0 ? psk + b * 16 : 0x80000000u, 0, 16);
    PRE(1)
    f32x2 cc[4][3][2], ss[4][3][2];
    const unsigned char* cb = smem + LDS_CS + wave * RECW + r * (DH * 2) + c * 8;
    asm volatile("s_waitcnt vmcnt(12)" ::: "memory");
#pragma unroll
    for (int k = 0; k < 4; ++k)
#pragma unroll
        for (int x = 0; x < 3; ++x) {
            const u32x2 t = *reinterpret_cast<const u32x2*>(cb + k * 1536 + 128 * x);
            cc[k][x][0] = f32x2{bflo(t[0]), bfhi(t[0])}; cc[k][x][1] = f32x2{bflo(t[1]), bfhi(t[1])};
            asm volatile("" : "+v"(cc[k][x][0]), "+v"(cc[k][x][1]));
        }
    PRE(2)
    loadq();
    const unsigned char* scp = smem + LDS_SC + (c & 4) * 1024 + (16 * wave + 4 * (c & 3) + r) * 2;
    unsigned sc = *reinterpret_cast<const unsigned short*>(scp);
    auto loadsin = [&](int k) {
        const unsigned char* sb1 = cb + (c < 8 ? (64 + HALF) * 2 : (64 - HALF) * 2);
        const unsigned sg = c < 8 ? 0u : 0x80008000u;
        u32x2 t[3];
        t[0] = *reinterpret_cast<const u32x2*>(cb + 6144 + k * 1536 + HALF * 2);
        t[1] = *reinterpret_cast<const u32x2*>(sb1 + 6144 + k * 1536) ^ u32x2{sg, sg};
        t[2] = *reinterpret_cast<const u32x2*>(cb + 6144 + k * 1536 + (128 - HALF) * 2) ^ u32x2{0x80008000u, 0x80008000u};
#pragma unroll
        for (int x = 0; x < 3; ++x) {
            ss[k][x][0] = f32x2{bflo(t[x][0]), bfhi(t[x][0])}; ss[k][x][1] = f32x2{bflo(t[x][1]), bfhi(t[x][1])};
            asm volatile("" : "+v"(ss[k][x][0]), "+v"(ss[k][x][1]));
        }
    };
    asm volatile("s_waitcnt vmcnt(12)" ::: "memory");
    loadsin(0); loadsin(1);
    PRE(3)
    asm volatile("s_waitcnt vmcnt(12)" ::: "memory");
    loadsin(2); loadsin(3);
    unsigned th1 = 0, th4 = 0;
    unsigned char* prec = smem + LDS_P + wave * (H * 128) + r * 32 + (c & 7) * 4;
    auto softmax = [&](float s, int h) {
        float m = fmaxf(s, dpp<0xb1>(s));
        m = xrow_max(fmaxf(m, dpp<0x4e>(m)));
        const float p = __builtin_amdgcn_exp2f(s - m);
        float l = p + dpp<0xb1>(p);
        l = xrow_sum(l + dpp<0x4e>(l));
        const float t = (c & 1) ? l : m;
        const int x = __builtin_amdgcn_update_dpp(__float_as_int(p), __float_as_int(t), 0x114, 0xf, 0xa, false);
        *reinterpret_cast<int*>(prec + h * 128) = x;
    };
    float sprev = 0.f;

#pragma unroll 1
    for (int h = 0; h < H; ++h) {
        const bool more = h + 1 < H;
        const rsrc_t nx = more ? kr : vr;
        const unsigned nso = more ? (h + 1) * hstride : 0;
        float scf = bflo(sc);
        asm volatile("" : "+v"(scf) :: "memory");
        SB();
        scp += more ? 128 : 4096 + 128;
        sc = *reinterpret_cast<const unsigned short*>(scp);
        SB();
        float sreg = 0.f;
#define KSTEP(k) { \
            const float kz = dpp<0x154 + (k)>(scf); \
            f32x2 kz2 = f32x2{kz, kz}; \
            asm("" : "+v"(kz2)); \
            f32x2 a2 = f32x2{0.f, 0.f}; \
            _Pragma("unroll") for (int x = 0; x < 3; ++x) { \
                _Pragma("unroll") for (int e = 0; e < 2; ++e) { \
                    const f32x2 w = __builtin_elementwise_fma(f32x2{qb[x][2 * e], qb[x][2 * e + 1]}, ss[k][x][e], f32x2{qa[x][2 * e], qa[x][2 * e + 1]} * cc[k][x][e]); \
                    const f32x2 kf = f32x2{(float)buf[k].d[x][2 * e], (float)buf[k].d[x][2 * e + 1]} - kz2; \
                    a2 = __builtin_elementwise_fma(kf, w, a2); \
                } \
            } \
            float sa = a2[0] + a2[1]; \
            asm volatile("" : "+v"(sa) :: "memory"); \
            SB(); pace(true); issue(buf[k], nx, nso + (k) * (4 * DH * 4)); SB(); \
            if ((k) == 0) { softmax(sprev, (h - 1) & (H - 1)); SB(); }   \
            if ((k) == 1) { rotq(); SB(); } \
            if ((k) == 2) { \
                if (wave == 1 && h == ((OPT & 4) ? 1 + ((b >> 3) & 3) : 4 + ((b >> 3) & 15))) \
                    asm volatile("s_mov_b32 m0, %2\n\ts_nop 0\n\tglobal_load_lds_dwordx4 %0, %1 sc1" :: "v"(psk + (lane & 7) * 16), "s"(a.part), "s"(lds0 + LDS_SK) : "memory"); \
                if (h == ((OPT & 4) ? 6 : 25)) {        \
                    const u32x2 e = *reinterpret_cast<const u32x2*>(smem + LDS_SK + (lane & 7) * 16); \
                    const int d = (int)(tdma - e[0]); \
                    if (__all(e[1] == t32 && d > -400 && d < 400)) { \
                        int sum = d + __builtin_amdgcn_mov_dpp(d, 0xb1, 0xf, 0xf, true); \
                        sum += __builtin_amdgcn_mov_dpp(sum, 0x4e, 0xf, 0xf, true); \
                        sum += __builtin_amdgcn_mov_dpp(sum, 0x124, 0xf, 0xf, true); \
                        const int adj = (__builtin_amdgcn_readfirstlane(sum) * ((OPT & 4) ? 73 : 112) + 4096) >> 13;        \
                        step -= adj; tmin -= adj; \
                        if ((OPT & 64) && (OPT & 384) && b < ((OPT & 256) ? 256 : 16) && wave == 0) {        \
                            unsigned t0, t1, t2, t3; \
                            const unsigned ar = (((__builtin_amdgcn_readfirstlane(e[0]) >> 13) & (NAREA - 1)) + 1) * AREA; \
                            asm volatile("s_load_dword %0, %4, %5\n\ts_load_dword %1, %4, %6\n\ts_load_dword %2, %4, %7\n\ts_load_dword %3, %4, %8\n\ts_waitcnt lgkmcnt(0)" : "=&s"(t0), "=&s"(t1), "=&s"(t2), "=&s"(t3) : "s"(a.part), "s"(ar), "s"(ar + AREA - 128), "s"(ar + (2u << 20) + 128), "s"(ar + AREA - (2u << 20) - 128) : "memory"); \
                            vt = now32() << 4; \
                        } \
                    } \
                } \
                SB(); \
            } \
            if ((k) == 3) { loadq(); SB(); } \
            sa = row16_allsum(sa); \
            sreg = (c & 3) == (k) ? sa : sreg; }
        KSTEP(0) KSTEP(1) KSTEP(2) KSTEP(3)
        sprev = sreg * (scf * SCALE_LOG2);
        if (DS) { if (h == 7) th1 = now32(); }
    }
    softmax(sprev, H - 1);
    if (DS) th4 = now32();

    const unsigned tg = (unsigned)(tag >> (16 * (lane & 3))) << 16;
    unsigned rl = lds0 + LDS_REC + lane * 4, rh = rl + 2 * RECW;
    auto pack = [&](f32x2 t, float u) { return u32x2{pk_bf16(t[0], t[1]), pk_bf16(u, 0.f) | tg}; };
    auto ldrec = [&](unsigned o, f32x2 (&p)[6]) {
        asm volatile("ds_read2st64_b32 %0, %6 offset1:1\n\tds_read2st64_b32 %1, %6 offset0:96 offset1:97\n\tds_read2st64_b32 %2, %7 offset1:1\n\tds_read2st64_b32 %3, %7 offset0:96 offset1:97\n\t"
                     "ds_read2st64_b32 %4, %6 offset0:2 offset1:98\n\tds_read2st64_b32 %5, %7 offset0:2 offset1:98"
                     : "=&v"(p[0]), "=&v"(p[1]), "=&v"(p[2]), "=&v"(p[3]), "=&v"(p[4]), "=&v"(p[5]) : "v"(rl + o), "v"(rh + o) : "memory");
    };
    auto merge = [&](int hd) {
        f32x2 p[6], ma, mb;
        asm volatile("ds_read2st64_b32 %0, %2 offset1:16\n\tds_read2st64_b32 %1, %2 offset0:32 offset1:48" : "=&v"(ma), "=&v"(mb) : "v"(lds0 + LDS_P + hd * 128 + 16) : "memory");
        ldrec(hd * (DH * 4), p);
        asm volatile("s_waitcnt lgkmcnt(6)" : "+v"(ma), "+v"(mb) :: "memory");
        const float M = __builtin_amdgcn_fmed3f(__builtin_amdgcn_fmed3f(ma[0], ma[1], __builtin_inff()), __builtin_amdgcn_fmed3f(mb[0], mb[1], __builtin_inff()), __builtin_inff());
        ma = f32x2{__builtin_amdgcn_exp2f(ma[0] - M), __builtin_amdgcn_exp2f(ma[1] - M)};
        mb = f32x2{__builtin_amdgcn_exp2f(mb[0] - M), __builtin_amdgcn_exp2f(mb[1] - M)};
        asm volatile("s_waitcnt lgkmcnt(0)" : "+v"(ma), "+v"(mb), "+v"(p[0]), "+v"(p[1]), "+v"(p[2]), "+v"(p[3]), "+v"(p[4]), "+v"(p[5]) :: "memory");
        const f32x2 u = __builtin_elementwise_fma(mb, p[5], ma * p[4]);
        return pack(ma[0] * p[0] + ma[1] * p[1] + mb[0] * p[2] + mb[1] * p[3], u[0] + u[1]);
    };
    constexpr int NC = 4 * H;
    const int cn = sb;
    const unsigned hc = 4 * (cn & 7) + ((cn >> 3) & 3);
    unsigned old = 0, area = 0, fo = 0, shift = 0;
    u32x4 flv = {};
    float gl = 0.f, mw = 0.f;
    f32x2 fs[4] = {};
    constexpr int LDS_ML = LDS_QRAW + 2048;
#define BAD(y) ((unsigned)(y)[1] != t32 || (unsigned)(y)[3] != (unsigned)tag)
    auto ev_ml = [&]() {
        f32x2 ml[4];
#pragma unroll
        for (int w = 0; w < 4; ++w) ml[w] = *reinterpret_cast<const f32x2*>(smem + LDS_P + w * (H * 128) + (8 * wave + (lane & 7)) * 128 + 16);
        const float M = fmaxf(fmaxf(ml[0][0], ml[1][0]), fmaxf(ml[2][0], ml[3][0]));
        const float L = (__builtin_amdgcn_exp2f(ml[0][0] - M) * ml[0][1] + __builtin_amdgcn_exp2f(ml[1][0] - M) * ml[1][1]) + (__builtin_amdgcn_exp2f(ml[2][0] - M) * ml[2][1] + __builtin_amdgcn_exp2f(ml[3][0] - M) * ml[3][1]);
        flv = u32x4{__float_as_uint(M), t32, __float_as_uint(L), (unsigned)tag};
        asm volatile("" : "+v"(flv));
    };
    auto ev_area = [&]() {
        u32x4 e = *reinterpret_cast<const u32x4*>(smem + LDS_SK);
        float mv[4];
#pragma unroll
        for (int w = 0; w < 4; ++w) mv[w] = *reinterpret_cast<const float*>(smem + LDS_P + ((wave + w) & 3) * (H * 128) + (H - 1) * 128 + 16);
        gl = __builtin_amdgcn_exp2f(mv[0] - fmaxf(fmaxf(mv[0], mv[1]), fmaxf(mv[2], mv[3])));
        if (__builtin_expect(__any(BAD(e)), 0)) {
#pragma unroll 1
            for (int n = 0; n < (1 << 20) && BAD(e); ++n)
                asm volatile("global_load_dwordx4 %0, %1, %2 sc1\n\ts_waitcnt vmcnt(0)" : "=v"(e) : "v"(psk), "s"(a.part) : "memory");
        }
        area = (((__builtin_amdgcn_readfirstlane(e[0]) >> 13) & (NAREA - 1)) + 1) * AREA + 4096;
        asm volatile("" : "+s"(area), "+v"(gl));
    };
    auto ev_put = [&]() {
        asm volatile("s_mov_b64 exec, 0xff\n\tbuffer_store_dwordx4 %0, %1, %2, %3 offen sc1\n\ts_mov_b64 exec, -1" :: "v"(flv), "v"(4 * (rl - lds0 - LDS_REC)), "s"(pr), "s"(area + pfl + (wave * nb + sb) * 128) : "memory");
    };
    auto ev_get = [&]() {
        unsigned l = lane;
        asm volatile("" : "+v"(l));
        fo = area + pfl + ((hc >> 3) * nb + 8 * wave) * 128 + (hc & 7) * 16 + (((l & 56) << 2) + (l & 7)) * 128;
        asm volatile("s_mov_b32 m0, %2\n\ts_nop 0\n\tglobal_load_lds_dwordx4 %0, %1 sc1" :: "v"(fo), "s"(a.part), "s"(lds0 + LDS_ML + wave * 1024) : "memory");
    };
    auto ev_w1 = [&]() {
        u32x4 y = *reinterpret_cast<const u32x4*>(smem + LDS_ML + wave * 1024 + lane * 16);
        if (__builtin_expect(__any(BAD(y)), 0)) {
#pragma unroll 1
            for (int n = 0; n < (1 << 20) && BAD(y); ++n)
                asm volatile("global_load_dwordx4 %0, %1, %2 sc1\n\ts_waitcnt vmcnt(0)" : "=v"(y) : "v"(fo), "s"(a.part) : "memory");
        }
        const float m = __uint_as_float(y[0]);
        float u;
        asm volatile("s_nop 1\n\tv_max_f32_dpp %0, %2, %2 row_ror:8 row_mask:0xf bank_mask:0xf\n\ts_nop 1\n\tv_max_f32_dpp %0, %0, %0 row_ror:4 row_mask:0xf bank_mask:0xf\n\ts_nop 1\n\t"
                     "v_max_f32_dpp %0, %0, %0 row_ror:2 row_mask:0xf bank_mask:0xf\n\ts_nop 1\n\tv_max_f32_dpp %0, %0, %0 row_ror:1 row_mask:0xf bank_mask:0xf\n\tv_mov_b32 %1, %0\n\ts_nop 1\n\t"
                     "v_permlane16_swap_b32 %0, %1\n\ts_nop 0\n\tv_max_f32 %0, %0, %1\n\tv_mov_b32 %1, %0\n\ts_nop 1\n\tv_permlane32_swap_b32 %0, %1\n\ts_nop 0\n\tv_max_f32 %0, %0, %1" : "=&v"(mw), "=&v"(u) : "v"(m));
        fs[0][0] = __builtin_amdgcn_exp2f(m - mw);
        fs[0][1] = fs[0][0] * __uint_as_float(y[2]);
        asm volatile("" : "+v"(fs[0]));
    };
    auto ev_w2 = [&]() {
        const f32x2 f = fs[0];
        unsigned bp = (lane >> 3) * 4;
        asm volatile("ds_bpermute_b32 %0, %8, %9\n\tds_bpermute_b32 %1, %8, %9 offset:32\n\tds_bpermute_b32 %2, %8, %9 offset:64\n\tds_bpermute_b32 %3, %8, %9 offset:96\n\t"
                     "ds_bpermute_b32 %4, %8, %9 offset:128\n\tds_bpermute_b32 %5, %8, %9 offset:160\n\tds_bpermute_b32 %6, %8, %9 offset:192\n\tds_bpermute_b32 %7, %8, %9 offset:224"
                     : "=&v"(fs[0][0]), "=&v"(fs[0][1]), "=&v"(fs[1][0]), "=&v"(fs[1][1]), "=&v"(fs[2][0]), "=&v"(fs[2][1]), "=&v"(fs[3][0]), "=&v"(fs[3][1]) : "v"(bp), "v"(f[0]) : "memory");
        *reinterpret_cast<f32x2*>(smem + LDS_QRAW + 1024 + wave * 8) = f32x2{mw, xrow_sum(row16_allsum(f[1]))};
        asm volatile("s_waitcnt lgkmcnt(0)" : "+v"(fs[0]), "+v"(fs[1]), "+v"(fs[2]), "+v"(fs[3]) :: "memory");
    };
    constexpr int L0 = (EV >> 12) & 63, HP = (EV >> 18) & 31;
    const int hpub = HP && b < 32 && wave == 2 ? HP : -9, hrd = HP && wave == 1 ? H - 2 : -9;
    auto ev_pub = [&]() {
        asm volatile("s_mov_b64 exec, 1\n\tbuffer_store_dwordx2 %0, off, %1, %2 sc1\n\ts_mov_b64 exec, -1" :: "v"(u32x2{vt - tmin - sched + step, t32}), "s"(pr), "s"(area - 2048 + b * 8) : "memory");
    };
    auto ev_rd = [&]() {
        asm volatile("s_mov_b32 m0, %2\n\ts_mov_b64 exec, 0xffff\n\tbuffer_load_dwordx4 %0, %1, %3 offen sc1 lds\n\ts_mov_b64 exec, -1" :: "v"(4 * (rl - lds0 - LDS_REC)), "s"(pr), "s"(lds0 + LDS_ML + 4096), "s"(area - 2048) : "memory");
    };
    constexpr int G1 = EV & 15, G2 = (EV >> 4) & 15, G3 = (EV >> 8) & 15;
    const int hput = G1 + G2 + 1 + ((b >> 3) & 3), hget = cn < NC ? G1 + G2 + 6 + ((b >> 3) & 3) : -9;
    float pv = *reinterpret_cast<const float*>(prec);
    f32x2 acc[3][2];
    float y;
    auto prep = [&]() {
        y = bflo(sc);
        y = (c & 4) ? y : y * pv;
        asm volatile("" : "+v"(y) :: "memory");
        SB();
        scp += 128;
        sc = *reinterpret_cast<const unsigned short*>(scp);
        prec += 128;
        pv = *reinterpret_cast<const float*>(prec);
        SB();
    };
#define VCOMP(k) { \
        const float pw = dpp<0x150 + (k)>(y); \
        const float vz = dpp<0x154 + (k)>(y); \
        f32x2 pw2 = f32x2{pw, pw}, vz2 = f32x2{vz, vz}; \
        asm("" : "+v"(pw2), "+v"(vz2)); \
        _Pragma("unroll") for (int x = 0; x < 3; ++x) \
            _Pragma("unroll") for (int e = 0; e < 2; ++e) { \
                const f32x2 vf = f32x2{(float)buf[k].d[x][2 * e], (float)buf[k].d[x][2 * e + 1]} - vz2; \
                acc[x][e] = (k) == 0 ? pw2 * vf : __builtin_elementwise_fma(pw2, vf, acc[x][e]); \
            } \
        asm volatile("" : "+v"(acc[0][0]), "+v"(acc[0][1]), "+v"(acc[1][0]), "+v"(acc[1][1]), "+v"(acc[2][0]), "+v"(acc[2][1]) :: "memory"); \
        SB(); }
#define VSTEP(k, hn) { \
        VCOMP(k) pace(true); issue(buf[k], vr, (hn) * hstride + (k) * (4 * DH * 4)); SB(); \
        if ((k) == 1) { tmin = (hn) == H - 1 ? 0 : tmin; } \
        if ((k) == 1 && (hn) == G1) { asm volatile("s_waitcnt lgkmcnt(0)\n\ts_barrier" ::: "memory"); SB(); } \
        if ((k) == 2 && (hn) == G1) { ev_ml(); SB(); } \
        if ((k) == 2 && (hn) == G1 + G2) { ev_area(); SB(); } \
        if ((k) == 1 && (OPT & 1024)) { vth = WIN & 15; asm volatile("" : "+s"(vth)); } \
        if ((k) == 1 && (hn) == hput) { ev_put(); if (OPT & 1024) vth = 15; SB(); } \
        if ((k) == 1 && (hn) == hget) { ev_get(); if (OPT & 1024) vth = 15; SB(); } \
        if ((k) == 2 && (hn) == hget + 2) { ev_w1(); SB(); } \
        if (HP && (k) == 2 && (hn) == hpub) { ev_pub(); SB(); } \
        if (HP && (k) == 1 && (hn) == hrd) { ev_rd(); SB(); } \
        if ((k) == 1 && (hn) == hget + 2 + G3) { ev_w2(); SB(); } }
    auto record = [&](unsigned ra, unsigned ca) {
        f32x4 o;
#pragma unroll
        for (int e = 0; e < 4; ++e) {
            const float t0 = sw32(acc[0][e >> 1][e & 1], acc[2][e >> 1][e & 1]);
            const float t1 = sw32(acc[1][e >> 1][e & 1], acc[1][e >> 1][e & 1]);
            o[e] = sw16(t0, t1);
        }
        asm volatile("s_mov_b64 exec, %5\n\tds_write_b128 %1, %2\n\ts_mov_b64 exec, 1\n\tds_add_rtn_u32 %0, %3, %4\n\ts_mov_b64 exec, -1"
                     : "+v"(old) : "v"(ra), "v"(o), "v"(ca), "v"(1u), "s"(0xffffffffffffull) : "memory");
        SB();
    };
#pragma unroll 1
    for (int h = 0; h < H - 1; ++h) {
        prep();
        VSTEP(0, h + 1)
        asm volatile("s_waitcnt lgkmcnt(0)" : "+v"(old) :: "memory");
        if (__builtin_amdgcn_readfirstlane(old) == 3) *reinterpret_cast<u32x2*>(smem + LDS_REC + (h - 1) * (DH * 4) + lane * 8) = merge(h - 1);
        SB();
        VSTEP(1, h + 1) VSTEP(2, h + 1) VSTEP(3, h + 1)
        record(lds0 + LDS_REC + h * (DH * 4) + wave * RECW + lane * 16, lds0 + LDS_CNT + h * 4);
    }
    asm volatile("s_branch .Lkv_t%c0\n\t.p2align 12,,3327\n.Lkv_t%c0:" :: "i"(ID));
    y = bflo(sc);
    y = (c & 4) ? y : y * (pv * gl);
    VCOMP(0)
    asm volatile("s_waitcnt lgkmcnt(0)\n\ts_barrier" ::: "memory");
    u32x4 ev[4];
#pragma unroll
    for (int p = 0; p < 4; ++p) ev[p] = *reinterpret_cast<const u32x4*>(smem + LDS_REC + (8 * wave + 2 * p + (lane >> 5)) * (DH * 4) + (lane & 31) * 16);
    const f32x4 a0 = *reinterpret_cast<const f32x4*>(smem + LDS_QRAW + 1024), a1 = *reinterpret_cast<const f32x4*>(smem + LDS_QRAW + 1040);
    const u32x2 lt = *reinterpret_cast<const u32x2*>(smem + LDS_ML + 4096 + (lane & 31) * 8);
    SB();
    VCOMP(1)
    if (HP) {
        int v = lt[1] == t32 ? (int)lt[0] : 0, u;
        asm volatile("s_nop 1\n\tv_max_i32_dpp %0, %0, %0 row_ror:8 row_mask:0xf bank_mask:0xf\n\ts_nop 1\n\tv_max_i32_dpp %0, %0, %0 row_ror:4 row_mask:0xf bank_mask:0xf\n\ts_nop 1\n\t"
                     "v_max_i32_dpp %0, %0, %0 row_ror:2 row_mask:0xf bank_mask:0xf\n\ts_nop 1\n\tv_max_i32_dpp %0, %0, %0 row_ror:1 row_mask:0xf bank_mask:0xf\n\ts_nop 0" : "+v"(v));
        u = max(max(__builtin_amdgcn_readlane(v, 0), __builtin_amdgcn_readlane(v, 16)) - L0 * 16, 0);
        shift = min(u, 1024 * 16);
    }
    unsigned eo = (lane >> 5) * (nb * LS) + (lane & 31) * 16, e8 = lane * 8, ra = lds0 + LDS_REC + (H - 1) * (DH * 4) + wave * RECW + lane * 16, ca = lds0 + LDS_CNT + (H - 1) * 4;
    unsigned es[5];
#pragma unroll
    for (int p = 0; p < 4; ++p) es[p] = area + ((8 * wave + 2 * p) * nb + sb) * LS;
    es[4] = area + ((H - 1) * nb + sb) * LS;
    unsigned tx = sched + shift - step - stag + EX * 16 + (sb >> 3) * GS * 4;
    if (wave == 3) { const u32x2 t = merge(H - 2); ev[3][0] = t[0]; ev[3][1] = t[1]; }
    {
        const float M = fmaxf(fmaxf(a0[0], a0[2]), fmaxf(a1[0], a1[2]));
        const float gw = __builtin_amdgcn_exp2f(mw - M) * __builtin_amdgcn_rcpf((__builtin_amdgcn_exp2f(a0[0] - M) * a0[1] + __builtin_amdgcn_exp2f(a0[2] - M) * a0[3]) + (__builtin_amdgcn_exp2f(a1[0] - M) * a1[1] + __builtin_amdgcn_exp2f(a1[2] - M) * a1[3]));
#pragma unroll
        for (int i = 0; i < 4; ++i) fs[i] *= gw;
    }
    rl += (H - 1) * (DH * 4); rh += (H - 1) * (DH * 4);
    asm volatile("" : "+v"(fs[0]), "+v"(fs[1]), "+v"(fs[2]), "+v"(fs[3]), "+v"(ev[0]), "+v"(ev[1]), "+v"(ev[2]), "+v"(ev[3]), "+v"(eo), "+v"(e8), "+v"(ra), "+v"(ca), "+v"(rl), "+v"(rh),
                      "+s"(es[0]), "+s"(es[1]), "+s"(es[2]), "+s"(es[3]), "+s"(es[4]), "+s"(tx));
    SB();
    VCOMP(2)
    while ((int)((now32() << 4) - tx) < 0) {}
    if constexpr ((OPT & 512) != 0)
    asm volatile("s_waitcnt vmcnt(0)\n\tbuffer_store_dwordx4 %3, %7, %9, %10 offen sc1\n\tbuffer_store_dwordx4 %4, %7, %9, %11 offen sc1\n\tbuffer_store_dwordx4 %5, %7, %9, %12 offen sc1\n\t"
                 "s_cmp_eq_u32 %14, 3\n\ts_cbranch_scc1 1f\n\tbuffer_store_dwordx4 %6, %7, %9, %13 offen sc1\n\ts_branch 2f\n1:\tbuffer_store_dwordx2 %15, %8, %9, %13 offen sc1\n2:"
                 : "+v"(buf[3].d[0]), "+v"(buf[3].d[1]), "+v"(buf[3].d[2]) : "v"(ev[0]), "v"(ev[1]), "v"(ev[2]), "v"(ev[3]), "v"(eo), "v"(e8), "s"(pr), "s"(es[0]), "s"(es[1]), "s"(es[2]), "s"(es[3]), "s"(wave),
                   "v"(u32x2{ev[3][0], ev[3][1]}) : "memory", "scc");
    else
    asm volatile("s_waitcnt vmcnt(0)\n\tbuffer_store_dwordx4 %3, %7, %9, %10 offen sc1\n\tbuffer_store_dwordx4 %4, %7, %9, %11 offen sc1\n\tbuffer_store_dwordx4 %5, %7, %9, %12 offen sc1\n\t"
                 "s_cmp_eq_u32 %14, 3\n\ts_cselect_b64 exec, 0, -1\n\tbuffer_store_dwordx4 %6, %7, %9, %13 offen sc1\n\ts_not_b64 exec, exec\n\tbuffer_store_dwordx2 %15, %8, %9, %13 offen sc1\n\ts_mov_b64 exec, -1"
                 : "+v"(buf[3].d[0]), "+v"(buf[3].d[1]), "+v"(buf[3].d[2]) : "v"(ev[0]), "v"(ev[1]), "v"(ev[2]), "v"(ev[3]), "v"(eo), "v"(e8), "s"(pr), "s"(es[0]), "s"(es[1]), "s"(es[2]), "s"(es[3]), "s"(wave),
                   "v"(u32x2{ev[3][0], ev[3][1]}) : "memory", "scc");
    if (DBG == 4) tbar = now32();
    SB();
    VCOMP(3)
    asm volatile("" :: "v"(ev[0]), "v"(ev[1]), "v"(ev[2]), "v"(ev[3]));
    record(ra, ca);
    asm volatile("s_waitcnt lgkmcnt(0)" : "+v"(old) :: "memory");
    if (__builtin_amdgcn_readfirstlane(old) == 3) {
        f32x2 p[6];
        ldrec(0, p);
        asm volatile("s_waitcnt lgkmcnt(0)" : "+v"(p[0]), "+v"(p[1]), "+v"(p[2]), "+v"(p[3]), "+v"(p[4]), "+v"(p[5]) :: "memory");
        const f32x2 u = p[4] + p[5];
        __builtin_amdgcn_raw_buffer_store_b64(pack((p[0] + p[1]) + (p[2] + p[3]), u[0] + u[1]), pr, e8, es[4], 16);
    }
    unsigned tend = DS ? now32() : 0;
    unsigned tfl = 0, tpl = 0;

    if (cn < NC) {
        unsigned tgt = sched + shift - stag + (hc == H - 1 ? HL : HOLD) * 16;
        unsigned rs[8];
#pragma unroll
        for (int i = 0; i < 8; ++i) rs[i] = area + (hc * nb + 32 * i + 8 * wave) * LS + (cn >> 5) * 128;
        const unsigned g = lane & 7, e = (lane >> 3) & 1;
        unsigned xo = (lane >> 3) * LS + g * 16;
        unsigned oo = r == 3 ? 0x80000000u : (hc * DH + (cn >> 5) * 16 + 2 * g + (r == 2 ? 128 + e : r + 64 * e)) * 2;
        unsigned po = lds0 + LDS_QRAW + lane * 4, wo = po + wave * 256;
        unsigned tb = (lane & 1) ? t32 : (unsigned)tag, ta = tb << 16;
        const rsrc_t orr = mkrsrc(a.out, H * DH * 2);
        asm volatile("" : "+s"(rs[0]), "+s"(rs[1]), "+s"(rs[2]), "+s"(rs[3]), "+s"(rs[4]), "+s"(rs[5]), "+s"(rs[6]), "+s"(rs[7]), "+v"(po), "+v"(wo), "+v"(xo), "+v"(oo), "+v"(ta), "+v"(tb), "+s"(tgt) : "s"(orr) : "memory");
        i32x4 x[8];
        while ((int)((now32() << 4) - tgt) < 0) {}
        if (DS) tfl = now32();
#define XL(i) x[i] = bld128<16>(pr, xo, rs[i]); SB();
#define XS(i) XL(i) __builtin_amdgcn_s_sleep((SL >> (2 * (i))) & 3); SB();
        XS(0) XS(1) XS(2) XS(3) XS(4) XS(5) XS(6) XL(7)
        if (DBG == 4) th4 = now32();
        SB();
        f32x2 o[3];
#pragma unroll 1
        for (int n = 0; n < (1 << 20); ++n) {
            unsigned bad = 0;
#pragma unroll
            for (int i = 0; i < 8; ++i) {
#pragma unroll
                for (int e = 0; e < 3; ++e) {
                    const f32x2 t = e < 2 ? f32x2{bflo(x[i][2 * e]), bfhi(x[i][2 * e])} : f32x2{bflo(x[i][1]), bflo(x[i][3])};
                    if (i == 0) asm volatile("v_pk_mul_f32 %0, %1, %2 op_sel_hi:[0,1]" : "=v"(o[e]) : "v"(fs[0]), "v"(t));
                    else if (i & 1) asm volatile("v_pk_fma_f32 %0, %1, %2, %0 op_sel:[1,0,0]" : "+v"(o[e]) : "v"(fs[i >> 1]), "v"(t));
                    else asm volatile("v_pk_fma_f32 %0, %1, %2, %0 op_sel_hi:[0,1,1]" : "+v"(o[e]) : "v"(fs[i >> 1]), "v"(t));
                }
                bad = __builtin_amdgcn_bitop3_b32(bad, x[i][1], ta, 0xf6);
                bad = __builtin_amdgcn_bitop3_b32(bad, x[i][3], tb, 0xf6);
                asm volatile("" : "+v"(bad));
                SB();
            }
            if (__builtin_expect(__all(bad < 0x10000u), 1)) break;
            asm volatile("" ::: "memory");
            XL(0) XL(1) XL(2) XL(3) XL(4) XL(5) XL(6) XL(7)
        }
        if (DS) { asm volatile("" : "+v"(o[0]), "+v"(o[1]), "+v"(o[2])); tpl = now32(); }
        const float w0 = sw16(sw32(o[0][0], o[2][0]), sw32(o[1][0], o[1][0])), w1 = sw16(sw32(o[0][1], o[2][1]), sw32(o[1][1], o[1][1]));
        asm volatile("ds_write_b32 %0, %1\n\ts_waitcnt lgkmcnt(0)\n\ts_barrier" :: "v"(wo), "v"((c < 8 ? w0 : w1) + dpp<0x128>(c < 8 ? w1 : w0)) : "memory");
        if (wave == 0) {
            f32x2 p0, p1;
            asm volatile("ds_read2st64_b32 %0, %2 offset1:1\n\tds_read2st64_b32 %1, %2 offset0:2 offset1:3\n\ts_waitcnt lgkmcnt(0)" : "=&v"(p0), "=&v"(p1) : "v"(po) : "memory");
            p0 += p1;
            __builtin_amdgcn_raw_buffer_store_b16((unsigned short)pk_bf16(p0[0] + p0[1], 0.f), orr, oo, 0, 0);
        }
        if (b == 0 && tid == 64) *reinterpret_cast<u64*>(a.part + pep) = tag;
        asm volatile(".Lkv_b%c0:" :: "i"(ID));
    }
    if (DBG) {
        const unsigned tc1 = now32();
        if (!DS) tend = tbar = th4 = tfl = tpl = tc1;
        const unsigned tl16 = sched - stag - step;
        u64* stp = reinterpret_cast<u64*>(a.part + STAMP_OFF) + (size_t)((DBG == 4 || DBG == 24 || DBG == 6 ? cn : b) * 4 + wave) * 8;

        if (DBG == 5 || DBG == 2) tbar = sched - (((tdma + ADV) << 4) + stag) - 33 * (TP / 2) - 223 * step + shift;
        if (lane == 0) { stp[0] = tstart; stp[1] = tbar; stp[2] = DBG >= 4 ? tend - (((tend << 4) - tl16) >> 4) : th1; stp[3] = DBG == 4 && cn >= NC ? tend : th4; stp[4] = tend; stp[5] = cn < NC ? tfl : tend; stp[6] = cn < NC ? tpl : tend; stp[7] = tc1; }
        if (DBG == 2) *reinterpret_cast<u32x4*>(a.part + STAMP_OFF + 65536 + (b * 4 + wave) * 1024 + lane * 16) = *reinterpret_cast<const u32x4*>(smem + LDS_TR + 16 + wave * 1024 + lane * 16);
    }
}

template <int TP, int ADV, int TMIN, int WIN, int OPT, int EX, int GS, int HOLD, int HL, int SL, int EV, int ID, int DBG>
static void launch43(void* const* d_in, const int* in_sizes, void* d_out, void* d_ws, hipStream_t stream) {
    typedef const unsigned short* P;
    Args a;
    a.qk = (const int*)d_in[1]; a.qv = (const int*)d_in[4];
    a.k_cos = (P)d_in[9]; a.k_sin = (P)d_in[10];
    a.q = (P)d_in[0]; a.q_cos = (P)d_in[7]; a.q_sin = (P)d_in[8];
    a.sc[0] = (P)d_in[2]; a.sc[1] = (P)d_in[3]; a.sc[2] = (P)d_in[5]; a.sc[3] = (P)d_in[6];
    a.part = (unsigned char*)d_ws;
    a.out = (unsigned short*)d_out;
    kv43_kernel<TP, ADV, TMIN, WIN, OPT, EX, GS, HOLD, HL, SL, EV, ID, DBG><<<Skv / RW, 256, 0, stream>>>(a);
}

extern "C" void kernel_launch(void* const* d_in, const int* in_sizes, int n_in, void* d_out, int out_size, void* d_ws, size_t ws_size, hipStream_t stream) {
    launch43<80*16-2,220,79*16,4+16*32+4096*20,38+64+128+512+1024,66,3,85,100,0x2aaa,5+16*1+256*1,0,0>(d_in, in_sizes, d_out, d_ws, stream);
}
```

```cpp
#include <hip/hip_runtime.h>
#include <hip/hip_bf16.h>
#include <cstdint>
#define KALIGN 256
#define STAMP_OFF (200 << 20)
using bf16 = __hip_bfloat16;
using i32x4 = __attribute__((ext_vector_type(4))) int;
using u32x4 = __attribute__((ext_vector_type(4))) unsigned;
using u32x2 = __attribute__((ext_vector_type(2))) unsigned;
using f32x2 = __attribute__((ext_vector_type(2))) float;
using f32x4 = __attribute__((ext_vector_type(4))) float;

constexpr int DH = 192;
constexpr int HALF = 96;
constexpr int RW = 64;
constexpr int MAXH = 32;

__device__ __forceinline__ float bflo(unsigned u) { return __uint_as_float(u << 16); }
__device__ __forceinline__ float bfhi(unsigned u) { return __uint_as_float(u & 0xffff0000u); }
__device__ __forceinline__ float rbf(float x) { return __bfloat162float(__float2bfloat16(x)); }

template <int CTRL> __device__ __forceinline__ float dpp(float x) {
    return __builtin_bit_cast(float, __builtin_amdgcn_mov_dpp(__builtin_bit_cast(int, x), CTRL, 0xf, 0xf, true));
}
__device__ __forceinline__ float row16_allsum(float x) {
    x += dpp<0x128>(x);
    x += dpp<0x124>(x);
    x += dpp<0x122>(x);
    x += dpp<0x121>(x);
    return x;
}
__device__ __forceinline__ float row16_allmax(float x) {
    x = fmaxf(x, dpp<0x128>(x));
    x = fmaxf(x, dpp<0x124>(x));
    x = fmaxf(x, dpp<0x122>(x));
    x = fmaxf(x, dpp<0x121>(x));
    return x;
}
__device__ __forceinline__ float xrow_max(float x) {
    auto s = __builtin_amdgcn_permlane16_swap(__float_as_uint(x), __float_as_uint(x), false, false);
    x = fmaxf(__uint_as_float(s[0]), __uint_as_float(s[1]));
    auto t = __builtin_amdgcn_permlane32_swap(__float_as_uint(x), __float_as_uint(x), false, false);
    return fmaxf(__uint_as_float(t[0]), __uint_as_float(t[1]));
}
__device__ __forceinline__ float xrow_sum(float x) {
    auto s = __builtin_amdgcn_permlane16_swap(__float_as_uint(x), __float_as_uint(x), false, false);
    x = __uint_as_float(s[0]) + __uint_as_float(s[1]);
    auto t = __builtin_amdgcn_permlane32_swap(__float_as_uint(x), __float_as_uint(x), false, false);
    return __uint_as_float(t[0]) + __uint_as_float(t[1]);
}
__device__ __forceinline__ float sw32(float a, float b) {
    auto t = __builtin_amdgcn_permlane32_swap(__float_as_uint(a), __float_as_uint(b), false, false);
    return __uint_as_float(t[0]) + __uint_as_float(t[1]);
}
__device__ __forceinline__ float sw16(float a, float b) {
    auto t = __builtin_amdgcn_permlane16_swap(__float_as_uint(a), __float_as_uint(b), false, false);
    return __uint_as_float(t[0]) + __uint_as_float(t[1]);
}

#define SB() __builtin_amdgcn_sched_barrier(0)
typedef __amdgpu_buffer_rsrc_t rsrc_t;
__device__ __forceinline__ rsrc_t mkrsrc(const void* p, unsigned bytes) {
    return __builtin_amdgcn_make_buffer_rsrc(const_cast<void*>(p), 0, bytes, 0x00020000);
}
template <int AUX> __device__ __forceinline__ i32x4 bld128(rsrc_t r, unsigned voff, unsigned soff) {
    return __builtin_bit_cast(i32x4, __builtin_amdgcn_raw_buffer_load_b128(r, voff, soff, AUX));
}
template <int AUX> __device__ __forceinline__ unsigned bld16(rsrc_t r, unsigned voff, unsigned soff) {
    return __builtin_amdgcn_raw_buffer_load_b16(r, voff, soff, AUX);
}

struct HG { i32x4 d[3]; };
typedef unsigned long long u64;
typedef __attribute__((address_space(3))) unsigned char lds_u8;

__device__ __forceinline__ unsigned pk_bf16(float lo, float hi) {
    unsigned r;
    asm("v_cvt_pk_bf16_f32 %0, %1, %2" : "=v"(r) : "v"(lo), "v"(hi));
    return r;
}
__device__ __forceinline__ unsigned now32() { return (unsigned)__builtin_amdgcn_s_memrealtime(); }

constexpr int LDS_REC = 0;
constexpr int RECW = MAXH * DH * 4;
constexpr int LDS_CS = 0;
constexpr int LDS_QT = 12288;
constexpr int LDS_PF = 14336;
constexpr int LDS_QRAW = MAXH * 4 * DH * 4;
constexpr int LDS_Q = LDS_QRAW + MAXH * DH * 2;
constexpr int LDS_P = LDS_Q + 4 * DH * 4;
constexpr int LDS_SC = LDS_P + 4 * MAXH * 128;
constexpr int LDS_CNT = LDS_SC + 4 * MAXH * RW * 2;
constexpr int LDS_SK = LDS_CNT + 256;
constexpr int LDS_TR = LDS_SK + 1024;
constexpr int LDS_BYTES = LDS_TR + 4096 + 64;

struct Args {
    const int* qk; const int* qv;
    const unsigned short* k_cos; const unsigned short* k_sin;
    const unsigned short* q; const unsigned short* q_cos; const unsigned short* q_sin;
    unsigned char* part;
    unsigned short* out;
    const unsigned short* sc[4];
};
constexpr unsigned Skv = 16384;
constexpr float SCALE_LOG2 = 0.10411754627697264f;

template <int TP, int ADV, int TMIN, int WIN, int OPT, int EX, int GS, int HOLD, int HL, int SL, int EV, int ID, int DBG>
__global__ __launch_bounds__(256) __attribute__((aligned(KALIGN))) void kv46_kernel(Args a)
{
    __shared__ __attribute__((aligned(16))) unsigned char smem[LDS_BYTES];
    const unsigned tstart = DBG ? now32() : 0;
    constexpr bool DS = (DBG && DBG < 5) || DBG == 24;
    constexpr int H = MAXH, ES = 7, PFN = 16;
    const int b = blockIdx.x;
    const int tid = threadIdx.x;
    const int lane = tid & 63;
    const int wave = __builtin_amdgcn_readfirstlane(tid >> 6);
    typedef const unsigned short* const __attribute__((address_space(4))) * karg_t;
    const unsigned short* sp = ((karg_t)__builtin_amdgcn_kernarg_segment_ptr())[offsetof(Args, sc) / 8 + wave];
    if (b < PFN) {
        u64 pc; unsigned len, tl;
        asm volatile("s_getpc_b64 %0\n.Lkv_a%c3:\n\ts_mov_b32 %1, .Lkv_b%c3-.Lkv_a%c3-16\n\ts_mov_b32 %2, .Lkv_t%c3-.Lkv_a%c3" : "=s"(pc), "=s"(len), "=s"(tl) : "i"(ID));
        const unsigned o = tid * 16;
        asm volatile("s_mov_b32 m0, %5\n\ts_nop 0\n\tglobal_load_lds_dwordx4 %0, %4\n\tglobal_load_lds_dwordx4 %1, %4\n\tglobal_load_lds_dwordx4 %2, %4\n\tglobal_load_lds_dwordx4 %3, %4"
                     :: "v"(o), "v"(o + 4096), "v"(min(o + 8192, tl)), "v"(min(o + tl, len)), "s"(pc), "s"((unsigned)(size_t)(lds_u8*)smem + LDS_PF) : "memory");
    }
    SB();
    const int r = lane >> 4;
    const int c = lane & 15;
    constexpr unsigned hstride = Skv * (DH * 4);
    constexpr unsigned nb = Skv / RW;
    const unsigned lds0 = (unsigned)(size_t)(lds_u8*)smem;
    const unsigned pos = (b & 7) * (nb >> 3) + (b >> 3);
    const unsigned sb = ((((b >> 3) * 13) & 31) << 3) | (b & 7);
    const unsigned row0 = RW * pos + 16 * wave;

    constexpr unsigned LS = 512, psk = 0, pep = 128, AREA = 6u << 20, NAREA = 32, pfl = H * nb * LS;
    const rsrc_t pr = mkrsrc(a.part, (NAREA + 1) * AREA);
    u64 t64;
    {
        unsigned vs = ((lane >> 3) * Skv + RW * pos + (lane & 7) * 8) * 2, v1 = vs + 16 * Skv, v2 = vs + 32 * Skv, v3 = vs + 48 * Skv;
        unsigned ds = lds0 + LDS_SC + wave * 4096;
        asm volatile("" : "+v"(vs), "+v"(v1), "+v"(v2), "+v"(v3), "+s"(ds));
        SB();
        asm volatile("s_memrealtime %0\n\ts_mov_b32 m0, %6\n\ts_nop 0\n\tglobal_load_lds_dwordx4 %1, %5 nt\n\t"
                     "s_add_u32 m0, m0, 1024\n\ts_nop 0\n\tglobal_load_lds_dwordx4 %2, %5 nt\n\t"
                     "s_add_u32 m0, m0, 1024\n\ts_nop 0\n\tglobal_load_lds_dwordx4 %3, %5 nt\n\t"
                     "s_add_u32 m0, m0, 1024\n\ts_nop 0\n\tglobal_load_lds_dwordx4 %4, %5 nt\n\ts_waitcnt lgkmcnt(0)"
                     : "=&s"(t64) : "v"(vs), "v"(v1), "v"(v2), "v"(v3), "s"(sp), "s"(ds),
                       "s"(a.qk), "s"(a.qv), "s"(a.k_cos), "s"(a.k_sin), "s"(a.q), "s"(a.q_cos), "s"(a.q_sin), "s"(a.part), "s"(a.out) : "memory", "scc");
    }
    SB();
    const unsigned tdma = (unsigned)t64;
#define DMA3(V, A, B, P) "s_mov_b32 m0, " B "\n\ts_nop 0\n\t" \
                     "global_load_lds_dwordx4 " V ", " A P "\n\t" \
                     "global_load_lds_dwordx4 " V ", " A " offset:1024" P "\n\t" \
                     "global_load_lds_dwordx4 " V ", " A " offset:2048" P "\n\t"
    asm volatile(DMA3("%0", "%1", "%2", "") "s_mov_b32 m0, %4\n\ts_nop 0\n\tglobal_load_lds_dwordx4 %3, off"
                 :: "v"(lane * 16), "s"(a.q + wave * 1536), "s"(lds0 + LDS_QRAW + wave * 3072),
                    "v"(lane < 48 ? (const unsigned char*)((lane < 24 ? a.q_cos : a.q_sin - DH) + 8 * lane) : a.part + pep), "s"(lds0 + LDS_QT + wave * RECW) : "memory");
    SB();
    const int stag = (OPT & 32) ? (int)(((sb & ~7) + wave * 2 + (b & 1)) * (TP / 2)) / 256 - TP / 4 : (int)(((OPT & 2) ? (((b >> 3) * 26 + wave * 2 + (b & 1)) & 63) : ((((b >> 3) * 4 + wave) * 37) & 63)) * (TP / 2)) / 64 - TP / 4;
    unsigned sched = ((tdma + ADV) << 4) + stag - 4 * (TP / 2);
    unsigned vt = tdma << 4;
    int tru = -4;
    unsigned step = TP / 2, tmin = TMIN / 2, vth = WIN & 15;
    auto pace = [&](bool ctl = false) {
        unsigned tgt = sched;
        sched += step;
        if ((int)(vt - tgt) > 0) tgt = vt;
        unsigned t = now32() << 4;
        while ((int)(t - tgt) < 0) { __builtin_amdgcn_s_sleep(1); t = now32() << 4; }
        vt = ((int)(t - 128 - tgt) > 0 ? t - 128 : tgt) + tmin;
        if (ctl) {
            if constexpr (WIN > 0 && WIN < 16) asm volatile("s_waitcnt vmcnt(%0)" :: "i"(WIN) : "memory");
            if constexpr (WIN >= 16) {
                const unsigned vm = __builtin_amdgcn_s_getreg(7 | (3 << 11));
                if (vm > vth && (int)(vt - sched) < (WIN >> 12) * 16) vt += (vm - vth) * ((WIN >> 4) & 255);
            }
        }
        if constexpr (DBG == 2) { *reinterpret_cast<unsigned*>(smem + LDS_TR + 16 + wave * 1024 + (tru & 255) * 4) = ((t - (sched - step)) >> 4) | ((OPT & 8) ? (__builtin_amdgcn_s_getreg(7 | (3 << 11)) << 16) : 0); ++tru; }
    };
    {
        unsigned dst = lds0 + LDS_CS + wave * RECW;
        const unsigned v0 = lane * 16 + row0 * (DH * 2);
#pragma unroll 1
        for (int j = 0; j < 4; ++j) {
            pace();
            asm volatile(DMA3("%0", "%1", "%2", " nt") :: "v"(v0), "s"((j < 2 ? a.k_cos : a.k_sin) + (j & 1) * 8 * DH), "s"(dst) : "memory");
            dst += 3072;
        }
    }
    if (tid < MAXH) *reinterpret_cast<unsigned*>(smem + LDS_CNT + tid * 4) = 0;
    const rsrc_t kr = mkrsrc(a.qk, H * hstride);
    const rsrc_t vr = mkrsrc(a.qv, H * hstride);
    const unsigned kvoff = (row0 + r) * (DH * 4) + c * 16;
    auto issue = [&](HG& g, rsrc_t rs, unsigned so) {
#pragma unroll
        for (int x = 0; x < 3; ++x) g.d[x] = bld128<2>(rs, kvoff + 256 * x, so);
    };
    HG buf[4];
#define PRE(g) SB(); pace(); issue(buf[g], kr, (g) * (4 * DH * 4)); SB();
    asm volatile("s_waitcnt vmcnt(12) lgkmcnt(0)\n\ts_barrier" ::: "memory");
    unsigned tbar = DS ? now32() : 0;
    PRE(0)
    const unsigned qoff = (lane < 48 ? lane : lane - 48) * 4;
    const unsigned* qt = reinterpret_cast<const unsigned*>(smem + LDS_QT + wave * RECW + qoff);
    unsigned qc0 = qt[0], qc1 = qt[HALF / 2], qs0 = qt[DH / 2], qs1 = qt[(DH + HALF) / 2];
    u64 tag;
    {
        const u32x2 e = *reinterpret_cast<const u32x2*>(smem + LDS_QT + wave * RECW + 768);
        tag = (((u64)__builtin_amdgcn_readfirstlane(e[1]) << 32) | __builtin_amdgcn_readfirstlane(e[0])) * 6364136223846793005ull + 1442695040888963407ull;
        asm volatile("" : "+s"(tag));
    }
    const unsigned t32 = tag >> 32;
    const unsigned char* qraw = smem + LDS_QRAW + qoff;
    unsigned char* qrot = smem + LDS_Q + wave * (DH * 4) + 2 * qoff;
    auto rotq = [&]() {
        const unsigned q_lo = *reinterpret_cast<const unsigned*>(qraw), q_hi = *reinterpret_cast<const unsigned*>(qraw + HALF * 2);
        qraw += DH * 2;
        const f32x2 lo = f32x2{bflo(q_lo), bfhi(q_lo)}, hi = f32x2{bflo(q_hi), bfhi(q_hi)};
        const f32x2 y1 = lo * f32x2{bflo(qc0), bfhi(qc0)}, y2 = -hi * f32x2{bflo(qs0), bfhi(qs0)};
        const f32x2 y3 = hi * f32x2{bflo(qc1), bfhi(qc1)}, y4 = lo * f32x2{bflo(qs1), bfhi(qs1)};
        const unsigned x1 = pk_bf16(y1[0], y1[1]), x2 = pk_bf16(y2[0], y2[1]), x3 = pk_bf16(y3[0], y3[1]), x4 = pk_bf16(y4[0], y4[1]);
        const f32x2 z1 = f32x2{bflo(x1), bfhi(x1)} + f32x2{bflo(x2), bfhi(x2)}, z2 = f32x2{bflo(x3), bfhi(x3)} + f32x2{bflo(x4), bfhi(x4)};
        const unsigned l2 = pk_bf16(z1[0], z1[1]), h2 = pk_bf16(z2[0], z2[1]);
        *reinterpret_cast<f32x2*>(qrot) = f32x2{bflo(l2), bfhi(l2)};
        *reinterpret_cast<f32x2*>(qrot + HALF * 4) = f32x2{bflo(h2), bfhi(h2)};
        asm volatile("" ::: "memory");
    };
    f32x4 qa[3], qb[3];
    const unsigned char* qh = smem + LDS_Q + wave * (DH * 4) + 16 * c;
    const unsigned char* qh1 = qh + (c < 8 ? 64 + HALF : 64 - HALF) * 4;
    auto loadq = [&]() {
        asm volatile("" ::: "memory");
#pragma unroll
        for (int x = 0; x < 3; ++x) qa[x] = *reinterpret_cast<const f32x4*>(qh + 256 * x);
        qb[0] = *reinterpret_cast<const f32x4*>(qh + HALF * 4);
        qb[1] = *reinterpret_cast<const f32x4*>(qh1);
        qb[2] = *reinterpret_cast<const f32x4*>(qh + (128 - HALF) * 4);
        asm volatile("" ::: "memory");
    };
    rotq();
    constexpr unsigned SKS = (OPT & 4096) ? 256 : 16;
    if (b < 8 && wave == 3) __builtin_amdgcn_raw_buffer_store_b128(u32x4{tdma, t32, tdma, (unsigned)tag}, pr, lane < ((OPT & 4096) ? 8 : 1) ? psk + b * SKS + lane * 16 : 0x80000000u, 0, 16);
    PRE(1)
    f32x2 cc[4][3][2], ss[4][3][2];
    const unsigned char* cb = smem + LDS_CS + wave * RECW + r * (DH * 2) + c * 8;
    asm volatile("s_waitcnt vmcnt(12)" ::: "memory");
#pragma unroll
    for (int k = 0; k < 4; ++k)
#pragma unroll
        for (int x = 0; x < 3; ++x) {
            const u32x2 t = *reinterpret_cast<const u32x2*>(cb + k * 1536 + 128 * x);
            cc[k][x][0] = f32x2{bflo(t[0]), bfhi(t[0])}; cc[k][x][1] = f32x2{bflo(t[1]), bfhi(t[1])};
            asm volatile("" : "+v"(cc[k][x][0]), "+v"(cc[k][x][1]));
        }
    PRE(2)
    loadq();
    const unsigned char* scp = smem + LDS_SC + (c & 4) * 1024 + (16 * wave + 4 * (c & 3) + r) * 2;
    unsigned sc = *reinterpret_cast<const unsigned short*>(scp);
    auto loadsin = [&](int k) {
        const unsigned char* sb1 = cb + (c < 8 ? (64 + HALF) * 2 : (64 - HALF) * 2);
        const unsigned sg = c < 8 ? 0u : 0x80008000u;
        u32x2 t[3];
        t[0] = *reinterpret_cast<const u32x2*>(cb + 6144 + k * 1536 + HALF * 2);
        t[1] = *reinterpret_cast<const u32x2*>(sb1 + 6144 + k * 1536) ^ u32x2{sg, sg};
        t[2] = *reinterpret_cast<const u32x2*>(cb + 6144 + k * 1536 + (128 - HALF) * 2) ^ u32x2{0x80008000u, 0x80008000u};
#pragma unroll
        for (int x = 0; x < 3; ++x) {
            ss[k][x][0] = f32x2{bflo(t[x][0]), bfhi(t[x][0])}; ss[k][x][1] = f32x2{bflo(t[x][1]), bfhi(t[x][1])};
            asm volatile("" : "+v"(ss[k][x][0]), "+v"(ss[k][x][1]));
        }
    };
    asm volatile("s_waitcnt vmcnt(12)" ::: "memory");
    loadsin(0); loadsin(1);
    PRE(3)
    asm volatile("s_waitcnt vmcnt(12)" ::: "memory");
    loadsin(2); loadsin(3);
    unsigned th1 = 0, th4 = 0;
    unsigned char* prec = smem + LDS_P + wave * (H * 128) + r * 32 + (c & 7) * 4;
    auto softmax = [&](float s, int h) {
        float m = fmaxf(s, dpp<0xb1>(s));
        m = xrow_max(fmaxf(m, dpp<0x4e>(m)));
        const float p = __builtin_amdgcn_exp2f(s - m);
        float l = p + dpp<0xb1>(p);
        l = xrow_sum(l + dpp<0x4e>(l));
        const float t = (c & 1) ? l : m;
        const int x = __builtin_amdgcn_update_dpp(__float_as_int(p), __float_as_int(t), 0x114, 0xf, 0xa, false);
        *reinterpret_cast<int*>(prec + h * 128) = x;
    };
    float sprev = 0.f;

#pragma unroll 1
    for (int h = 0; h < H; ++h) {
        const bool more = h + 1 < H;
        const rsrc_t nx = more ? kr : vr;
        const unsigned nso = more ? (h + 1) * hstride : 0;
        float scf = bflo(sc);
        asm volatile("" : "+v"(scf) :: "memory");
        SB();
        scp += more ? 128 : 4096 + 128;
        sc = *reinterpret_cast<const unsigned short*>(scp);
        SB();
        float sreg = 0.f;
#define KSTEP(k) { \
            const float kz = dpp<0x154 + (k)>(scf); \
            f32x2 kz2 = f32x2{kz, kz}; \
            asm("" : "+v"(kz2)); \
            f32x2 a2 = f32x2{0.f, 0.f}; \
            _Pragma("unroll") for (int x = 0; x < 3; ++x) { \
                _Pragma("unroll") for (int e = 0; e < 2; ++e) { \
                    const f32x2 w = __builtin_elementwise_fma(f32x2{qb[x][2 * e], qb[x][2 * e + 1]}, ss[k][x][e], f32x2{qa[x][2 * e], qa[x][2 * e + 1]} * cc[k][x][e]); \
                    const f32x2 kf = f32x2{(float)buf[k].d[x][2 * e], (float)buf[k].d[x][2 * e + 1]} - kz2; \
                    a2 = __builtin_elementwise_fma(kf, w, a2); \
                } \
            } \
            float sa = a2[0] + a2[1]; \
            asm volatile("" : "+v"(sa) :: "memory"); \
            SB(); pace(true); issue(buf[k], nx, nso + (k) * (4 * DH * 4)); SB(); \
            if ((k) == 0) { softmax(sprev, (h - 1) & (H - 1)); SB(); }   \
            if ((k) == 1) { rotq(); SB(); } \
            if ((k) == 2) { \
                if (wave == 1 && h == ((OPT & 4) ? 1 + ((b >> 3) & 3) : 4 + ((b >> 3) & 15))) \
                    asm volatile("s_mov_b32 m0, %2\n\ts_nop 0\n\tglobal_load_lds_dwordx4 %0, %1 sc1" :: "v"(psk + (lane & 7) * SKS), "s"(a.part), "s"(lds0 + LDS_SK) : "memory"); \
                if (h == ((OPT & 4) ? 6 : 25)) {        \
                    const u32x2 e = *reinterpret_cast<const u32x2*>(smem + LDS_SK + (lane & 7) * 16); \
                    const int d = (int)(tdma - e[0]); \
                    if (__all(e[1] == t32 && d > -400 && d < 400)) { \
                        int sum = d + __builtin_amdgcn_mov_dpp(d, 0xb1, 0xf, 0xf, true); \
                        sum += __builtin_amdgcn_mov_dpp(sum, 0x4e, 0xf, 0xf, true); \
                        sum += __builtin_amdgcn_mov_dpp(sum, 0x124, 0xf, 0xf, true); \
                        const int adj = (__builtin_amdgcn_readfirstlane(sum) * ((OPT & 4) ? 73 : 112) + 4096) >> 13;        \
                        step -= adj; tmin -= adj; \
                        if ((OPT & 64) && (OPT & 384) && b < ((OPT & 256) ? 256 : 16) && wave == 0) {        \
                            unsigned t0, t1, t2, t3; \
                            const unsigned ar = (((__builtin_amdgcn_readfirstlane(e[0]) >> 13) & (NAREA - 1)) + 1) * AREA; \
                            asm volatile("s_load_dword %0, %4, %5\n\ts_load_dword %1, %4, %6\n\ts_load_dword %2, %4, %7\n\ts_load_dword %3, %4, %8\n\ts_waitcnt lgkmcnt(0)" : "=&s"(t0), "=&s"(t1), "=&s"(t2), "=&s"(t3) : "s"(a.part), "s"(ar), "s"(ar + AREA - 128), "s"(ar + (2u << 20) + 128), "s"(ar + AREA - (2u << 20) - 128) : "memory"); \
                            vt = now32() << 4; \
                        } \
                    } \
                } \
                SB(); \
            } \
            if ((k) == 3) { loadq(); SB(); } \
            sa = row16_allsum(sa); \
            sreg = (c & 3) == (k) ? sa : sreg; }
        KSTEP(0) KSTEP(1) KSTEP(2) KSTEP(3)
        sprev = sreg * (scf * SCALE_LOG2);
        if (DS) { if (h == 7) th1 = now32(); }
    }
    softmax(sprev, H - 1);
    if (DS) th4 = now32();

    const unsigned tg = (unsigned)(tag >> (16 * (lane & 3))) << 16;
    unsigned rl = lds0 + LDS_REC + lane * 4, rh = rl + 2 * RECW;
    auto pack = [&](f32x2 t, float u) { return u32x2{pk_bf16(t[0], t[1]), pk_bf16(u, 0.f) | tg}; };
    auto ldrec = [&](unsigned o, f32x2 (&p)[6]) {
        asm volatile("ds_read2st64_b32 %0, %6 offset1:1\n\tds_read2st64_b32 %1, %6 offset0:96 offset1:97\n\tds_read2st64_b32 %2, %7 offset1:1\n\tds_read2st64_b32 %3, %7 offset0:96 offset1:97\n\t"
                     "ds_read2st64_b32 %4, %6 offset0:2 offset1:98\n\tds_read2st64_b32 %5, %7 offset0:2 offset1:98"
                     : "=&v"(p[0]), "=&v"(p[1]), "=&v"(p[2]), "=&v"(p[3]), "=&v"(p[4]), "=&v"(p[5]) : "v"(rl + o), "v"(rh + o) : "memory");
    };
    auto merge = [&](int hd) {
        f32x2 p[6], ma, mb;
        asm volatile("ds_read2st64_b32 %0, %2 offset1:16\n\tds_read2st64_b32 %1, %2 offset0:32 offset1:48" : "=&v"(ma), "=&v"(mb) : "v"(lds0 + LDS_P + hd * 128 + 16) : "memory");
        ldrec(hd * (DH * 4), p);
        asm volatile("s_waitcnt lgkmcnt(6)" : "+v"(ma), "+v"(mb) :: "memory");
        const float M = __builtin_amdgcn_fmed3f(__builtin_amdgcn_fmed3f(ma[0], ma[1], __builtin_inff()), __builtin_amdgcn_fmed3f(mb[0], mb[1], __builtin_inff()), __builtin_inff());
        ma = f32x2{__builtin_amdgcn_exp2f(ma[0] - M), __builtin_amdgcn_exp2f(ma[1] - M)};
        mb = f32x2{__builtin_amdgcn_exp2f(mb[0] - M), __builtin_amdgcn_exp2f(mb[1] - M)};
        asm volatile("s_waitcnt lgkmcnt(0)" : "+v"(ma), "+v"(mb), "+v"(p[0]), "+v"(p[1]), "+v"(p[2]), "+v"(p[3]), "+v"(p[4]), "+v"(p[5]) :: "memory");
        const f32x2 u = __builtin_elementwise_fma(mb, p[5], ma * p[4]);
        return pack(ma[0] * p[0] + ma[1] * p[1] + mb[0] * p[2] + mb[1] * p[3], u[0] + u[1]);
    };
    constexpr int NC = 4 * H;
    const int cn = sb;
    const unsigned hc = 4 * (cn & 7) + ((cn >> 3) & 3);
    unsigned old = 0, area = 0, fo = 0;
    u32x4 flv = {};
    float gl = 0.f, mw = 0.f;
    f32x2 fs[4] = {};
    constexpr int LDS_ML = LDS_QRAW + 2048;
#define BAD(y) ((unsigned)(y)[1] != t32 || (unsigned)(y)[3] != (unsigned)tag)
    auto ev_ml = [&]() {
        f32x2 ml[4];
#pragma unroll
        for (int w = 0; w < 4; ++w) ml[w] = *reinterpret_cast<const f32x2*>(smem + LDS_P + w * (H * 128) + (8 * wave + (lane & 7)) * 128 + 16);
        const float M = fmaxf(fmaxf(ml[0][0], ml[1][0]), fmaxf(ml[2][0], ml[3][0]));
        const float L = (__builtin_amdgcn_exp2f(ml[0][0] - M) * ml[0][1] + __builtin_amdgcn_exp2f(ml[1][0] - M) * ml[1][1]) + (__builtin_amdgcn_exp2f(ml[2][0] - M) * ml[2][1] + __builtin_amdgcn_exp2f(ml[3][0] - M) * ml[3][1]);
        flv = u32x4{__float_as_uint(M), t32, __float_as_uint(L), (unsigned)tag};
        asm volatile("" : "+v"(flv));
    };
    auto ev_area = [&]() {
        u32x4 e = *reinterpret_cast<const u32x4*>(smem + LDS_SK);
        float mv[4];
#pragma unroll
        for (int w = 0; w < 4; ++w) mv[w] = *reinterpret_cast<const float*>(smem + LDS_P + ((wave + w) & 3) * (H * 128) + (H - 1) * 128 + 16);
        gl = __builtin_amdgcn_exp2f(mv[0] - fmaxf(fmaxf(mv[0], mv[1]), fmaxf(mv[2], mv[3])));
        if (__builtin_expect(__any(BAD(e)), 0)) {
#pragma unroll 1
            for (int n = 0; n < (1 << 20) && BAD(e); ++n)
                asm volatile("global_load_dwordx4 %0, %1, %2 sc1\n\ts_waitcnt vmcnt(0)" : "=v"(e) : "v"(psk), "s"(a.part) : "memory");
        }
        area = (((__builtin_amdgcn_readfirstlane(e[0]) >> 13) & (NAREA - 1)) + 1) * AREA + 4096;
        asm volatile("" : "+s"(area), "+v"(gl));
    };
    auto ev_put = [&]() {
        asm volatile("s_mov_b64 exec, 0xff\n\tbuffer_store_dwordx4 %0, %1, %2, %3 offen sc1\n\ts_mov_b64 exec, -1" :: "v"(flv), "v"(4 * (rl - lds0 - LDS_REC)), "s"(pr), "s"(area + pfl + (wave * nb + sb) * 128) : "memory");
    };
    auto ev_get = [&]() {
        unsigned l = lane;
        asm volatile("" : "+v"(l));
        fo = area + pfl + ((hc >> 3) * nb + 8 * wave) * 128 + (hc & 7) * 16 + (((l & 56) << 2) + (l & 7)) * 128;
        asm volatile("s_mov_b32 m0, %2\n\ts_nop 0\n\tglobal_load_lds_dwordx4 %0, %1 sc1" :: "v"(fo), "s"(a.part), "s"(lds0 + LDS_ML + wave * 1024) : "memory");
    };
    auto ev_w1 = [&]() {
        u32x4 y = *reinterpret_cast<const u32x4*>(smem + LDS_ML + wave * 1024 + lane * 16);
        if (__builtin_expect(__any(BAD(y)), 0)) {
#pragma unroll 1
            for (int n = 0; n < (1 << 20) && BAD(y); ++n)
                asm volatile("global_load_dwordx4 %0, %1, %2 sc1\n\ts_waitcnt vmcnt(0)" : "=v"(y) : "v"(fo), "s"(a.part) : "memory");
        }
        const float m = __uint_as_float(y[0]);
        float u;
        asm volatile("s_nop 1\n\tv_max_f32_dpp %0, %2, %2 row_ror:8 row_mask:0xf bank_mask:0xf\n\ts_nop 1\n\tv_max_f32_dpp %0, %0, %0 row_ror:4 row_mask:0xf bank_mask:0xf\n\ts_nop 1\n\t"
                     "v_max_f32_dpp %0, %0, %0 row_ror:2 row_mask:0xf bank_mask:0xf\n\ts_nop 1\n\tv_max_f32_dpp %0, %0, %0 row_ror:1 row_mask:0xf bank_mask:0xf\n\tv_mov_b32 %1, %0\n\ts_nop 1\n\t"
                     "v_permlane16_swap_b32 %0, %1\n\ts_nop 0\n\tv_max_f32 %0, %0, %1\n\tv_mov_b32 %1, %0\n\ts_nop 1\n\tv_permlane32_swap_b32 %0, %1\n\ts_nop 0\n\tv_max_f32 %0, %0, %1" : "=&v"(mw), "=&v"(u) : "v"(m));
        fs[0][0] = __builtin_amdgcn_exp2f(m - mw);
        fs[0][1] = fs[0][0] * __uint_as_float(y[2]);
        asm volatile("" : "+v"(fs[0]));
    };
    auto ev_w2 = [&]() {
        const f32x2 f = fs[0];
        unsigned bp = (lane >> 3) * 4;
        asm volatile("ds_bpermute_b32 %0, %8, %9\n\tds_bpermute_b32 %1, %8, %9 offset:32\n\tds_bpermute_b32 %2, %8, %9 offset:64\n\tds_bpermute_b32 %3, %8, %9 offset:96\n\t"
                     "ds_bpermute_b32 %4, %8, %9 offset:128\n\tds_bpermute_b32 %5, %8, %9 offset:160\n\tds_bpermute_b32 %6, %8, %9 offset:192\n\tds_bpermute_b32 %7, %8, %9 offset:224"
                     : "=&v"(fs[0][0]), "=&v"(fs[0][1]), "=&v"(fs[1][0]), "=&v"(fs[1][1]), "=&v"(fs[2][0]), "=&v"(fs[2][1]), "=&v"(fs[3][0]), "=&v"(fs[3][1]) : "v"(bp), "v"(f[0]) : "memory");
        *reinterpret_cast<f32x2*>(smem + LDS_QRAW + 1024 + wave * 8) = f32x2{mw, xrow_sum(row16_allsum(f[1]))};
        asm volatile("s_waitcnt lgkmcnt(0)" : "+v"(fs[0]), "+v"(fs[1]), "+v"(fs[2]), "+v"(fs[3]) :: "memory");
    };
    constexpr int G1 = EV & 15, G2 = (EV >> 4) & 15, G3 = (EV >> 8) & 15;
    const int hput = G1 + G2 + 1 + ((b >> 3) & 3), hget = cn < NC ? G1 + G2 + 6 + ((b >> 3) & 3) : -9;
    const int htch = cn < NC && wave == 0 ? (EV >> 12) + ((b >> 3) & 3) : -9;
    float pv = *reinterpret_cast<const float*>(prec);
    f32x2 acc[3][2];
    float y;
    auto prep = [&]() {
        y = bflo(sc);
        y = (c & 4) ? y : y * pv;
        asm volatile("" : "+v"(y) :: "memory");
        SB();
        scp += 128;
        sc = *reinterpret_cast<const unsigned short*>(scp);
        prec += 128;
        pv = *reinterpret_cast<const float*>(prec);
        SB();
    };
#define VCOMP(k) { \
        const float pw = dpp<0x150 + (k)>(y); \
        const float vz = dpp<0x154 + (k)>(y); \
        f32x2 pw2 = f32x2{pw, pw}, vz2 = f32x2{vz, vz}; \
        asm("" : "+v"(pw2), "+v"(vz2)); \
        _Pragma("unroll") for (int x = 0; x < 3; ++x) \
            _Pragma("unroll") for (int e = 0; e < 2; ++e) { \
                const f32x2 vf = f32x2{(float)buf[k].d[x][2 * e], (float)buf[k].d[x][2 * e + 1]} - vz2; \
                acc[x][e] = (k) == 0 ? pw2 * vf : __builtin_elementwise_fma(pw2, vf, acc[x][e]); \
            } \
        asm volatile("" : "+v"(acc[0][0]), "+v"(acc[0][1]), "+v"(acc[1][0]), "+v"(acc[1][1]), "+v"(acc[2][0]), "+v"(acc[2][1]) :: "memory"); \
        SB(); }
#define VSTEP(k, hn) { \
        VCOMP(k) pace(true); issue(buf[k], vr, (hn) * hstride + (k) * (4 * DH * 4)); SB(); \
        if ((k) == 1) { tmin = (hn) == H - 1 ? 0 : tmin; } \
        if ((k) == 1 && (hn) == G1) { asm volatile("s_waitcnt lgkmcnt(0)\n\ts_barrier" ::: "memory"); SB(); } \
        if ((k) == 2 && (hn) == G1) { ev_ml(); SB(); } \
        if ((k) == 2 && (hn) == G1 + G2) { ev_area(); SB(); } \
        if ((k) == 1 && (OPT & 1024)) { vth = WIN & 15; asm volatile("" : "+s"(vth)); } \
        if ((k) == 1 && (hn) == hput) { ev_put(); if (OPT & 1024) vth = 15; SB(); } \
        if ((k) == 1 && (hn) == hget) { ev_get(); if (OPT & 1024) vth = 15; SB(); } \
        if ((k) == 2 && (hn) == hget + 2) { ev_w1(); SB(); } \
        if ((OPT & 2048) && (k) == 2 && (hn) == htch) { asm volatile("s_mov_b32 m0, %2\n\ts_mov_b64 exec, 63\n\tglobal_load_lds_dword %0, %1\n\ts_mov_b64 exec, -1" :: "v"(hc * (DH * 2) + 16 * (rl - lds0 - LDS_REC)), "s"(a.out), "s"(lds0 + LDS_ML + 4096) : "memory"); vth = 15; SB(); } \
        if ((k) == 1 && (hn) == hget + 2 + G3) { ev_w2(); SB(); } }
    auto record = [&](unsigned ra, unsigned ca) {
        f32x4 o;
#pragma unroll
        for (int e = 0; e < 4; ++e) {
            const float t0 = sw32(acc[0][e >> 1][e & 1], acc[2][e >> 1][e & 1]);
            const float t1 = sw32(acc[1][e >> 1][e & 1], acc[1][e >> 1][e & 1]);
            o[e] = sw16(t0, t1);
        }
        asm volatile("s_mov_b64 exec, %5\n\tds_write_b128 %1, %2\n\ts_mov_b64 exec, 1\n\tds_add_rtn_u32 %0, %3, %4\n\ts_mov_b64 exec, -1"
                     : "+v"(old) : "v"(ra), "v"(o), "v"(ca), "v"(1u), "s"(0xffffffffffffull) : "memory");
        SB();
    };
#pragma unroll 1
    for (int h = 0; h < H - 1; ++h) {
        prep();
        VSTEP(0, h + 1)
        asm volatile("s_waitcnt lgkmcnt(0)" : "+v"(old) :: "memory");
        if (__builtin_amdgcn_readfirstlane(old) == 3) *reinterpret_cast<u32x2*>(smem + LDS_REC + (h - 1) * (DH * 4) + lane * 8) = merge(h - 1);
        SB();
        VSTEP(1, h + 1) VSTEP(2, h + 1) VSTEP(3, h + 1)
        record(lds0 + LDS_REC + h * (DH * 4) + wave * RECW + lane * 16, lds0 + LDS_CNT + h * 4);
    }
    asm volatile("s_branch .Lkv_t%c0\n\t.p2align 12,,3327\n.Lkv_t%c0:" :: "i"(ID));
    y = bflo(sc);
    y = (c & 4) ? y : y * (pv * gl);
    VCOMP(0)
    asm volatile("s_waitcnt lgkmcnt(0)\n\ts_barrier" ::: "memory");
    u32x4 ev[4];
#pragma unroll
    for (int p = 0; p < 4; ++p) ev[p] = *reinterpret_cast<const u32x4*>(smem + LDS_REC + (8 * wave + 2 * p + (lane >> 5)) * (DH * 4) + (lane & 31) * 16);
    const f32x4 a0 = *reinterpret_cast<const f32x4*>(smem + LDS_QRAW + 1024), a1 = *reinterpret_cast<const f32x4*>(smem + LDS_QRAW + 1040);
    SB();
    VCOMP(1)
    unsigned eo = (lane >> 5) * (nb * LS) + (lane & 31) * 16, e8 = lane * 8, ra = lds0 + LDS_REC + (H - 1) * (DH * 4) + wave * RECW + lane * 16, ca = lds0 + LDS_CNT + (H - 1) * 4;
    unsigned es[5];
#pragma unroll
    for (int p = 0; p < 4; ++p) es[p] = area + ((8 * wave + 2 * p) * nb + sb) * LS;
    es[4] = area + ((H - 1) * nb + sb) * LS;
    unsigned tx = sched - step - stag + EX * 16 + (sb >> 3) * GS * 4;
    if (wave == 3) { const u32x2 t = merge(H - 2); ev[3][0] = t[0]; ev[3][1] = t[1]; }
    {
        const float M = fmaxf(fmaxf(a0[0], a0[2]), fmaxf(a1[0], a1[2]));
        const float gw = __builtin_amdgcn_exp2f(mw - M) * __builtin_amdgcn_rcpf((__builtin_amdgcn_exp2f(a0[0] - M) * a0[1] + __builtin_amdgcn_exp2f(a0[2] - M) * a0[3]) + (__builtin_amdgcn_exp2f(a1[0] - M) * a1[1] + __builtin_amdgcn_exp2f(a1[2] - M) * a1[3]));
#pragma unroll
        for (int i = 0; i < 4; ++i) fs[i] *= gw;
    }
    rl += (H - 1) * (DH * 4); rh += (H - 1) * (DH * 4);
    asm volatile("" : "+v"(fs[0]), "+v"(fs[1]), "+v"(fs[2]), "+v"(fs[3]), "+v"(ev[0]), "+v"(ev[1]), "+v"(ev[2]), "+v"(ev[3]), "+v"(eo), "+v"(e8), "+v"(ra), "+v"(ca), "+v"(rl), "+v"(rh),
                      "+s"(es[0]), "+s"(es[1]), "+s"(es[2]), "+s"(es[3]), "+s"(es[4]), "+s"(tx));
    SB();
    VCOMP(2)
    while ((int)((now32() << 4) - tx) < 0) {}
    if constexpr ((OPT & 512) != 0)
    asm volatile("s_waitcnt vmcnt(0)\n\tbuffer_store_dwordx4 %3, %7, %9, %10 offen sc1\n\tbuffer_store_dwordx4 %4, %7, %9, %11 offen sc1\n\tbuffer_store_dwordx4 %5, %7, %9, %12 offen sc1\n\t"
                 "s_cmp_eq_u32 %14, 3\n\ts_cbranch_scc1 1f\n\tbuffer_store_dwordx4 %6, %7, %9, %13 offen sc1\n\ts_branch 2f\n1:\tbuffer_store_dwordx2 %15, %8, %9, %13 offen sc1\n2:"
                 : "+v"(buf[3].d[0]), "+v"(buf[3].d[1]), "+v"(buf[3].d[2]) : "v"(ev[0]), "v"(ev[1]), "v"(ev[2]), "v"(ev[3]), "v"(eo), "v"(e8), "s"(pr), "s"(es[0]), "s"(es[1]), "s"(es[2]), "s"(es[3]), "s"(wave),
                   "v"(u32x2{ev[3][0], ev[3][1]}) : "memory", "scc");
    else
    asm volatile("s_waitcnt vmcnt(0)\n\tbuffer_store_dwordx4 %3, %7, %9, %10 offen sc1\n\tbuffer_store_dwordx4 %4, %7, %9, %11 offen sc1\n\tbuffer_store_dwordx4 %5, %7, %9, %12 offen sc1\n\t"
                 "s_cmp_eq_u32 %14, 3\n\ts_cselect_b64 exec, 0, -1\n\tbuffer_store_dwordx4 %6, %7, %9, %13 offen sc1\n\ts_not_b64 exec, exec\n\tbuffer_store_dwordx2 %15, %8, %9, %13 offen sc1\n\ts_mov_b64 exec, -1"
                 : "+v"(buf[3].d[0]), "+v"(buf[3].d[1]), "+v"(buf[3].d[2]) : "v"(ev[0]), "v"(ev[1]), "v"(ev[2]), "v"(ev[3]), "v"(eo), "v"(e8), "s"(pr), "s"(es[0]), "s"(es[1]), "s"(es[2]), "s"(es[3]), "s"(wave),
                   "v"(u32x2{ev[3][0], ev[3][1]}) : "memory", "scc");
    if (DBG == 4) tbar = now32();
    SB();
    VCOMP(3)
    asm volatile("" :: "v"(ev[0]), "v"(ev[1]), "v"(ev[2]), "v"(ev[3]));
    record(ra, ca);
    asm volatile("s_waitcnt lgkmcnt(0)" : "+v"(old) :: "memory");
    if (__builtin_amdgcn_readfirstlane(old) == 3) {
        f32x2 p[6];
        ldrec(0, p);
        asm volatile("s_waitcnt lgkmcnt(0)" : "+v"(p[0]), "+v"(p[1]), "+v"(p[2]), "+v"(p[3]), "+v"(p[4]), "+v"(p[5]) :: "memory");
        const f32x2 u = p[4] + p[5];
        __builtin_amdgcn_raw_buffer_store_b64(pack((p[0] + p[1]) + (p[2] + p[3]), u[0] + u[1]), pr, e8, es[4], 16);
    }
    unsigned tend = DS ? now32() : 0;
    unsigned tfl = 0, tpl = 0;

    if (cn < NC) {
        unsigned tgt = sched - stag + (hc == H - 1 ? HL : HOLD) * 16;
        unsigned rs[8];
#pragma unroll
        for (int i = 0; i < 8; ++i) rs[i] = area + (hc * nb + 32 * i + 8 * wave) * LS + (cn >> 5) * 128;
        const unsigned g = lane & 7, e = (lane >> 3) & 1;
        unsigned xo = (lane >> 3) * LS + g * 16;
        unsigned oo = r == 3 ? 0x80000000u : (hc * DH + (cn >> 5) * 16 + 2 * g + (r == 2 ? 128 + e : r + 64 * e)) * 2;
        unsigned po = lds0 + LDS_QRAW + lane * 4, wo = po + wave * 256;
        unsigned tb = (lane & 1) ? t32 : (unsigned)tag, ta = tb << 16;
        const rsrc_t orr = mkrsrc(a.out, H * DH * 2);
        asm volatile("" : "+s"(rs[0]), "+s"(rs[1]), "+s"(rs[2]), "+s"(rs[3]), "+s"(rs[4]), "+s"(rs[5]), "+s"(rs[6]), "+s"(rs[7]), "+v"(po), "+v"(wo), "+v"(xo), "+v"(oo), "+v"(ta), "+v"(tb), "+s"(tgt) : "s"(orr) : "memory");
        i32x4 x[8];
        while ((int)((now32() << 4) - tgt) < 0) {}
        if (DS) tfl = now32();
#define XL(i) x[i] = bld128<16>(pr, xo, rs[i]); SB();
#define XS(i) XL(i) __builtin_amdgcn_s_sleep((SL >> (2 * (i))) & 3); SB();
        XS(0) XS(1) XS(2) XS(3) XS(4) XS(5) XS(6) XL(7)
        if (DBG == 4) th4 = now32();
        SB();
        f32x2 o[3];
#pragma unroll 1
        for (int n = 0; n < (1 << 20); ++n) {
            unsigned bad = 0;
#pragma unroll
            for (int i = 0; i < 8; ++i) {
#pragma unroll
                for (int e = 0; e < 3; ++e) {
                    const f32x2 t = e < 2 ? f32x2{bflo(x[i][2 * e]), bfhi(x[i][2 * e])} : f32x2{bflo(x[i][1]), bflo(x[i][3])};
                    if (i == 0) asm volatile("v_pk_mul_f32 %0, %1, %2 op_sel_hi:[0,1]" : "=v"(o[e]) : "v"(fs[0]), "v"(t));
                    else if (i & 1) asm volatile("v_pk_fma_f32 %0, %1, %2, %0 op_sel:[1,0,0]" : "+v"(o[e]) : "v"(fs[i >> 1]), "v"(t));
                    else asm volatile("v_pk_fma_f32 %0, %1, %2, %0 op_sel_hi:[0,1,1]" : "+v"(o[e]) : "v"(fs[i >> 1]), "v"(t));
                }
                bad = __builtin_amdgcn_bitop3_b32(bad, x[i][1], ta, 0xf6);
                bad = __builtin_amdgcn_bitop3_b32(bad, x[i][3], tb, 0xf6);
                asm volatile("" : "+v"(bad));
                SB();
            }
            if (__builtin_expect(__all(bad < 0x10000u), 1)) break;
            asm volatile("" ::: "memory");
            XL(0) XL(1) XL(2) XL(3) XL(4) XL(5) XL(6) XL(7)
        }
        if (DS) { asm volatile("" : "+v"(o[0]), "+v"(o[1]), "+v"(o[2])); tpl = now32(); }
        const float w0 = sw16(sw32(o[0][0], o[2][0]), sw32(o[1][0], o[1][0])), w1 = sw16(sw32(o[0][1], o[2][1]), sw32(o[1][1], o[1][1]));
        asm volatile("ds_write_b32 %0, %1\n\ts_waitcnt lgkmcnt(0)\n\ts_barrier" :: "v"(wo), "v"((c < 8 ? w0 : w1) + dpp<0x128>(c < 8 ? w1 : w0)) : "memory");
        if (wave == 0) {
            f32x2 p0, p1;
            asm volatile("ds_read2st64_b32 %0, %2 offset1:1\n\tds_read2st64_b32 %1, %2 offset0:2 offset1:3\n\ts_waitcnt lgkmcnt(0)" : "=&v"(p0), "=&v"(p1) : "v"(po) : "memory");
            p0 += p1;
            __builtin_amdgcn_raw_buffer_store_b16((unsigned short)pk_bf16(p0[0] + p0[1], 0.f), orr, oo, 0, (OPT & 8192) ? 16 : (OPT & 16384) ? 2 : 0);
        }
        if (b == 0 && tid == 64) *reinterpret_cast<u64*>(a.part + pep) = tag;
        asm volatile(".Lkv_b%c0:" :: "i"(ID));
    }
    if (DBG) {
        const unsigned tc1 = now32();
        if (!DS) tend = tbar = th4 = tfl = tpl = tc1;
        const unsigned tl16 = sched - stag - step;
        u64* stp = reinterpret_cast<u64*>(a.part + STAMP_OFF) + (size_t)((DBG == 4 || DBG == 24 || DBG == 6 ? cn : b) * 4 + wave) * 8;

        if (lane == 0) { stp[0] = tstart; stp[1] = tbar; stp[2] = DBG >= 4 ? tend - (((tend << 4) - tl16) >> 4) : th1; stp[3] = DBG == 4 && cn >= NC ? tend : th4; stp[4] = tend; stp[5] = cn < NC ? tfl : tend; stp[6] = cn < NC ? tpl : tend; stp[7] = tc1; }
        if (DBG == 2) *reinterpret_cast<u32x4*>(a.part + STAMP_OFF + 65536 + (b * 4 + wave) * 1024 + lane * 16) = *reinterpret_cast<const u32x4*>(smem + LDS_TR + 16 + wave * 1024 + lane * 16);
    }
}

template <int TP, int ADV, int TMIN, int WIN, int OPT, int EX, int GS, int HOLD, int HL, int SL, int EV, int ID, int DBG>
static void launch46(void* const* d_in, const int* in_sizes, void* d_out, void* d_ws, hipStream_t stream) {
    typedef const unsigned short* P;
    Args a;
    a.qk = (const int*)d_in[1]; a.qv = (const int*)d_in[4];
    a.k_cos = (P)d_in[9]; a.k_sin = (P)d_in[10];
    a.q = (P)d_in[0]; a.q_cos = (P)d_in[7]; a.q_sin = (P)d_in[8];
    a.sc[0] = (P)d_in[2]; a.sc[1] = (P)d_in[3]; a.sc[2] = (P)d_in[5]; a.sc[3] = (P)d_in[6];
    a.part = (unsigned char*)d_ws;
    a.out = (unsigned short*)d_out;
    kv46_kernel<TP, ADV, TMIN, WIN, OPT, EX, GS, HOLD, HL, SL, EV, ID, DBG><<<Skv / RW, 256, 0, stream>>>(a);
}

extern "C" void kernel_launch(void* const* d_in, const int* in_sizes, int n_in, void* d_out, int out_size, void* d_ws, size_t ws_size, hipStream_t stream) {
    launch46<80*16+2,220,79*16,4+16*32+4096*20,38+64+128+512+1024,66,3,85,100,0x2aaa,5+16*1+256*1,0,0>(d_in, in_sizes, d_out, d_ws, stream);
}
```

```cpp
#include <hip/hip_runtime.h>
#include <hip/hip_bf16.h>
#include <cstdint>
#define KALIGN 256
#define STAMP_OFF (200 << 20)
using bf16 = __hip_bfloat16;
using i32x4 = __attribute__((ext_vector_type(4))) int;
using u32x4 = __attribute__((ext_vector_type(4))) unsigned;
using u32x2 = __attribute__((ext_vector_type(2))) unsigned;
using f32x2 = __attribute__((ext_vector_type(2))) float;
using f32x4 = __attribute__((ext_vector_type(4))) float;

constexpr int DH = 192;
constexpr int HALF = 96;
constexpr int RW = 64;
constexpr int MAXH = 32;

__device__ __forceinline__ float bflo(unsigned u) { return __uint_as_float(u << 16); }
__device__ __forceinline__ float bfhi(unsigned u) { return __uint_as_float(u & 0xffff0000u); }
__device__ __forceinline__ float rbf(float x) { return __bfloat162float(__float2bfloat16(x)); }

template <int CTRL> __device__ __forceinline__ float dpp(float x) {
    return __builtin_bit_cast(float, __builtin_amdgcn_mov_dpp(__builtin_bit_cast(int, x), CTRL, 0xf, 0xf, true));
}
__device__ __forceinline__ float row16_allsum(float x) {
    x += dpp<0x128>(x);
    x += dpp<0x124>(x);
    x += dpp<0x122>(x);
    x += dpp<0x121>(x);
    return x;
}
__device__ __forceinline__ float row16_allmax(float x) {
    x = fmaxf(x, dpp<0x128>(x));
    x = fmaxf(x, dpp<0x124>(x));
    x = fmaxf(x, dpp<0x122>(x));
    x = fmaxf(x, dpp<0x121>(x));
    return x;
}
__device__ __forceinline__ float xrow_max(float x) {
    auto s = __builtin_amdgcn_permlane16_swap(__float_as_uint(x), __float_as_uint(x), false, false);
    x = fmaxf(__uint_as_float(s[0]), __uint_as_float(s[1]));
    auto t = __builtin_amdgcn_permlane32_swap(__float_as_uint(x), __float_as_uint(x), false, false);
    return fmaxf(__uint_as_float(t[0]), __uint_as_float(t[1]));
}
__device__ __forceinline__ float xrow_sum(float x) {
    auto s = __builtin_amdgcn_permlane16_swap(__float_as_uint(x), __float_as_uint(x), false, false);
    x = __uint_as_float(s[0]) + __uint_as_float(s[1]);
    auto t = __builtin_amdgcn_permlane32_swap(__float_as_uint(x), __float_as_uint(x), false, false);
    return __uint_as_float(t[0]) + __uint_as_float(t[1]);
}
__device__ __forceinline__ float sw32(float a, float b) {
    auto t = __builtin_amdgcn_permlane32_swap(__float_as_uint(a), __float_as_uint(b), false, false);
    return __uint_as_float(t[0]) + __uint_as_float(t[1]);
}
__device__ __forceinline__ float sw16(float a, float b) {
    auto t = __builtin_amdgcn_permlane16_swap(__float_as_uint(a), __float_as_uint(b), false, false);
    return __uint_as_float(t[0]) + __uint_as_float(t[1]);
}

#define SB() __builtin_amdgcn_sched_barrier(0)
typedef __amdgpu_buffer_rsrc_t rsrc_t;
__device__ __forceinline__ rsrc_t mkrsrc(const void* p, unsigned bytes) {
    return __builtin_amdgcn_make_buffer_rsrc(const_cast<void*>(p), 0, bytes, 0x00020000);
}
template <int AUX> __device__ __forceinline__ i32x4 bld128(rsrc_t r, unsigned voff, unsigned soff) {
    return __builtin_bit_cast(i32x4, __builtin_amdgcn_raw_buffer_load_b128(r, voff, soff, AUX));
}
template <int AUX> __device__ __forceinline__ unsigned bld16(rsrc_t r, unsigned voff, unsigned soff) {
    return __builtin_amdgcn_raw_buffer_load_b16(r, voff, soff, AUX);
}

struct HG { i32x4 d[3]; };
typedef unsigned long long u64;
typedef __attribute__((address_space(3))) unsigned char lds_u8;

__device__ __forceinline__ unsigned pk_bf16(float lo, float hi) {
    unsigned r;
    asm("v_cvt_pk_bf16_f32 %0, %1, %2" : "=v"(r) : "v"(lo), "v"(hi));
    return r;
}
__device__ __forceinline__ unsigned now32() { return (unsigned)__builtin_amdgcn_s_memrealtime(); }

constexpr int LDS_REC = 0;
constexpr int RECW = MAXH * DH * 4;
constexpr int LDS_CS = 0;
constexpr int LDS_QT = 12288;
constexpr int LDS_PF = 14336;
constexpr int LDS_QRAW = MAXH * 4 * DH * 4;
constexpr int LDS_Q = LDS_QRAW + MAXH * DH * 2;
constexpr int LDS_P = LDS_Q + 4 * DH * 4;
constexpr int LDS_SC = LDS_P + 4 * MAXH * 128;
constexpr int LDS_CNT = LDS_SC + 4 * MAXH * RW * 2;
constexpr int LDS_SK = LDS_CNT + 256;
constexpr int LDS_TR = LDS_SK + 1024;
constexpr int LDS_BYTES = LDS_TR + 4096 + 64;

struct Args {
    const int* qk; const int* qv;
    const unsigned short* k_cos; const unsigned short* k_sin;
    const unsigned short* q; const unsigned short* q_cos; const unsigned short* q_sin;
    unsigned char* part;
    unsigned short* out;
    const unsigned short* sc[4];
};
constexpr unsigned Skv = 16384;
constexpr float SCALE_LOG2 = 0.10411754627697264f;

template <int TP, int ADV, int TMIN, int WIN, int OPT, int EX, int GS, int HOLD, int HL, int SL, int EV, int ID, int DBG>
__global__ __launch_bounds__(256) __attribute__((aligned(KALIGN))) void kv46_kernel(Args a)
{
    __shared__ __attribute__((aligned(16))) unsigned char smem[LDS_BYTES];
    const unsigned tstart = DBG ? now32() : 0;
    constexpr bool DS = (DBG && DBG < 5) || DBG == 24;
    constexpr int H = MAXH, ES = 7, PFN = 16;
    const int b = blockIdx.x;
    const int tid = threadIdx.x;
    const int lane = tid & 63;
    const int wave = __builtin_amdgcn_readfirstlane(tid >> 6);
    typedef const unsigned short* const __attribute__((address_space(4))) * karg_t;
    const unsigned short* sp = ((karg_t)__builtin_amdgcn_kernarg_segment_ptr())[offsetof(Args, sc) / 8 + wave];
    if (b < PFN) {
        u64 pc; unsigned len, tl;
        asm volatile("s_getpc_b64 %0\n.Lkv_a%c3:\n\ts_mov_b32 %1, .Lkv_b%c3-.Lkv_a%c3-16\n\ts_mov_b32 %2, .Lkv_t%c3-.Lkv_a%c3" : "=s"(pc), "=s"(len), "=s"(tl) : "i"(ID));
        const unsigned o = tid * 16;
        asm volatile("s_mov_b32 m0, %5\n\ts_nop 0\n\tglobal_load_lds_dwordx4 %0, %4\n\tglobal_load_lds_dwordx4 %1, %4\n\tglobal_load_lds_dwordx4 %2, %4\n\tglobal_load_lds_dwordx4 %3, %4"
                     :: "v"(o), "v"(o + 4096), "v"(min(o + 8192, tl)), "v"(min(o + tl, len)), "s"(pc), "s"((unsigned)(size_t)(lds_u8*)smem + LDS_PF) : "memory");
    }
    SB();
    const int r = lane >> 4;
    const int c = lane & 15;
    constexpr unsigned hstride = Skv * (DH * 4);
    constexpr unsigned nb = Skv / RW;
    const unsigned lds0 = (unsigned)(size_t)(lds_u8*)smem;
    const unsigned pos = (b & 7) * (nb >> 3) + (b >> 3);
    const unsigned sb = ((((b >> 3) * 13) & 31) << 3) | (b & 7);
    const unsigned row0 = RW * pos + 16 * wave;

    constexpr unsigned LS = 512, psk = 0, pep = 128, AREA = 6u << 20, NAREA = 32, pfl = H * nb * LS;
    const rsrc_t pr = mkrsrc(a.part, (NAREA + 1) * AREA);
    u64 t64;
    {
        unsigned vs = ((lane >> 3) * Skv + RW * pos + (lane & 7) * 8) * 2, v1 = vs + 16 * Skv, v2 = vs + 32 * Skv, v3 = vs + 48 * Skv;
        unsigned ds = lds0 + LDS_SC + wave * 4096;
        asm volatile("" : "+v"(vs), "+v"(v1), "+v"(v2), "+v"(v3), "+s"(ds));
        SB();
        asm volatile("s_memrealtime %0\n\ts_mov_b32 m0, %6\n\ts_nop 0\n\tglobal_load_lds_dwordx4 %1, %5 nt\n\t"
                     "s_add_u32 m0, m0, 1024\n\ts_nop 0\n\tglobal_load_lds_dwordx4 %2, %5 nt\n\t"
                     "s_add_u32 m0, m0, 1024\n\ts_nop 0\n\tglobal_load_lds_dwordx4 %3, %5 nt\n\t"
                     "s_add_u32 m0, m0, 1024\n\ts_nop 0\n\tglobal_load_lds_dwordx4 %4, %5 nt\n\ts_waitcnt lgkmcnt(0)"
                     : "=&s"(t64) : "v"(vs), "v"(v1), "v"(v2), "v"(v3), "s"(sp), "s"(ds),
                       "s"(a.qk), "s"(a.qv), "s"(a.k_cos), "s"(a.k_sin), "s"(a.q), "s"(a.q_cos), "s"(a.q_sin), "s"(a.part), "s"(a.out) : "memory", "scc");
    }
    SB();
    const unsigned tdma = (unsigned)t64;
#define DMA3(V, A, B, P) "s_mov_b32 m0, " B "\n\ts_nop 0\n\t" \
                     "global_load_lds_dwordx4 " V ", " A P "\n\t" \
                     "global_load_lds_dwordx4 " V ", " A " offset:1024" P "\n\t" \
                     "global_load_lds_dwordx4 " V ", " A " offset:2048" P "\n\t"
    asm volatile(DMA3("%0", "%1", "%2", "") "s_mov_b32 m0, %4\n\ts_nop 0\n\tglobal_load_lds_dwordx4 %3, off"
                 :: "v"(lane * 16), "s"(a.q + wave * 1536), "s"(lds0 + LDS_QRAW + wave * 3072),
                    "v"(lane < 48 ? (const unsigned char*)((lane < 24 ? a.q_cos : a.q_sin - DH) + 8 * lane) : a.part + pep), "s"(lds0 + LDS_QT + wave * RECW) : "memory");
    if (OPT & 32768) {
        const unsigned char* pp = wave == 0 ? (const unsigned char*)a.k_cos + row0 * (DH * 2) : wave == 1 ? (const unsigned char*)a.k_sin + row0 * (DH * 2) : (const unsigned char*)a.qk + row0 * (DH * 4) + (wave - 2) * hstride;
        if ((OPT & 65536) || b < 32) asm volatile("s_mov_b32 m0, %2\n\ts_mov_b64 exec, 1\n\tglobal_load_lds_dword %0, %1 nt\n\ts_mov_b64 exec, -1" :: "v"(0), "s"(pp), "s"(lds0 + LDS_PF) : "memory");
    }
    SB();
    const int stag = (OPT & 32) ? (int)(((sb & ~7) + wave * 2 + (b & 1)) * (TP / 2)) / 256 - TP / 4 : (int)(((OPT & 2) ? (((b >> 3) * 26 + wave * 2 + (b & 1)) & 63) : ((((b >> 3) * 4 + wave) * 37) & 63)) * (TP / 2)) / 64 - TP / 4;
    unsigned sched = ((tdma + ADV) << 4) + stag - 4 * (TP / 2);
    unsigned vt = tdma << 4;
    int tru = -4;
    unsigned step = TP / 2, tmin = TMIN / 2, vth = WIN & 15;
    auto pace = [&](bool ctl = false) {
        unsigned tgt = sched;
        sched += step;
        if ((int)(vt - tgt) > 0) tgt = vt;
        unsigned t = now32() << 4;
        while ((int)(t - tgt) < 0) { __builtin_amdgcn_s_sleep(1); t = now32() << 4; }
        vt = ((int)(t - 128 - tgt) > 0 ? t - 128 : tgt) + tmin;
        if (ctl) {
            if constexpr (WIN > 0 && WIN < 16) asm volatile("s_waitcnt vmcnt(%0)" :: "i"(WIN) : "memory");
            if constexpr (WIN >= 16) {
                const unsigned vm = __builtin_amdgcn_s_getreg(7 | (3 << 11));
                if (vm > vth && (int)(vt - sched) < (WIN >> 12) * 16) vt += (vm - vth) * ((WIN >> 4) & 255);
            }
        }
        if constexpr (DBG == 2) { *reinterpret_cast<unsigned*>(smem + LDS_TR + 16 + wave * 1024 + (tru & 255) * 4) = ((t - (sched - step)) >> 4) | ((OPT & 8) ? (__builtin_amdgcn_s_getreg(7 | (3 << 11)) << 16) : 0); ++tru; }
    };
    {
        unsigned dst = lds0 + LDS_CS + wave * RECW;
        const unsigned v0 = lane * 16 + row0 * (DH * 2);
#pragma unroll 1
        for (int j = 0; j < 4; ++j) {
            pace();
            asm volatile(DMA3("%0", "%1", "%2", " nt") :: "v"(v0), "s"((j < 2 ? a.k_cos : a.k_sin) + (j & 1) * 8 * DH), "s"(dst) : "memory");
            dst += 3072;
        }
    }
    if (tid < MAXH) *reinterpret_cast<unsigned*>(smem + LDS_CNT + tid * 4) = 0;
    const rsrc_t kr = mkrsrc(a.qk, H * hstride);
    const rsrc_t vr = mkrsrc(a.qv, H * hstride);
    const unsigned kvoff = (row0 + r) * (DH * 4) + c * 16;
    auto issue = [&](HG& g, rsrc_t rs, unsigned so) {
#pragma unroll
        for (int x = 0; x < 3; ++x) g.d[x] = bld128<2>(rs, kvoff + 256 * x, so);
    };
    HG buf[4];
#define PRE(g) SB(); pace(); issue(buf[g], kr, (g) * (4 * DH * 4)); SB();
    asm volatile("s_waitcnt vmcnt(12) lgkmcnt(0)\n\ts_barrier" ::: "memory");
    unsigned tbar = DS ? now32() : 0;
    PRE(0)
    const unsigned qoff = (lane < 48 ? lane : lane - 48) * 4;
    const unsigned* qt = reinterpret_cast<const unsigned*>(smem + LDS_QT + wave * RECW + qoff);
    unsigned qc0 = qt[0], qc1 = qt[HALF / 2], qs0 = qt[DH / 2], qs1 = qt[(DH + HALF) / 2];
    u64 tag;
    {
        const u32x2 e = *reinterpret_cast<const u32x2*>(smem + LDS_QT + wave * RECW + 768);
        tag = (((u64)__builtin_amdgcn_readfirstlane(e[1]) << 32) | __builtin_amdgcn_readfirstlane(e[0])) * 6364136223846793005ull + 1442695040888963407ull;
        asm volatile("" : "+s"(tag));
    }
    const unsigned t32 = tag >> 32;
    const unsigned char* qraw = smem + LDS_QRAW + qoff;
    unsigned char* qrot = smem + LDS_Q + wave * (DH * 4) + 2 * qoff;
    auto rotq = [&]() {
        const unsigned q_lo = *reinterpret_cast<const unsigned*>(qraw), q_hi = *reinterpret_cast<const unsigned*>(qraw + HALF * 2);
        qraw += DH * 2;
        const f32x2 lo = f32x2{bflo(q_lo), bfhi(q_lo)}, hi = f32x2{bflo(q_hi), bfhi(q_hi)};
        const f32x2 y1 = lo * f32x2{bflo(qc0), bfhi(qc0)}, y2 = -hi * f32x2{bflo(qs0), bfhi(qs0)};
        const f32x2 y3 = hi * f32x2{bflo(qc1), bfhi(qc1)}, y4 = lo * f32x2{bflo(qs1), bfhi(qs1)};
        const unsigned x1 = pk_bf16(y1[0], y1[1]), x2 = pk_bf16(y2[0], y2[1]), x3 = pk_bf16(y3[0], y3[1]), x4 = pk_bf16(y4[0], y4[1]);
        const f32x2 z1 = f32x2{bflo(x1), bfhi(x1)} + f32x2{bflo(x2), bfhi(x2)}, z2 = f32x2{bflo(x3), bfhi(x3)} + f32x2{bflo(x4), bfhi(x4)};
        const unsigned l2 = pk_bf16(z1[0], z1[1]), h2 = pk_bf16(z2[0], z2[1]);
        *reinterpret_cast<f32x2*>(qrot) = f32x2{bflo(l2), bfhi(l2)};
        *reinterpret_cast<f32x2*>(qrot + HALF * 4) = f32x2{bflo(h2), bfhi(h2)};
        asm volatile("" ::: "memory");
    };
    f32x4 qa[3], qb[3];
    const unsigned char* qh = smem + LDS_Q + wave * (DH * 4) + 16 * c;
    const unsigned char* qh1 = qh + (c < 8 ? 64 + HALF : 64 - HALF) * 4;
    auto loadq = [&]() {
        asm volatile("" ::: "memory");
#pragma unroll
        for (int x = 0; x < 3; ++x) qa[x] = *reinterpret_cast<const f32x4*>(qh + 256 * x);
        qb[0] = *reinterpret_cast<const f32x4*>(qh + HALF * 4);
        qb[1] = *reinterpret_cast<const f32x4*>(qh1);
        qb[2] = *reinterpret_cast<const f32x4*>(qh + (128 - HALF) * 4);
        asm volatile("" ::: "memory");
    };
    rotq();
    constexpr unsigned SKS = (OPT & 4096) ? 256 : 16;
    if (b < 8 && wave == 3) __builtin_amdgcn_raw_buffer_store_b128(u32x4{tdma, t32, tdma, (unsigned)tag}, pr, lane < ((OPT & 4096) ? 8 : 1) ? psk + b * SKS + lane * 16 : 0x80000000u, 0, 16);
    PRE(1)
    f32x2 cc[4][3][2], ss[4][3][2];
    const unsigned char* cb = smem + LDS_CS + wave * RECW + r * (DH * 2) + c * 8;
    asm volatile("s_waitcnt vmcnt(12)" ::: "memory");
#pragma unroll
    for (int k = 0; k < 4; ++k)
#pragma unroll
        for (int x = 0; x < 3; ++x) {
            const u32x2 t = *reinterpret_cast<const u32x2*>(cb + k * 1536 + 128 * x);
            cc[k][x][0] = f32x2{bflo(t[0]), bfhi(t[0])}; cc[k][x][1] = f32x2{bflo(t[1]), bfhi(t[1])};
            asm volatile("" : "+v"(cc[k][x][0]), "+v"(cc[k][x][1]));
        }
    PRE(2)
    loadq();
    const unsigned char* scp = smem + LDS_SC + (c & 4) * 1024 + (16 * wave + 4 * (c & 3) + r) * 2;
    unsigned sc = *reinterpret_cast<const unsigned short*>(scp);
    auto loadsin = [&](int k) {
        const unsigned char* sb1 = cb + (c < 8 ? (64 + HALF) * 2 : (64 - HALF) * 2);
        const unsigned sg = c < 8 ? 0u : 0x80008000u;
        u32x2 t[3];
        t[0] = *reinterpret_cast<const u32x2*>(cb + 6144 + k * 1536 + HALF * 2);
        t[1] = *reinterpret_cast<const u32x2*>(sb1 + 6144 + k * 1536) ^ u32x2{sg, sg};
        t[2] = *reinterpret_cast<const u32x2*>(cb + 6144 + k * 1536 + (128 - HALF) * 2) ^ u32x2{0x80008000u, 0x80008000u};
#pragma unroll
        for (int x = 0; x < 3; ++x) {
            ss[k][x][0] = f32x2{bflo(t[x][0]), bfhi(t[x][0])}; ss[k][x][1] = f32x2{bflo(t[x][1]), bfhi(t[x][1])};
            asm volatile("" : "+v"(ss[k][x][0]), "+v"(ss[k][x][1]));
        }
    };
    asm volatile("s_waitcnt vmcnt(12)" ::: "memory");
    loadsin(0); loadsin(1);
    PRE(3)
    asm volatile("s_waitcnt vmcnt(12)" ::: "memory");
    loadsin(2); loadsin(3);
    unsigned th1 = 0, th4 = 0;
    unsigned char* prec = smem + LDS_P + wave * (H * 128) + r * 32 + (c & 7) * 4;
    auto softmax = [&](float s, int h) {
        float m = fmaxf(s, dpp<0xb1>(s));
        m = xrow_max(fmaxf(m, dpp<0x4e>(m)));
        const float p = __builtin_amdgcn_exp2f(s - m);
        float l = p + dpp<0xb1>(p);
        l = xrow_sum(l + dpp<0x4e>(l));
        const float t = (c & 1) ? l : m;
        const int x = __builtin_amdgcn_update_dpp(__float_as_int(p), __float_as_int(t), 0x114, 0xf, 0xa, false);
        *reinterpret_cast<int*>(prec + h * 128) = x;
    };
    float sprev = 0.f;

#pragma unroll 1
    for (int h = 0; h < H; ++h) {
        const bool more = h + 1 < H;
        const rsrc_t nx = more ? kr : vr;
        const unsigned nso = more ? (h + 1) * hstride : 0;
        float scf = bflo(sc);
        asm volatile("" : "+v"(scf) :: "memory");
        SB();
        scp += more ? 128 : 4096 + 128;
        sc = *reinterpret_cast<const unsigned short*>(scp);
        SB();
        float sreg = 0.f;
#define KSTEP(k) { \
            const float kz = dpp<0x154 + (k)>(scf); \
            f32x2 kz2 = f32x2{kz, kz}; \
            asm("" : "+v"(kz2)); \
            f32x2 a2 = f32x2{0.f, 0.f}; \
            _Pragma("unroll") for (int x = 0; x < 3; ++x) { \
                _Pragma("unroll") for (int e = 0; e < 2; ++e) { \
                    const f32x2 w = __builtin_elementwise_fma(f32x2{qb[x][2 * e], qb[x][2 * e + 1]}, ss[k][x][e], f32x2{qa[x][2 * e], qa[x][2 * e + 1]} * cc[k][x][e]); \
                    const f32x2 kf = f32x2{(float)buf[k].d[x][2 * e], (float)buf[k].d[x][2 * e + 1]} - kz2; \
                    a2 = __builtin_elementwise_fma(kf, w, a2); \
                } \
            } \
            float sa = a2[0] + a2[1]; \
            asm volatile("" : "+v"(sa) :: "memory"); \
            SB(); pace(true); issue(buf[k], nx, nso + (k) * (4 * DH * 4)); SB(); \
            if ((k) == 0) { softmax(sprev, (h - 1) & (H - 1)); SB(); }   \
            if ((k) == 1) { rotq(); SB(); } \
            if ((k) == 2) { \
                if (wave == 1 && h == ((OPT & 4) ? 1 + ((b >> 3) & 3) : 4 + ((b >> 3) & 15))) \
                    asm volatile("s_mov_b32 m0, %2\n\ts_nop 0\n\tglobal_load_lds_dwordx4 %0, %1 sc1" :: "v"(psk + (lane & 7) * SKS), "s"(a.part), "s"(lds0 + LDS_SK) : "memory"); \
                if (h == ((OPT & 4) ? 6 : 25)) {        \
                    const u32x2 e = *reinterpret_cast<const u32x2*>(smem + LDS_SK + (lane & 7) * 16); \
                    const int d = (int)(tdma - e[0]); \
                    if (__all(e[1] == t32 && d > -400 && d < 400)) { \
                        int sum = d + __builtin_amdgcn_mov_dpp(d, 0xb1, 0xf, 0xf, true); \
                        sum += __builtin_amdgcn_mov_dpp(sum, 0x4e, 0xf, 0xf, true); \
                        sum += __builtin_amdgcn_mov_dpp(sum, 0x124, 0xf, 0xf, true); \
                        const int adj = (__builtin_amdgcn_readfirstlane(sum) * ((OPT & 4) ? 73 : 112) + 4096) >> 13;        \
                        step -= adj; tmin -= adj; \
                        if ((OPT & 64) && (OPT & 384) && b < ((OPT & 256) ? 256 : 16) && wave == 0) {        \
                            unsigned t0, t1, t2, t3; \
                            const unsigned ar = (((__builtin_amdgcn_readfirstlane(e[0]) >> 13) & (NAREA - 1)) + 1) * AREA; \
                            asm volatile("s_load_dword %0, %4, %5\n\ts_load_dword %1, %4, %6\n\ts_load_dword %2, %4, %7\n\ts_load_dword %3, %4, %8\n\ts_waitcnt lgkmcnt(0)" : "=&s"(t0), "=&s"(t1), "=&s"(t2), "=&s"(t3) : "s"(a.part), "s"(ar), "s"(ar + AREA - 128), "s"(ar + (2u << 20) + 128), "s"(ar + AREA - (2u << 20) - 128) : "memory"); \
                            vt = now32() << 4; \
                        } \
                    } \
                } \
                SB(); \
            } \
            if ((k) == 3) { loadq(); SB(); } \
            sa = row16_allsum(sa); \
            sreg = (c & 3) == (k) ? sa : sreg; }
        KSTEP(0) KSTEP(1) KSTEP(2) KSTEP(3)
        sprev = sreg * (scf * SCALE_LOG2);
        if (DS) { if (h == 7) th1 = now32(); }
    }
    softmax(sprev, H - 1);
    if (DS) th4 = now32();

    const unsigned tg = (unsigned)(tag >> (16 * (lane & 3))) << 16;
    unsigned rl = lds0 + LDS_REC + lane * 4, rh = rl + 2 * RECW;
    auto pack = [&](f32x2 t, float u) { return u32x2{pk_bf16(t[0], t[1]), pk_bf16(u, 0.f) | tg}; };
    auto ldrec = [&](unsigned o, f32x2 (&p)[6]) {
        asm volatile("ds_read2st64_b32 %0, %6 offset1:1\n\tds_read2st64_b32 %1, %6 offset0:96 offset1:97\n\tds_read2st64_b32 %2, %7 offset1:1\n\tds_read2st64_b32 %3, %7 offset0:96 offset1:97\n\t"
                     "ds_read2st64_b32 %4, %6 offset0:2 offset1:98\n\tds_read2st64_b32 %5, %7 offset0:2 offset1:98"
                     : "=&v"(p[0]), "=&v"(p[1]), "=&v"(p[2]), "=&v"(p[3]), "=&v"(p[4]), "=&v"(p[5]) : "v"(rl + o), "v"(rh + o) : "memory");
    };
    auto merge = [&](int hd) {
        f32x2 p[6], ma, mb;
        asm volatile("ds_read2st64_b32 %0, %2 offset1:16\n\tds_read2st64_b32 %1, %2 offset0:32 offset1:48" : "=&v"(ma), "=&v"(mb) : "v"(lds0 + LDS_P + hd * 128 + 16) : "memory");
        ldrec(hd * (DH * 4), p);
        asm volatile("s_waitcnt lgkmcnt(6)" : "+v"(ma), "+v"(mb) :: "memory");
        const float M = __builtin_amdgcn_fmed3f(__builtin_amdgcn_fmed3f(ma[0], ma[1], __builtin_inff()), __builtin_amdgcn_fmed3f(mb[0], mb[1], __builtin_inff()), __builtin_inff());
        ma = f32x2{__builtin_amdgcn_exp2f(ma[0] - M), __builtin_amdgcn_exp2f(ma[1] - M)};
        mb = f32x2{__builtin_amdgcn_exp2f(mb[0] - M), __builtin_amdgcn_exp2f(mb[1] - M)};
        asm volatile("s_waitcnt lgkmcnt(0)" : "+v"(ma), "+v"(mb), "+v"(p[0]), "+v"(p[1]), "+v"(p[2]), "+v"(p[3]), "+v"(p[4]), "+v"(p[5]) :: "memory");
        const f32x2 u = __builtin_elementwise_fma(mb, p[5], ma * p[4]);
        return pack(ma[0] * p[0] + ma[1] * p[1] + mb[0] * p[2] + mb[1] * p[3], u[0] + u[1]);
    };
    constexpr int NC = 4 * H;
    const int cn = sb;
    const unsigned hc = 4 * (cn & 7) + ((cn >> 3) & 3);
    unsigned old = 0, area = 0, fo = 0;
    u32x4 flv = {};
    float gl = 0.f, mw = 0.f;
    f32x2 fs[4] = {};
    constexpr int LDS_ML = LDS_QRAW + 2048;
#define BAD(y) ((unsigned)(y)[1] != t32 || (unsigned)(y)[3] != (unsigned)tag)
    auto ev_ml = [&]() {
        f32x2 ml[4];
#pragma unroll
        for (int w = 0; w < 4; ++w) ml[w] = *reinterpret_cast<const f32x2*>(smem + LDS_P + w * (H * 128) + (8 * wave + (lane & 7)) * 128 + 16);
        const float M = fmaxf(fmaxf(ml[0][0], ml[1][0]), fmaxf(ml[2][0], ml[3][0]));
        const float L = (__builtin_amdgcn_exp2f(ml[0][0] - M) * ml[0][1] + __builtin_amdgcn_exp2f(ml[1][0] - M) * ml[1][1]) + (__builtin_amdgcn_exp2f(ml[2][0] - M) * ml[2][1] + __builtin_amdgcn_exp2f(ml[3][0] - M) * ml[3][1]);
        flv = u32x4{__float_as_uint(M), t32, __float_as_uint(L), (unsigned)tag};
        asm volatile("" : "+v"(flv));
    };
    auto ev_area = [&]() {
        u32x4 e = *reinterpret_cast<const u32x4*>(smem + LDS_SK);
        float mv[4];
#pragma unroll
        for (int w = 0; w < 4; ++w) mv[w] = *reinterpret_cast<const float*>(smem + LDS_P + ((wave + w) & 3) * (H * 128) + (H - 1) * 128 + 16);
        gl = __builtin_amdgcn_exp2f(mv[0] - fmaxf(fmaxf(mv[0], mv[1]), fmaxf(mv[2], mv[3])));
        if (__builtin_expect(__any(BAD(e)), 0)) {
#pragma unroll 1
            for (int n = 0; n < (1 << 20) && BAD(e); ++n)
                asm volatile("global_load_dwordx4 %0, %1, %2 sc1\n\ts_waitcnt vmcnt(0)" : "=v"(e) : "v"(psk), "s"(a.part) : "memory");
        }
        area = (((__builtin_amdgcn_readfirstlane(e[0]) >> 13) & (NAREA - 1)) + 1) * AREA + 4096;
        asm volatile("" : "+s"(area), "+v"(gl));
    };
    auto ev_put = [&]() {
        asm volatile("s_mov_b64 exec, 0xff\n\tbuffer_store_dwordx4 %0, %1, %2, %3 offen sc1\n\ts_mov_b64 exec, -1" :: "v"(flv), "v"(4 * (rl - lds0 - LDS_REC)), "s"(pr), "s"(area + pfl + (wave * nb + sb) * 128) : "memory");
    };
    auto ev_get = [&]() {
        unsigned l = lane;
        asm volatile("" : "+v"(l));
        fo = area + pfl + ((hc >> 3) * nb + 8 * wave) * 128 + (hc & 7) * 16 + (((l & 56) << 2) + (l & 7)) * 128;
        asm volatile("s_mov_b32 m0, %2\n\ts_nop 0\n\tglobal_load_lds_dwordx4 %0, %1 sc1" :: "v"(fo), "s"(a.part), "s"(lds0 + LDS_ML + wave * 1024) : "memory");
    };
    auto ev_w1 = [&]() {
        u32x4 y = *reinterpret_cast<const u32x4*>(smem + LDS_ML + wave * 1024 + lane * 16);
        if (__builtin_expect(__any(BAD(y)), 0)) {
#pragma unroll 1
            for (int n = 0; n < (1 << 20) && BAD(y); ++n)
                asm volatile("global_load_dwordx4 %0, %1, %2 sc1\n\ts_waitcnt vmcnt(0)" : "=v"(y) : "v"(fo), "s"(a.part) : "memory");
        }
        const float m = __uint_as_float(y[0]);
        float u;
        asm volatile("s_nop 1\n\tv_max_f32_dpp %0, %2, %2 row_ror:8 row_mask:0xf bank_mask:0xf\n\ts_nop 1\n\tv_max_f32_dpp %0, %0, %0 row_ror:4 row_mask:0xf bank_mask:0xf\n\ts_nop 1\n\t"
                     "v_max_f32_dpp %0, %0, %0 row_ror:2 row_mask:0xf bank_mask:0xf\n\ts_nop 1\n\tv_max_f32_dpp %0, %0, %0 row_ror:1 row_mask:0xf bank_mask:0xf\n\tv_mov_b32 %1, %0\n\ts_nop 1\n\t"
                     "v_permlane16_swap_b32 %0, %1\n\ts_nop 0\n\tv_max_f32 %0, %0, %1\n\tv_mov_b32 %1, %0\n\ts_nop 1\n\tv_permlane32_swap_b32 %0, %1\n\ts_nop 0\n\tv_max_f32 %0, %0, %1" : "=&v"(mw), "=&v"(u) : "v"(m));
        fs[0][0] = __builtin_amdgcn_exp2f(m - mw);
        fs[0][1] = fs[0][0] * __uint_as_float(y[2]);
        asm volatile("" : "+v"(fs[0]));
    };
    auto ev_w2 = [&]() {
        const f32x2 f = fs[0];
        unsigned bp = (lane >> 3) * 4;
        asm volatile("ds_bpermute_b32 %0, %8, %9\n\tds_bpermute_b32 %1, %8, %9 offset:32\n\tds_bpermute_b32 %2, %8, %9 offset:64\n\tds_bpermute_b32 %3, %8, %9 offset:96\n\t"
                     "ds_bpermute_b32 %4, %8, %9 offset:128\n\tds_bpermute_b32 %5, %8, %9 offset:160\n\tds_bpermute_b32 %6, %8, %9 offset:192\n\tds_bpermute_b32 %7, %8, %9 offset:224"
                     : "=&v"(fs[0][0]), "=&v"(fs[0][1]), "=&v"(fs[1][0]), "=&v"(fs[1][1]), "=&v"(fs[2][0]), "=&v"(fs[2][1]), "=&v"(fs[3][0]), "=&v"(fs[3][1]) : "v"(bp), "v"(f[0]) : "memory");
        *reinterpret_cast<f32x2*>(smem + LDS_QRAW + 1024 + wave * 8) = f32x2{mw, xrow_sum(row16_allsum(f[1]))};
        asm volatile("s_waitcnt lgkmcnt(0)" : "+v"(fs[0]), "+v"(fs[1]), "+v"(fs[2]), "+v"(fs[3]) :: "memory");
    };
    constexpr int G1 = EV & 15, G2 = (EV >> 4) & 15, G3 = (EV >> 8) & 15;
    const int hput = G1 + G2 + 1 + ((b >> 3) & 3), hget = cn < NC ? G1 + G2 + 6 + ((b >> 3) & 3) : -9;
    const int htch = cn < NC && wave == 0 ? (EV >> 12) + ((b >> 3) & 3) : -9;
    float pv = *reinterpret_cast<const float*>(prec);
    f32x2 acc[3][2];
    float y;
    auto prep = [&]() {
        y = bflo(sc);
        y = (c & 4) ? y : y * pv;
        asm volatile("" : "+v"(y) :: "memory");
        SB();
        scp += 128;
        sc = *reinterpret_cast<const unsigned short*>(scp);
        prec += 128;
        pv = *reinterpret_cast<const float*>(prec);
        SB();
    };
#define VCOMP(k) { \
        const float pw = dpp<0x150 + (k)>(y); \
        const float vz = dpp<0x154 + (k)>(y); \
        f32x2 pw2 = f32x2{pw, pw}, vz2 = f32x2{vz, vz}; \
        asm("" : "+v"(pw2), "+v"(vz2)); \
        _Pragma("unroll") for (int x = 0; x < 3; ++x) \
            _Pragma("unroll") for (int e = 0; e < 2; ++e) { \
                const f32x2 vf = f32x2{(float)buf[k].d[x][2 * e], (float)buf[k].d[x][2 * e + 1]} - vz2; \
                acc[x][e] = (k) == 0 ? pw2 * vf : __builtin_elementwise_fma(pw2, vf, acc[x][e]); \
            } \
        asm volatile("" : "+v"(acc[0][0]), "+v"(acc[0][1]), "+v"(acc[1][0]), "+v"(acc[1][1]), "+v"(acc[2][0]), "+v"(acc[2][1]) :: "memory"); \
        SB(); }
#define VSTEP(k, hn) { \
        VCOMP(k) pace(true); issue(buf[k], vr, (hn) * hstride + (k) * (4 * DH * 4)); SB(); \
        if ((k) == 1) { tmin = (hn) == H - 1 ? 0 : tmin; } \
        if ((k) == 1 && (hn) == G1) { asm volatile("s_waitcnt lgkmcnt(0)\n\ts_barrier" ::: "memory"); SB(); } \
        if ((k) == 2 && (hn) == G1) { ev_ml(); SB(); } \
        if ((k) == 2 && (hn) == G1 + G2) { ev_area(); SB(); } \
        if ((k) == 1 && (OPT & 1024)) { vth = WIN & 15; asm volatile("" : "+s"(vth)); } \
        if ((k) == 1 && (hn) == hput) { ev_put(); if (OPT & 1024) vth = 15; SB(); } \
        if ((k) == 1 && (hn) == hget) { ev_get(); if (OPT & 1024) vth = 15; SB(); } \
        if ((k) == 2 && (hn) == hget + 2) { ev_w1(); SB(); } \
        if ((OPT & 2048) && (k) == 2 && (hn) == htch) { asm volatile("s_mov_b32 m0, %2\n\ts_mov_b64 exec, 63\n\tglobal_load_lds_dword %0, %1\n\ts_mov_b64 exec, -1" :: "v"(hc * (DH * 2) + 16 * (rl - lds0 - LDS_REC)), "s"(a.out), "s"(lds0 + LDS_ML + 4096) : "memory"); vth = 15; SB(); } \
        if ((k) == 1 && (hn) == hget + 2 + G3) { ev_w2(); SB(); } }
    auto record = [&](unsigned ra, unsigned ca) {
        f32x4 o;
#pragma unroll
        for (int e = 0; e < 4; ++e) {
            const float t0 = sw32(acc[0][e >> 1][e & 1], acc[2][e >> 1][e & 1]);
            const float t1 = sw32(acc[1][e >> 1][e & 1], acc[1][e >> 1][e & 1]);
            o[e] = sw16(t0, t1);
        }
        asm volatile("s_mov_b64 exec, %5\n\tds_write_b128 %1, %2\n\ts_mov_b64 exec, 1\n\tds_add_rtn_u32 %0, %3, %4\n\ts_mov_b64 exec, -1"
                     : "+v"(old) : "v"(ra), "v"(o), "v"(ca), "v"(1u), "s"(0xffffffffffffull) : "memory");
        SB();
    };
#pragma unroll 1
    for (int h = 0; h < H - 1; ++h) {
        prep();
        VSTEP(0, h + 1)
        asm volatile("s_waitcnt lgkmcnt(0)" : "+v"(old) :: "memory");
        if (__builtin_amdgcn_readfirstlane(old) == 3) *reinterpret_cast<u32x2*>(smem + LDS_REC + (h - 1) * (DH * 4) + lane * 8) = merge(h - 1);
        SB();
        VSTEP(1, h + 1) VSTEP(2, h + 1) VSTEP(3, h + 1)
        record(lds0 + LDS_REC + h * (DH * 4) + wave * RECW + lane * 16, lds0 + LDS_CNT + h * 4);
    }
    asm volatile("s_branch .Lkv_t%c0\n\t.p2align 12,,3327\n.Lkv_t%c0:" :: "i"(ID));
    y = bflo(sc);
    y = (c & 4) ? y : y * (pv * gl);
    VCOMP(0)
    asm volatile("s_waitcnt lgkmcnt(0)\n\ts_barrier" ::: "memory");
    u32x4 ev[4];
#pragma unroll
    for (int p = 0; p < 4; ++p) ev[p] = *reinterpret_cast<const u32x4*>(smem + LDS_REC + (8 * wave + 2 * p + (lane >> 5)) * (DH * 4) + (lane & 31) * 16);
    const f32x4 a0 = *reinterpret_cast<const f32x4*>(smem + LDS_QRAW + 1024), a1 = *reinterpret_cast<const f32x4*>(smem + LDS_QRAW + 1040);
    SB();
    VCOMP(1)
    unsigned eo = (lane >> 5) * (nb * LS) + (lane & 31) * 16, e8 = lane * 8, ra = lds0 + LDS_REC + (H - 1) * (DH * 4) + wave * RECW + lane * 16, ca = lds0 + LDS_CNT + (H - 1) * 4;
    unsigned es[5];
#pragma unroll
    for (int p = 0; p < 4; ++p) es[p] = area + ((8 * wave + 2 * p) * nb + sb) * LS;
    es[4] = area + ((H - 1) * nb + sb) * LS;
    unsigned tx = sched - step - stag + EX * 16 + (sb >> 3) * GS * 4;
    if (wave == 3) { const u32x2 t = merge(H - 2); ev[3][0] = t[0]; ev[3][1] = t[1]; }
    {
        const float M = fmaxf(fmaxf(a0[0], a0[2]), fmaxf(a1[0], a1[2]));
        const float gw = __builtin_amdgcn_exp2f(mw - M) * __builtin_amdgcn_rcpf((__builtin_amdgcn_exp2f(a0[0] - M) * a0[1] + __builtin_amdgcn_exp2f(a0[2] - M) * a0[3]) + (__builtin_amdgcn_exp2f(a1[0] - M) * a1[1] + __builtin_amdgcn_exp2f(a1[2] - M) * a1[3]));
#pragma unroll
        for (int i = 0; i < 4; ++i) fs[i] *= gw;
    }
    rl += (H - 1) * (DH * 4); rh += (H - 1) * (DH * 4);
    asm volatile("" : "+v"(fs[0]), "+v"(fs[1]), "+v"(fs[2]), "+v"(fs[3]), "+v"(ev[0]), "+v"(ev[1]), "+v"(ev[2]), "+v"(ev[3]), "+v"(eo), "+v"(e8), "+v"(ra), "+v"(ca), "+v"(rl), "+v"(rh),
                      "+s"(es[0]), "+s"(es[1]), "+s"(es[2]), "+s"(es[3]), "+s"(es[4]), "+s"(tx));
    SB();
    VCOMP(2)
    while ((int)((now32() << 4) - tx) < 0) {}
    if constexpr ((OPT & 512) != 0)
    asm volatile("s_waitcnt vmcnt(0)\n\tbuffer_store_dwordx4 %3, %7, %9, %10 offen sc1\n\tbuffer_store_dwordx4 %4, %7, %9, %11 offen sc1\n\tbuffer_store_dwordx4 %5, %7, %9, %12 offen sc1\n\t"
                 "s_cmp_eq_u32 %14, 3\n\ts_cbranch_scc1 1f\n\tbuffer_store_dwordx4 %6, %7, %9, %13 offen sc1\n\ts_branch 2f\n1:\tbuffer_store_dwordx2 %15, %8, %9, %13 offen sc1\n2:"
                 : "+v"(buf[3].d[0]), "+v"(buf[3].d[1]), "+v"(buf[3].d[2]) : "v"(ev[0]), "v"(ev[1]), "v"(ev[2]), "v"(ev[3]), "v"(eo), "v"(e8), "s"(pr), "s"(es[0]), "s"(es[1]), "s"(es[2]), "s"(es[3]), "s"(wave),
                   "v"(u32x2{ev[3][0], ev[3][1]}) : "memory", "scc");
    else
    asm volatile("s_waitcnt vmcnt(0)\n\tbuffer_store_dwordx4 %3, %7, %9, %10 offen sc1\n\tbuffer_store_dwordx4 %4, %7, %9, %11 offen sc1\n\tbuffer_store_dwordx4 %5, %7, %9, %12 offen sc1\n\t"
                 "s_cmp_eq_u32 %14, 3\n\ts_cselect_b64 exec, 0, -1\n\tbuffer_store_dwordx4 %6, %7, %9, %13 offen sc1\n\ts_not_b64 exec, exec\n\tbuffer_store_dwordx2 %15, %8, %9, %13 offen sc1\n\ts_mov_b64 exec, -1"
                 : "+v"(buf[3].d[0]), "+v"(buf[3].d[1]), "+v"(buf[3].d[2]) : "v"(ev[0]), "v"(ev[1]), "v"(ev[2]), "v"(ev[3]), "v"(eo), "v"(e8), "s"(pr), "s"(es[0]), "s"(es[1]), "s"(es[2]), "s"(es[3]), "s"(wave),
                   "v"(u32x2{ev[3][0], ev[3][1]}) : "memory", "scc");
    if (DBG == 4) tbar = now32();
    SB();
    VCOMP(3)
    asm volatile("" :: "v"(ev[0]), "v"(ev[1]), "v"(ev[2]), "v"(ev[3]));
    record(ra, ca);
    asm volatile("s_waitcnt lgkmcnt(0)" : "+v"(old) :: "memory");
    if (__builtin_amdgcn_readfirstlane(old) == 3) {
        f32x2 p[6];
        ldrec(0, p);
        asm volatile("s_waitcnt lgkmcnt(0)" : "+v"(p[0]), "+v"(p[1]), "+v"(p[2]), "+v"(p[3]), "+v"(p[4]), "+v"(p[5]) :: "memory");
        const f32x2 u = p[4] + p[5];
        __builtin_amdgcn_raw_buffer_store_b64(pack((p[0] + p[1]) + (p[2] + p[3]), u[0] + u[1]), pr, e8, es[4], 16);
    }
    unsigned tend = DS ? now32() : 0;
    unsigned tfl = 0, tpl = 0;

    if (cn < NC) {
        unsigned tgt = sched - stag + (hc == H - 1 ? HL : HOLD) * 16;
        unsigned rs[8];
#pragma unroll
        for (int i = 0; i < 8; ++i) rs[i] = area + (hc * nb + 32 * i + 8 * wave) * LS + (cn >> 5) * 128;
        const unsigned g = lane & 7, e = (lane >> 3) & 1;
        unsigned xo = (lane >> 3) * LS + g * 16;
        unsigned oo = r == 3 ? 0x80000000u : (hc * DH + (cn >> 5) * 16 + 2 * g + (r == 2 ? 128 + e : r + 64 * e)) * 2;
        unsigned po = lds0 + LDS_QRAW + lane * 4, wo = po + wave * 256;
        unsigned tb = (lane & 1) ? t32 : (unsigned)tag, ta = tb << 16;
        const rsrc_t orr = mkrsrc(a.out, H * DH * 2);
        asm volatile("" : "+s"(rs[0]), "+s"(rs[1]), "+s"(rs[2]), "+s"(rs[3]), "+s"(rs[4]), "+s"(rs[5]), "+s"(rs[6]), "+s"(rs[7]), "+v"(po), "+v"(wo), "+v"(xo), "+v"(oo), "+v"(ta), "+v"(tb), "+s"(tgt) : "s"(orr) : "memory");
        i32x4 x[8];
        while ((int)((now32() << 4) - tgt) < 0) {}
        if (DS) tfl = now32();
#define XL(i) x[i] = bld128<16>(pr, xo, rs[i]); SB();
#define XS(i) XL(i) __builtin_amdgcn_s_sleep((SL >> (2 * (i))) & 3); SB();
        XS(0) XS(1) XS(2) XS(3) XS(4) XS(5) XS(6) XL(7)
        if (DBG == 4) th4 = now32();
        SB();
        f32x2 o[3];
#pragma unroll 1
        for (int n = 0; n < (1 << 20); ++n) {
            unsigned bad = 0;
#pragma unroll
            for (int i = 0; i < 8; ++i) {
#pragma unroll
                for (int e = 0; e < 3; ++e) {
                    const f32x2 t = e < 2 ? f32x2{bflo(x[i][2 * e]), bfhi(x[i][2 * e])} : f32x2{bflo(x[i][1]), bflo(x[i][3])};
                    if (i == 0) asm volatile("v_pk_mul_f32 %0, %1, %2 op_sel_hi:[0,1]" : "=v"(o[e]) : "v"(fs[0]), "v"(t));
                    else if (i & 1) asm volatile("v_pk_fma_f32 %0, %1, %2, %0 op_sel:[1,0,0]" : "+v"(o[e]) : "v"(fs[i >> 1]), "v"(t));
                    else asm volatile("v_pk_fma_f32 %0, %1, %2, %0 op_sel_hi:[0,1,1]" : "+v"(o[e]) : "v"(fs[i >> 1]), "v"(t));
                }
                bad = __builtin_amdgcn_bitop3_b32(bad, x[i][1], ta, 0xf6);
                bad = __builtin_amdgcn_bitop3_b32(bad, x[i][3], tb, 0xf6);
                asm volatile("" : "+v"(bad));
                SB();
            }
            if (__builtin_expect(__all(bad < 0x10000u), 1)) break;
            asm volatile("" ::: "memory");
            XL(0) XL(1) XL(2) XL(3) XL(4) XL(5) XL(6) XL(7)
        }
        if (DS) { asm volatile("" : "+v"(o[0]), "+v"(o[1]), "+v"(o[2])); tpl = now32(); }
        const float w0 = sw16(sw32(o[0][0], o[2][0]), sw32(o[1][0], o[1][0])), w1 = sw16(sw32(o[0][1], o[2][1]), sw32(o[1][1], o[1][1]));
        asm volatile("ds_write_b32 %0, %1\n\ts_waitcnt lgkmcnt(0)\n\ts_barrier" :: "v"(wo), "v"((c < 8 ? w0 : w1) + dpp<0x128>(c < 8 ? w1 : w0)) : "memory");
        if (wave == 0) {
            f32x2 p0, p1;
            asm volatile("ds_read2st64_b32 %0, %2 offset1:1\n\tds_read2st64_b32 %1, %2 offset0:2 offset1:3\n\ts_waitcnt lgkmcnt(0)" : "=&v"(p0), "=&v"(p1) : "v"(po) : "memory");
            p0 += p1;
            __builtin_amdgcn_raw_buffer_store_b16((unsigned short)pk_bf16(p0[0] + p0[1], 0.f), orr, oo, 0, (OPT & 8192) ? 16 : (OPT & 16384) ? 2 : 0);
        }
        if (b == 0 && tid == 64) *reinterpret_cast<u64*>(a.part + pep) = tag;
        asm volatile(".Lkv_b%c0:" :: "i"(ID));
    }
    if (DBG) {
        const unsigned tc1 = now32();
        if (!DS) tend = tbar = th4 = tfl = tpl = tc1;
        const unsigned tl16 = sched - stag - step;
        u64* stp = reinterpret_cast<u64*>(a.part + STAMP_OFF) + (size_t)((DBG == 4 || DBG == 24 || DBG == 6 ? cn : b) * 4 + wave) * 8;

        if (lane == 0) { stp[0] = tstart; stp[1] = tbar; stp[2] = DBG >= 4 ? tend - (((tend << 4) - tl16) >> 4) : th1; stp[3] = DBG == 4 && cn >= NC ? tend : th4; stp[4] = tend; stp[5] = cn < NC ? tfl : tend; stp[6] = cn < NC ? tpl : tend; stp[7] = tc1; }
        if (DBG == 2) *reinterpret_cast<u32x4*>(a.part + STAMP_OFF + 65536 + (b * 4 + wave) * 1024 + lane * 16) = *reinterpret_cast<const u32x4*>(smem + LDS_TR + 16 + wave * 1024 + lane * 16);
    }
}

template <int TP, int ADV, int TMIN, int WIN, int OPT, int EX, int GS, int HOLD, int HL, int SL, int EV, int ID, int DBG>
static void launch46(void* const* d_in, const int* in_sizes, void* d_out, void* d_ws, hipStream_t stream) {
    typedef const unsigned short* P;
    Args a;
    a.qk = (const int*)d_in[1]; a.qv = (const int*)d_in[4];
    a.k_cos = (P)d_in[9]; a.k_sin = (P)d_in[10];
    a.q = (P)d_in[0]; a.q_cos = (P)d_in[7]; a.q_sin = (P)d_in[8];
    a.sc[0] = (P)d_in[2]; a.sc[1] = (P)d_in[3]; a.sc[2] = (P)d_in[5]; a.sc[3] = (P)d_in[6];
    a.part = (unsigned char*)d_ws;
    a.out = (unsigned short*)d_out;
    kv46_kernel<TP, ADV, TMIN, WIN, OPT, EX, GS, HOLD, HL, SL, EV, ID, DBG><<<Skv / RW, 256, 0, stream>>>(a);
}

extern "C" void kernel_launch(void* const* d_in, const int* in_sizes, int n_in, void* d_out, int out_size, void* d_ws, size_t ws_size, hipStream_t stream) {
    launch46<80*16-6,220,78*16+8,4+16*32+4096*28,38+64+128+512+1024,66,3,85,100,0x2aaa,5+16*1+256*1,0,0>(d_in, in_sizes, d_out, d_ws, stream);
}
```

```cpp
#include <hip/hip_runtime.h>
#include <hip/hip_bf16.h>
#include <cstdint>
#define KALIGN 256
#define STAMP_OFF (200 << 20)
using bf16 = __hip_bfloat16;
using i32x4 = __attribute__((ext_vector_type(4))) int;
using u32x4 = __attribute__((ext_vector_type(4))) unsigned;
using u32x2 = __attribute__((ext_vector_type(2))) unsigned;
using f32x2 = __attribute__((ext_vector_type(2))) float;
using f32x4 = __attribute__((ext_vector_type(4))) float;

constexpr int DH = 192;
constexpr int HALF = 96;
constexpr int RW = 64;
constexpr int MAXH = 32;

__device__ __forceinline__ float bflo(unsigned u) { return __uint_as_float(u << 16); }
__device__ __forceinline__ float bfhi(unsigned u) { return __uint_as_float(u & 0xffff0000u); }
__device__ __forceinline__ float rbf(float x) { return __bfloat162float(__float2bfloat16(x)); }

template <int CTRL> __device__ __forceinline__ float dpp(float x) {
    return __builtin_bit_cast(float, __builtin_amdgcn_mov_dpp(__builtin_bit_cast(int, x), CTRL, 0xf, 0xf, true));
}
__device__ __forceinline__ float row16_allsum(float x) {
    x += dpp<0x128>(x);
    x += dpp<0x124>(x);
    x += dpp<0x122>(x);
    x += dpp<0x121>(x);
    return x;
}
__device__ __forceinline__ float row16_allmax(float x) {
    x = fmaxf(x, dpp<0x128>(x));
    x = fmaxf(x, dpp<0x124>(x));
    x = fmaxf(x, dpp<0x122>(x));
    x = fmaxf(x, dpp<0x121>(x));
    return x;
}
__device__ __forceinline__ float xrow_max(float x) {
    auto s = __builtin_amdgcn_permlane16_swap(__float_as_uint(x), __float_as_uint(x), false, false);
    x = fmaxf(__uint_as_float(s[0]), __uint_as_float(s[1]));
    auto t = __builtin_amdgcn_permlane32_swap(__float_as_uint(x), __float_as_uint(x), false, false);
    return fmaxf(__uint_as_float(t[0]), __uint_as_float(t[1]));
}
__device__ __forceinline__ float xrow_sum(float x) {
    auto s = __builtin_amdgcn_permlane16_swap(__float_as_uint(x), __float_as_uint(x), false, false);
    x = __uint_as_float(s[0]) + __uint_as_float(s[1]);
    auto t = __builtin_amdgcn_permlane32_swap(__float_as_uint(x), __float_as_uint(x), false, false);
    return __uint_as_float(t[0]) + __uint_as_float(t[1]);
}
__device__ __forceinline__ float sw32(float a, float b) {
    auto t = __builtin_amdgcn_permlane32_swap(__float_as_uint(a), __float_as_uint(b), false, false);
    return __uint_as_float(t[0]) + __uint_as_float(t[1]);
}
__device__ __forceinline__ float sw16(float a, float b) {
    auto t = __builtin_amdgcn_permlane16_swap(__float_as_uint(a), __float_as_uint(b), false, false);
    return __uint_as_float(t[0]) + __uint_as_float(t[1]);
}

#define SB() __builtin_amdgcn_sched_barrier(0)
typedef __amdgpu_buffer_rsrc_t rsrc_t;
__device__ __forceinline__ rsrc_t mkrsrc(const void* p, unsigned bytes) {
    return __builtin_amdgcn_make_buffer_rsrc(const_cast<void*>(p), 0, bytes, 0x00020000);
}
template <int AUX> __device__ __forceinline__ i32x4 bld128(rsrc_t r, unsigned voff, unsigned soff) {
    return __builtin_bit_cast(i32x4, __builtin_amdgcn_raw_buffer_load_b128(r, voff, soff, AUX));
}
template <int AUX> __device__ __forceinline__ unsigned bld16(rsrc_t r, unsigned voff, unsigned soff) {
    return __builtin_amdgcn_raw_buffer_load_b16(r, voff, soff, AUX);
}

struct HG { i32x4 d[3]; };
typedef unsigned long long u64;
typedef __attribute__((address_space(3))) unsigned char lds_u8;

__device__ __forceinline__ unsigned pk_bf16(float lo, float hi) {
    unsigned r;
    asm("v_cvt_pk_bf16_f32 %0, %1, %2" : "=v"(r) : "v"(lo), "v"(hi));
    return r;
}
__device__ __forceinline__ unsigned now32() { return (unsigned)__builtin_amdgcn_s_memrealtime(); }

constexpr int LDS_REC = 0;
constexpr int RECW = MAXH * DH * 4;
constexpr int LDS_CS = 0;
constexpr int LDS_QT = 12288;
constexpr int LDS_PF = 14336;
constexpr int LDS_QRAW = MAXH * 4 * DH * 4;
constexpr int LDS_Q = LDS_QRAW + MAXH * DH * 2;
constexpr int LDS_P = LDS_Q + 4 * DH * 4;
constexpr int LDS_SC = LDS_P + 4 * MAXH * 128;
constexpr int LDS_CNT = LDS_SC + 4 * MAXH * RW * 2;
constexpr int LDS_SK = LDS_CNT + 256;
constexpr int LDS_TR = LDS_SK + 1024;
constexpr int LDS_BYTES = LDS_TR + 4096 + 64;

struct Args {
    const int* qk; const int* qv;
    const unsigned short* k_cos; const unsigned short* k_sin;
    const unsigned short* q; const unsigned short* q_cos; const unsigned short* q_sin;
    unsigned char* part;
    unsigned short* out;
    const unsigned short* sc[4];
};
constexpr unsigned Skv = 16384;
constexpr float SCALE_LOG2 = 0.10411754627697264f;

template <int TP, int ADV, int TMIN, int WIN, int HC, int OPT, int EX, int GS, int HOLD, int HL, int SL, int EV, int ID, int DBG>
__global__ __launch_bounds__(256) __attribute__((aligned(KALIGN))) void kv49_kernel(Args a)
{
    __shared__ __attribute__((aligned(16))) unsigned char smem[LDS_BYTES];
    const unsigned tstart = DBG ? now32() : 0;
    constexpr bool DS = (DBG && DBG < 5) || DBG == 24;
    constexpr int H = MAXH, ES = 7, PFN = 16;
    const int b = blockIdx.x;
    const int tid = threadIdx.x;
    const int lane = tid & 63;
    const int wave = __builtin_amdgcn_readfirstlane(tid >> 6);
    typedef const unsigned short* const __attribute__((address_space(4))) * karg_t;
    const unsigned short* sp = ((karg_t)__builtin_amdgcn_kernarg_segment_ptr())[offsetof(Args, sc) / 8 + wave];
    if (b < PFN) {
        u64 pc; unsigned len, tl;
        asm volatile("s_getpc_b64 %0\n.Lkv_a%c3:\n\ts_mov_b32 %1, .Lkv_b%c3-.Lkv_a%c3-16\n\ts_mov_b32 %2, .Lkv_t%c3-.Lkv_a%c3" : "=s"(pc), "=s"(len), "=s"(tl) : "i"(ID));
        const unsigned o = tid * 16;
        asm volatile("s_mov_b32 m0, %5\n\ts_nop 0\n\tglobal_load_lds_dwordx4 %0, %4\n\tglobal_load_lds_dwordx4 %1, %4\n\tglobal_load_lds_dwordx4 %2, %4\n\tglobal_load_lds_dwordx4 %3, %4"
                     :: "v"(o), "v"(o + 4096), "v"(min(o + 8192, tl)), "v"(min(o + tl, len)), "s"(pc), "s"((unsigned)(size_t)(lds_u8*)smem + LDS_PF) : "memory");
    }
    SB();
    const int r = lane >> 4;
    const int c = lane & 15;
    constexpr unsigned hstride = Skv * (DH * 4);
    constexpr unsigned nb = Skv / RW;
    const unsigned lds0 = (unsigned)(size_t)(lds_u8*)smem;
    const unsigned pos = (b & 7) * (nb >> 3) + (b >> 3);
    const unsigned sb = ((((b >> 3) * 13) & 31) << 3) | (b & 7);
    const unsigned row0 = RW * pos + 16 * wave;

    constexpr unsigned LS = 512, psk = 0, pep = 128, AREA = 6u << 20, NAREA = 32, pfl = H * nb * LS;
    const rsrc_t pr = mkrsrc(a.part, (NAREA + 1) * AREA);
    u64 t64;
    {
        unsigned vs = ((lane >> 3) * Skv + RW * pos + (lane & 7) * 8) * 2, v1 = vs + 16 * Skv, v2 = vs + 32 * Skv, v3 = vs + 48 * Skv;
        unsigned ds = lds0 + LDS_SC + wave * 4096;
        asm volatile("" : "+v"(vs), "+v"(v1), "+v"(v2), "+v"(v3), "+s"(ds));
        SB();
        asm volatile("s_memrealtime %0\n\ts_mov_b32 m0, %6\n\ts_nop 0\n\tglobal_load_lds_dwordx4 %1, %5 nt\n\t"
                     "s_add_u32 m0, m0, 1024\n\ts_nop 0\n\tglobal_load_lds_dwordx4 %2, %5 nt\n\t"
                     "s_add_u32 m0, m0, 1024\n\ts_nop 0\n\tglobal_load_lds_dwordx4 %3, %5 nt\n\t"
                     "s_add_u32 m0, m0, 1024\n\ts_nop 0\n\tglobal_load_lds_dwordx4 %4, %5 nt\n\ts_waitcnt lgkmcnt(0)"
                     : "=&s"(t64) : "v"(vs), "v"(v1), "v"(v2), "v"(v3), "s"(sp), "s"(ds),
                       "s"(a.qk), "s"(a.qv), "s"(a.k_cos), "s"(a.k_sin), "s"(a.q), "s"(a.q_cos), "s"(a.q_sin), "s"(a.part), "s"(a.out) : "memory", "scc");
    }
    SB();
    const unsigned tdma = (unsigned)t64;
#define DMA3(V, A, B, P) "s_mov_b32 m0, " B "\n\ts_nop 0\n\t" \
                     "global_load_lds_dwordx4 " V ", " A P "\n\t" \
                     "global_load_lds_dwordx4 " V ", " A " offset:1024" P "\n\t" \
                     "global_load_lds_dwordx4 " V ", " A " offset:2048" P "\n\t"
    asm volatile(DMA3("%0", "%1", "%2", "") "s_mov_b32 m0, %4\n\ts_nop 0\n\tglobal_load_lds_dwordx4 %3, off"
                 :: "v"(lane * 16), "s"(a.q + wave * 1536), "s"(lds0 + LDS_QRAW + wave * 3072),
                    "v"(lane < 48 ? (const unsigned char*)((lane < 24 ? a.q_cos : a.q_sin - DH) + 8 * lane) : a.part + pep), "s"(lds0 + LDS_QT + wave * RECW) : "memory");
    if (OPT & 32768) {
        const unsigned char* pp = wave == 0 ? (const unsigned char*)a.k_cos + row0 * (DH * 2) : wave == 1 ? (const unsigned char*)a.k_sin + row0 * (DH * 2) : (const unsigned char*)a.qk + row0 * (DH * 4) + (wave - 2) * hstride;
        if ((OPT & 65536) || b < 32) asm volatile("s_mov_b32 m0, %2\n\ts_mov_b64 exec, 1\n\tglobal_load_lds_dword %0, %1 nt\n\ts_mov_b64 exec, -1" :: "v"(0), "s"(pp), "s"(lds0 + LDS_PF) : "memory");
    }
    SB();
    const int stag = (OPT & 32) ? (int)(((sb & ~7) + wave * 2 + (b & 1)) * (TP / 2)) / 256 - TP / 4 : (int)(((OPT & 2) ? (((b >> 3) * 26 + wave * 2 + (b & 1)) & 63) : ((((b >> 3) * 4 + wave) * 37) & 63)) * (TP / 2)) / 64 - TP / 4;
    unsigned sched = ((tdma + ADV) << 4) + stag - 4 * (TP / 2);
    unsigned vt = tdma << 4;
    int tru = -4;
    unsigned calm = vt + 1200 * 16;
    unsigned step = TP / 2, tmin = TMIN / 2, vth = WIN & 15;
    auto pace = [&](bool ctl = false) {
        unsigned tgt = sched;
        sched += step;
        if ((int)(vt - tgt) > 0) tgt = vt;
        unsigned t = now32() << 4;
        while ((int)(t - tgt) < 0) { __builtin_amdgcn_s_sleep(1); t = now32() << 4; }
        vt = ((int)(t - 128 - tgt) > 0 ? t - 128 : tgt) + tmin;
        if (ctl) {
            if constexpr (WIN > 0 && WIN < 16) asm volatile("s_waitcnt vmcnt(%0)" :: "i"(WIN) : "memory");
            if constexpr (WIN >= 16) {
                const unsigned vm = __builtin_amdgcn_s_getreg(7 | (3 << 11));
                constexpr int TH2 = HC & 15, D2 = (HC >> 4) & 255, BIG = (HC >> 12) & 255, RB = (HC >> 20) & 255;
                if constexpr (RB != 0) { if ((int)(t - tgt) > RB * 16 && (int)(t - calm) > 0) { sched += 220 * 16; calm = t + 900 * 16; } }
                if (vm > vth) {
                    if ((int)(vt - sched) < (WIN >> 12) * 16) vt += (vm - vth) * ((WIN >> 4) & 255);
                    else if (TH2 && vm > TH2 && (int)(vt - sched) > BIG * 16) vt += (vm - TH2) * D2;
                }
            }
        }
        if constexpr (DBG == 2) { *reinterpret_cast<unsigned*>(smem + LDS_TR + 16 + wave * 1024 + (tru & 255) * 4) = ((t - (sched - step)) >> 4) | ((OPT & 8) ? (__builtin_amdgcn_s_getreg(7 | (3 << 11)) << 16) : 0); ++tru; }
    };
    {
        unsigned dst = lds0 + LDS_CS + wave * RECW;
        const unsigned v0 = lane * 16 + row0 * (DH * 2);
#pragma unroll 1
        for (int j = 0; j < 4; ++j) {
            pace();
            asm volatile(DMA3("%0", "%1", "%2", " nt") :: "v"(v0), "s"((j < 2 ? a.k_cos : a.k_sin) + (j & 1) * 8 * DH), "s"(dst) : "memory");
            dst += 3072;
        }
    }
    if (tid < MAXH) *reinterpret_cast<unsigned*>(smem + LDS_CNT + tid * 4) = 0;
    const rsrc_t kr = mkrsrc(a.qk, H * hstride);
    const rsrc_t vr = mkrsrc(a.qv, H * hstride);
    const unsigned kvoff = (row0 + r) * (DH * 4) + c * 16;
    auto issue = [&](HG& g, rsrc_t rs, unsigned so) {
#pragma unroll
        for (int x = 0; x < 3; ++x) g.d[x] = bld128<2>(rs, kvoff + 256 * x, so);
    };
    HG buf[4];
#define PRE(g) SB(); pace(); issue(buf[g], kr, (g) * (4 * DH * 4)); SB();
    asm volatile("s_waitcnt vmcnt(12) lgkmcnt(0)\n\ts_barrier" ::: "memory");
    unsigned tbar = DS ? now32() : 0;
    PRE(0)
    const unsigned qoff = (lane < 48 ? lane : lane - 48) * 4;
    const unsigned* qt = reinterpret_cast<const unsigned*>(smem + LDS_QT + wave * RECW + qoff);
    unsigned qc0 = qt[0], qc1 = qt[HALF / 2], qs0 = qt[DH / 2], qs1 = qt[(DH + HALF) / 2];
    u64 tag;
    {
        const u32x2 e = *reinterpret_cast<const u32x2*>(smem + LDS_QT + wave * RECW + 768);
        tag = (((u64)__builtin_amdgcn_readfirstlane(e[1]) << 32) | __builtin_amdgcn_readfirstlane(e[0])) * 6364136223846793005ull + 1442695040888963407ull;
        asm volatile("" : "+s"(tag));
    }
    const unsigned t32 = tag >> 32;
    const unsigned char* qraw = smem + LDS_QRAW + qoff;
    unsigned char* qrot = smem + LDS_Q + wave * (DH * 4) + 2 * qoff;
    auto rotq = [&]() {
        const unsigned q_lo = *reinterpret_cast<const unsigned*>(qraw), q_hi = *reinterpret_cast<const unsigned*>(qraw + HALF * 2);
        qraw += DH * 2;
        const f32x2 lo = f32x2{bflo(q_lo), bfhi(q_lo)}, hi = f32x2{bflo(q_hi), bfhi(q_hi)};
        const f32x2 y1 = lo * f32x2{bflo(qc0), bfhi(qc0)}, y2 = -hi * f32x2{bflo(qs0), bfhi(qs0)};
        const f32x2 y3 = hi * f32x2{bflo(qc1), bfhi(qc1)}, y4 = lo * f32x2{bflo(qs1), bfhi(qs1)};
        const unsigned x1 = pk_bf16(y1[0], y1[1]), x2 = pk_bf16(y2[0], y2[1]), x3 = pk_bf16(y3[0], y3[1]), x4 = pk_bf16(y4[0], y4[1]);
        const f32x2 z1 = f32x2{bflo(x1), bfhi(x1)} + f32x2{bflo(x2), bfhi(x2)}, z2 = f32x2{bflo(x3), bfhi(x3)} + f32x2{bflo(x4), bfhi(x4)};
        const unsigned l2 = pk_bf16(z1[0], z1[1]), h2 = pk_bf16(z2[0], z2[1]);
        *reinterpret_cast<f32x2*>(qrot) = f32x2{bflo(l2), bfhi(l2)};
        *reinterpret_cast<f32x2*>(qrot + HALF * 4) = f32x2{bflo(h2), bfhi(h2)};
        asm volatile("" ::: "memory");
    };
    f32x4 qa[3], qb[3];
    const unsigned char* qh = smem + LDS_Q + wave * (DH * 4) + 16 * c;
    const unsigned char* qh1 = qh + (c < 8 ? 64 + HALF : 64 - HALF) * 4;
    auto loadq = [&]() {
        asm volatile("" ::: "memory");
#pragma unroll
        for (int x = 0; x < 3; ++x) qa[x] = *reinterpret_cast<const f32x4*>(qh + 256 * x);
        qb[0] = *reinterpret_cast<const f32x4*>(qh + HALF * 4);
        qb[1] = *reinterpret_cast<const f32x4*>(qh1);
        qb[2] = *reinterpret_cast<const f32x4*>(qh + (128 - HALF) * 4);
        asm volatile("" ::: "memory");
    };
    rotq();
    constexpr unsigned SKS = (OPT & 4096) ? 256 : 16;
    if (b < 8 && wave == 3) __builtin_amdgcn_raw_buffer_store_b128(u32x4{tdma, t32, tdma, (unsigned)tag}, pr, lane < ((OPT & 4096) ? 8 : 1) ? psk + b * SKS + lane * 16 : 0x80000000u, 0, 16);
    PRE(1)
    f32x2 cc[4][3][2], ss[4][3][2];
    const unsigned char* cb = smem + LDS_CS + wave * RECW + r * (DH * 2) + c * 8;
    asm volatile("s_waitcnt vmcnt(12)" ::: "memory");
#pragma unroll
    for (int k = 0; k < 4; ++k)
#pragma unroll
        for (int x = 0; x < 3; ++x) {
            const u32x2 t = *reinterpret_cast<const u32x2*>(cb + k * 1536 + 128 * x);
            cc[k][x][0] = f32x2{bflo(t[0]), bfhi(t[0])}; cc[k][x][1] = f32x2{bflo(t[1]), bfhi(t[1])};
            asm volatile("" : "+v"(cc[k][x][0]), "+v"(cc[k][x][1]));
        }
    PRE(2)
    loadq();
    const unsigned char* scp = smem + LDS_SC + (c & 4) * 1024 + (16 * wave + 4 * (c & 3) + r) * 2;
    unsigned sc = *reinterpret_cast<const unsigned short*>(scp);
    auto loadsin = [&](int k) {
        const unsigned char* sb1 = cb + (c < 8 ? (64 + HALF) * 2 : (64 - HALF) * 2);
        const unsigned sg = c < 8 ? 0u : 0x80008000u;
        u32x2 t[3];
        t[0] = *reinterpret_cast<const u32x2*>(cb + 6144 + k * 1536 + HALF * 2);
        t[1] = *reinterpret_cast<const u32x2*>(sb1 + 6144 + k * 1536) ^ u32x2{sg, sg};
        t[2] = *reinterpret_cast<const u32x2*>(cb + 6144 + k * 1536 + (128 - HALF) * 2) ^ u32x2{0x80008000u, 0x80008000u};
#pragma unroll
        for (int x = 0; x < 3; ++x) {
            ss[k][x][0] = f32x2{bflo(t[x][0]), bfhi(t[x][0])}; ss[k][x][1] = f32x2{bflo(t[x][1]), bfhi(t[x][1])};
            asm volatile("" : "+v"(ss[k][x][0]), "+v"(ss[k][x][1]));
        }
    };
    asm volatile("s_waitcnt vmcnt(12)" ::: "memory");
    loadsin(0); loadsin(1);
    PRE(3)
    asm volatile("s_waitcnt vmcnt(12)" ::: "memory");
    loadsin(2); loadsin(3);
    unsigned th1 = 0, th4 = 0;
    unsigned char* prec = smem + LDS_P + wave * (H * 128) + r * 32 + (c & 7) * 4;
    auto softmax = [&](float s, int h) {
        float m = fmaxf(s, dpp<0xb1>(s));
        m = xrow_max(fmaxf(m, dpp<0x4e>(m)));
        const float p = __builtin_amdgcn_exp2f(s - m);
        float l = p + dpp<0xb1>(p);
        l = xrow_sum(l + dpp<0x4e>(l));
        const float t = (c & 1) ? l : m;
        const int x = __builtin_amdgcn_update_dpp(__float_as_int(p), __float_as_int(t), 0x114, 0xf, 0xa, false);
        *reinterpret_cast<int*>(prec + h * 128) = x;
    };
    float sprev = 0.f;

#pragma unroll 1
    for (int h = 0; h < H; ++h) {
        const bool more = h + 1 < H;
        const rsrc_t nx = more ? kr : vr;
        const unsigned nso = more ? (h + 1) * hstride : 0;
        float scf = bflo(sc);
        asm volatile("" : "+v"(scf) :: "memory");
        SB();
        scp += more ? 128 : 4096 + 128;
        sc = *reinterpret_cast<const unsigned short*>(scp);
        SB();
        float sreg = 0.f;
#define KSTEP(k) { \
            const float kz = dpp<0x154 + (k)>(scf); \
            f32x2 kz2 = f32x2{kz, kz}; \
            asm("" : "+v"(kz2)); \
            f32x2 a2 = f32x2{0.f, 0.f}; \
            _Pragma("unroll") for (int x = 0; x < 3; ++x) { \
                _Pragma("unroll") for (int e = 0; e < 2; ++e) { \
                    const f32x2 w = __builtin_elementwise_fma(f32x2{qb[x][2 * e], qb[x][2 * e + 1]}, ss[k][x][e], f32x2{qa[x][2 * e], qa[x][2 * e + 1]} * cc[k][x][e]); \
                    const f32x2 kf = f32x2{(float)buf[k].d[x][2 * e], (float)buf[k].d[x][2 * e + 1]} - kz2; \
                    a2 = __builtin_elementwise_fma(kf, w, a2); \
                } \
            } \
            float sa = a2[0] + a2[1]; \
            asm volatile("" : "+v"(sa) :: "memory"); \
            SB(); pace(true); issue(buf[k], nx, nso + (k) * (4 * DH * 4)); SB(); \
            if ((k) == 0) { softmax(sprev, (h - 1) & (H - 1)); SB(); }   \
            if ((k) == 1) { rotq(); SB(); } \
            if ((k) == 2) { \
                if (wave == 1 && h == ((OPT & 4) ? 1 + ((b >> 3) & 3) : 4 + ((b >> 3) & 15))) \
                    asm volatile("s_mov_b32 m0, %2\n\ts_nop 0\n\tglobal_load_lds_dwordx4 %0, %1 sc1" :: "v"(psk + (lane & 7) * SKS), "s"(a.part), "s"(lds0 + LDS_SK) : "memory"); \
                if (h == ((OPT & 4) ? 6 : 25)) {        \
                    const u32x2 e = *reinterpret_cast<const u32x2*>(smem + LDS_SK + (lane & 7) * 16); \
                    const int d = (int)(tdma - e[0]); \
                    if (__all(e[1] == t32 && d > -400 && d < 400)) { \
                        int sum = d + __builtin_amdgcn_mov_dpp(d, 0xb1, 0xf, 0xf, true); \
                        sum += __builtin_amdgcn_mov_dpp(sum, 0x4e, 0xf, 0xf, true); \
                        sum += __builtin_amdgcn_mov_dpp(sum, 0x124, 0xf, 0xf, true); \
                        const int adj = (__builtin_amdgcn_readfirstlane(sum) * ((OPT & 4) ? 73 : 112) + 4096) >> 13;        \
                        step -= adj; tmin -= adj; \
                        if ((OPT & 64) && (OPT & 384) && b < ((OPT & 256) ? 256 : 16) && wave == 0) {        \
                            unsigned t0, t1, t2, t3; \
                            const unsigned ar = (((__builtin_amdgcn_readfirstlane(e[0]) >> 13) & (NAREA - 1)) + 1) * AREA; \
                            asm volatile("s_load_dword %0, %4, %5\n\ts_load_dword %1, %4, %6\n\ts_load_dword %2, %4, %7\n\ts_load_dword %3, %4, %8\n\ts_waitcnt lgkmcnt(0)" : "=&s"(t0), "=&s"(t1), "=&s"(t2), "=&s"(t3) : "s"(a.part), "s"(ar), "s"(ar + AREA - 128), "s"(ar + (2u << 20) + 128), "s"(ar + AREA - (2u << 20) - 128) : "memory"); \
                            vt = now32() << 4; \
                        } \
                    } \
                } \
                SB(); \
            } \
            if ((k) == 3) { loadq(); SB(); } \
            sa = row16_allsum(sa); \
            sreg = (c & 3) == (k) ? sa : sreg; }
        KSTEP(0) KSTEP(1) KSTEP(2) KSTEP(3)
        sprev = sreg * (scf * SCALE_LOG2);
        if (DS) { if (h == 7) th1 = now32(); }
    }
    softmax(sprev, H - 1);
    if (DS) th4 = now32();

    const unsigned tg = (unsigned)(tag >> (16 * (lane & 3))) << 16;
    unsigned rl = lds0 + LDS_REC + lane * 4, rh = rl + 2 * RECW;
    auto pack = [&](f32x2 t, float u) { return u32x2{pk_bf16(t[0], t[1]), pk_bf16(u, 0.f) | tg}; };
    auto ldrec = [&](unsigned o, f32x2 (&p)[6]) {
        asm volatile("ds_read2st64_b32 %0, %6 offset1:1\n\tds_read2st64_b32 %1, %6 offset0:96 offset1:97\n\tds_read2st64_b32 %2, %7 offset1:1\n\tds_read2st64_b32 %3, %7 offset0:96 offset1:97\n\t"
                     "ds_read2st64_b32 %4, %6 offset0:2 offset1:98\n\tds_read2st64_b32 %5, %7 offset0:2 offset1:98"
                     : "=&v"(p[0]), "=&v"(p[1]), "=&v"(p[2]), "=&v"(p[3]), "=&v"(p[4]), "=&v"(p[5]) : "v"(rl + o), "v"(rh + o) : "memory");
    };
    auto merge = [&](int hd) {
        f32x2 p[6], ma, mb;
        asm volatile("ds_read2st64_b32 %0, %2 offset1:16\n\tds_read2st64_b32 %1, %2 offset0:32 offset1:48" : "=&v"(ma), "=&v"(mb) : "v"(lds0 + LDS_P + hd * 128 + 16) : "memory");
        ldrec(hd * (DH * 4), p);
        asm volatile("s_waitcnt lgkmcnt(6)" : "+v"(ma), "+v"(mb) :: "memory");
        const float M = __builtin_amdgcn_fmed3f(__builtin_amdgcn_fmed3f(ma[0], ma[1], __builtin_inff()), __builtin_amdgcn_fmed3f(mb[0], mb[1], __builtin_inff()), __builtin_inff());
        ma = f32x2{__builtin_amdgcn_exp2f(ma[0] - M), __builtin_amdgcn_exp2f(ma[1] - M)};
        mb = f32x2{__builtin_amdgcn_exp2f(mb[0] - M), __builtin_amdgcn_exp2f(mb[1] - M)};
        asm volatile("s_waitcnt lgkmcnt(0)" : "+v"(ma), "+v"(mb), "+v"(p[0]), "+v"(p[1]), "+v"(p[2]), "+v"(p[3]), "+v"(p[4]), "+v"(p[5]) :: "memory");
        const f32x2 u = __builtin_elementwise_fma(mb, p[5], ma * p[4]);
        return pack(ma[0] * p[0] + ma[1] * p[1] + mb[0] * p[2] + mb[1] * p[3], u[0] + u[1]);
    };
    constexpr int NC = 4 * H;
    const int cn = sb;
    const unsigned hc = 4 * (cn & 7) + ((cn >> 3) & 3);
    unsigned old = 0, area = 0, fo = 0;
    u32x4 flv = {};
    float gl = 0.f, mw = 0.f;
    f32x2 fs[4] = {};
    constexpr int LDS_ML = LDS_QRAW + 2048;
#define BAD(y) ((unsigned)(y)[1] != t32 || (unsigned)(y)[3] != (unsigned)tag)
    auto ev_ml = [&]() {
        f32x2 ml[4];
#pragma unroll
        for (int w = 0; w < 4; ++w) ml[w] = *reinterpret_cast<const f32x2*>(smem + LDS_P + w * (H * 128) + (8 * wave + (lane & 7)) * 128 + 16);
        const float M = fmaxf(fmaxf(ml[0][0], ml[1][0]), fmaxf(ml[2][0], ml[3][0]));
        const float L = (__builtin_amdgcn_exp2f(ml[0][0] - M) * ml[0][1] + __builtin_amdgcn_exp2f(ml[1][0] - M) * ml[1][1]) + (__builtin_amdgcn_exp2f(ml[2][0] - M) * ml[2][1] + __builtin_amdgcn_exp2f(ml[3][0] - M) * ml[3][1]);
        flv = u32x4{__float_as_uint(M), t32, __float_as_uint(L), (unsigned)tag};
        asm volatile("" : "+v"(flv));
    };
    auto ev_area = [&]() {
        u32x4 e = *reinterpret_cast<const u32x4*>(smem + LDS_SK);
        float mv[4];
#pragma unroll
        for (int w = 0; w < 4; ++w) mv[w] = *reinterpret_cast<const float*>(smem + LDS_P + ((wave + w) & 3) * (H * 128) + (H - 1) * 128 + 16);
        gl = __builtin_amdgcn_exp2f(mv[0] - fmaxf(fmaxf(mv[0], mv[1]), fmaxf(mv[2], mv[3])));
        if (__builtin_expect(__any(BAD(e)), 0)) {
#pragma unroll 1
            for (int n = 0; n < (1 << 20) && BAD(e); ++n)
                asm volatile("global_load_dwordx4 %0, %1, %2 sc1\n\ts_waitcnt vmcnt(0)" : "=v"(e) : "v"(psk), "s"(a.part) : "memory");
        }
        area = (((__builtin_amdgcn_readfirstlane(e[0]) >> 13) & (NAREA - 1)) + 1) * AREA + 4096;
        asm volatile("" : "+s"(area), "+v"(gl));
    };
    auto ev_put = [&]() {
        asm volatile("s_mov_b64 exec, 0xff\n\tbuffer_store_dwordx4 %0, %1, %2, %3 offen sc1\n\ts_mov_b64 exec, -1" :: "v"(flv), "v"(4 * (rl - lds0 - LDS_REC)), "s"(pr), "s"(area + pfl + (wave * nb + sb) * 128) : "memory");
    };
    auto ev_get = [&]() {
        unsigned l = lane;
        asm volatile("" : "+v"(l));
        fo = area + pfl + ((hc >> 3) * nb + 8 * wave) * 128 + (hc & 7) * 16 + (((l & 56) << 2) + (l & 7)) * 128;
        asm volatile("s_mov_b32 m0, %2\n\ts_nop 0\n\tglobal_load_lds_dwordx4 %0, %1 sc1" :: "v"(fo), "s"(a.part), "s"(lds0 + LDS_ML + wave * 1024) : "memory");
    };
    auto ev_w1 = [&]() {
        u32x4 y = *reinterpret_cast<const u32x4*>(smem + LDS_ML + wave * 1024 + lane * 16);
        if (__builtin_expect(__any(BAD(y)), 0)) {
#pragma unroll 1
            for (int n = 0; n < (1 << 20) && BAD(y); ++n)
                asm volatile("global_load_dwordx4 %0, %1, %2 sc1\n\ts_waitcnt vmcnt(0)" : "=v"(y) : "v"(fo), "s"(a.part) : "memory");
        }
        const float m = __uint_as_float(y[0]);
        float u;
        asm volatile("s_nop 1\n\tv_max_f32_dpp %0, %2, %2 row_ror:8 row_mask:0xf bank_mask:0xf\n\ts_nop 1\n\tv_max_f32_dpp %0, %0, %0 row_ror:4 row_mask:0xf bank_mask:0xf\n\ts_nop 1\n\t"
                     "v_max_f32_dpp %0, %0, %0 row_ror:2 row_mask:0xf bank_mask:0xf\n\ts_nop 1\n\tv_max_f32_dpp %0, %0, %0 row_ror:1 row_mask:0xf bank_mask:0xf\n\tv_mov_b32 %1, %0\n\ts_nop 1\n\t"
                     "v_permlane16_swap_b32 %0, %1\n\ts_nop 0\n\tv_max_f32 %0, %0, %1\n\tv_mov_b32 %1, %0\n\ts_nop 1\n\tv_permlane32_swap_b32 %0, %1\n\ts_nop 0\n\tv_max_f32 %0, %0, %1" : "=&v"(mw), "=&v"(u) : "v"(m));
        fs[0][0] = __builtin_amdgcn_exp2f(m - mw);
        fs[0][1] = fs[0][0] * __uint_as_float(y[2]);
        asm volatile("" : "+v"(fs[0]));
    };
    auto ev_w2 = [&]() {
        const f32x2 f = fs[0];
        unsigned bp = (lane >> 3) * 4;
        asm volatile("ds_bpermute_b32 %0, %8, %9\n\tds_bpermute_b32 %1, %8, %9 offset:32\n\tds_bpermute_b32 %2, %8, %9 offset:64\n\tds_bpermute_b32 %3, %8, %9 offset:96\n\t"
                     "ds_bpermute_b32 %4, %8, %9 offset:128\n\tds_bpermute_b32 %5, %8, %9 offset:160\n\tds_bpermute_b32 %6, %8, %9 offset:192\n\tds_bpermute_b32 %7, %8, %9 offset:224"
                     : "=&v"(fs[0][0]), "=&v"(fs[0][1]), "=&v"(fs[1][0]), "=&v"(fs[1][1]), "=&v"(fs[2][0]), "=&v"(fs[2][1]), "=&v"(fs[3][0]), "=&v"(fs[3][1]) : "v"(bp), "v"(f[0]) : "memory");
        *reinterpret_cast<f32x2*>(smem + LDS_QRAW + 1024 + wave * 8) = f32x2{mw, xrow_sum(row16_allsum(f[1]))};
        asm volatile("s_waitcnt lgkmcnt(0)" : "+v"(fs[0]), "+v"(fs[1]), "+v"(fs[2]), "+v"(fs[3]) :: "memory");
    };
    constexpr int G1 = EV & 15, G2 = (EV >> 4) & 15, G3 = (EV >> 8) & 15;
    const int hput = G1 + G2 + 1 + ((b >> 3) & 3), hget = cn < NC ? G1 + G2 + 6 + ((b >> 3) & 3) : -9;
    const int htch = cn < NC && wave == 0 ? (EV >> 12) + ((b >> 3) & 3) : -9;
    float pv = *reinterpret_cast<const float*>(prec);
    f32x2 acc[3][2];
    float y;
    auto prep = [&]() {
        y = bflo(sc);
        y = (c & 4) ? y : y * pv;
        asm volatile("" : "+v"(y) :: "memory");
        SB();
        scp += 128;
        sc = *reinterpret_cast<const unsigned short*>(scp);
        prec += 128;
        pv = *reinterpret_cast<const float*>(prec);
        SB();
    };
#define VCOMP(k) { \
        const float pw = dpp<0x150 + (k)>(y); \
        const float vz = dpp<0x154 + (k)>(y); \
        f32x2 pw2 = f32x2{pw, pw}, vz2 = f32x2{vz, vz}; \
        asm("" : "+v"(pw2), "+v"(vz2)); \
        _Pragma("unroll") for (int x = 0; x < 3; ++x) \
            _Pragma("unroll") for (int e = 0; e < 2; ++e) { \
                const f32x2 vf = f32x2{(float)buf[k].d[x][2 * e], (float)buf[k].d[x][2 * e + 1]} - vz2; \
                acc[x][e] = (k) == 0 ? pw2 * vf : __builtin_elementwise_fma(pw2, vf, acc[x][e]); \
            } \
        asm volatile("" : "+v"(acc[0][0]), "+v"(acc[0][1]), "+v"(acc[1][0]), "+v"(acc[1][1]), "+v"(acc[2][0]), "+v"(acc[2][1]) :: "memory"); \
        SB(); }
#define VSTEP(k, hn) { \
        VCOMP(k) pace(true); issue(buf[k], vr, (hn) * hstride + (k) * (4 * DH * 4)); SB(); \
        if ((k) == 1) { tmin = (hn) == H - 1 ? 0 : tmin; } \
        if ((k) == 1 && (hn) == G1) { asm volatile("s_waitcnt lgkmcnt(0)\n\ts_barrier" ::: "memory"); SB(); } \
        if ((k) == 2 && (hn) == G1) { ev_ml(); SB(); } \
        if ((k) == 2 && (hn) == G1 + G2) { ev_area(); SB(); } \
        if ((k) == 1 && (OPT & 1024)) { vth = WIN & 15; asm volatile("" : "+s"(vth)); } \
        if ((k) == 1 && (hn) == hput) { ev_put(); if (OPT & 1024) vth = 15; SB(); } \
        if ((k) == 1 && (hn) == hget) { ev_get(); if (OPT & 1024) vth = 15; SB(); } \
        if ((k) == 2 && (hn) == hget + 2) { ev_w1(); SB(); } \
        if ((OPT & 2048) && (k) == 2 && (hn) == htch) { asm volatile("s_mov_b32 m0, %2\n\ts_mov_b64 exec, 63\n\tglobal_load_lds_dword %0, %1\n\ts_mov_b64 exec, -1" :: "v"(hc * (DH * 2) + 16 * (rl - lds0 - LDS_REC)), "s"(a.out), "s"(lds0 + LDS_ML + 4096) : "memory"); vth = 15; SB(); } \
        if ((k) == 1 && (hn) == hget + 2 + G3) { ev_w2(); SB(); } }
    auto record = [&](unsigned ra, unsigned ca) {
        f32x4 o;
#pragma unroll
        for (int e = 0; e < 4; ++e) {
            const float t0 = sw32(acc[0][e >> 1][e & 1], acc[2][e >> 1][e & 1]);
            const float t1 = sw32(acc[1][e >> 1][e & 1], acc[1][e >> 1][e & 1]);
            o[e] = sw16(t0, t1);
        }
        asm volatile("s_mov_b64 exec, %5\n\tds_write_b128 %1, %2\n\ts_mov_b64 exec, 1\n\tds_add_rtn_u32 %0, %3, %4\n\ts_mov_b64 exec, -1"
                     : "+v"(old) : "v"(ra), "v"(o), "v"(ca), "v"(1u), "s"(0xffffffffffffull) : "memory");
        SB();
    };
#pragma unroll 1
    for (int h = 0; h < H - 1; ++h) {
        prep();
        VSTEP(0, h + 1)
        asm volatile("s_waitcnt lgkmcnt(0)" : "+v"(old) :: "memory");
        if (__builtin_amdgcn_readfirstlane(old) == 3) *reinterpret_cast<u32x2*>(smem + LDS_REC + (h - 1) * (DH * 4) + lane * 8) = merge(h - 1);
        SB();
        VSTEP(1, h + 1) VSTEP(2, h + 1) VSTEP(3, h + 1)
        record(lds0 + LDS_REC + h * (DH * 4) + wave * RECW + lane * 16, lds0 + LDS_CNT + h * 4);
    }
    asm volatile("s_branch .Lkv_t%c0\n\t.p2align 12,,3327\n.Lkv_t%c0:" :: "i"(ID));
    y = bflo(sc);
    y = (c & 4) ? y : y * (pv * gl);
    VCOMP(0)
    asm volatile("s_waitcnt lgkmcnt(0)\n\ts_barrier" ::: "memory");
    u32x4 ev[4];
#pragma unroll
    for (int p = 0; p < 4; ++p) ev[p] = *reinterpret_cast<const u32x4*>(smem + LDS_REC + (8 * wave + 2 * p + (lane >> 5)) * (DH * 4) + (lane & 31) * 16);
    const f32x4 a0 = *reinterpret_cast<const f32x4*>(smem + LDS_QRAW + 1024), a1 = *reinterpret_cast<const f32x4*>(smem + LDS_QRAW + 1040);
    SB();
    VCOMP(1)
    unsigned eo = (lane >> 5) * (nb * LS) + (lane & 31) * 16, e8 = lane * 8, ra = lds0 + LDS_REC + (H - 1) * (DH * 4) + wave * RECW + lane * 16, ca = lds0 + LDS_CNT + (H - 1) * 4;
    unsigned es[5];
#pragma unroll
    for (int p = 0; p < 4; ++p) es[p] = area + ((8 * wave + 2 * p) * nb + sb) * LS;
    es[4] = area + ((H - 1) * nb + sb) * LS;
    unsigned tx = sched - step - stag + EX * 16 + (sb >> 3) * GS * 4;
    if (wave == 3) { const u32x2 t = merge(H - 2); ev[3][0] = t[0]; ev[3][1] = t[1]; }
    {
        const float M = fmaxf(fmaxf(a0[0], a0[2]), fmaxf(a1[0], a1[2]));
        const float gw = __builtin_amdgcn_exp2f(mw - M) * __builtin_amdgcn_rcpf((__builtin_amdgcn_exp2f(a0[0] - M) * a0[1] + __builtin_amdgcn_exp2f(a0[2] - M) * a0[3]) + (__builtin_amdgcn_exp2f(a1[0] - M) * a1[1] + __builtin_amdgcn_exp2f(a1[2] - M) * a1[3]));
#pragma unroll
        for (int i = 0; i < 4; ++i) fs[i] *= gw;
    }
    rl += (H - 1) * (DH * 4); rh += (H - 1) * (DH * 4);
    asm volatile("" : "+v"(fs[0]), "+v"(fs[1]), "+v"(fs[2]), "+v"(fs[3]), "+v"(ev[0]), "+v"(ev[1]), "+v"(ev[2]), "+v"(ev[3]), "+v"(eo), "+v"(e8), "+v"(ra), "+v"(ca), "+v"(rl), "+v"(rh),
                      "+s"(es[0]), "+s"(es[1]), "+s"(es[2]), "+s"(es[3]), "+s"(es[4]), "+s"(tx));
    SB();
    VCOMP(2)
    while ((int)((now32() << 4) - tx) < 0) {}
    if constexpr ((OPT & 512) != 0)
    asm volatile("s_waitcnt vmcnt(0)\n\tbuffer_store_dwordx4 %3, %7, %9, %10 offen sc1\n\tbuffer_store_dwordx4 %4, %7, %9, %11 offen sc1\n\tbuffer_store_dwordx4 %5, %7, %9, %12 offen sc1\n\t"
                 "s_cmp_eq_u32 %14, 3\n\ts_cbranch_scc1 1f\n\tbuffer_store_dwordx4 %6, %7, %9, %13 offen sc1\n\ts_branch 2f\n1:\tbuffer_store_dwordx2 %15, %8, %9, %13 offen sc1\n2:"
                 : "+v"(buf[3].d[0]), "+v"(buf[3].d[1]), "+v"(buf[3].d[2]) : "v"(ev[0]), "v"(ev[1]), "v"(ev[2]), "v"(ev[3]), "v"(eo), "v"(e8), "s"(pr), "s"(es[0]), "s"(es[1]), "s"(es[2]), "s"(es[3]), "s"(wave),
                   "v"(u32x2{ev[3][0], ev[3][1]}) : "memory", "scc");
    else
    asm volatile("s_waitcnt vmcnt(0)\n\tbuffer_store_dwordx4 %3, %7, %9, %10 offen sc1\n\tbuffer_store_dwordx4 %4, %7, %9, %11 offen sc1\n\tbuffer_store_dwordx4 %5, %7, %9, %12 offen sc1\n\t"
                 "s_cmp_eq_u32 %14, 3\n\ts_cselect_b64 exec, 0, -1\n\tbuffer_store_dwordx4 %6, %7, %9, %13 offen sc1\n\ts_not_b64 exec, exec\n\tbuffer_store_dwordx2 %15, %8, %9, %13 offen sc1\n\ts_mov_b64 exec, -1"
                 : "+v"(buf[3].d[0]), "+v"(buf[3].d[1]), "+v"(buf[3].d[2]) : "v"(ev[0]), "v"(ev[1]), "v"(ev[2]), "v"(ev[3]), "v"(eo), "v"(e8), "s"(pr), "s"(es[0]), "s"(es[1]), "s"(es[2]), "s"(es[3]), "s"(wave),
                   "v"(u32x2{ev[3][0], ev[3][1]}) : "memory", "scc");
    if (DBG == 4) tbar = now32();
    SB();
    VCOMP(3)
    asm volatile("" :: "v"(ev[0]), "v"(ev[1]), "v"(ev[2]), "v"(ev[3]));
    record(ra, ca);
    asm volatile("s_waitcnt lgkmcnt(0)" : "+v"(old) :: "memory");
    if (__builtin_amdgcn_readfirstlane(old) == 3) {
        f32x2 p[6];
        ldrec(0, p);
        asm volatile("s_waitcnt lgkmcnt(0)" : "+v"(p[0]), "+v"(p[1]), "+v"(p[2]), "+v"(p[3]), "+v"(p[4]), "+v"(p[5]) :: "memory");
        const f32x2 u = p[4] + p[5];
        __builtin_amdgcn_raw_buffer_store_b64(pack((p[0] + p[1]) + (p[2] + p[3]), u[0] + u[1]), pr, e8, es[4], 16);
    }
    unsigned tend = DS ? now32() : 0;
    unsigned tfl = 0, tpl = 0;

    if (cn < NC) {
        unsigned tgt = sched - stag + (hc == H - 1 ? HL : HOLD) * 16;
        unsigned rs[8];
#pragma unroll
        for (int i = 0; i < 8; ++i) rs[i] = area + (hc * nb + 32 * i + 8 * wave) * LS + (cn >> 5) * 128;
        const unsigned g = lane & 7, e = (lane >> 3) & 1;
        unsigned xo = (lane >> 3) * LS + g * 16;
        unsigned oo = r == 3 ? 0x80000000u : (hc * DH + (cn >> 5) * 16 + 2 * g + (r == 2 ? 128 + e : r + 64 * e)) * 2;
        unsigned po = lds0 + LDS_QRAW + lane * 4, wo = po + wave * 256;
        unsigned tb = (lane & 1) ? t32 : (unsigned)tag, ta = tb << 16;
        const rsrc_t orr = mkrsrc(a.out, H * DH * 2);
        asm volatile("" : "+s"(rs[0]), "+s"(rs[1]), "+s"(rs[2]), "+s"(rs[3]), "+s"(rs[4]), "+s"(rs[5]), "+s"(rs[6]), "+s"(rs[7]), "+v"(po), "+v"(wo), "+v"(xo), "+v"(oo), "+v"(ta), "+v"(tb), "+s"(tgt) : "s"(orr) : "memory");
        i32x4 x[8];
        while ((int)((now32() << 4) - tgt) < 0) {}
        if (DS) tfl = now32();
#define XL(i) x[i] = bld128<16>(pr, xo, rs[i]); SB();
#define XS(i) XL(i) __builtin_amdgcn_s_sleep((SL >> (2 * (i))) & 3); SB();
        XS(0) XS(1) XS(2) XS(3) XS(4) XS(5) XS(6) XL(7)
        if (DBG == 4) th4 = now32();
        SB();
        f32x2 o[3];
#pragma unroll 1
        for (int n = 0; n < (1 << 20); ++n) {
            unsigned bad = 0;
#pragma unroll
            for (int i = 0; i < 8; ++i) {
#pragma unroll
                for (int e = 0; e < 3; ++e) {
                    const f32x2 t = e < 2 ? f32x2{bflo(x[i][2 * e]), bfhi(x[i][2 * e])} : f32x2{bflo(x[i][1]), bflo(x[i][3])};
                    if (i == 0) asm volatile("v_pk_mul_f32 %0, %1, %2 op_sel_hi:[0,1]" : "=v"(o[e]) : "v"(fs[0]), "v"(t));
                    else if (i & 1) asm volatile("v_pk_fma_f32 %0, %1, %2, %0 op_sel:[1,0,0]" : "+v"(o[e]) : "v"(fs[i >> 1]), "v"(t));
                    else asm volatile("v_pk_fma_f32 %0, %1, %2, %0 op_sel_hi:[0,1,1]" : "+v"(o[e]) : "v"(fs[i >> 1]), "v"(t));
                }
                bad = __builtin_amdgcn_bitop3_b32(bad, x[i][1], ta, 0xf6);
                bad = __builtin_amdgcn_bitop3_b32(bad, x[i][3], tb, 0xf6);
                asm volatile("" : "+v"(bad));
                SB();
            }
            if (__builtin_expect(__all(bad < 0x10000u), 1)) break;
            asm volatile("" ::: "memory");
            XL(0) XL(1) XL(2) XL(3) XL(4) XL(5) XL(6) XL(7)
        }
        if (DS) { asm volatile("" : "+v"(o[0]), "+v"(o[1]), "+v"(o[2])); tpl = now32(); }
        const float w0 = sw16(sw32(o[0][0], o[2][0]), sw32(o[1][0], o[1][0])), w1 = sw16(sw32(o[0][1], o[2][1]), sw32(o[1][1], o[1][1]));
        asm volatile("ds_write_b32 %0, %1\n\ts_waitcnt lgkmcnt(0)\n\ts_barrier" :: "v"(wo), "v"((c < 8 ? w0 : w1) + dpp<0x128>(c < 8 ? w1 : w0)) : "memory");
        if (wave == 0) {
            f32x2 p0, p1;
            asm volatile("ds_read2st64_b32 %0, %2 offset1:1\n\tds_read2st64_b32 %1, %2 offset0:2 offset1:3\n\ts_waitcnt lgkmcnt(0)" : "=&v"(p0), "=&v"(p1) : "v"(po) : "memory");
            p0 += p1;
            __builtin_amdgcn_raw_buffer_store_b16((unsigned short)pk_bf16(p0[0] + p0[1], 0.f), orr, oo, 0, (OPT & 8192) ? 16 : (OPT & 16384) ? 2 : 0);
        }
        if (b == 0 && tid == 64) *reinterpret_cast<u64*>(a.part + pep) = tag;
        asm volatile(".Lkv_b%c0:" :: "i"(ID));
    }
    if (DBG) {
        const unsigned tc1 = now32();
        if (!DS) tend = tbar = th4 = tfl = tpl = tc1;
        const unsigned tl16 = sched - stag - step;
        u64* stp = reinterpret_cast<u64*>(a.part + STAMP_OFF) + (size_t)((DBG == 4 || DBG == 24 || DBG == 6 ? cn : b) * 4 + wave) * 8;

        if (lane == 0) { stp[0] = tstart; stp[1] = tbar; stp[2] = DBG >= 4 ? tend - (((tend << 4) - tl16) >> 4) : th1; stp[3] = DBG == 4 && cn >= NC ? tend : th4; stp[4] = tend; stp[5] = cn < NC ? tfl : tend; stp[6] = cn < NC ? tpl : tend; stp[7] = tc1; }
        if (DBG == 2) *reinterpret_cast<u32x4*>(a.part + STAMP_OFF + 65536 + (b * 4 + wave) * 1024 + lane * 16) = *reinterpret_cast<const u32x4*>(smem + LDS_TR + 16 + wave * 1024 + lane * 16);
    }
}

template <int TP, int ADV, int TMIN, int WIN, int HC, int OPT, int EX, int GS, int HOLD, int HL, int SL, int EV, int ID, int DBG>
static void launch49(void* const* d_in, const int* in_sizes, void* d_out, void* d_ws, hipStream_t stream) {
    typedef const unsigned short* P;
    Args a;
    a.qk = (const int*)d_in[1]; a.qv = (const int*)d_in[4];
    a.k_cos = (P)d_in[9]; a.k_sin = (P)d_in[10];
    a.q = (P)d_in[0]; a.q_cos = (P)d_in[7]; a.q_sin = (P)d_in[8];
    a.sc[0] = (P)d_in[2]; a.sc[1] = (P)d_in[3]; a.sc[2] = (P)d_in[5]; a.sc[3] = (P)d_in[6];
    a.part = (unsigned char*)d_ws;
    a.out = (unsigned short*)d_out;
    kv49_kernel<TP, ADV, TMIN, WIN, HC, OPT, EX, GS, HOLD, HL, SL, EV, ID, DBG><<<Skv / RW, 256, 0, stream>>>(a);
}

extern "C" void kernel_launch(void* const* d_in, const int* in_sizes, int n_in, void* d_out, int out_size, void* d_ws, size_t ws_size, hipStream_t stream) {
    launch49<80*16-2,220,78*16+8,4+16*32+4096*28,4+16*32+4096*70,38+64+128+512+1024,66,3,85,100,0x2aaa,5+16*1+256*1,0,0>(d_in, in_sizes, d_out, d_ws, stream);
}
```

```cpp
#include <hip/hip_runtime.h>
#include <hip/hip_bf16.h>
#include <cstdint>
#define KALIGN 256
#define STAMP_OFF (200 << 20)
using bf16 = __hip_bfloat16;
using i32x4 = __attribute__((ext_vector_type(4))) int;
using u32x4 = __attribute__((ext_vector_type(4))) unsigned;
using u32x2 = __attribute__((ext_vector_type(2))) unsigned;
using f32x2 = __attribute__((ext_vector_type(2))) float;
using f32x4 = __attribute__((ext_vector_type(4))) float;

constexpr int DH = 192;
constexpr int HALF = 96;
constexpr int RW = 64;
constexpr int MAXH = 32;

__device__ __forceinline__ float bflo(unsigned u) { return __uint_as_float(u << 16); }
__device__ __forceinline__ float bfhi(unsigned u) { return __uint_as_float(u & 0xffff0000u); }
__device__ __forceinline__ float rbf(float x) { return __bfloat162float(__float2bfloat16(x)); }

template <int CTRL> __device__ __forceinline__ float dpp(float x) {
    return __builtin_bit_cast(float, __builtin_amdgcn_mov_dpp(__builtin_bit_cast(int, x), CTRL, 0xf, 0xf, true));
}
__device__ __forceinline__ float row16_allsum(float x) {
    x += dpp<0x128>(x);
    x += dpp<0x124>(x);
    x += dpp<0x122>(x);
    x += dpp<0x121>(x);
    return x;
}
__device__ __forceinline__ float row16_allmax(float x) {
    x = fmaxf(x, dpp<0x128>(x));
    x = fmaxf(x, dpp<0x124>(x));
    x = fmaxf(x, dpp<0x122>(x));
    x = fmaxf(x, dpp<0x121>(x));
    return x;
}
__device__ __forceinline__ float xrow_max(float x) {
    auto s = __builtin_amdgcn_permlane16_swap(__float_as_uint(x), __float_as_uint(x), false, false);
    x = fmaxf(__uint_as_float(s[0]), __uint_as_float(s[1]));
    auto t = __builtin_amdgcn_permlane32_swap(__float_as_uint(x), __float_as_uint(x), false, false);
    return fmaxf(__uint_as_float(t[0]), __uint_as_float(t[1]));
}
__device__ __forceinline__ float xrow_sum(float x) {
    auto s = __builtin_amdgcn_permlane16_swap(__float_as_uint(x), __float_as_uint(x), false, false);
    x = __uint_as_float(s[0]) + __uint_as_float(s[1]);
    auto t = __builtin_amdgcn_permlane32_swap(__float_as_uint(x), __float_as_uint(x), false, false);
    return __uint_as_float(t[0]) + __uint_as_float(t[1]);
}
__device__ __forceinline__ float sw32(float a, float b) {
    auto t = __builtin_amdgcn_permlane32_swap(__float_as_uint(a), __float_as_uint(b), false, false);
    return __uint_as_float(t[0]) + __uint_as_float(t[1]);
}
__device__ __forceinline__ float sw16(float a, float b) {
    auto t = __builtin_amdgcn_permlane16_swap(__float_as_uint(a), __float_as_uint(b), false, false);
    return __uint_as_float(t[0]) + __uint_as_float(t[1]);
}

#define SB() __builtin_amdgcn_sched_barrier(0)
typedef __amdgpu_buffer_rsrc_t rsrc_t;
__device__ __forceinline__ rsrc_t mkrsrc(const void* p, unsigned bytes) {
    return __builtin_amdgcn_make_buffer_rsrc(const_cast<void*>(p), 0, bytes, 0x00020000);
}
template <int AUX> __device__ __forceinline__ i32x4 bld128(rsrc_t r, unsigned voff, unsigned soff) {
    return __builtin_bit_cast(i32x4, __builtin_amdgcn_raw_buffer_load_b128(r, voff, soff, AUX));
}
template <int AUX> __device__ __forceinline__ unsigned bld16(rsrc_t r, unsigned voff, unsigned soff) {
    return __builtin_amdgcn_raw_buffer_load_b16(r, voff, soff, AUX);
}

struct HG { i32x4 d[3]; };
typedef unsigned long long u64;
typedef __attribute__((address_space(3))) unsigned char lds_u8;

__device__ __forceinline__ unsigned pk_bf16(float lo, float hi) {
    unsigned r;
    asm("v_cvt_pk_bf16_f32 %0, %1, %2" : "=v"(r) : "v"(lo), "v"(hi));
    return r;
}
__device__ __forceinline__ unsigned now32() { return (unsigned)__builtin_amdgcn_s_memrealtime(); }

constexpr int LDS_REC = 0;
constexpr int RECW = MAXH * DH * 4;
constexpr int LDS_CS = 0;
constexpr int LDS_QT = 12288;
constexpr int LDS_PF = 14336;
constexpr int LDS_QRAW = MAXH * 4 * DH * 4;
constexpr int LDS_Q = LDS_QRAW + MAXH * DH * 2;
constexpr int LDS_P = LDS_Q + 4 * DH * 4;
constexpr int LDS_SC = LDS_P + 4 * MAXH * 128;
constexpr int LDS_CNT = LDS_SC + 4 * MAXH * RW * 2;
constexpr int LDS_SK = LDS_CNT + 256;
constexpr int LDS_TR = LDS_SK + 1024;
constexpr int LDS_BYTES = LDS_TR + 4096 + 64;

struct Args {
    const int* qk; const int* qv;
    const unsigned short* k_cos; const unsigned short* k_sin;
    const unsigned short* q; const unsigned short* q_cos; const unsigned short* q_sin;
    unsigned char* part;
    unsigned short* out;
    const unsigned short* sc[4];
};
constexpr unsigned Skv = 16384;
constexpr float SCALE_LOG2 = 0.10411754627697264f;

template <int TP, int ADV, int TMIN, int WIN, int OPT, int EX, int GS, int HOLD, int HL, int SL, int EV, int ID, int DBG>
__global__ __launch_bounds__(256) __attribute__((aligned(KALIGN))) void kv43_kernel(Args a)
{
    __shared__ __attribute__((aligned(16))) unsigned char smem[LDS_BYTES];
    unsigned tstart; asm volatile("s_memrealtime s[100:101]" ::: "s100", "s101");
    constexpr bool DS = (DBG && DBG < 5) || DBG == 24;
    constexpr int H = MAXH, ES = 7, PFN = 16;
    const int b = blockIdx.x;
    const int tid = threadIdx.x;
    const int lane = tid & 63;
    const int wave = __builtin_amdgcn_readfirstlane(tid >> 6);
    typedef const unsigned short* const __attribute__((address_space(4))) * karg_t;
    const unsigned short* sp = ((karg_t)__builtin_amdgcn_kernarg_segment_ptr())[offsetof(Args, sc) / 8 + wave];
    if (b < PFN) {
        u64 pc; unsigned len, tl;
        asm volatile("s_getpc_b64 %0\n.Lkv_a%c3:\n\ts_mov_b32 %1, .Lkv_b%c3-.Lkv_a%c3-16\n\ts_mov_b32 %2, .Lkv_t%c3-.Lkv_a%c3" : "=s"(pc), "=s"(len), "=s"(tl) : "i"(ID));
        const unsigned o = tid * 16;
        asm volatile("s_mov_b32 m0, %5\n\ts_nop 0\n\tglobal_load_lds_dwordx4 %0, %4\n\tglobal_load_lds_dwordx4 %1, %4\n\tglobal_load_lds_dwordx4 %2, %4\n\tglobal_load_lds_dwordx4 %3, %4"
                     :: "v"(o), "v"(o + 4096), "v"(min(o + 8192, tl)), "v"(min(o + tl, len)), "s"(pc), "s"((unsigned)(size_t)(lds_u8*)smem + LDS_PF) : "memory");
    }
    SB();
    const int r = lane >> 4;
    const int c = lane & 15;
    constexpr unsigned hstride = Skv * (DH * 4);
    constexpr unsigned nb = Skv / RW;
    const unsigned lds0 = (unsigned)(size_t)(lds_u8*)smem;
    const unsigned pos = (b & 7) * (nb >> 3) + (b >> 3);
    const unsigned sb = ((((b >> 3) * 13) & 31) << 3) | (b & 7);
    const unsigned row0 = RW * pos + 16 * wave;

    constexpr unsigned LS = 512, psk = 0, pep = 128, AREA = 6u << 20, NAREA = 32, pfl = H * nb * LS;
    const rsrc_t pr = mkrsrc(a.part, (NAREA + 1) * AREA);
    u64 t64;
    {
        unsigned vs = ((lane >> 3) * Skv + RW * pos + (lane & 7) * 8) * 2, v1 = vs + 16 * Skv, v2 = vs + 32 * Skv, v3 = vs + 48 * Skv;
        unsigned ds = lds0 + LDS_SC + wave * 4096;
        asm volatile("" : "+v"(vs), "+v"(v1), "+v"(v2), "+v"(v3), "+s"(ds));
        SB();
        asm volatile("s_memrealtime %0\n\ts_mov_b32 m0, %6\n\ts_nop 0\n\tglobal_load_lds_dwordx4 %1, %5 nt\n\t"
                     "s_add_u32 m0, m0, 1024\n\ts_nop 0\n\tglobal_load_lds_dwordx4 %2, %5 nt\n\t"
                     "s_add_u32 m0, m0, 1024\n\ts_nop 0\n\tglobal_load_lds_dwordx4 %3, %5 nt\n\t"
                     "s_add_u32 m0, m0, 1024\n\ts_nop 0\n\tglobal_load_lds_dwordx4 %4, %5 nt\n\ts_waitcnt lgkmcnt(0)"
                     : "=&s"(t64) : "v"(vs), "v"(v1), "v"(v2), "v"(v3), "s"(sp), "s"(ds),
                       "s"(a.qk), "s"(a.qv), "s"(a.k_cos), "s"(a.k_sin), "s"(a.q), "s"(a.q_cos), "s"(a.q_sin), "s"(a.part), "s"(a.out) : "memory", "scc");
    }
    SB();
    const unsigned tdma = (unsigned)t64;
    asm volatile("s_mov_b32 %0, s100" : "=s"(tstart));
    int skew = 0;
#define DMA3(V, A, B, P) "s_mov_b32 m0, " B "\n\ts_nop 0\n\t" \
                     "global_load_lds_dwordx4 " V ", " A P "\n\t" \
                     "global_load_lds_dwordx4 " V ", " A " offset:1024" P "\n\t" \
                     "global_load_lds_dwordx4 " V ", " A " offset:2048" P "\n\t"
    asm volatile(DMA3("%0", "%1", "%2", "") "s_mov_b32 m0, %4\n\ts_nop 0\n\tglobal_load_lds_dwordx4 %3, off"
                 :: "v"(lane * 16), "s"(a.q + wave * 1536), "s"(lds0 + LDS_QRAW + wave * 3072),
                    "v"(lane < 48 ? (const unsigned char*)((lane < 24 ? a.q_cos : a.q_sin - DH) + 8 * lane) : a.part + pep), "s"(lds0 + LDS_QT + wave * RECW) : "memory");
    SB();
    const int stag = (OPT & 32) ? (int)(((sb & ~7) + wave * 2 + (b & 1)) * (TP / 2)) / 256 - TP / 4 : (int)(((OPT & 2) ? (((b >> 3) * 26 + wave * 2 + (b & 1)) & 63) : ((((b >> 3) * 4 + wave) * 37) & 63)) * (TP / 2)) / 64 - TP / 4;
    unsigned sched = ((tdma + ADV) << 4) + stag - 4 * (TP / 2);
    unsigned vt = tdma << 4;
    int tru = -4;
    unsigned slip = 0, run = 0;
    unsigned step = TP / 2, tmin = TMIN / 2, vth = WIN & 15;
    auto pace = [&](bool ctl = false) {
        unsigned tgt = sched;
        sched += step;
        if ((int)(vt - tgt) > 0) tgt = vt;
        unsigned t = now32() << 4;
        while ((int)(t - tgt) < 0) { __builtin_amdgcn_s_sleep(1); t = now32() << 4; }
        vt = ((int)(t - 128 - tgt) > 0 ? t - 128 : tgt) + tmin;
        if (ctl) {
            if constexpr (WIN > 0 && WIN < 16) asm volatile("s_waitcnt vmcnt(%0)" :: "i"(WIN) : "memory");
            if constexpr (WIN >= 16) {
                const unsigned vm = __builtin_amdgcn_s_getreg(7 | (3 << 11));
                constexpr int EPS = (WIN >> 20) & 15, NP = (WIN >> 24) & 15, TH2 = (WIN >> 28) & 7;
                if (NP) run = vm > vth ? run + 1 : 0;
                if (vm > vth) {
                    if ((int)(vt - sched) < ((WIN >> 12) & 255) * 16 || (NP && run >= NP)) vt += (vm - vth) * ((WIN >> 4) & 255);
                    else if (TH2) { if (vm > TH2) vt += (vm - TH2) * ((WIN >> 4) & 255); }
                    else if (EPS) { sched += EPS; if (DBG == 2) slip += EPS; }
                }
            }
        }
        if constexpr (DBG == 2) { *reinterpret_cast<unsigned*>(smem + LDS_TR + 16 + wave * 1024 + (tru & 255) * 4) = ((t - (sched - slip - step)) >> 4) | ((OPT & 8) ? (__builtin_amdgcn_s_getreg(7 | (3 << 11)) << 16) : 0); ++tru; }
    };
    {
        unsigned dst = lds0 + LDS_CS + wave * RECW;
        const unsigned v0 = lane * 16 + row0 * (DH * 2);
#pragma unroll 1
        for (int j = 0; j < 4; ++j) {
            pace();
            asm volatile(DMA3("%0", "%1", "%2", " nt") :: "v"(v0), "s"((j < 2 ? a.k_cos : a.k_sin) + (j & 1) * 8 * DH), "s"(dst) : "memory");
            dst += 3072;
        }
    }
    if (tid < MAXH) *reinterpret_cast<unsigned*>(smem + LDS_CNT + tid * 4) = 0;
    const rsrc_t kr = mkrsrc(a.qk, H * hstride);
    const rsrc_t vr = mkrsrc(a.qv, H * hstride);
    const unsigned kvoff = (row0 + r) * (DH * 4) + c * 16;
    auto issue = [&](HG& g, rsrc_t rs, unsigned so) {
#pragma unroll
        for (int x = 0; x < 3; ++x) g.d[x] = bld128<2>(rs, kvoff + 256 * x, so);
    };
    HG buf[4];
#define PRE(g) SB(); pace(); issue(buf[g], kr, (g) * (4 * DH * 4)); SB();
    asm volatile("s_waitcnt vmcnt(12) lgkmcnt(0)\n\ts_barrier" ::: "memory");
    unsigned tbar = DS ? now32() : 0;
    PRE(0)
    const unsigned qoff = (lane < 48 ? lane : lane - 48) * 4;
    const unsigned* qt = reinterpret_cast<const unsigned*>(smem + LDS_QT + wave * RECW + qoff);
    unsigned qc0 = qt[0], qc1 = qt[HALF / 2], qs0 = qt[DH / 2], qs1 = qt[(DH + HALF) / 2];
    u64 tag;
    {
        const u32x2 e = *reinterpret_cast<const u32x2*>(smem + LDS_QT + wave * RECW + 768);
        tag = (((u64)__builtin_amdgcn_readfirstlane(e[1]) << 32) | __builtin_amdgcn_readfirstlane(e[0])) * 6364136223846793005ull + 1442695040888963407ull;
        asm volatile("" : "+s"(tag));
    }
    const unsigned t32 = tag >> 32;
    const unsigned char* qraw = smem + LDS_QRAW + qoff;
    unsigned char* qrot = smem + LDS_Q + wave * (DH * 4) + 2 * qoff;
    auto rotq = [&]() {
        const unsigned q_lo = *reinterpret_cast<const unsigned*>(qraw), q_hi = *reinterpret_cast<const unsigned*>(qraw + HALF * 2);
        qraw += DH * 2;
        const f32x2 lo = f32x2{bflo(q_lo), bfhi(q_lo)}, hi = f32x2{bflo(q_hi), bfhi(q_hi)};
        const f32x2 y1 = lo * f32x2{bflo(qc0), bfhi(qc0)}, y2 = -hi * f32x2{bflo(qs0), bfhi(qs0)};
        const f32x2 y3 = hi * f32x2{bflo(qc1), bfhi(qc1)}, y4 = lo * f32x2{bflo(qs1), bfhi(qs1)};
        const unsigned x1 = pk_bf16(y1[0], y1[1]), x2 = pk_bf16(y2[0], y2[1]), x3 = pk_bf16(y3[0], y3[1]), x4 = pk_bf16(y4[0], y4[1]);
        const f32x2 z1 = f32x2{bflo(x1), bfhi(x1)} + f32x2{bflo(x2), bfhi(x2)}, z2 = f32x2{bflo(x3), bfhi(x3)} + f32x2{bflo(x4), bfhi(x4)};
        const unsigned l2 = pk_bf16(z1[0], z1[1]), h2 = pk_bf16(z2[0], z2[1]);
        *reinterpret_cast<f32x2*>(qrot) = f32x2{bflo(l2), bfhi(l2)};
        *reinterpret_cast<f32x2*>(qrot + HALF * 4) = f32x2{bflo(h2), bfhi(h2)};
        asm volatile("" ::: "memory");
    };
    f32x4 qa[3], qb[3];
    const unsigned char* qh = smem + LDS_Q + wave * (DH * 4) + 16 * c;
    const unsigned char* qh1 = qh + (c < 8 ? 64 + HALF : 64 - HALF) * 4;
    auto loadq = [&]() {
        asm volatile("" ::: "memory");
#pragma unroll
        for (int x = 0; x < 3; ++x) qa[x] = *reinterpret_cast<const f32x4*>(qh + 256 * x);
        qb[0] = *reinterpret_cast<const f32x4*>(qh + HALF * 4);
        qb[1] = *reinterpret_cast<const f32x4*>(qh1);
        qb[2] = *reinterpret_cast<const f32x4*>(qh + (128 - HALF) * 4);
        asm volatile("" ::: "memory");
    };
    rotq();
    if (b < 8 && wave == 3) __builtin_amdgcn_raw_buffer_store_b128(u32x4{tdma, t32, tdma, (unsigned)tag}, pr, lane == 0 ? psk + b * 16 : 0x80000000u, 0, 16);
    PRE(1)
    f32x2 cc[4][3][2], ss[4][3][2];
    const unsigned char* cb = smem + LDS_CS + wave * RECW + r * (DH * 2) + c * 8;
    asm volatile("s_waitcnt vmcnt(12)" ::: "memory");
#pragma unroll
    for (int k = 0; k < 4; ++k)
#pragma unroll
        for (int x = 0; x < 3; ++x) {
            const u32x2 t = *reinterpret_cast<const u32x2*>(cb + k * 1536 + 128 * x);
            cc[k][x][0] = f32x2{bflo(t[0]), bfhi(t[0])}; cc[k][x][1] = f32x2{bflo(t[1]), bfhi(t[1])};
            asm volatile("" : "+v"(cc[k][x][0]), "+v"(cc[k][x][1]));
        }
    PRE(2)
    loadq();
    const unsigned char* scp = smem + LDS_SC + (c & 4) * 1024 + (16 * wave + 4 * (c & 3) + r) * 2;
    unsigned sc = *reinterpret_cast<const unsigned short*>(scp);
    auto loadsin = [&](int k) {
        const unsigned char* sb1 = cb + (c < 8 ? (64 + HALF) * 2 : (64 - HALF) * 2);
        const unsigned sg = c < 8 ? 0u : 0x80008000u;
        u32x2 t[3];
        t[0] = *reinterpret_cast<const u32x2*>(cb + 6144 + k * 1536 + HALF * 2);
        t[1] = *reinterpret_cast<const u32x2*>(sb1 + 6144 + k * 1536) ^ u32x2{sg, sg};
        t[2] = *reinterpret_cast<const u32x2*>(cb + 6144 + k * 1536 + (128 - HALF) * 2) ^ u32x2{0x80008000u, 0x80008000u};
#pragma unroll
        for (int x = 0; x < 3; ++x) {
            ss[k][x][0] = f32x2{bflo(t[x][0]), bfhi(t[x][0])}; ss[k][x][1] = f32x2{bflo(t[x][1]), bfhi(t[x][1])};
            asm volatile("" : "+v"(ss[k][x][0]), "+v"(ss[k][x][1]));
        }
    };
    asm volatile("s_waitcnt vmcnt(12)" ::: "memory");
    loadsin(0); loadsin(1);
    PRE(3)
    asm volatile("s_waitcnt vmcnt(12)" ::: "memory");
    loadsin(2); loadsin(3);
    unsigned th1 = 0, th4 = 0;
    unsigned char* prec = smem + LDS_P + wave * (H * 128) + r * 32 + (c & 7) * 4;
    auto softmax = [&](float s, int h) {
        float m = fmaxf(s, dpp<0xb1>(s));
        m = xrow_max(fmaxf(m, dpp<0x4e>(m)));
        const float p = __builtin_amdgcn_exp2f(s - m);
        float l = p + dpp<0xb1>(p);
        l = xrow_sum(l + dpp<0x4e>(l));
        const float t = (c & 1) ? l : m;
        const int x = __builtin_amdgcn_update_dpp(__float_as_int(p), __float_as_int(t), 0x114, 0xf, 0xa, false);
        *reinterpret_cast<int*>(prec + h * 128) = x;
    };
    float sprev = 0.f;

#pragma unroll 1
    for (int h = 0; h < H; ++h) {
        const bool more = h + 1 < H;
        const rsrc_t nx = more ? kr : vr;
        const unsigned nso = more ? (h + 1) * hstride : 0;
        float scf = bflo(sc);
        asm volatile("" : "+v"(scf) :: "memory");
        SB();
        scp += more ? 128 : 4096 + 128;
        sc = *reinterpret_cast<const unsigned short*>(scp);
        SB();
        float sreg = 0.f;
#define KSTEP(k) { \
            const float kz = dpp<0x154 + (k)>(scf); \
            f32x2 kz2 = f32x2{kz, kz}; \
            asm("" : "+v"(kz2)); \
            f32x2 a2 = f32x2{0.f, 0.f}; \
            _Pragma("unroll") for (int x = 0; x < 3; ++x) { \
                _Pragma("unroll") for (int e = 0; e < 2; ++e) { \
                    const f32x2 w = __builtin_elementwise_fma(f32x2{qb[x][2 * e], qb[x][2 * e + 1]}, ss[k][x][e], f32x2{qa[x][2 * e], qa[x][2 * e + 1]} * cc[k][x][e]); \
                    const f32x2 kf = f32x2{(float)buf[k].d[x][2 * e], (float)buf[k].d[x][2 * e + 1]} - kz2; \
                    a2 = __builtin_elementwise_fma(kf, w, a2); \
                } \
            } \
            float sa = a2[0] + a2[1]; \
            asm volatile("" : "+v"(sa) :: "memory"); \
            SB(); pace(true); issue(buf[k], nx, nso + (k) * (4 * DH * 4)); SB(); \
            if ((k) == 0) { softmax(sprev, (h - 1) & (H - 1)); SB(); }   \
            if ((k) == 1) { rotq(); SB(); } \
            if ((k) == 2) { \
                if (wave == 1 && h == ((OPT & 4) ? 1 + ((b >> 3) & 3) : 4 + ((b >> 3) & 15))) \
                    asm volatile("s_mov_b32 m0, %2\n\ts_nop 0\n\tglobal_load_lds_dwordx4 %0, %1 sc1" :: "v"(psk + (lane & 7) * 16), "s"(a.part), "s"(lds0 + LDS_SK) : "memory"); \
                if (h == ((OPT & 4) ? 6 : 25)) {        \
                    const u32x2 e = *reinterpret_cast<const u32x2*>(smem + LDS_SK + (lane & 7) * 16); \
                    const int d = (int)(tdma - e[0]); \
                    if (__all(e[1] == t32 && d > -400 && d < 400)) { \
                        int sum = d + __builtin_amdgcn_mov_dpp(d, 0xb1, 0xf, 0xf, true); \
                        sum += __builtin_amdgcn_mov_dpp(sum, 0x4e, 0xf, 0xf, true); \
                        sum += __builtin_amdgcn_mov_dpp(sum, 0x124, 0xf, 0xf, true); \
                        const int adj = (__builtin_amdgcn_readfirstlane(sum) * ((OPT & 4) ? 73 : 112) + 4096) >> 13;        \
                        step -= adj; tmin -= adj; skew = __builtin_amdgcn_readfirstlane(sum) / 8; \
                        if ((OPT & 64) && (OPT & 384) && b < ((OPT & 256) ? 256 : 16) && wave == 0) {        \
                            unsigned t0, t1, t2, t3; \
                            const unsigned ar = (((__builtin_amdgcn_readfirstlane(e[0]) >> 13) & (NAREA - 1)) + 1) * AREA; \
                            asm volatile("s_load_dword %0, %4, %5\n\ts_load_dword %1, %4, %6\n\ts_load_dword %2, %4, %7\n\ts_load_dword %3, %4, %8\n\ts_waitcnt lgkmcnt(0)" : "=&s"(t0), "=&s"(t1), "=&s"(t2), "=&s"(t3) : "s"(a.part), "s"(ar), "s"(ar + AREA - 128), "s"(ar + (2u << 20) + 128), "s"(ar + AREA - (2u << 20) - 128) : "memory"); \
                            vt = now32() << 4; \
                        } \
                    } \
                } \
                SB(); \
            } \
            if ((k) == 3) { loadq(); SB(); } \
            sa = row16_allsum(sa); \
            sreg = (c & 3) == (k) ? sa : sreg; }
        KSTEP(0) KSTEP(1) KSTEP(2) KSTEP(3)
        sprev = sreg * (scf * SCALE_LOG2);
        if (h == 3) th1 = (int)(vt - sched) > 0 ? (vt - sched) >> 4 : 0;
    }
    softmax(sprev, H - 1);
    th4 = (int)(vt - sched) > 0 ? (vt - sched) >> 4 : 0;

    const unsigned tg = (unsigned)(tag >> (16 * (lane & 3))) << 16;
    unsigned rl = lds0 + LDS_REC + lane * 4, rh = rl + 2 * RECW;
    auto pack = [&](f32x2 t, float u) { return u32x2{pk_bf16(t[0], t[1]), pk_bf16(u, 0.f) | tg}; };
    auto ldrec = [&](unsigned o, f32x2 (&p)[6]) {
        asm volatile("ds_read2st64_b32 %0, %6 offset1:1\n\tds_read2st64_b32 %1, %6 offset0:96 offset1:97\n\tds_read2st64_b32 %2, %7 offset1:1\n\tds_read2st64_b32 %3, %7 offset0:96 offset1:97\n\t"
                     "ds_read2st64_b32 %4, %6 offset0:2 offset1:98\n\tds_read2st64_b32 %5, %7 offset0:2 offset1:98"
                     : "=&v"(p[0]), "=&v"(p[1]), "=&v"(p[2]), "=&v"(p[3]), "=&v"(p[4]), "=&v"(p[5]) : "v"(rl + o), "v"(rh + o) : "memory");
    };
    auto merge = [&](int hd) {
        f32x2 p[6], ma, mb;
        asm volatile("ds_read2st64_b32 %0, %2 offset1:16\n\tds_read2st64_b32 %1, %2 offset0:32 offset1:48" : "=&v"(ma), "=&v"(mb) : "v"(lds0 + LDS_P + hd * 128 + 16) : "memory");
        ldrec(hd * (DH * 4), p);
        asm volatile("s_waitcnt lgkmcnt(6)" : "+v"(ma), "+v"(mb) :: "memory");
        const float M = __builtin_amdgcn_fmed3f(__builtin_amdgcn_fmed3f(ma[0], ma[1], __builtin_inff()), __builtin_amdgcn_fmed3f(mb[0], mb[1], __builtin_inff()), __builtin_inff());
        ma = f32x2{__builtin_amdgcn_exp2f(ma[0] - M), __builtin_amdgcn_exp2f(ma[1] - M)};
        mb = f32x2{__builtin_amdgcn_exp2f(mb[0] - M), __builtin_amdgcn_exp2f(mb[1] - M)};
        asm volatile("s_waitcnt lgkmcnt(0)" : "+v"(ma), "+v"(mb), "+v"(p[0]), "+v"(p[1]), "+v"(p[2]), "+v"(p[3]), "+v"(p[4]), "+v"(p[5]) :: "memory");
        const f32x2 u = __builtin_elementwise_fma(mb, p[5], ma * p[4]);
        return pack(ma[0] * p[0] + ma[1] * p[1] + mb[0] * p[2] + mb[1] * p[3], u[0] + u[1]);
    };
    constexpr int NC = 4 * H;
    const int cn = sb;
    const unsigned hc = 4 * (cn & 7) + ((cn >> 3) & 3);
    unsigned old = 0, area = 0, fo = 0, shift = 0;
    u32x4 flv = {};
    float gl = 0.f, mw = 0.f;
    f32x2 fs[4] = {};
    constexpr int LDS_ML = LDS_QRAW + 2048;
#define BAD(y) ((unsigned)(y)[1] != t32 || (unsigned)(y)[3] != (unsigned)tag)
    auto ev_ml = [&]() {
        f32x2 ml[4];
#pragma unroll
        for (int w = 0; w < 4; ++w) ml[w] = *reinterpret_cast<const f32x2*>(smem + LDS_P + w * (H * 128) + (8 * wave + (lane & 7)) * 128 + 16);
        const float M = fmaxf(fmaxf(ml[0][0], ml[1][0]), fmaxf(ml[2][0], ml[3][0]));
        const float L = (__builtin_amdgcn_exp2f(ml[0][0] - M) * ml[0][1] + __builtin_amdgcn_exp2f(ml[1][0] - M) * ml[1][1]) + (__builtin_amdgcn_exp2f(ml[2][0] - M) * ml[2][1] + __builtin_amdgcn_exp2f(ml[3][0] - M) * ml[3][1]);
        flv = u32x4{__float_as_uint(M), t32, __float_as_uint(L), (unsigned)tag};
        asm volatile("" : "+v"(flv));
    };
    auto ev_area = [&]() {
        u32x4 e = *reinterpret_cast<const u32x4*>(smem + LDS_SK);
        float mv[4];
#pragma unroll
        for (int w = 0; w < 4; ++w) mv[w] = *reinterpret_cast<const float*>(smem + LDS_P + ((wave + w) & 3) * (H * 128) + (H - 1) * 128 + 16);
        gl = __builtin_amdgcn_exp2f(mv[0] - fmaxf(fmaxf(mv[0], mv[1]), fmaxf(mv[2], mv[3])));
        if (__builtin_expect(__any(BAD(e)), 0)) {
#pragma unroll 1
            for (int n = 0; n < (1 << 20) && BAD(e); ++n)
                asm volatile("global_load_dwordx4 %0, %1, %2 sc1\n\ts_waitcnt vmcnt(0)" : "=v"(e) : "v"(psk), "s"(a.part) : "memory");
        }
        area = (((__builtin_amdgcn_readfirstlane(e[0]) >> 13) & (NAREA - 1)) + 1) * AREA + 4096;
        asm volatile("" : "+s"(area), "+v"(gl));
    };
    auto ev_put = [&]() {
        asm volatile("s_mov_b64 exec, 0xff\n\tbuffer_store_dwordx4 %0, %1, %2, %3 offen sc1\n\ts_mov_b64 exec, -1" :: "v"(flv), "v"(4 * (rl - lds0 - LDS_REC)), "s"(pr), "s"(area + pfl + (wave * nb + sb) * 128) : "memory");
    };
    auto ev_get = [&]() {
        unsigned l = lane;
        asm volatile("" : "+v"(l));
        fo = area + pfl + ((hc >> 3) * nb + 8 * wave) * 128 + (hc & 7) * 16 + (((l & 56) << 2) + (l & 7)) * 128;
        asm volatile("s_mov_b32 m0, %2\n\ts_nop 0\n\tglobal_load_lds_dwordx4 %0, %1 sc1" :: "v"(fo), "s"(a.part), "s"(lds0 + LDS_ML + wave * 1024) : "memory");
    };
    auto ev_w1 = [&]() {
        u32x4 y = *reinterpret_cast<const u32x4*>(smem + LDS_ML + wave * 1024 + lane * 16);
        if (__builtin_expect(__any(BAD(y)), 0)) {
#pragma unroll 1
            for (int n = 0; n < (1 << 20) && BAD(y); ++n)
                asm volatile("global_load_dwordx4 %0, %1, %2 sc1\n\ts_waitcnt vmcnt(0)" : "=v"(y) : "v"(fo), "s"(a.part) : "memory");
        }
        const float m = __uint_as_float(y[0]);
        float u;
        asm volatile("s_nop 1\n\tv_max_f32_dpp %0, %2, %2 row_ror:8 row_mask:0xf bank_mask:0xf\n\ts_nop 1\n\tv_max_f32_dpp %0, %0, %0 row_ror:4 row_mask:0xf bank_mask:0xf\n\ts_nop 1\n\t"
                     "v_max_f32_dpp %0, %0, %0 row_ror:2 row_mask:0xf bank_mask:0xf\n\ts_nop 1\n\tv_max_f32_dpp %0, %0, %0 row_ror:1 row_mask:0xf bank_mask:0xf\n\tv_mov_b32 %1, %0\n\ts_nop 1\n\t"
                     "v_permlane16_swap_b32 %0, %1\n\ts_nop 0\n\tv_max_f32 %0, %0, %1\n\tv_mov_b32 %1, %0\n\ts_nop 1\n\tv_permlane32_swap_b32 %0, %1\n\ts_nop 0\n\tv_max_f32 %0, %0, %1" : "=&v"(mw), "=&v"(u) : "v"(m));
        fs[0][0] = __builtin_amdgcn_exp2f(m - mw);
        fs[0][1] = fs[0][0] * __uint_as_float(y[2]);
        asm volatile("" : "+v"(fs[0]));
    };
    auto ev_w2 = [&]() {
        const f32x2 f = fs[0];
        unsigned bp = (lane >> 3) * 4;
        asm volatile("ds_bpermute_b32 %0, %8, %9\n\tds_bpermute_b32 %1, %8, %9 offset:32\n\tds_bpermute_b32 %2, %8, %9 offset:64\n\tds_bpermute_b32 %3, %8, %9 offset:96\n\t"
                     "ds_bpermute_b32 %4, %8, %9 offset:128\n\tds_bpermute_b32 %5, %8, %9 offset:160\n\tds_bpermute_b32 %6, %8, %9 offset:192\n\tds_bpermute_b32 %7, %8, %9 offset:224"
                     : "=&v"(fs[0][0]), "=&v"(fs[0][1]), "=&v"(fs[1][0]), "=&v"(fs[1][1]), "=&v"(fs[2][0]), "=&v"(fs[2][1]), "=&v"(fs[3][0]), "=&v"(fs[3][1]) : "v"(bp), "v"(f[0]) : "memory");
        *reinterpret_cast<f32x2*>(smem + LDS_QRAW + 1024 + wave * 8) = f32x2{mw, xrow_sum(row16_allsum(f[1]))};
        asm volatile("s_waitcnt lgkmcnt(0)" : "+v"(fs[0]), "+v"(fs[1]), "+v"(fs[2]), "+v"(fs[3]) :: "memory");
    };
    constexpr int L0 = (EV >> 12) & 63, HP = (EV >> 18) & 31;
    const int hpub = HP && b < 32 && wave == 2 ? HP : -9, hrd = HP && wave == 1 ? H - 2 : -9;
    auto ev_pub = [&]() {
        asm volatile("s_mov_b64 exec, 1\n\tbuffer_store_dwordx2 %0, off, %1, %2 sc1\n\ts_mov_b64 exec, -1" :: "v"(u32x2{vt - tmin - sched + step, t32}), "s"(pr), "s"(area - 2048 + b * 8) : "memory");
    };
    auto ev_rd = [&]() {
        asm volatile("s_mov_b32 m0, %2\n\ts_mov_b64 exec, 0xffff\n\tbuffer_load_dwordx4 %0, %1, %3 offen sc1 lds\n\ts_mov_b64 exec, -1" :: "v"(4 * (rl - lds0 - LDS_REC)), "s"(pr), "s"(lds0 + LDS_ML + 4096), "s"(area - 2048) : "memory");
    };
    constexpr int G1 = EV & 15, G2 = (EV >> 4) & 15, G3 = (EV >> 8) & 15;
    const int hput = G1 + G2 + 1 + ((b >> 3) & 3), hget = cn < NC ? G1 + G2 + 6 + ((b >> 3) & 3) : -9;
    float pv = *reinterpret_cast<const float*>(prec);
    f32x2 acc[3][2];
    float y;
    auto prep = [&]() {
        y = bflo(sc);
        y = (c & 4) ? y : y * pv;
        asm volatile("" : "+v"(y) :: "memory");
        SB();
        scp += 128;
        sc = *reinterpret_cast<const unsigned short*>(scp);
        prec += 128;
        pv = *reinterpret_cast<const float*>(prec);
        SB();
    };
#define VCOMP(k) { \
        const float pw = dpp<0x150 + (k)>(y); \
        const float vz = dpp<0x154 + (k)>(y); \
        f32x2 pw2 = f32x2{pw, pw}, vz2 = f32x2{vz, vz}; \
        asm("" : "+v"(pw2), "+v"(vz2)); \
        _Pragma("unroll") for (int x = 0; x < 3; ++x) \
            _Pragma("unroll") for (int e = 0; e < 2; ++e) { \
                const f32x2 vf = f32x2{(float)buf[k].d[x][2 * e], (float)buf[k].d[x][2 * e + 1]} - vz2; \
                acc[x][e] = (k) == 0 ? pw2 * vf : __builtin_elementwise_fma(pw2, vf, acc[x][e]); \
            } \
        asm volatile("" : "+v"(acc[0][0]), "+v"(acc[0][1]), "+v"(acc[1][0]), "+v"(acc[1][1]), "+v"(acc[2][0]), "+v"(acc[2][1]) :: "memory"); \
        SB(); }
#define VSTEP(k, hn) { \
        VCOMP(k) pace(true); issue(buf[k], vr, (hn) * hstride + (k) * (4 * DH * 4)); SB(); \
        if ((k) == 1) { tmin = (hn) == H - 1 ? 0 : tmin; } \
        if ((k) == 1 && (hn) == G1) { asm volatile("s_waitcnt lgkmcnt(0)\n\ts_barrier" ::: "memory"); SB(); } \
        if ((k) == 2 && (hn) == G1) { ev_ml(); SB(); } \
        if ((k) == 2 && (hn) == G1 + G2) { ev_area(); SB(); } \
        if ((k) == 1 && (OPT & 1024)) { vth = WIN & 15; asm volatile("" : "+s"(vth)); } \
        if ((k) == 1 && (hn) == hput) { ev_put(); if (OPT & 1024) vth = 15; SB(); } \
        if ((k) == 1 && (hn) == hget) { ev_get(); if (OPT & 1024) vth = 15; SB(); } \
        if ((k) == 2 && (hn) == hget + 2) { ev_w1(); SB(); } \
        if (HP && (k) == 2 && (hn) == hpub) { ev_pub(); SB(); } \
        if (HP && (k) == 1 && (hn) == hrd) { ev_rd(); SB(); } \
        if ((k) == 1 && (hn) == hget + 2 + G3) { ev_w2(); SB(); } }
    auto record = [&](unsigned ra, unsigned ca) {
        f32x4 o;
#pragma unroll
        for (int e = 0; e < 4; ++e) {
            const float t0 = sw32(acc[0][e >> 1][e & 1], acc[2][e >> 1][e & 1]);
            const float t1 = sw32(acc[1][e >> 1][e & 1], acc[1][e >> 1][e & 1]);
            o[e] = sw16(t0, t1);
        }
        asm volatile("s_mov_b64 exec, %5\n\tds_write_b128 %1, %2\n\ts_mov_b64 exec, 1\n\tds_add_rtn_u32 %0, %3, %4\n\ts_mov_b64 exec, -1"
                     : "+v"(old) : "v"(ra), "v"(o), "v"(ca), "v"(1u), "s"(0xffffffffffffull) : "memory");
        SB();
    };
#pragma unroll 1
    for (int h = 0; h < H - 1; ++h) {
        prep();
        VSTEP(0, h + 1)
        asm volatile("s_waitcnt lgkmcnt(0)" : "+v"(old) :: "memory");
        if (__builtin_amdgcn_readfirstlane(old) == 3) *reinterpret_cast<u32x2*>(smem + LDS_REC + (h - 1) * (DH * 4) + lane * 8) = merge(h - 1);
        SB();
        VSTEP(1, h + 1) VSTEP(2, h + 1) VSTEP(3, h + 1)
        record(lds0 + LDS_REC + h * (DH * 4) + wave * RECW + lane * 16, lds0 + LDS_CNT + h * 4);
    }
    const unsigned lv = ((now32() << 4) - (sched - step)) >> 4;
    asm volatile("s_branch .Lkv_t%c0\n\t.p2align 12,,3327\n.Lkv_t%c0:" :: "i"(ID));
    y = bflo(sc);
    y = (c & 4) ? y : y * (pv * gl);
    VCOMP(0)
    asm volatile("s_waitcnt lgkmcnt(0)\n\ts_barrier" ::: "memory");
    u32x4 ev[4];
#pragma unroll
    for (int p = 0; p < 4; ++p) ev[p] = *reinterpret_cast<const u32x4*>(smem + LDS_REC + (8 * wave + 2 * p + (lane >> 5)) * (DH * 4) + (lane & 31) * 16);
    const f32x4 a0 = *reinterpret_cast<const f32x4*>(smem + LDS_QRAW + 1024), a1 = *reinterpret_cast<const f32x4*>(smem + LDS_QRAW + 1040);
    const u32x2 lt = *reinterpret_cast<const u32x2*>(smem + LDS_ML + 4096 + (lane & 31) * 8);
    SB();
    VCOMP(1)
    if (HP) {
        int v = lt[1] == t32 ? (int)lt[0] : 0, u;
        asm volatile("s_nop 1\n\tv_max_i32_dpp %0, %0, %0 row_ror:8 row_mask:0xf bank_mask:0xf\n\ts_nop 1\n\tv_max_i32_dpp %0, %0, %0 row_ror:4 row_mask:0xf bank_mask:0xf\n\ts_nop 1\n\t"
                     "v_max_i32_dpp %0, %0, %0 row_ror:2 row_mask:0xf bank_mask:0xf\n\ts_nop 1\n\tv_max_i32_dpp %0, %0, %0 row_ror:1 row_mask:0xf bank_mask:0xf\n\ts_nop 0" : "+v"(v));
        u = max(max(__builtin_amdgcn_readlane(v, 0), __builtin_amdgcn_readlane(v, 16)) - L0 * 16, 0);
        shift = min(u, 1024 * 16);
    }
    unsigned eo = (lane >> 5) * (nb * LS) + (lane & 31) * 16, e8 = lane * 8, ra = lds0 + LDS_REC + (H - 1) * (DH * 4) + wave * RECW + lane * 16, ca = lds0 + LDS_CNT + (H - 1) * 4;
    unsigned es[5];
#pragma unroll
    for (int p = 0; p < 4; ++p) es[p] = area + ((8 * wave + 2 * p) * nb + sb) * LS;
    es[4] = area + ((H - 1) * nb + sb) * LS;
    unsigned tx = sched + shift - step - stag + EX * 16 + (sb >> 3) * GS * 4;
    if (wave == 3) { const u32x2 t = merge(H - 2); ev[3][0] = t[0]; ev[3][1] = t[1]; }
    {
        const float M = fmaxf(fmaxf(a0[0], a0[2]), fmaxf(a1[0], a1[2]));
        const float gw = __builtin_amdgcn_exp2f(mw - M) * __builtin_amdgcn_rcpf((__builtin_amdgcn_exp2f(a0[0] - M) * a0[1] + __builtin_amdgcn_exp2f(a0[2] - M) * a0[3]) + (__builtin_amdgcn_exp2f(a1[0] - M) * a1[1] + __builtin_amdgcn_exp2f(a1[2] - M) * a1[3]));
#pragma unroll
        for (int i = 0; i < 4; ++i) fs[i] *= gw;
    }
    rl += (H - 1) * (DH * 4); rh += (H - 1) * (DH * 4);
    asm volatile("" : "+v"(fs[0]), "+v"(fs[1]), "+v"(fs[2]), "+v"(fs[3]), "+v"(ev[0]), "+v"(ev[1]), "+v"(ev[2]), "+v"(ev[3]), "+v"(eo), "+v"(e8), "+v"(ra), "+v"(ca), "+v"(rl), "+v"(rh),
                      "+s"(es[0]), "+s"(es[1]), "+s"(es[2]), "+s"(es[3]), "+s"(es[4]), "+s"(tx));
    SB();
    VCOMP(2)
    while ((int)((now32() << 4) - tx) < 0) {}
    if constexpr ((OPT & 512) != 0)
    asm volatile("s_waitcnt vmcnt(0)\n\tbuffer_store_dwordx4 %3, %7, %9, %10 offen sc1\n\tbuffer_store_dwordx4 %4, %7, %9, %11 offen sc1\n\tbuffer_store_dwordx4 %5, %7, %9, %12 offen sc1\n\t"
                 "s_cmp_eq_u32 %14, 3\n\ts_cbranch_scc1 1f\n\tbuffer_store_dwordx4 %6, %7, %9, %13 offen sc1\n\ts_branch 2f\n1:\tbuffer_store_dwordx2 %15, %8, %9, %13 offen sc1\n2:"
                 : "+v"(buf[3].d[0]), "+v"(buf[3].d[1]), "+v"(buf[3].d[2]) : "v"(ev[0]), "v"(ev[1]), "v"(ev[2]), "v"(ev[3]), "v"(eo), "v"(e8), "s"(pr), "s"(es[0]), "s"(es[1]), "s"(es[2]), "s"(es[3]), "s"(wave),
                   "v"(u32x2{ev[3][0], ev[3][1]}) : "memory", "scc");
    else
    asm volatile("s_waitcnt vmcnt(0)\n\tbuffer_store_dwordx4 %3, %7, %9, %10 offen sc1\n\tbuffer_store_dwordx4 %4, %7, %9, %11 offen sc1\n\tbuffer_store_dwordx4 %5, %7, %9, %12 offen sc1\n\t"
                 "s_cmp_eq_u32 %14, 3\n\ts_cselect_b64 exec, 0, -1\n\tbuffer_store_dwordx4 %6, %7, %9, %13 offen sc1\n\ts_not_b64 exec, exec\n\tbuffer_store_dwordx2 %15, %8, %9, %13 offen sc1\n\ts_mov_b64 exec, -1"
                 : "+v"(buf[3].d[0]), "+v"(buf[3].d[1]), "+v"(buf[3].d[2]) : "v"(ev[0]), "v"(ev[1]), "v"(ev[2]), "v"(ev[3]), "v"(eo), "v"(e8), "s"(pr), "s"(es[0]), "s"(es[1]), "s"(es[2]), "s"(es[3]), "s"(wave),
                   "v"(u32x2{ev[3][0], ev[3][1]}) : "memory", "scc");
    if (DBG == 4) tbar = now32();
    SB();
    VCOMP(3)
    asm volatile("" :: "v"(ev[0]), "v"(ev[1]), "v"(ev[2]), "v"(ev[3]));
    record(ra, ca);
    asm volatile("s_waitcnt lgkmcnt(0)" : "+v"(old) :: "memory");
    if (__builtin_amdgcn_readfirstlane(old) == 3) {
        f32x2 p[6];
        ldrec(0, p);
        asm volatile("s_waitcnt lgkmcnt(0)" : "+v"(p[0]), "+v"(p[1]), "+v"(p[2]), "+v"(p[3]), "+v"(p[4]), "+v"(p[5]) :: "memory");
        const f32x2 u = p[4] + p[5];
        __builtin_amdgcn_raw_buffer_store_b64(pack((p[0] + p[1]) + (p[2] + p[3]), u[0] + u[1]), pr, e8, es[4], 16);
    }
    unsigned tend = DS ? now32() : 0;
    unsigned tfl = 0, tpl = 0;

    if (cn < NC) {
        unsigned tgt = sched + shift - stag + (hc == H - 1 ? HL : HOLD) * 16;
        unsigned rs[8];
#pragma unroll
        for (int i = 0; i < 8; ++i) rs[i] = area + (hc * nb + 32 * i + 8 * wave) * LS + (cn >> 5) * 128;
        const unsigned g = lane & 7, e = (lane >> 3) & 1;
        unsigned xo = (lane >> 3) * LS + g * 16;
        unsigned oo = r == 3 ? 0x80000000u : (hc * DH + (cn >> 5) * 16 + 2 * g + (r == 2 ? 128 + e : r + 64 * e)) * 2;
        unsigned po = lds0 + LDS_QRAW + lane * 4, wo = po + wave * 256;
        unsigned tb = (lane & 1) ? t32 : (unsigned)tag, ta = tb << 16;
        const rsrc_t orr = mkrsrc(a.out, H * DH * 2);
        asm volatile("" : "+s"(rs[0]), "+s"(rs[1]), "+s"(rs[2]), "+s"(rs[3]), "+s"(rs[4]), "+s"(rs[5]), "+s"(rs[6]), "+s"(rs[7]), "+v"(po), "+v"(wo), "+v"(xo), "+v"(oo), "+v"(ta), "+v"(tb), "+s"(tgt) : "s"(orr) : "memory");
        i32x4 x[8];
        while ((int)((now32() << 4) - tgt) < 0) {}
        if (DS) tfl = now32();
#define XL(i) x[i] = bld128<16>(pr, xo, rs[i]); SB();
#define XS(i) XL(i) __builtin_amdgcn_s_sleep((SL >> (2 * (i))) & 3); SB();
        XS(0) XS(1) XS(2) XS(3) XS(4) XS(5) XS(6) XL(7)
        if (DBG == 4) th4 = now32();
        SB();
        f32x2 o[3];
#pragma unroll 1
        for (int n = 0; n < (1 << 20); ++n) {
            unsigned bad = 0;
#pragma unroll
            for (int i = 0; i < 8; ++i) {
#pragma unroll
                for (int e = 0; e < 3; ++e) {
                    const f32x2 t = e < 2 ? f32x2{bflo(x[i][2 * e]), bfhi(x[i][2 * e])} : f32x2{bflo(x[i][1]), bflo(x[i][3])};
                    if (i == 0) asm volatile("v_pk_mul_f32 %0, %1, %2 op_sel_hi:[0,1]" : "=v"(o[e]) : "v"(fs[0]), "v"(t));
                    else if (i & 1) asm volatile("v_pk_fma_f32 %0, %1, %2, %0 op_sel:[1,0,0]" : "+v"(o[e]) : "v"(fs[i >> 1]), "v"(t));
                    else asm volatile("v_pk_fma_f32 %0, %1, %2, %0 op_sel_hi:[0,1,1]" : "+v"(o[e]) : "v"(fs[i >> 1]), "v"(t));
                }
                bad = __builtin_amdgcn_bitop3_b32(bad, x[i][1], ta, 0xf6);
                bad = __builtin_amdgcn_bitop3_b32(bad, x[i][3], tb, 0xf6);
                asm volatile("" : "+v"(bad));
                SB();
            }
            if (__builtin_expect(__all(bad < 0x10000u), 1)) break;
            asm volatile("" ::: "memory");
            XL(0) XL(1) XL(2) XL(3) XL(4) XL(5) XL(6) XL(7)
        }
        if (DS) { asm volatile("" : "+v"(o[0]), "+v"(o[1]), "+v"(o[2])); tpl = now32(); }
        const float w0 = sw16(sw32(o[0][0], o[2][0]), sw32(o[1][0], o[1][0])), w1 = sw16(sw32(o[0][1], o[2][1]), sw32(o[1][1], o[1][1]));
        asm volatile("ds_write_b32 %0, %1\n\ts_waitcnt lgkmcnt(0)\n\ts_barrier" :: "v"(wo), "v"((c < 8 ? w0 : w1) + dpp<0x128>(c < 8 ? w1 : w0)) : "memory");
        if (wave == 0) {
            f32x2 p0, p1;
            asm volatile("ds_read2st64_b32 %0, %2 offset1:1\n\tds_read2st64_b32 %1, %2 offset0:2 offset1:3\n\ts_waitcnt lgkmcnt(0)" : "=&v"(p0), "=&v"(p1) : "v"(po) : "memory");
            p0 += p1;
            __builtin_amdgcn_raw_buffer_store_b16((unsigned short)pk_bf16(p0[0] + p0[1], 0.f), orr, oo, 0, 0);
        }
        if (b == 0 && tid == 64) *reinterpret_cast<u64*>(a.part + pep) = tag;
        asm volatile(".Lkv_b%c0:" :: "i"(ID));
    }
    if (wave == 0) {
        const unsigned t1 = now32();
        const unsigned tg = tstart + 15000 + 0;
        for (int n = 0; n < 100000 && (int)(now32() - tg) < 0; ++n) __builtin_amdgcn_s_sleep(4);
    }
    if (DBG) {
        const unsigned tc1 = now32();
        if (!DS) tend = tbar = th4 = tfl = tpl = tc1;
        const unsigned tl16 = sched - stag - step;
        u64* stp = reinterpret_cast<u64*>(a.part + STAMP_OFF) + (size_t)((DBG == 4 || DBG == 24 || DBG == 6 ? cn : b) * 4 + wave) * 8;

        if (DBG == 5 || DBG == 2) tbar = sched - (((tdma + ADV) << 4) + stag) - 33 * (TP / 2) - 223 * step + shift;
        if (lane == 0) { stp[0] = tstart; stp[1] = tbar; stp[2] = DBG >= 4 ? tend - (((tend << 4) - tl16) >> 4) : th1; stp[3] = DBG == 4 && cn >= NC ? tend : th4; stp[4] = tend; stp[5] = cn < NC ? tfl : tend; stp[6] = cn < NC ? tpl : tend; stp[7] = tc1; }
        if (DBG == 2) *reinterpret_cast<u32x4*>(a.part + STAMP_OFF + 65536 + (b * 4 + wave) * 1024 + lane * 16) = *reinterpret_cast<const u32x4*>(smem + LDS_TR + 16 + wave * 1024 + lane * 16);
    }
}

template <int TP, int ADV, int TMIN, int WIN, int OPT, int EX, int GS, int HOLD, int HL, int SL, int EV, int ID, int DBG>
static void launch43(void* const* d_in, const int* in_sizes, void* d_out, void* d_ws, hipStream_t stream) {
    typedef const unsigned short* P;
    Args a;
    a.qk = (const int*)d_in[1]; a.qv = (const int*)d_in[4];
    a.k_cos = (P)d_in[9]; a.k_sin = (P)d_in[10];
    a.q = (P)d_in[0]; a.q_cos = (P)d_in[7]; a.q_sin = (P)d_in[8];
    a.sc[0] = (P)d_in[2]; a.sc[1] = (P)d_in[3]; a.sc[2] = (P)d_in[5]; a.sc[3] = (P)d_in[6];
    a.part = (unsigned char*)d_ws;
    a.out = (unsigned short*)d_out;
    kv43_kernel<TP, ADV, TMIN, WIN, OPT, EX, GS, HOLD, HL, SL, EV, ID, DBG><<<Skv / RW, 256, 0, stream>>>(a);
}

extern "C" void kernel_launch(void* const* d_in, const int* in_sizes, int n_in, void* d_out, int out_size, void* d_ws, size_t ws_size, hipStream_t stream) {
    launch43<80*16+2,220,79*16,4+16*32+4096*20,38+64+128+512+1024,66,3,85,100,0x2aaa,5+16*1+256*1,0,0>(d_in, in_sizes, d_out, d_ws, stream);
}
```

```cpp
#include <hip/hip_runtime.h>
#include <hip/hip_bf16.h>
#include <cstdint>
#define KALIGN 256
#define STAMP_OFF (200 << 20)
using bf16 = __hip_bfloat16;
using i32x4 = __attribute__((ext_vector_type(4))) int;
using u32x4 = __attribute__((ext_vector_type(4))) unsigned;
using u32x2 = __attribute__((ext_vector_type(2))) unsigned;
using f32x2 = __attribute__((ext_vector_type(2))) float;
using f32x4 = __attribute__((ext_vector_type(4))) float;

constexpr int DH = 192;
constexpr int HALF = 96;
constexpr int RW = 64;
constexpr int MAXH = 32;

__device__ __forceinline__ float bflo(unsigned u) { return __uint_as_float(u << 16); }
__device__ __forceinline__ float bfhi(unsigned u) { return __uint_as_float(u & 0xffff0000u); }
__device__ __forceinline__ float rbf(float x) { return __bfloat162float(__float2bfloat16(x)); }

template <int CTRL> __device__ __forceinline__ float dpp(float x) {
    return __builtin_bit_cast(float, __builtin_amdgcn_mov_dpp(__builtin_bit_cast(int, x), CTRL, 0xf, 0xf, true));
}
__device__ __forceinline__ float row16_allsum(float x) {
    x += dpp<0x128>(x);
    x += dpp<0x124>(x);
    x += dpp<0x122>(x);
    x += dpp<0x121>(x);
    return x;
}
__device__ __forceinline__ float row16_allmax(float x) {
    x = fmaxf(x, dpp<0x128>(x));
    x = fmaxf(x, dpp<0x124>(x));
    x = fmaxf(x, dpp<0x122>(x));
    x = fmaxf(x, dpp<0x121>(x));
    return x;
}
__device__ __forceinline__ float xrow_max(float x) {
    auto s = __builtin_amdgcn_permlane16_swap(__float_as_uint(x), __float_as_uint(x), false, false);
    x = fmaxf(__uint_as_float(s[0]), __uint_as_float(s[1]));
    auto t = __builtin_amdgcn_permlane32_swap(__float_as_uint(x), __float_as_uint(x), false, false);
    return fmaxf(__uint_as_float(t[0]), __uint_as_float(t[1]));
}
__device__ __forceinline__ float xrow_sum(float x) {
    auto s = __builtin_amdgcn_permlane16_swap(__float_as_uint(x), __float_as_uint(x), false, false);
    x = __uint_as_float(s[0]) + __uint_as_float(s[1]);
    auto t = __builtin_amdgcn_permlane32_swap(__float_as_uint(x), __float_as_uint(x), false, false);
    return __uint_as_float(t[0]) + __uint_as_float(t[1]);
}
__device__ __forceinline__ float sw32(float a, float b) {
    auto t = __builtin_amdgcn_permlane32_swap(__float_as_uint(a), __float_as_uint(b), false, false);
    return __uint_as_float(t[0]) + __uint_as_float(t[1]);
}
__device__ __forceinline__ float sw16(float a, float b) {
    auto t = __builtin_amdgcn_permlane16_swap(__float_as_uint(a), __float_as_uint(b), false, false);
    return __uint_as_float(t[0]) + __uint_as_float(t[1]);
}

#define SB() __builtin_amdgcn_sched_barrier(0)
typedef __amdgpu_buffer_rsrc_t rsrc_t;
__device__ __forceinline__ rsrc_t mkrsrc(const void* p, unsigned bytes) {
    return __builtin_amdgcn_make_buffer_rsrc(const_cast<void*>(p), 0, bytes, 0x00020000);
}
template <int AUX> __device__ __forceinline__ i32x4 bld128(rsrc_t r, unsigned voff, unsigned soff) {
    return __builtin_bit_cast(i32x4, __builtin_amdgcn_raw_buffer_load_b128(r, voff, soff, AUX));
}
template <int AUX> __device__ __forceinline__ unsigned bld16(rsrc_t r, unsigned voff, unsigned soff) {
    return __builtin_amdgcn_raw_buffer_load_b16(r, voff, soff, AUX);
}

struct HG { i32x4 d[3]; };
typedef unsigned long long u64;
typedef __attribute__((address_space(3))) unsigned char lds_u8;

__device__ __forceinline__ unsigned pk_bf16(float lo, float hi) {
    unsigned r;
    asm("v_cvt_pk_bf16_f32 %0, %1, %2" : "=v"(r) : "v"(lo), "v"(hi));
    return r;
}
__device__ __forceinline__ unsigned now32() { return (unsigned)__builtin_amdgcn_s_memrealtime(); }

constexpr int LDS_REC = 0;
constexpr int RECW = MAXH * DH * 4;
constexpr int LDS_CS = 0;
constexpr int LDS_QT = 12288;
constexpr int LDS_PF = 14336;
constexpr int LDS_QRAW = MAXH * 4 * DH * 4;
constexpr int LDS_Q = LDS_QRAW + MAXH * DH * 2;
constexpr int LDS_P = LDS_Q + 4 * DH * 4;
constexpr int LDS_SC = LDS_P + 4 * MAXH * 128;
constexpr int LDS_CNT = LDS_SC + 4 * MAXH * RW * 2;
constexpr int LDS_SK = LDS_CNT + 256;
constexpr int LDS_TR = LDS_SK + 1024;
constexpr int LDS_BYTES = LDS_TR + 4096 + 64;

struct Args {
    const int* qk; const int* qv;
    const unsigned short* k_cos; const unsigned short* k_sin;
    const unsigned short* q; const unsigned short* q_cos; const unsigned short* q_sin;
    unsigned char* part;
    unsigned short* out;
    const unsigned short* sc[4];
};
constexpr unsigned Skv = 16384;
constexpr float SCALE_LOG2 = 0.10411754627697264f;

template <int TP, int ADV, int TMIN, int WIN, int HC, int HX, int OPT, int EX, int GS, int HOLD, int HL, int SL, int EV, int ID, int DBG>
__global__ __launch_bounds__(256) __attribute__((aligned(KALIGN))) void kv65_kernel(Args a)
{
    __shared__ __attribute__((aligned(16))) unsigned char smem[LDS_BYTES];
    const unsigned tstart = DBG ? now32() : 0;
    constexpr bool DS = (DBG && DBG < 5) || DBG == 24;
    constexpr int H = MAXH, ES = 7, PFN = 16;
    const int b = blockIdx.x;
    const int tid = threadIdx.x;
    const int lane = tid & 63;
    const int wave = __builtin_amdgcn_readfirstlane(tid >> 6);
    typedef const unsigned short* const __attribute__((address_space(4))) * karg_t;
    const unsigned short* sp = ((karg_t)__builtin_amdgcn_kernarg_segment_ptr())[offsetof(Args, sc) / 8 + wave];
    if (b < PFN) {
        u64 pc; unsigned len, tl;
        asm volatile("s_getpc_b64 %0\n.Lkv_a%c3:\n\ts_mov_b32 %1, .Lkv_b%c3-.Lkv_a%c3-16\n\ts_mov_b32 %2, .Lkv_t%c3-.Lkv_a%c3" : "=s"(pc), "=s"(len), "=s"(tl) : "i"(ID));
        const unsigned o = tid * 16;
        asm volatile("s_mov_b32 m0, %5\n\ts_nop 0\n\tglobal_load_lds_dwordx4 %0, %4\n\tglobal_load_lds_dwordx4 %1, %4\n\tglobal_load_lds_dwordx4 %2, %4\n\tglobal_load_lds_dwordx4 %3, %4"
                     :: "v"(o), "v"(o + 4096), "v"(min(o + 8192, tl)), "v"(min(o + tl, len)), "s"(pc), "s"((unsigned)(size_t)(lds_u8*)smem + LDS_PF) : "memory");
    }
    SB();
    const int r = lane >> 4;
    const int c = lane & 15;
    constexpr unsigned hstride = Skv * (DH * 4);
    constexpr unsigned nb = Skv / RW;
    const unsigned lds0 = (unsigned)(size_t)(lds_u8*)smem;
    const unsigned pos = (b & 7) * (nb >> 3) + (b >> 3);
    const unsigned sb = ((((b >> 3) * 13) & 31) << 3) | (b & 7);
    const unsigned row0 = RW * pos + 16 * wave;

    constexpr unsigned LS = 512, psk = 0, pep = 128, AREA = 6u << 20, NAREA = 32, pfl = H * nb * LS;
    const rsrc_t pr = mkrsrc(a.part, (NAREA + 1) * AREA);
    u64 t64;
    {
        unsigned vs = ((lane >> 3) * Skv + RW * pos + (lane & 7) * 8) * 2, v1 = vs + 16 * Skv, v2 = vs + 32 * Skv, v3 = vs + 48 * Skv;
        unsigned ds = lds0 + LDS_SC + wave * 4096;
        asm volatile("" : "+v"(vs), "+v"(v1), "+v"(v2), "+v"(v3), "+s"(ds));
        SB();
        asm volatile("s_memrealtime %0\n\ts_mov_b32 m0, %6\n\ts_nop 0\n\tglobal_load_lds_dwordx4 %1, %5 nt\n\t"
                     "s_add_u32 m0, m0, 1024\n\ts_nop 0\n\tglobal_load_lds_dwordx4 %2, %5 nt\n\t"
                     "s_add_u32 m0, m0, 1024\n\ts_nop 0\n\tglobal_load_lds_dwordx4 %3, %5 nt\n\t"
                     "s_add_u32 m0, m0, 1024\n\ts_nop 0\n\tglobal_load_lds_dwordx4 %4, %5 nt\n\ts_waitcnt lgkmcnt(0)"
                     : "=&s"(t64) : "v"(vs), "v"(v1), "v"(v2), "v"(v3), "s"(sp), "s"(ds),
                       "s"(a.qk), "s"(a.qv), "s"(a.k_cos), "s"(a.k_sin), "s"(a.q), "s"(a.q_cos), "s"(a.q_sin), "s"(a.part), "s"(a.out) : "memory", "scc");
    }
    SB();
    const unsigned tdma = (unsigned)t64;
#define DMA3(V, A, B, P) "s_mov_b32 m0, " B "\n\ts_nop 0\n\t" \
                     "global_load_lds_dwordx4 " V ", " A P "\n\t" \
                     "global_load_lds_dwordx4 " V ", " A " offset:1024" P "\n\t" \
                     "global_load_lds_dwordx4 " V ", " A " offset:2048" P "\n\t"
    asm volatile(DMA3("%0", "%1", "%2", "") "s_mov_b32 m0, %4\n\ts_nop 0\n\tglobal_load_lds_dwordx4 %3, off"
                 :: "v"(lane * 16), "s"(a.q + wave * 1536), "s"(lds0 + LDS_QRAW + wave * 3072),
                    "v"(lane < 48 ? (const unsigned char*)((lane < 24 ? a.q_cos : a.q_sin - DH) + 8 * lane) : a.part + pep), "s"(lds0 + LDS_QT + wave * RECW) : "memory");
    if ((OPT & 131072) && (b >> 4) == 1 && wave == 3)
        asm volatile("s_mov_b32 m0, %2\n\ts_mov_b64 exec, 15\n\tglobal_load_lds_dword %0, %1\n\ts_mov_b64 exec, -1" :: "v"((((tdma >> 13) & (NAREA - 1)) + 1) * AREA + lane * 0x160000 + b * 128), "s"(a.part), "s"(lds0 + LDS_PF) : "memory");
    if ((OPT & 524288) && (b >> 4) == 1 && wave == 2)
        asm volatile("s_mov_b32 m0, %3\n\ts_mov_b64 exec, 3\n\tglobal_load_lds_dword %0, %1\n\tglobal_load_lds_dword %0, %2 nt\n\ts_mov_b64 exec, -1" :: "v"(lane * (H * DH * 2 - 4)), "s"(a.out), "s"((OPT & 256) ? (const void*)a.qv : (const void*)a.out), "s"(lds0 + LDS_PF) : "memory");
    if (OPT & 32768) {
        const unsigned char* pp = wave == 0 ? (const unsigned char*)a.k_cos + row0 * (DH * 2) : wave == 1 ? (const unsigned char*)a.k_sin + row0 * (DH * 2) : (const unsigned char*)a.qk + row0 * (DH * 4) + (wave - 2) * hstride;
        if ((OPT & 65536) || b < 32) asm volatile("s_mov_b32 m0, %2\n\ts_mov_b64 exec, 1\n\tglobal_load_lds_dword %0, %1 nt\n\ts_mov_b64 exec, -1" :: "v"(0), "s"(pp), "s"(lds0 + LDS_PF) : "memory");
    }
    SB();
    const int stag = (OPT & 32) ? (int)(((sb & ~7) + wave * 2 + (b & 1)) * (TP / 2)) / 256 - TP / 4 : (int)(((OPT & 2) ? (((b >> 3) * 26 + wave * 2 + (b & 1)) & 63) : ((((b >> 3) * 4 + wave) * 37) & 63)) * (TP / 2)) / 64 - TP / 4;
    constexpr int PX = (ADV >> 10) & 255, PY = (ADV >> 18) & 255;
    unsigned sched = ((tdma + (ADV & 1023)) << 4) + stag - 4 * (TP / 2);
    unsigned vt = tdma << 4;
    int tru = -4;
    constexpr int HD = HX & 255, FC = (HX >> 8) & 255, FT = (HX >> 16) & 255, RA = (HX >> 24) * 4;
    unsigned calm = vt + ((HC >> 20) & 255) * 160;
    int big = HD && !(HC >> 30) ? 1 << 30 : ((HC >> 12) & 255) * 16, fast = FT * 16;
    unsigned slip = 0, pvm = 0, ppvm = 0;
    constexpr bool SH = (OPT >> 18) & 1;
    constexpr int M2 = (HC >> 28) & 3, SKL = (EV >> 24) & 127;
    unsigned step = TP / 2, tmin = TMIN / 2, vth = WIN & 15;
    auto pace = [&](bool ctl = false) {
        unsigned tgt = sched;
        sched += step;
        if ((int)(vt - tgt) > 0) tgt = vt;
        unsigned t = now32() << 4;
        while ((int)(t - tgt) < 0) { __builtin_amdgcn_s_sleep(1); t = now32() << 4; }
        vt = ((int)(t - 128 - tgt) > 0 ? t - 128 : tgt) + tmin;
        if (ctl) {
            if constexpr (WIN > 0 && WIN < 16) asm volatile("s_waitcnt vmcnt(%0)" :: "i"(WIN) : "memory");
            if constexpr (WIN >= 16) {
                const unsigned vm = __builtin_amdgcn_s_getreg(7 | (3 << 11));
                constexpr int TH2 = HC & 15, D2 = (HC >> 4) & 255, BIG = (HC >> 12) & 255;
                if constexpr (HD != 0) { if ((int)(t - tgt) > HD * 16 && (int)(t - calm) > 0) { big = BIG * 16; fast = 1 << 30; if (RA) { sched += RA * 16; calm = t + 900 * 16; if (DBG == 2 || SH) slip += RA * 16; if (SH) *reinterpret_cast<volatile unsigned*>(smem + LDS_CNT + 128) = slip; } } }
                if constexpr (FC != 0) { if ((int)(vt - sched) > fast) vt -= FC; }
                if (vm > vth) {
                    if ((int)(vt - sched) < (WIN >> 12) * 16) vt += (vm - vth) * ((WIN >> 4) & 255);
                    else if (TH2 && (M2 == 0 ? vm : M2 == 3 ? min(pvm, ppvm) : pvm) > TH2 && (int)(vt - sched) > big) vt += ((M2 == 2 ? min(vm, pvm) : vm) - TH2) * D2;
                }
                ppvm = pvm; pvm = vm;
            }
        }
        if constexpr (DBG == 2) { *reinterpret_cast<unsigned*>(smem + LDS_TR + 16 + wave * 1024 + (tru & 255) * 4) = ((t - (sched - slip - step)) >> 4) | ((OPT & 8) ? (__builtin_amdgcn_s_getreg(7 | (3 << 11)) << 16) : 0); ++tru; }
    };
    {
        unsigned dst = lds0 + LDS_CS + wave * RECW;
        const unsigned v0 = lane * 16 + row0 * (DH * 2);
#pragma unroll 1
        for (int j = 0; j < 4; ++j) {
            pace();
            sched += PX;
            asm volatile(DMA3("%0", "%1", "%2", " nt") :: "v"(v0), "s"((j < 2 ? a.k_cos : a.k_sin) + (j & 1) * 8 * DH), "s"(dst) : "memory");
            dst += 3072;
        }
    }
    if (tid <= MAXH) *reinterpret_cast<unsigned*>(smem + LDS_CNT + tid * 4) = 0;
    const rsrc_t kr = mkrsrc(a.qk, H * hstride);
    const rsrc_t vr = mkrsrc(a.qv, H * hstride);
    const unsigned kvoff = (row0 + r) * (DH * 4) + c * 16;
    auto issue = [&](HG& g, rsrc_t rs, unsigned so) {
#pragma unroll
        for (int x = 0; x < 3; ++x) g.d[x] = bld128<2>(rs, kvoff + 256 * x, so);
    };
    HG buf[4];
#define PRE(g) SB(); pace(); sched += PY; issue(buf[g], kr, (g) * (4 * DH * 4)); SB();
    asm volatile("s_waitcnt vmcnt(12) lgkmcnt(0)\n\ts_barrier" ::: "memory");
    unsigned tbar = DS ? now32() : 0;
    PRE(0)
    const unsigned qoff = (lane < 48 ? lane : lane - 48) * 4;
    const unsigned* qt = reinterpret_cast<const unsigned*>(smem + LDS_QT + wave * RECW + qoff);
    unsigned qc0 = qt[0], qc1 = qt[HALF / 2], qs0 = qt[DH / 2], qs1 = qt[(DH + HALF) / 2];
    u64 tag;
    {
        const u32x2 e = *reinterpret_cast<const u32x2*>(smem + LDS_QT + wave * RECW + 768);
        tag = (((u64)__builtin_amdgcn_readfirstlane(e[1]) << 32) | __builtin_amdgcn_readfirstlane(e[0])) * 6364136223846793005ull + 1442695040888963407ull;
        asm volatile("" : "+s"(tag));
    }
    const unsigned t32 = tag >> 32;
    const unsigned char* qraw = smem + LDS_QRAW + qoff;
    unsigned char* qrot = smem + LDS_Q + wave * (DH * 4) + 2 * qoff;
    auto rotq = [&]() {
        const unsigned q_lo = *reinterpret_cast<const unsigned*>(qraw), q_hi = *reinterpret_cast<const unsigned*>(qraw + HALF * 2);
        qraw += DH * 2;
        const f32x2 lo = f32x2{bflo(q_lo), bfhi(q_lo)}, hi = f32x2{bflo(q_hi), bfhi(q_hi)};
        const f32x2 y1 = lo * f32x2{bflo(qc0), bfhi(qc0)}, y2 = -hi * f32x2{bflo(qs0), bfhi(qs0)};
        const f32x2 y3 = hi * f32x2{bflo(qc1), bfhi(qc1)}, y4 = lo * f32x2{bflo(qs1), bfhi(qs1)};
        const unsigned x1 = pk_bf16(y1[0], y1[1]), x2 = pk_bf16(y2[0], y2[1]), x3 = pk_bf16(y3[0], y3[1]), x4 = pk_bf16(y4[0], y4[1]);
        const f32x2 z1 = f32x2{bflo(x1), bfhi(x1)} + f32x2{bflo(x2), bfhi(x2)}, z2 = f32x2{bflo(x3), bfhi(x3)} + f32x2{bflo(x4), bfhi(x4)};
        const unsigned l2 = pk_bf16(z1[0], z1[1]), h2 = pk_bf16(z2[0], z2[1]);
        *reinterpret_cast<f32x2*>(qrot) = f32x2{bflo(l2), bfhi(l2)};
        *reinterpret_cast<f32x2*>(qrot + HALF * 4) = f32x2{bflo(h2), bfhi(h2)};
        asm volatile("" ::: "memory");
    };
    f32x4 qa[3], qb[3];
    const unsigned char* qh = smem + LDS_Q + wave * (DH * 4) + 16 * c;
    const unsigned char* qh1 = qh + (c < 8 ? 64 + HALF : 64 - HALF) * 4;
    auto loadq = [&]() {
        asm volatile("" ::: "memory");
#pragma unroll
        for (int x = 0; x < 3; ++x) qa[x] = *reinterpret_cast<const f32x4*>(qh + 256 * x);
        qb[0] = *reinterpret_cast<const f32x4*>(qh + HALF * 4);
        qb[1] = *reinterpret_cast<const f32x4*>(qh1);
        qb[2] = *reinterpret_cast<const f32x4*>(qh + (128 - HALF) * 4);
        asm volatile("" ::: "memory");
    };
    rotq();
    constexpr unsigned SKS = (OPT & 4096) ? 256 : 16;
    if (b < 8 && wave == 3) __builtin_amdgcn_raw_buffer_store_b128(u32x4{tdma, t32, tdma, (unsigned)tag}, pr, lane < ((OPT & 4096) ? 8 : 1) ? psk + b * SKS + lane * 16 : 0x80000000u, 0, 16);
    PRE(1)
    f32x2 cc[4][3][2], ss[4][3][2];
    const unsigned char* cb = smem + LDS_CS + wave * RECW + r * (DH * 2) + c * 8;
    asm volatile("s_waitcnt vmcnt(12)" ::: "memory");
#pragma unroll
    for (int k = 0; k < 4; ++k)
#pragma unroll
        for (int x = 0; x < 3; ++x) {
            const u32x2 t = *reinterpret_cast<const u32x2*>(cb + k * 1536 + 128 * x);
            cc[k][x][0] = f32x2{bflo(t[0]), bfhi(t[0])}; cc[k][x][1] = f32x2{bflo(t[1]), bfhi(t[1])};
            asm volatile("" : "+v"(cc[k][x][0]), "+v"(cc[k][x][1]));
        }
    PRE(2)
    loadq();
    const unsigned char* scp = smem + LDS_SC + (c & 4) * 1024 + (16 * wave + 4 * (c & 3) + r) * 2;
    unsigned sc = *reinterpret_cast<const unsigned short*>(scp);
    auto loadsin = [&](int k) {
        const unsigned char* sb1 = cb + (c < 8 ? (64 + HALF) * 2 : (64 - HALF) * 2);
        const unsigned sg = c < 8 ? 0u : 0x80008000u;
        u32x2 t[3];
        t[0] = *reinterpret_cast<const u32x2*>(cb + 6144 + k * 1536 + HALF * 2);
        t[1] = *reinterpret_cast<const u32x2*>(sb1 + 6144 + k * 1536) ^ u32x2{sg, sg};
        t[2] = *reinterpret_cast<const u32x2*>(cb + 6144 + k * 1536 + (128 - HALF) * 2) ^ u32x2{0x80008000u, 0x80008000u};
#pragma unroll
        for (int x = 0; x < 3; ++x) {
            ss[k][x][0] = f32x2{bflo(t[x][0]), bfhi(t[x][0])}; ss[k][x][1] = f32x2{bflo(t[x][1]), bfhi(t[x][1])};
            asm volatile("" : "+v"(ss[k][x][0]), "+v"(ss[k][x][1]));
        }
    };
    asm volatile("s_waitcnt vmcnt(12)" ::: "memory");
    loadsin(0); loadsin(1);
    PRE(3)
    asm volatile("s_waitcnt vmcnt(12)" ::: "memory");
    loadsin(2); loadsin(3);
    unsigned th1 = 0, th4 = 0;
    unsigned char* prec = smem + LDS_P + wave * (H * 128) + r * 32 + (c & 7) * 4;
    auto softmax = [&](float s, int h) {
        float m = fmaxf(s, dpp<0xb1>(s));
        m = xrow_max(fmaxf(m, dpp<0x4e>(m)));
        const float p = __builtin_amdgcn_exp2f(s - m);
        float l = p + dpp<0xb1>(p);
        l = xrow_sum(l + dpp<0x4e>(l));
        const float t = (c & 1) ? l : m;
        const int x = __builtin_amdgcn_update_dpp(__float_as_int(p), __float_as_int(t), 0x114, 0xf, 0xa, false);
        *reinterpret_cast<int*>(prec + h * 128) = x;
    };
    float sprev = 0.f;
    auto share = [&]() {
        if (!SH) return;
        const unsigned x = __builtin_amdgcn_readfirstlane(*reinterpret_cast<volatile unsigned*>(smem + LDS_CNT + 128));
        if (x > slip) { sched += x - slip; slip = x; calm = vt + 600 * 16; }
    };

#pragma unroll 1
    for (int h = 0; h < H; ++h) {
        const bool more = h + 1 < H;
        const rsrc_t nx = more ? kr : vr;
        const unsigned nso = more ? (h + 1) * hstride : 0;
        float scf = bflo(sc);
        asm volatile("" : "+v"(scf) :: "memory");
        SB();
        scp += more ? 128 : 4096 + 128;
        sc = *reinterpret_cast<const unsigned short*>(scp);
        SB();
        float sreg = 0.f;
#define KSTEP(k) { \
            const float kz = dpp<0x154 + (k)>(scf); \
            f32x2 kz2 = f32x2{kz, kz}; \
            asm("" : "+v"(kz2)); \
            f32x2 a2 = f32x2{0.f, 0.f}; \
            _Pragma("unroll") for (int x = 0; x < 3; ++x) { \
                _Pragma("unroll") for (int e = 0; e < 2; ++e) { \
                    const f32x2 w = __builtin_elementwise_fma(f32x2{qb[x][2 * e], qb[x][2 * e + 1]}, ss[k][x][e], f32x2{qa[x][2 * e], qa[x][2 * e + 1]} * cc[k][x][e]); \
                    const f32x2 kf = f32x2{(float)buf[k].d[x][2 * e], (float)buf[k].d[x][2 * e + 1]} - kz2; \
                    a2 = __builtin_elementwise_fma(kf, w, a2); \
                } \
            } \
            float sa = a2[0] + a2[1]; \
            asm volatile("" : "+v"(sa) :: "memory"); \
            SB(); pace(true); issue(buf[k], nx, nso + (k) * (4 * DH * 4)); SB(); \
            if ((k) == 0) { softmax(sprev, (h - 1) & (H - 1)); SB(); }   \
            if ((k) == 1) { rotq(); SB(); share(); SB(); } \
            if ((k) == 2) { \
                if (wave == 1 && h == ((OPT & 4) ? 1 + ((b >> 3) & 3) : 4 + ((b >> 3) & 15))) \
                    asm volatile("s_mov_b32 m0, %2\n\ts_nop 0\n\tglobal_load_lds_dwordx4 %0, %1 sc1" :: "v"(psk + (lane & 7) * SKS), "s"(a.part), "s"(lds0 + LDS_SK) : "memory"); \
                if (h == ((OPT & 4) ? 6 : 25)) {        \
                    const u32x2 e = *reinterpret_cast<const u32x2*>(smem + LDS_SK + (lane & 7) * 16); \
                    const int d = (int)(tdma - e[0]); \
                    if (__all(e[1] == t32 && d > -400 && d < 400)) { \
                        int sum = d + __builtin_amdgcn_mov_dpp(d, 0xb1, 0xf, 0xf, true); \
                        sum += __builtin_amdgcn_mov_dpp(sum, 0x4e, 0xf, 0xf, true); \
                        sum += __builtin_amdgcn_mov_dpp(sum, 0x124, 0xf, 0xf, true); \
                        if (SKL) {        \
                            int mn = min(d, __builtin_amdgcn_mov_dpp(d, 0xb1, 0xf, 0xf, true)); \
                            mn = min(mn, __builtin_amdgcn_mov_dpp(mn, 0x4e, 0xf, 0xf, true)); \
                            mn = min(mn, __builtin_amdgcn_mov_dpp(mn, 0x124, 0xf, 0xf, true)); \
                            sum = min(sum, 8 * (mn + SKL)); \
                        } \
                        const int adj = (__builtin_amdgcn_readfirstlane(sum) * ((OPT & 4) ? 73 : 112) + 4096) >> 13;        \
                        step -= adj; tmin -= adj; \
                        if (OPT & 1) sched -= 2 * __builtin_amdgcn_readfirstlane(sum) - 224 * adj;        \
                        if ((OPT & 64) && (OPT & 128) && b < 16 && wave == 0) {        \
                            unsigned t0, t1, t2, t3; \
                            const unsigned ar = (((__builtin_amdgcn_readfirstlane(e[0]) >> 13) & (NAREA - 1)) + 1) * AREA; \
                            asm volatile("s_load_dword %0, %4, %5\n\ts_load_dword %1, %4, %6\n\ts_load_dword %2, %4, %7\n\ts_load_dword %3, %4, %8\n\ts_waitcnt lgkmcnt(0)" : "=&s"(t0), "=&s"(t1), "=&s"(t2), "=&s"(t3) : "s"(a.part), "s"(ar), "s"(ar + AREA - 128), "s"(ar + (2u << 20) + 128), "s"(ar + AREA - (2u << 20) - 128) : "memory"); \
                            vt = now32() << 4; \
                        } \
                    } \
                } \
                SB(); \
            } \
            if ((k) == 3) { loadq(); SB(); } \
            sa = row16_allsum(sa); \
            sreg = (c & 3) == (k) ? sa : sreg; }
        KSTEP(0) KSTEP(1) KSTEP(2) KSTEP(3)
        sprev = sreg * (scf * SCALE_LOG2);
        if (DS) { if (h == 7) th1 = now32(); }
    }
    softmax(sprev, H - 1);
    if (DS) th4 = now32();

    const unsigned tg = (unsigned)(tag >> (16 * (lane & 3))) << 16;
    unsigned rl = lds0 + LDS_REC + lane * 4, rh = rl + 2 * RECW;
    auto pack = [&](f32x2 t, float u) { return u32x2{pk_bf16(t[0], t[1]), pk_bf16(u, 0.f) | tg}; };
    auto ldrec = [&](unsigned o, f32x2 (&p)[6]) {
        asm volatile("ds_read2st64_b32 %0, %6 offset1:1\n\tds_read2st64_b32 %1, %6 offset0:96 offset1:97\n\tds_read2st64_b32 %2, %7 offset1:1\n\tds_read2st64_b32 %3, %7 offset0:96 offset1:97\n\t"
                     "ds_read2st64_b32 %4, %6 offset0:2 offset1:98\n\tds_read2st64_b32 %5, %7 offset0:2 offset1:98"
                     : "=&v"(p[0]), "=&v"(p[1]), "=&v"(p[2]), "=&v"(p[3]), "=&v"(p[4]), "=&v"(p[5]) : "v"(rl + o), "v"(rh + o) : "memory");
    };
    auto merge = [&](int hd) {
        f32x2 p[6], ma, mb;
        f32x4 xa, xb;
        asm volatile("ds_read2st64_b64 %0, %2 offset1:8\n\tds_read2st64_b64 %1, %2 offset0:16 offset1:24" : "=&v"(xa), "=&v"(xb) : "v"(lds0 + LDS_P + hd * 128 + 16) : "memory");
        ldrec(hd * (DH * 4), p);
        asm volatile("s_waitcnt lgkmcnt(6)" : "+v"(xa), "+v"(xb) :: "memory");
        const float M = __builtin_amdgcn_fmed3f(__builtin_amdgcn_fmed3f(xa[0], xa[2], __builtin_inff()), __builtin_amdgcn_fmed3f(xb[0], xb[2], __builtin_inff()), __builtin_inff());
        ma = f32x2{__builtin_amdgcn_exp2f(xa[0] - M), __builtin_amdgcn_exp2f(xa[2] - M)};
        mb = f32x2{__builtin_amdgcn_exp2f(xb[0] - M), __builtin_amdgcn_exp2f(xb[2] - M)};
        const float rL = __builtin_amdgcn_rcpf((ma[0] * xa[1] + ma[1] * xa[3]) + (mb[0] * xb[1] + mb[1] * xb[3]));
        ma *= rL; mb *= rL;
        asm volatile("s_waitcnt lgkmcnt(0)" : "+v"(ma), "+v"(mb), "+v"(p[0]), "+v"(p[1]), "+v"(p[2]), "+v"(p[3]), "+v"(p[4]), "+v"(p[5]) :: "memory");
        const f32x2 u = __builtin_elementwise_fma(mb, p[5], ma * p[4]);
        return pack(ma[0] * p[0] + ma[1] * p[1] + mb[0] * p[2] + mb[1] * p[3], u[0] + u[1]);
    };
    constexpr unsigned LSTR = 128 << ((OPT >> 20) & 7);
    constexpr int NC = 4 * H;
    const int cn = sb;
    const unsigned hc = 4 * (cn & 7) + ((cn >> 3) & 3);
    unsigned old = 0, area = 0, fo = 0;
    unsigned flv = 0;
    float gl = 0.f, mw = 0.f;
    f32x2 fs[4] = {};
    constexpr int LDS_ML = LDS_QRAW + 2048;
#define BAD(y) ((unsigned)(y)[1] != t32 || (unsigned)(y)[3] != (unsigned)tag)
    auto ev_all = [&]() {
        unsigned n;
        asm volatile("1:\tds_read_b32 %0, %1\n\ts_waitcnt lgkmcnt(0)\n\tv_cmp_ne_u32 vcc, 4, %0\n\ts_cbranch_vccnz 1b" : "=&v"(n) : "v"(lds0 + LDS_CNT) : "memory", "vcc");
    };
    auto quiet = [&]() { const unsigned x = vt + 250 * 16; if (HD && (int)(x - calm) > 0) calm = x; };
    constexpr int MK = (OPT >> 26) & 3;
    auto ev_ml = [&]() {
        f32x2 ml[4];
#pragma unroll
        for (int w = 0; w < 4; ++w) ml[w] = *reinterpret_cast<const f32x2*>(smem + LDS_P + ((wave + w) & 3) * (H * 128) + (lane & 31) * 128 + 16);
        const float M = fmaxf(fmaxf(ml[0][0], ml[1][0]), fmaxf(ml[2][0], ml[3][0]));
        const float e0 = __builtin_amdgcn_exp2f(ml[0][0] - M);
        const float L = (e0 * ml[0][1] + __builtin_amdgcn_exp2f(ml[1][0] - M) * ml[1][1]) + (__builtin_amdgcn_exp2f(ml[2][0] - M) * ml[2][1] + __builtin_amdgcn_exp2f(ml[3][0] - M) * ml[3][1]);
        flv = ((__float_as_uint(M + __builtin_amdgcn_logf(L)) + 0x80) & 0xffffff00u) | ((unsigned)(tag >> (8 * (lane & 7))) & 255);
        gl = __uint_as_float(__builtin_amdgcn_readlane(__float_as_uint(e0 * __builtin_amdgcn_rcpf(L)), H - 1));
        asm volatile("" : "+v"(flv), "+s"(gl));
    };
    auto ev_area = [&]() {
        u32x4 e = *reinterpret_cast<const u32x4*>(smem + LDS_SK);
        if (__builtin_expect(__any(BAD(e)), 0)) {
#pragma unroll 1
            for (int n = 0; n < (1 << 20) && BAD(e); ++n)
                asm volatile("global_load_dwordx4 %0, %1, %2 sc1\n\ts_waitcnt vmcnt(0)" : "=v"(e) : "v"(psk), "s"(a.part) : "memory");
        }
        area = (((__builtin_amdgcn_readfirstlane(e[0]) >> 13) & (NAREA - 1)) + 1) * AREA + 4096;
        asm volatile("" : "+s"(area));
    };
    auto ev_put = [&]() {
        asm volatile("s_mov_b32 exec_hi, 0\n\tbuffer_store_dword %0, %1, %2, %3 offen sc1\n\ts_mov_b32 exec_hi, -1" :: "v"(flv), "v"(rl - lds0 - LDS_REC), "s"(pr), "s"(area + pfl + sb * LSTR) : "memory");
    };
    auto ev_get = [&]() {
        unsigned l = lane;
        asm volatile("" : "+v"(l));
        fo = area + pfl + 8 * wave * LSTR + (cn & 7) * 16 + (((l & 56) << 2) + (l & 7)) * LSTR;
        asm volatile("s_mov_b32 m0, %2\n\ts_nop 0\n\tglobal_load_lds_dwordx4 %0, %1 sc1" :: "v"(fo), "s"(a.part), "s"(lds0 + LDS_ML + wave * 1024) : "memory");
    };
    auto ev_w1 = [&]() {
        u32x4 y = *reinterpret_cast<const u32x4*>(smem + LDS_ML + wave * 1024 + lane * 16);
        const unsigned tq = (cn & 1) ? t32 : (unsigned)tag;
#define BADW(y) (__builtin_amdgcn_perm(__builtin_amdgcn_perm((y)[3], (y)[2], 0x0400), __builtin_amdgcn_perm((y)[1], (y)[0], 0x0400), 0x05040100) != tq)
        if (!MK && __builtin_expect(__any(BADW(y)), 0)) {
#pragma unroll 1
            for (int n = 0; n < (1 << 20) && BADW(y); ++n)
                asm volatile("global_load_dwordx4 %0, %1, %2 sc1\n\ts_waitcnt vmcnt(0)" : "=v"(y) : "v"(fo), "s"(a.part) : "memory");
        }
        const float m = __uint_as_float(((cn & 16) ? ((cn & 8) ? y[3] : y[2]) : ((cn & 8) ? y[1] : y[0])) & 0xffffff00u);
        float u;
        asm volatile("s_nop 1\n\tv_max_f32_dpp %0, %2, %2 row_ror:8 row_mask:0xf bank_mask:0xf\n\ts_nop 1\n\tv_max_f32_dpp %0, %0, %0 row_ror:4 row_mask:0xf bank_mask:0xf\n\ts_nop 1\n\t"
                     "v_max_f32_dpp %0, %0, %0 row_ror:2 row_mask:0xf bank_mask:0xf\n\ts_nop 1\n\tv_max_f32_dpp %0, %0, %0 row_ror:1 row_mask:0xf bank_mask:0xf\n\tv_mov_b32 %1, %0\n\ts_nop 1\n\t"
                     "v_permlane16_swap_b32 %0, %1\n\ts_nop 0\n\tv_max_f32 %0, %0, %1\n\tv_mov_b32 %1, %0\n\ts_nop 1\n\tv_permlane32_swap_b32 %0, %1\n\ts_nop 0\n\tv_max_f32 %0, %0, %1" : "=&v"(mw), "=&v"(u) : "v"(m));
        fs[0][0] = __builtin_amdgcn_exp2f(m - mw);
        asm volatile("" : "+v"(fs[0]));
    };
    auto ev_w2 = [&]() {
        const f32x2 f = fs[0];
        unsigned bp = (lane >> 3) * 4;
        asm volatile("ds_bpermute_b32 %0, %8, %9\n\tds_bpermute_b32 %1, %8, %9 offset:32\n\tds_bpermute_b32 %2, %8, %9 offset:64\n\tds_bpermute_b32 %3, %8, %9 offset:96\n\t"
                     "ds_bpermute_b32 %4, %8, %9 offset:128\n\tds_bpermute_b32 %5, %8, %9 offset:160\n\tds_bpermute_b32 %6, %8, %9 offset:192\n\tds_bpermute_b32 %7, %8, %9 offset:224"
                     : "=&v"(fs[0][0]), "=&v"(fs[0][1]), "=&v"(fs[1][0]), "=&v"(fs[1][1]), "=&v"(fs[2][0]), "=&v"(fs[2][1]), "=&v"(fs[3][0]), "=&v"(fs[3][1]) : "v"(bp), "v"(f[0]) : "memory");
        *reinterpret_cast<f32x2*>(smem + LDS_QRAW + 1024 + wave * 8) = f32x2{mw, xrow_sum(row16_allsum(f[0]))};
        asm volatile("s_waitcnt lgkmcnt(0)" : "+v"(fs[0]), "+v"(fs[1]), "+v"(fs[2]), "+v"(fs[3]) :: "memory");
    };
    constexpr int G1 = EV & 15, G2 = (EV >> 4) & 15, G3 = (EV >> 8) & 15, GW = (EV >> 20) & 7;
    const int hput = wave == ((b >> 5) & 3) ? G1 + G2 + 1 + ((b >> 3) & 3) : -9, hget = cn < NC ? G1 + G2 + 6 + (GW == 0 ? (b >> 3) & 3 : GW == 1 ? cn >> 3 : GW == 2 ? ((cn >> 3) + 4 * wave) & 15 : GW == 3 ? ((cn >> 3) + 2 * cn) & 15 : (cn >> 3) & 7) : -9;
    const int htch = cn < NC && wave == 0 ? ((EV >> 12) & 31) + ((b >> 3) & 3) : -9;
    float pv = *reinterpret_cast<const float*>(prec);
    f32x2 acc[3][2];
    float y;
    auto prep = [&]() {
        y = bflo(sc);
        y = (c & 4) ? y : y * pv;
        asm volatile("" : "+v"(y) :: "memory");
        SB();
        scp += 128;
        sc = *reinterpret_cast<const unsigned short*>(scp);
        prec += 128;
        pv = *reinterpret_cast<const float*>(prec);
        SB();
    };
#define VCOMP(k) { \
        const float pw = dpp<0x150 + (k)>(y); \
        const float vz = dpp<0x154 + (k)>(y); \
        f32x2 pw2 = f32x2{pw, pw}, vz2 = f32x2{vz, vz}; \
        asm("" : "+v"(pw2), "+v"(vz2)); \
        _Pragma("unroll") for (int x = 0; x < 3; ++x) \
            _Pragma("unroll") for (int e = 0; e < 2; ++e) { \
                const f32x2 vf = f32x2{(float)buf[k].d[x][2 * e], (float)buf[k].d[x][2 * e + 1]} - vz2; \
                acc[x][e] = (k) == 0 ? pw2 * vf : __builtin_elementwise_fma(pw2, vf, acc[x][e]); \
            } \
        asm volatile("" : "+v"(acc[0][0]), "+v"(acc[0][1]), "+v"(acc[1][0]), "+v"(acc[1][1]), "+v"(acc[2][0]), "+v"(acc[2][1]) :: "memory"); \
        SB(); }
#define VSTEP(k, hn) { \
        VCOMP(k) pace(true); issue(buf[k], vr, (hn) * hstride + (k) * (4 * DH * 4)); SB(); \
        if ((k) == 1) { tmin = (hn) == H - 1 ? 0 : tmin; share(); SB(); } \
        if ((k) == 1 && (hn) == G1) { ev_all(); SB(); } \
        if ((k) == 2 && (hn) == G1) { ev_ml(); SB(); } \
        if ((k) == 2 && (hn) == G1 + G2) { ev_area(); SB(); } \
        if ((k) == 1 && (OPT & 1024)) { vth = WIN & 15; asm volatile("" : "+s"(vth)); } \
        if ((k) == 1 && (hn) == hput) { if (!(MK & 1)) ev_put(); if (OPT & 1024) vth = 15; quiet(); SB(); } \
        if ((k) == 1 && (hn) == hget) { if (!(MK & 2)) ev_get(); if (OPT & 1024) vth = 15; quiet(); SB(); } \
        if ((k) == 2 && (hn) == hget + 2) { ev_w1(); SB(); } \
        if ((OPT & 2048) && (k) == 2 && (hn) == htch) { asm volatile("s_mov_b32 m0, %2\n\ts_mov_b64 exec, 63\n\tglobal_load_lds_dword %0, %1\n\ts_mov_b64 exec, -1" :: "v"(hc * (DH * 2) + 16 * (rl - lds0 - LDS_REC)), "s"(a.out), "s"(lds0 + LDS_ML + 4096) : "memory"); vth = 15; SB(); } \
        if ((k) == 1 && (hn) == hget + 2 + G3) { ev_w2(); SB(); } }
    auto record = [&](unsigned ra, unsigned ca) {
        f32x4 o;
#pragma unroll
        for (int e = 0; e < 4; ++e) {
            const float t0 = sw32(acc[0][e >> 1][e & 1], acc[2][e >> 1][e & 1]);
            const float t1 = sw32(acc[1][e >> 1][e & 1], acc[1][e >> 1][e & 1]);
            o[e] = sw16(t0, t1);
        }
        asm volatile("s_mov_b64 exec, %5\n\tds_write_b128 %1, %2\n\ts_mov_b64 exec, 1\n\tds_add_rtn_u32 %0, %3, %4\n\ts_mov_b64 exec, -1"
                     : "+v"(old) : "v"(ra), "v"(o), "v"(ca), "v"(1u), "s"(0xffffffffffffull) : "memory");
        SB();
    };
#pragma unroll 1
    for (int h = 0; h < H - 1; ++h) {
        prep();
        VSTEP(0, h + 1)
        asm volatile("s_waitcnt lgkmcnt(0)" : "+v"(old) :: "memory");
        if (__builtin_amdgcn_readfirstlane(old) == 3) *reinterpret_cast<u32x2*>(smem + LDS_REC + (h - 1) * (DH * 4) + lane * 8) = merge(h - 1);
        SB();
        VSTEP(1, h + 1) VSTEP(2, h + 1) VSTEP(3, h + 1)
        record(lds0 + LDS_REC + h * (DH * 4) + wave * RECW + lane * 16, lds0 + LDS_CNT + h * 4);
    }
    asm volatile("s_branch .Lkv_t%c0\n\t.p2align 12,,3327\n.Lkv_t%c0:" :: "i"(ID));
    y = bflo(sc);
    y = (c & 4) ? y : y * (pv * gl);
    VCOMP(0)
    asm volatile("s_waitcnt lgkmcnt(0)\n\ts_barrier" ::: "memory");
    u32x4 ev[4];
#pragma unroll
    for (int p = 0; p < 4; ++p) ev[p] = *reinterpret_cast<const u32x4*>(smem + LDS_REC + (8 * wave + 2 * p + (lane >> 5)) * (DH * 4) + (lane & 31) * 16);
    const f32x4 a0 = *reinterpret_cast<const f32x4*>(smem + LDS_QRAW + 1024), a1 = *reinterpret_cast<const f32x4*>(smem + LDS_QRAW + 1040);
    SB();
    VCOMP(1)
    unsigned eo = (lane >> 5) * (nb * LS) + (lane & 31) * 16, e8 = lane * 8, ra = lds0 + LDS_REC + (H - 1) * (DH * 4) + wave * RECW + lane * 16, ca = lds0 + LDS_CNT + (H - 1) * 4;
    unsigned es[5];
#pragma unroll
    for (int p = 0; p < 4; ++p) es[p] = area + ((8 * wave + 2 * p) * nb + sb) * LS;
    es[4] = area + ((H - 1) * nb + sb) * LS;
    unsigned tx = sched - step - stag + EX * 16 + (sb >> 3) * GS * 4;
    if (wave == 3) { const u32x2 t = merge(H - 2); ev[3][0] = t[0]; ev[3][1] = t[1]; }
    {
        const float M = fmaxf(fmaxf(a0[0], a0[2]), fmaxf(a1[0], a1[2]));
        const float gw = __builtin_amdgcn_exp2f(mw - M) * __builtin_amdgcn_rcpf((__builtin_amdgcn_exp2f(a0[0] - M) * a0[1] + __builtin_amdgcn_exp2f(a0[2] - M) * a0[3]) + (__builtin_amdgcn_exp2f(a1[0] - M) * a1[1] + __builtin_amdgcn_exp2f(a1[2] - M) * a1[3]));
#pragma unroll
        for (int i = 0; i < 4; ++i) fs[i] *= gw;
    }
    rl += (H - 1) * (DH * 4); rh += (H - 1) * (DH * 4);
    asm volatile("" : "+v"(fs[0]), "+v"(fs[1]), "+v"(fs[2]), "+v"(fs[3]), "+v"(ev[0]), "+v"(ev[1]), "+v"(ev[2]), "+v"(ev[3]), "+v"(eo), "+v"(e8), "+v"(ra), "+v"(ca), "+v"(rl), "+v"(rh),
                      "+s"(es[0]), "+s"(es[1]), "+s"(es[2]), "+s"(es[3]), "+s"(es[4]), "+s"(tx));
    unsigned tgt = sched - stag + (hc == H - 1 ? HL : HOLD) * 16;
    unsigned rs[8];
#pragma unroll
    for (int i = 0; i < 8; ++i) rs[i] = area + (hc * nb + 32 * i + 8 * wave) * LS + (cn >> 5) * 128;
    const unsigned g = lane & 7, e = (lane >> 3) & 1;
    unsigned xo = (lane >> 3) * LS + g * 16;
    unsigned oo = r == 3 ? 0x80000000u : (hc * DH + (cn >> 5) * 16 + 2 * g + (r == 2 ? 128 + e : r + 64 * e)) * 2;
    unsigned po = lds0 + LDS_QRAW + lane * 4, wo = po + wave * 256;
    unsigned tb = (lane & 1) ? t32 : (unsigned)tag, ta = tb << 16;
    const rsrc_t orr = mkrsrc(a.out, H * DH * 2);
    asm volatile("" : "+s"(rs[0]), "+s"(rs[1]), "+s"(rs[2]), "+s"(rs[3]), "+s"(rs[4]), "+s"(rs[5]), "+s"(rs[6]), "+s"(rs[7]), "+v"(po), "+v"(wo), "+v"(xo), "+v"(oo), "+v"(ta), "+v"(tb), "+s"(tgt) : "s"(orr) : "memory");
    SB();
    VCOMP(2)
    while ((int)((now32() << 4) - tx) < 0) {}
    if constexpr ((OPT & 512) != 0)
    asm volatile("s_waitcnt vmcnt(0)\n\tbuffer_store_dwordx4 %3, %7, %9, %10 offen sc1\n\tbuffer_store_dwordx4 %4, %7, %9, %11 offen sc1\n\tbuffer_store_dwordx4 %5, %7, %9, %12 offen sc1\n\t"
                 "s_cmp_eq_u32 %14, 3\n\ts_cbranch_scc1 1f\n\tbuffer_store_dwordx4 %6, %7, %9, %13 offen sc1\n\ts_branch 2f\n1:\tbuffer_store_dwordx2 %15, %8, %9, %13 offen sc1\n2:"
                 : "+v"(buf[3].d[0]), "+v"(buf[3].d[1]), "+v"(buf[3].d[2]) : "v"(ev[0]), "v"(ev[1]), "v"(ev[2]), "v"(ev[3]), "v"(eo), "v"(e8), "s"(pr), "s"(es[0]), "s"(es[1]), "s"(es[2]), "s"(es[3]), "s"(wave),
                   "v"(u32x2{ev[3][0], ev[3][1]}) : "memory", "scc");
    else
    asm volatile("s_waitcnt vmcnt(0)\n\tbuffer_store_dwordx4 %3, %7, %9, %10 offen sc1\n\tbuffer_store_dwordx4 %4, %7, %9, %11 offen sc1\n\tbuffer_store_dwordx4 %5, %7, %9, %12 offen sc1\n\t"
                 "s_cmp_eq_u32 %14, 3\n\ts_cselect_b64 exec, 0, -1\n\tbuffer_store_dwordx4 %6, %7, %9, %13 offen sc1\n\ts_not_b64 exec, exec\n\tbuffer_store_dwordx2 %15, %8, %9, %13 offen sc1\n\ts_mov_b64 exec, -1"
                 : "+v"(buf[3].d[0]), "+v"(buf[3].d[1]), "+v"(buf[3].d[2]) : "v"(ev[0]), "v"(ev[1]), "v"(ev[2]), "v"(ev[3]), "v"(eo), "v"(e8), "s"(pr), "s"(es[0]), "s"(es[1]), "s"(es[2]), "s"(es[3]), "s"(wave),
                   "v"(u32x2{ev[3][0], ev[3][1]}) : "memory", "scc");
    if (DBG == 4) tbar = now32();
    SB();
    VCOMP(3)
    asm volatile("" :: "v"(ev[0]), "v"(ev[1]), "v"(ev[2]), "v"(ev[3]));
    record(ra, ca);
    asm volatile("s_waitcnt lgkmcnt(0)" : "+v"(old) :: "memory");
    if (__builtin_amdgcn_readfirstlane(old) == 3) {
        f32x2 p[6];
        ldrec(0, p);
        asm volatile("s_waitcnt lgkmcnt(0)" : "+v"(p[0]), "+v"(p[1]), "+v"(p[2]), "+v"(p[3]), "+v"(p[4]), "+v"(p[5]) :: "memory");
        const f32x2 u = p[4] + p[5];
        __builtin_amdgcn_raw_buffer_store_b64(pack((p[0] + p[1]) + (p[2] + p[3]), u[0] + u[1]), pr, e8, es[4], 16);
    }
    if ((OPT & 16) && b == 0 && wave == 1) __builtin_amdgcn_raw_buffer_store_b64(u32x2{(unsigned)tag, t32}, pr, lane == 0 ? pep : 0x80000000u, 0, 16);
    unsigned tend = DS ? now32() : 0;
    unsigned tfl = 0, tpl = 0;

    if (cn < NC) {
        i32x4 x[8];
        while ((int)((now32() << 4) - tgt) < 0) {}
        if (DS) tfl = now32();
#define XL(i) x[i] = bld128<16>(pr, xo, rs[i]); SB();
#define XS(i) XL(i) __builtin_amdgcn_s_sleep((SL >> (2 * (i))) & 3); SB();
        XS(0) XS(1) XS(2) XS(3) XS(4) XS(5) XS(6) XL(7)
        if (DBG == 4) th4 = now32();
        SB();
        f32x2 o[3];
#pragma unroll 1
        for (int n = 0; n < (1 << 20); ++n) {
            unsigned bad = 0;
#pragma unroll
            for (int i = 0; i < 8; ++i) {
#pragma unroll
                for (int e = 0; e < 3; ++e) {
                    const f32x2 t = e < 2 ? f32x2{bflo(x[i][2 * e]), bfhi(x[i][2 * e])} : f32x2{bflo(x[i][1]), bflo(x[i][3])};
                    if (i == 0) asm volatile("v_pk_mul_f32 %0, %1, %2 op_sel_hi:[0,1]" : "=v"(o[e]) : "v"(fs[0]), "v"(t));
                    else if (i & 1) asm volatile("v_pk_fma_f32 %0, %1, %2, %0 op_sel:[1,0,0]" : "+v"(o[e]) : "v"(fs[i >> 1]), "v"(t));
                    else asm volatile("v_pk_fma_f32 %0, %1, %2, %0 op_sel_hi:[0,1,1]" : "+v"(o[e]) : "v"(fs[i >> 1]), "v"(t));
                }
                bad = __builtin_amdgcn_bitop3_b32(bad, x[i][1], ta, 0xf6);
                bad = __builtin_amdgcn_bitop3_b32(bad, x[i][3], tb, 0xf6);
                asm volatile("" : "+v"(bad));
                SB();
            }
            if (__builtin_expect(__all(bad < 0x10000u), 1)) break;
            asm volatile("" ::: "memory");
            XL(0) XL(1) XL(2) XL(3) XL(4) XL(5) XL(6) XL(7)
        }
        asm volatile("" : "+v"(o[0]), "+v"(o[1]), "+v"(o[2]));
        if (DS) tpl = now32();
        const float w0 = sw16(sw32(o[0][0], o[2][0]), sw32(o[1][0], o[1][0])), w1 = sw16(sw32(o[0][1], o[2][1]), sw32(o[1][1], o[1][1]));
        asm volatile("ds_write_b32 %0, %1\n\ts_waitcnt lgkmcnt(0)\n\ts_barrier" :: "v"(wo), "v"((c < 8 ? w0 : w1) + dpp<0x128>(c < 8 ? w1 : w0)) : "memory");
        if (wave == 0) {
            f32x2 p0, p1;
            asm volatile("ds_read2st64_b32 %0, %2 offset1:1\n\tds_read2st64_b32 %1, %2 offset0:2 offset1:3\n\ts_waitcnt lgkmcnt(0)" : "=&v"(p0), "=&v"(p1) : "v"(po) : "memory");
            p0 += p1;
            __builtin_amdgcn_raw_buffer_store_b16((unsigned short)pk_bf16(p0[0] + p0[1], 0.f), orr, oo, 0, (OPT & 8192) ? 16 : (OPT & 16384) ? 2 : 0);
        }
        if (!(OPT & 16) && b == 0 && tid == 64) *reinterpret_cast<u64*>(a.part + pep) = tag;
        asm volatile(".Lkv_b%c0:" :: "i"(ID));
    }
    if (DBG) {
        const unsigned tc1 = now32();
        if (!DS) tend = tbar = th4 = tfl = tpl = tc1;
        const unsigned tl16 = sched - stag - step;
        u64* stp = reinterpret_cast<u64*>(a.part + STAMP_OFF) + (size_t)((DBG == 4 || DBG == 24 || DBG == 6 ? cn : b) * 4 + wave) * 8;

        if (lane == 0) { stp[0] = tstart; stp[1] = tbar; stp[2] = DBG >= 4 ? tend - (((tend << 4) - tl16) >> 4) : th1; stp[3] = DBG == 4 && cn >= NC ? tend : th4; stp[4] = tend; stp[5] = cn < NC ? tfl : tend; stp[6] = cn < NC ? tpl : tend; stp[7] = tc1; }
        if (DBG == 2) *reinterpret_cast<u32x4*>(a.part + STAMP_OFF + 65536 + (b * 4 + wave) * 1024 + lane * 16) = *reinterpret_cast<const u32x4*>(smem + LDS_TR + 16 + wave * 1024 + lane * 16);
    }
}

template <int TP, int ADV, int TMIN, int WIN, int HC, int HX, int OPT, int EX, int GS, int HOLD, int HL, int SL, int EV, int ID, int DBG>
static void launch65(void* const* d_in, const int* in_sizes, void* d_out, void* d_ws, hipStream_t stream) {
    typedef const unsigned short* P;
    Args a;
    a.qk = (const int*)d_in[1]; a.qv = (const int*)d_in[4];
    a.k_cos = (P)d_in[9]; a.k_sin = (P)d_in[10];
    a.q = (P)d_in[0]; a.q_cos = (P)d_in[7]; a.q_sin = (P)d_in[8];
    a.sc[0] = (P)d_in[2]; a.sc[1] = (P)d_in[3]; a.sc[2] = (P)d_in[5]; a.sc[3] = (P)d_in[6];
    a.part = (unsigned char*)d_ws;
    a.out = (unsigned short*)d_out;
    kv65_kernel<TP, ADV, TMIN, WIN, HC, HX, OPT, EX, GS, HOLD, HL, SL, EV, ID, DBG><<<Skv / RW, 256, 0, stream>>>(a);
}

extern "C" void kernel_launch(void* const* d_in, const int* in_sizes, int n_in, void* d_out, int out_size, void* d_ws, size_t ws_size, hipStream_t stream) {
    launch65<80*16-2,220+(32<<10),78*16+8,4+16*32+4096*28,4+16*32+4096*70+(1<<28)+(1<<30)+(60<<20),70+(62<<24),38+64+512+1024+17+131072,66,3,85,100,0x2aaa,5+16*1+256*1+(4<<20)+(20<<24),0,0>(d_in, in_sizes, d_out, d_ws, stream);
}
```

```cpp
#include <hip/hip_runtime.h>
#include <hip/hip_bf16.h>
#include <cstdint>
#define KALIGN 256
#define STAMP_OFF (200 << 20)
using bf16 = __hip_bfloat16;
using i32x4 = __attribute__((ext_vector_type(4))) int;
using u32x4 = __attribute__((ext_vector_type(4))) unsigned;
using u32x2 = __attribute__((ext_vector_type(2))) unsigned;
using f32x2 = __attribute__((ext_vector_type(2))) float;
using f32x4 = __attribute__((ext_vector_type(4))) float;

constexpr int DH = 192;
constexpr int HALF = 96;
constexpr int RW = 64;
constexpr int MAXH = 32;

__device__ __forceinline__ float bflo(unsigned u) { return __uint_as_float(u << 16); }
__device__ __forceinline__ float bfhi(unsigned u) { return __uint_as_float(u & 0xffff0000u); }
__device__ __forceinline__ float rbf(float x) { return __bfloat162float(__float2bfloat16(x)); }

template <int CTRL> __device__ __forceinline__ float dpp(float x) {
    return __builtin_bit_cast(float, __builtin_amdgcn_mov_dpp(__builtin_bit_cast(int, x), CTRL, 0xf, 0xf, true));
}
__device__ __forceinline__ float row16_allsum(float x) {
    x += dpp<0x128>(x);
    x += dpp<0x124>(x);
    x += dpp<0x122>(x);
    x += dpp<0x121>(x);
    return x;
}
__device__ __forceinline__ float row16_allmax(float x) {
    x = fmaxf(x, dpp<0x128>(x));
    x = fmaxf(x, dpp<0x124>(x));
    x = fmaxf(x, dpp<0x122>(x));
    x = fmaxf(x, dpp<0x121>(x));
    return x;
}
__device__ __forceinline__ float xrow_max(float x) {
    auto s = __builtin_amdgcn_permlane16_swap(__float_as_uint(x), __float_as_uint(x), false, false);
    x = fmaxf(__uint_as_float(s[0]), __uint_as_float(s[1]));
    auto t = __builtin_amdgcn_permlane32_swap(__float_as_uint(x), __float_as_uint(x), false, false);
    return fmaxf(__uint_as_float(t[0]), __uint_as_float(t[1]));
}
__device__ __forceinline__ float xrow_sum(float x) {
    auto s = __builtin_amdgcn_permlane16_swap(__float_as_uint(x), __float_as_uint(x), false, false);
    x = __uint_as_float(s[0]) + __uint_as_float(s[1]);
    auto t = __builtin_amdgcn_permlane32_swap(__float_as_uint(x), __float_as_uint(x), false, false);
    return __uint_as_float(t[0]) + __uint_as_float(t[1]);
}
__device__ __forceinline__ float sw32(float a, float b) {
    auto t = __builtin_amdgcn_permlane32_swap(__float_as_uint(a), __float_as_uint(b), false, false);
    return __uint_as_float(t[0]) + __uint_as_float(t[1]);
}
__device__ __forceinline__ float sw16(float a, float b) {
    auto t = __builtin_amdgcn_permlane16_swap(__float_as_uint(a), __float_as_uint(b), false, false);
    return __uint_as_float(t[0]) + __uint_as_float(t[1]);
}

#define SB() __builtin_amdgcn_sched_barrier(0)
typedef __amdgpu_buffer_rsrc_t rsrc_t;
__device__ __forceinline__ rsrc_t mkrsrc(const void* p, unsigned bytes) {
    return __builtin_amdgcn_make_buffer_rsrc(const_cast<void*>(p), 0, bytes, 0x00020000);
}
template <int AUX> __device__ __forceinline__ i32x4 bld128(rsrc_t r, unsigned voff, unsigned soff) {
    return __builtin_bit_cast(i32x4, __builtin_amdgcn_raw_buffer_load_b128(r, voff, soff, AUX));
}
template <int AUX> __device__ __forceinline__ unsigned bld16(rsrc_t r, unsigned voff, unsigned soff) {
    return __builtin_amdgcn_raw_buffer_load_b16(r, voff, soff, AUX);
}

struct HG { i32x4 d[3]; };
typedef unsigned long long u64;
typedef __attribute__((address_space(3))) unsigned char lds_u8;

__device__ __forceinline__ unsigned pk_bf16(float lo, float hi) {
    unsigned r;
    asm("v_cvt_pk_bf16_f32 %0, %1, %2" : "=v"(r) : "v"(lo), "v"(hi));
    return r;
}
__device__ __forceinline__ unsigned now32() { return (unsigned)__builtin_amdgcn_s_memrealtime(); }

constexpr int LDS_REC = 0;
constexpr int RECW = MAXH * DH * 4;
constexpr int LDS_CS = 0;
constexpr int LDS_QT = 12288;
constexpr int LDS_PF = 14336;
constexpr int LDS_QRAW = MAXH * 4 * DH * 4;
constexpr int LDS_Q = LDS_QRAW + MAXH * DH * 2;
constexpr int LDS_P = LDS_Q + 4 * DH * 4;
constexpr int LDS_SC = LDS_P + 4 * MAXH * 128;
constexpr int LDS_CNT = LDS_SC + 4 * MAXH * RW * 2;
constexpr int LDS_SK = LDS_CNT + 256;
constexpr int LDS_TR = LDS_SK + 1024;
constexpr int LDS_BYTES = LDS_TR + 4096 + 64;

struct Args {
    const int* qk; const int* qv;
    const unsigned short* k_cos; const unsigned short* k_sin;
    const unsigned short* q; const unsigned short* q_cos; const unsigned short* q_sin;
    unsigned char* part;
    unsigned short* out;
    const unsigned short* sc[4];
};
constexpr unsigned Skv = 16384;
constexpr float SCALE_LOG2 = 0.10411754627697264f;

template <int TP, int ADV, int PX, int TMIN, int CAP, int HD, int RA, int SKL, int FC, int F, int EX, int GS, int HOLD, int HL, int SL, int ID, int DBG>
__global__ __launch_bounds__(256) __attribute__((aligned(KALIGN))) void kv71_kernel(Args a)
{
    __shared__ __attribute__((aligned(16))) unsigned char smem[LDS_BYTES];
    const unsigned tstart = DBG ? now32() : 0;
    constexpr bool DS = (DBG && DBG < 5) || DBG == 24;
    constexpr int H = MAXH, PFN = 16;
    const int b = blockIdx.x;
    const int tid = threadIdx.x;
    const int lane = tid & 63;
    const int wave = __builtin_amdgcn_readfirstlane(tid >> 6);
    typedef const unsigned short* const __attribute__((address_space(4))) * karg_t;
    const unsigned short* sp = ((karg_t)__builtin_amdgcn_kernarg_segment_ptr())[offsetof(Args, sc) / 8 + wave];
    if (b < PFN) {
        u64 pc; unsigned len, tl;
        asm volatile("s_getpc_b64 %0\n.Lkv_a%c3:\n\ts_mov_b32 %1, .Lkv_b%c3-.Lkv_a%c3-16\n\ts_mov_b32 %2, .Lkv_t%c3-.Lkv_a%c3" : "=s"(pc), "=s"(len), "=s"(tl) : "i"(ID));
        const unsigned o = tid * 16;
        asm volatile("s_mov_b32 m0, %5\n\ts_nop 0\n\tglobal_load_lds_dwordx4 %0, %4\n\tglobal_load_lds_dwordx4 %1, %4\n\tglobal_load_lds_dwordx4 %2, %4\n\tglobal_load_lds_dwordx4 %3, %4"
                     :: "v"(o), "v"(o + 4096), "v"(min(o + 8192, tl)), "v"(min(o + tl, len)), "s"(pc), "s"((unsigned)(size_t)(lds_u8*)smem + LDS_PF) : "memory");
    }
    SB();
    const int r = lane >> 4;
    const int c = lane & 15;
    constexpr unsigned hstride = Skv * (DH * 4);
    constexpr unsigned nb = Skv / RW;
    const unsigned lds0 = (unsigned)(size_t)(lds_u8*)smem;
    const unsigned pos = (b & 7) * (nb >> 3) + (b >> 3);
    const unsigned sb = ((((b >> 3) * 13) & 31) << 3) | (b & 7);
    const unsigned row0 = RW * pos + 16 * wave;

    constexpr unsigned LS = 512, psk = 0, pep = 128, AREA = 6u << 20, NAREA = 32, pfl = H * nb * LS;
    const rsrc_t pr = mkrsrc(a.part, (NAREA + 1) * AREA);
    u64 t64;
    {
        unsigned vs = ((lane >> 3) * Skv + RW * pos + (lane & 7) * 8) * 2, v1 = vs + 16 * Skv, v2 = vs + 32 * Skv, v3 = vs + 48 * Skv;
        unsigned ds = lds0 + LDS_SC + wave * 4096;
        asm volatile("" : "+v"(vs), "+v"(v1), "+v"(v2), "+v"(v3), "+s"(ds));
        SB();
        asm volatile("s_memrealtime %0\n\ts_mov_b32 m0, %6\n\ts_nop 0\n\tglobal_load_lds_dwordx4 %1, %5 nt\n\t"
                     "s_add_u32 m0, m0, 1024\n\ts_nop 0\n\tglobal_load_lds_dwordx4 %2, %5 nt\n\t"
                     "s_add_u32 m0, m0, 1024\n\ts_nop 0\n\tglobal_load_lds_dwordx4 %3, %5 nt\n\t"
                     "s_add_u32 m0, m0, 1024\n\ts_nop 0\n\tglobal_load_lds_dwordx4 %4, %5 nt\n\ts_waitcnt lgkmcnt(0)"
                     : "=&s"(t64) : "v"(vs), "v"(v1), "v"(v2), "v"(v3), "s"(sp), "s"(ds),
                       "s"(a.qk), "s"(a.qv), "s"(a.k_cos), "s"(a.k_sin), "s"(a.q), "s"(a.q_cos), "s"(a.q_sin), "s"(a.part), "s"(a.out) : "memory", "scc");
    }
    SB();
    const unsigned tdma = (unsigned)t64;
#define DMA3(V, A, B, P) "s_mov_b32 m0, " B "\n\ts_nop 0\n\t" \
                     "global_load_lds_dwordx4 " V ", " A P "\n\t" \
                     "global_load_lds_dwordx4 " V ", " A " offset:1024" P "\n\t" \
                     "global_load_lds_dwordx4 " V ", " A " offset:2048" P "\n\t"
    asm volatile(DMA3("%0", "%1", "%2", "") "s_mov_b32 m0, %4\n\ts_nop 0\n\tglobal_load_lds_dwordx4 %3, off"
                 :: "v"(lane * 16), "s"(a.q + wave * 1536), "s"(lds0 + LDS_QRAW + wave * 3072),
                    "v"(lane < 48 ? (const unsigned char*)((lane < 24 ? a.q_cos : a.q_sin - DH) + 8 * lane) : a.part + pep), "s"(lds0 + LDS_QT + wave * RECW) : "memory");
    if ((b >> 4) == 1 && wave == 3)
        asm volatile("s_mov_b32 m0, %2\n\ts_mov_b64 exec, 15\n\tglobal_load_lds_dword %0, %1\n\ts_mov_b64 exec, -1" :: "v"((((tdma >> 13) & (NAREA - 1)) + 1) * AREA + lane * 0x160000 + b * 128), "s"(a.part), "s"(lds0 + LDS_PF) : "memory");
    SB();
    constexpr int TH = 4, DL = 32, BIG = 70;
    const int stag = (int)(((sb & ~7) + wave * 2 + (b & 1)) * (TP / 2)) / 256 - TP / 4;
    const unsigned nb16 = -(tdma << 4);
    int sched = ADV * 16 + stag - 4 * (TP / 2);
    int vt = 0;
    int calm = 600 * 16, step = TP / 2, tmin = TMIN / 2, vth = TH, pvm = 0;
    int tru = -4, slip = 0;
    int tgt0, tnow, vm, tf;
    auto pace = [&]() {
        if (DBG == 2 && (F & 16)) tf = (now32() << 4) + nb16;
        if (F & 1)
        asm volatile("s_max_i32 %0, %3, %4\n1:\ts_memrealtime vcc\n\ts_waitcnt lgkmcnt(0)\n\ts_lshl4_add_u32 %1, vcc_lo, %5\n\ts_cmp_lt_i32 %1, %0\n\ts_cbranch_scc1 1b\n\t"
                     "s_getreg_b32 %2, hwreg(HW_REG_IB_STS, 0, 4)" : "=&s"(tgt0), "=&s"(tnow), "=s"(vm) : "s"(sched), "s"(vt), "s"(nb16) : "scc", "vcc", "memory");
        else
        asm volatile("s_max_i32 %0, %3, %4\n\ts_branch 2f\n1:\ts_sleep 1\n2:\ts_memrealtime vcc\n\ts_waitcnt lgkmcnt(0)\n\ts_lshl4_add_u32 %1, vcc_lo, %5\n\ts_cmp_lt_i32 %1, %0\n\ts_cbranch_scc1 1b\n\t"
                     "s_getreg_b32 %2, hwreg(HW_REG_IB_STS, 0, 4)" : "=&s"(tgt0), "=&s"(tnow), "=s"(vm) : "s"(sched), "s"(vt), "s"(nb16) : "scc", "vcc", "memory");
    };
    auto steer = [&](bool ctl, int k = 0) {
        sched += step;
        if (!ctl) vt = max(tnow - 128, tgt0) + tmin;
        else {
            vt = tgt0 + tmin;
            if (__builtin_expect(tnow - tgt0 > 128, 0)) {
                asm volatile("");
                vt = tnow - 128 + tmin;
                if (tnow - tgt0 > HD * 16 && tnow > calm) { sched += RA * 16; calm = tnow + 900 * 16; if (DBG == 2) slip += RA * 16; }
            }
            if (__builtin_expect(vm > vth, 0)) {
                asm volatile("" : "+s"(pvm));
                if (vt - sched < CAP * 16 || (pvm > TH && vt - sched > BIG * 16)) vt += (vm - TH) * DL;
            }
            if (FC && ((FC >> 20) >> k & 1)) {
                if (__builtin_expect(vt - sched > ((FC >> 8) & 255) * 16, 0)) {
                    asm volatile("" : "+s"(pvm));
                    if (max(vm, pvm) <= ((FC >> 16) & 15)) vt -= FC & 255;
                }
            }
            pvm = vm;
            if ((F >> 5) & 7) asm volatile(".rept %0\n\ts_nop 7\n\t.endr" :: "i"(3 * ((F >> 5) & 7)));
        }
        if constexpr (DBG == 2) { *reinterpret_cast<unsigned*>(smem + LDS_TR + 16 + wave * 1024 + (tru & 255) * 4) = ((unsigned)(tnow - (sched - slip - step)) >> 4) | ((F & 8) ? vm << 16 : (F & 16) ? ((tnow - tf) >> 4) << 16 : 0); ++tru; }
    };
    {
        unsigned dst = lds0 + LDS_CS + wave * RECW;
        const unsigned v0 = lane * 16 + row0 * (DH * 2);
#pragma unroll 1
        for (int j = 0; j < 4; ++j) {
            pace();
            asm volatile(DMA3("%0", "%1", "%2", " nt") :: "v"(v0), "s"((j < 2 ? a.k_cos : a.k_sin) + (j & 1) * 8 * DH), "s"(dst) : "memory");
            steer(false);
            sched += PX;
            dst += 3072;
        }
    }
    if (tid < MAXH) *reinterpret_cast<unsigned*>(smem + LDS_CNT + tid * 4) = 0;
    const rsrc_t kr = mkrsrc(a.qk, H * hstride);
    const rsrc_t vr = mkrsrc(a.qv, H * hstride);
    unsigned kvo[4][3];
#pragma unroll
    for (int i = 0; i < 12; ++i) { kvo[i / 3][i % 3] = (row0 + r + i / 3 * 4) * (DH * 4) + c * 16 + i % 3 * 256; asm volatile("" : "+v"(kvo[i / 3][i % 3])); }
    auto issue = [&](HG& g, int k, rsrc_t rs, unsigned so) {
#pragma unroll
        for (int x = 0; x < 3; ++x) g.d[x] = bld128<2>(rs, kvo[k][x], so);
    };
    HG buf[4];
#define PRE(g) SB(); pace(); issue(buf[g], g, kr, 0); SB(); steer(false); SB();
    asm volatile("s_waitcnt vmcnt(12) lgkmcnt(0)\n\ts_barrier" ::: "memory");
    unsigned tbar = DS ? now32() : 0;
    PRE(0)
    const unsigned qoff = (lane < 48 ? lane : lane - 48) * 4;
    const unsigned* qt = reinterpret_cast<const unsigned*>(smem + LDS_QT + wave * RECW + qoff);
    unsigned qc0 = qt[0], qc1 = qt[HALF / 2], qs0 = qt[DH / 2], qs1 = qt[(DH + HALF) / 2];
    u64 tag;
    {
        const u32x2 e = *reinterpret_cast<const u32x2*>(smem + LDS_QT + wave * RECW + 768);
        tag = (((u64)__builtin_amdgcn_readfirstlane(e[1]) << 32) | __builtin_amdgcn_readfirstlane(e[0])) * 6364136223846793005ull + 1442695040888963407ull;
        asm volatile("" : "+s"(tag));
    }
    const unsigned t32 = tag >> 32;
    unsigned qraw = lds0 + LDS_QRAW + qoff;
    unsigned char* qrot = smem + LDS_Q + wave * (DH * 4) + 2 * qoff;
    unsigned q_lo, q_hi;
    auto getq = [&]() {
        asm volatile("" : "+v"(qraw));
        q_lo = *(const __attribute__((address_space(3))) unsigned*)(size_t)qraw, q_hi = *(const __attribute__((address_space(3))) unsigned*)(size_t)(qraw + HALF * 2);
        qraw += DH * 2;
    };
    auto rotq = [&]() {
        const f32x2 lo = f32x2{bflo(q_lo), bfhi(q_lo)}, hi = f32x2{bflo(q_hi), bfhi(q_hi)};
        const f32x2 y1 = lo * f32x2{bflo(qc0), bfhi(qc0)}, y2 = -hi * f32x2{bflo(qs0), bfhi(qs0)};
        const f32x2 y3 = hi * f32x2{bflo(qc1), bfhi(qc1)}, y4 = lo * f32x2{bflo(qs1), bfhi(qs1)};
        const unsigned x1 = pk_bf16(y1[0], y1[1]), x2 = pk_bf16(y2[0], y2[1]), x3 = pk_bf16(y3[0], y3[1]), x4 = pk_bf16(y4[0], y4[1]);
        const f32x2 z1 = f32x2{bflo(x1), bfhi(x1)} + f32x2{bflo(x2), bfhi(x2)}, z2 = f32x2{bflo(x3), bfhi(x3)} + f32x2{bflo(x4), bfhi(x4)};
        const unsigned l2 = pk_bf16(z1[0], z1[1]), h2 = pk_bf16(z2[0], z2[1]);
        *reinterpret_cast<f32x2*>(qrot) = f32x2{bflo(l2), bfhi(l2)};
        *reinterpret_cast<f32x2*>(qrot + HALF * 4) = f32x2{bflo(h2), bfhi(h2)};
        asm volatile("" ::: "memory");
    };
    f32x4 qa[3], qb[3];
    const unsigned char* qh = smem + LDS_Q + wave * (DH * 4) + 16 * c;
    const unsigned char* qh1 = qh + (c < 8 ? 64 + HALF : 64 - HALF) * 4;
    auto loadq = [&]() {
        asm volatile("" ::: "memory");
        qa[0] = *reinterpret_cast<const f32x4*>(qh); SB();
        qb[0] = *reinterpret_cast<const f32x4*>(qh + HALF * 4); SB();
        qa[1] = *reinterpret_cast<const f32x4*>(qh + 256); SB();
        qb[1] = *reinterpret_cast<const f32x4*>(qh1); SB();
        qa[2] = *reinterpret_cast<const f32x4*>(qh + 512); SB();
        qb[2] = *reinterpret_cast<const f32x4*>(qh + (128 - HALF) * 4);
        asm volatile("" ::: "memory");
    };
    getq(); rotq();
    if ((b >> 3) == 4 && wave == 3) __builtin_amdgcn_raw_buffer_store_b128(u32x4{tdma, t32, tdma, (unsigned)tag}, pr, lane == 0 ? psk + (b & 7) * 16 : 0x80000000u, 0, 16);
    PRE(1)
    f32x2 cc[4][3][2], ss[4][3][2];
    const unsigned char* cb = smem + LDS_CS + wave * RECW + r * (DH * 2) + c * 8;
    asm volatile("s_waitcnt vmcnt(12)" ::: "memory");
#pragma unroll
    for (int k = 0; k < 4; ++k)
#pragma unroll
        for (int x = 0; x < 3; ++x) {
            const u32x2 t = *reinterpret_cast<const u32x2*>(cb + k * 1536 + 128 * x);
            cc[k][x][0] = f32x2{bflo(t[0]), bfhi(t[0])}; cc[k][x][1] = f32x2{bflo(t[1]), bfhi(t[1])};
            asm volatile("" : "+v"(cc[k][x][0]), "+v"(cc[k][x][1]));
        }
    PRE(2)
    loadq();
    const unsigned char* scp = smem + LDS_SC + (c & 1) * 4096 + (16 * wave + (c & 12) + r) * 2;
    unsigned sc = *reinterpret_cast<const unsigned short*>(scp);
    auto loadsin = [&](int k) {
        const unsigned char* sb1 = cb + (c < 8 ? (64 + HALF) * 2 : (64 - HALF) * 2);
        const unsigned sg = c < 8 ? 0u : 0x80008000u;
        u32x2 t[3];
        t[0] = *reinterpret_cast<const u32x2*>(cb + 6144 + k * 1536 + HALF * 2);
        t[1] = *reinterpret_cast<const u32x2*>(sb1 + 6144 + k * 1536) ^ u32x2{sg, sg};
        t[2] = *reinterpret_cast<const u32x2*>(cb + 6144 + k * 1536 + (128 - HALF) * 2) ^ u32x2{0x80008000u, 0x80008000u};
#pragma unroll
        for (int x = 0; x < 3; ++x) {
            ss[k][x][0] = f32x2{bflo(t[x][0]), bfhi(t[x][0])}; ss[k][x][1] = f32x2{bflo(t[x][1]), bfhi(t[x][1])};
            asm volatile("" : "+v"(ss[k][x][0]), "+v"(ss[k][x][1]));
        }
    };
    asm volatile("s_waitcnt vmcnt(12)" ::: "memory");
    loadsin(0); loadsin(1);
    PRE(3)
    asm volatile("s_waitcnt vmcnt(12)" ::: "memory");
    loadsin(2); loadsin(3);
    unsigned th1 = 0, th4 = 0;
    const unsigned char* prec = smem + LDS_P + wave * (H * 128) + r * 32 + (c & 12);
    unsigned pw_ = lds0 + LDS_P + wave * (H * 128) + r * 32 + (c & 12) + (c & 3) * 128;
    float sm, sp_, sacc = 0.f;
    auto softmax1 = [&]() {
        float u;
        asm volatile("v_max_f32_dpp %0, %2, %2 row_ror:4 row_mask:0xf bank_mask:0xf\n\ts_nop 1\n\tv_max_f32_dpp %0, %0, %0 row_ror:8 row_mask:0xf bank_mask:0xf\n\tv_mov_b32 %1, %0\n\ts_nop 1\n\t"
                     "v_permlane16_swap_b32 %0, %1\n\tv_max_f32 %0, %0, %1\n\tv_mov_b32 %1, %0\n\ts_nop 1\n\tv_permlane32_swap_b32 %0, %1\n\tv_max_f32 %0, %0, %1" : "=&v"(sm), "=&v"(u) : "v"(sacc));
        sp_ = __builtin_amdgcn_exp2f(sacc - sm);
        asm volatile("" : "+v"(sp_));
    };
    auto softmax2 = [&]() {
        float l = sp_ + dpp<0x124>(sp_);
        l = xrow_sum(l + dpp<0x128>(l));
        asm volatile("ds_write2_b32 %0, %1, %2 offset1:4" :: "v"(pw_), "v"(sp_), "v"((c & 4) ? l : sm) : "memory");
        pw_ += 512;
    };

    auto skew = [&](int h) {
        if (wave == 1 && h == 1 + ((b >> 3) & 3))
            asm volatile("s_mov_b32 m0, %2\n\ts_nop 0\n\tglobal_load_lds_dwordx4 %0, %1 sc1" :: "v"(psk + (lane & 7) * 16), "s"(a.part), "s"(lds0 + LDS_SK) : "memory");
        if (h == 6) {
            const u32x2 e = *reinterpret_cast<const u32x2*>(smem + LDS_SK + (lane & 7) * 16);
            const int d = (int)(tdma - e[0]);
            if (__all(e[1] == t32 && d > -400 && d < 400)) {
                int sum = d + __builtin_amdgcn_mov_dpp(d, 0xb1, 0xf, 0xf, true), mn = min(d, __builtin_amdgcn_mov_dpp(d, 0xb1, 0xf, 0xf, true));
                sum += __builtin_amdgcn_mov_dpp(sum, 0x4e, 0xf, 0xf, true); mn = min(mn, __builtin_amdgcn_mov_dpp(mn, 0x4e, 0xf, 0xf, true));
                sum += __builtin_amdgcn_mov_dpp(sum, 0x124, 0xf, 0xf, true); mn = min(mn, __builtin_amdgcn_mov_dpp(mn, 0x124, 0xf, 0xf, true));
                sum = __builtin_amdgcn_readfirstlane(min(sum, 8 * (mn + SKL)));
                const int adj = (sum * 73 + 4096) >> 13;
                step -= adj; tmin -= adj;
                sched -= 2 * sum - 224 * adj;
            }
        }
    };

    rsrc_t nx = kr;
    unsigned nso = 0, sm4 = 0x11111110;
    asm volatile("" : "+s"(sm4));
#pragma unroll 1
    for (int h = 0; h < H; ++h) {
        float scf = bflo(sc);
        asm volatile("" : "+v"(scf) :: "memory");
        SB();
        nso += hstride;
        scp += 128;
        if (__builtin_expect(h == H - 1, 0)) { asm volatile(""); nx = vr; nso = 0; scp += 4096; }
        sc = *reinterpret_cast<const unsigned short*>(scp);
        SB();
        float st[2];
#define KSTEP(k) { \
            __builtin_amdgcn_s_waitcnt(0xf79); SB();        \
            const float kz = dpp<0x151 + 4 * (k)>(scf); \
            const f32x2 kz2 = f32x2{kz, kz}; \
            f32x2 a2 = f32x2{0.f, 0.f}; \
            _Pragma("unroll") for (int x = 0; x < 3; ++x) { \
                _Pragma("unroll") for (int e = 0; e < 2; ++e) { \
                    const f32x2 w = __builtin_elementwise_fma(f32x2{qb[x][2 * e], qb[x][2 * e + 1]}, ss[k][x][e], f32x2{qa[x][2 * e], qa[x][2 * e + 1]} * cc[k][x][e]); \
                    const f32x2 kf = f32x2{(float)buf[k].d[x][2 * e], (float)buf[k].d[x][2 * e + 1]} - kz2; \
                    a2 = __builtin_elementwise_fma(kf, w, a2); \
                } \
            } \
            float sa = a2[0] + a2[1]; \
            asm volatile("" : "+v"(sa) :: "memory"); \
            SB(); pace(); issue(buf[k], k, nx, nso); SB(); steer(true, k); SB(); \
              \
            if ((k) < 2) asm volatile("v_add_f32_dpp %0, %1, %1 row_ror:8 row_mask:0xf bank_mask:0xf" : "=v"(st[(k) & 1]) : "v"(sa)); \
            else asm volatile("v_add_f32_dpp %0, %1, %1 row_ror:8 row_mask:0xf bank_mask:0xc" : "+v"(st[(k) & 1]) : "v"(sa)); \
            if ((k) == 0) { getq(); SB(); if (sm4 >> h & 1) softmax1(); SB(); } \
            if ((k) == 1) { rotq(); SB(); } \
            if ((k) == 2) { \
                asm volatile("" : "+s"(sm4)); \
                if (sm4 >> h & 1) softmax2(); \
                SB(); \
                if (__builtin_expect(h <= 6, 0)) skew(h); \
                SB(); \
            } \
            if ((k) == 3) { loadq(); SB(); } }
        KSTEP(0) KSTEP(1) KSTEP(2) KSTEP(3)
        asm volatile("v_add_f32_dpp %0, %0, %0 row_shl:4 row_mask:0xf bank_mask:0x5\n\tv_add_f32_dpp %0, %3, %3 row_shr:4 row_mask:0xf bank_mask:0xa\n\t"
                     "v_mul_f32 %1, %4, %1\n\tv_mov_b32_dpp %2, %2 quad_perm:[1,2,3,3] row_mask:0xf bank_mask:0xf\n\t"
                     "v_add_f32_dpp %0, %0, %0 quad_perm:[1,0,3,2] row_mask:0xf bank_mask:0xf\n\ts_nop 1\n\tv_add_f32_dpp %0, %0, %0 quad_perm:[2,3,0,1] row_mask:0xf bank_mask:0xf\n\t"
                     "v_mul_f32_dpp %0, %1, %0 quad_perm:[0,0,0,0] row_mask:0xf bank_mask:0xf\n\tv_cndmask_b32 %2, %2, %0, %5"
                     : "+v"(st[0]), "+v"(scf), "+v"(sacc) : "v"(st[1]), "s"(SCALE_LOG2), "s"(0x8888888888888888ull));
        if (DS) { if (h == 7) th1 = now32(); }
    }
    if (DS) th4 = now32();

    const unsigned tg = (unsigned)(tag >> (16 * (lane & 3))) << 16;
    unsigned rl = lds0 + LDS_REC + lane * 4, rh = rl + 2 * RECW;
    auto pack = [&](f32x2 t, float u) { return u32x2{pk_bf16(t[0], t[1]), pk_bf16(u, 0.f) | tg}; };
    auto ldrec = [&](unsigned o, f32x2 (&p)[6]) {
        asm volatile("ds_read2st64_b32 %0, %6 offset1:1\n\tds_read2st64_b32 %1, %6 offset0:96 offset1:97\n\tds_read2st64_b32 %2, %7 offset1:1\n\tds_read2st64_b32 %3, %7 offset0:96 offset1:97\n\t"
                     "ds_read2st64_b32 %4, %6 offset0:2 offset1:98\n\tds_read2st64_b32 %5, %7 offset0:2 offset1:98"
                     : "=&v"(p[0]), "=&v"(p[1]), "=&v"(p[2]), "=&v"(p[3]), "=&v"(p[4]), "=&v"(p[5]) : "v"(rl + o), "v"(rh + o) : "memory");
    };
    auto sum4 = [&](unsigned o) {
        f32x2 p[6];
        ldrec(o, p);
        asm volatile("s_waitcnt lgkmcnt(0)" : "+v"(p[0]), "+v"(p[1]), "+v"(p[2]), "+v"(p[3]), "+v"(p[4]), "+v"(p[5]) :: "memory");
        const f32x2 u = p[4] + p[5];
        return pack((p[0] + p[1]) + (p[2] + p[3]), u[0] + u[1]);
    };
    auto merge = [&](int hd) {
        f32x2 p[6], ma, mb;
        f32x4 xa, xb;
        asm volatile("ds_read2st64_b64 %0, %2 offset1:8\n\tds_read2st64_b64 %1, %2 offset0:16 offset1:24" : "=&v"(xa), "=&v"(xb) : "v"(lds0 + LDS_P + hd * 128 + 16) : "memory");
        ldrec(hd * (DH * 4), p);
        asm volatile("s_waitcnt lgkmcnt(6)" : "+v"(xa), "+v"(xb) :: "memory");
        const float M = __builtin_amdgcn_fmed3f(__builtin_amdgcn_fmed3f(xa[0], xa[2], __builtin_inff()), __builtin_amdgcn_fmed3f(xb[0], xb[2], __builtin_inff()), __builtin_inff());
        ma = f32x2{__builtin_amdgcn_exp2f(xa[0] - M), __builtin_amdgcn_exp2f(xa[2] - M)};
        mb = f32x2{__builtin_amdgcn_exp2f(xb[0] - M), __builtin_amdgcn_exp2f(xb[2] - M)};
        const float rL = __builtin_amdgcn_rcpf((ma[0] * xa[1] + ma[1] * xa[3]) + (mb[0] * xb[1] + mb[1] * xb[3]));
        ma *= rL; mb *= rL;
        asm volatile("s_waitcnt lgkmcnt(0)" : "+v"(ma), "+v"(mb), "+v"(p[0]), "+v"(p[1]), "+v"(p[2]), "+v"(p[3]), "+v"(p[4]), "+v"(p[5]) :: "memory");
        const f32x2 u = __builtin_elementwise_fma(mb, p[5], ma * p[4]);
        return pack(ma[0] * p[0] + ma[1] * p[1] + mb[0] * p[2] + mb[1] * p[3], u[0] + u[1]);
    };
    constexpr unsigned LSTR = 128;
    constexpr int NC = 4 * H;
    const int cn = sb;
    const unsigned hc = 4 * (cn & 7) + ((cn >> 3) & 3);
    unsigned old = 0, area = 0, fo = 0;
    unsigned flv = 0;
    float gw = 1.f, mw = 0.f;
    f32x2 fs[4] = {};
    constexpr int LDS_ML = LDS_QRAW + 2048;
#define BAD(y) ((unsigned)(y)[1] != t32 || (unsigned)(y)[3] != (unsigned)tag)
    auto ev_all = [&]() {
        unsigned n;
        asm volatile("1:\tds_read_b32 %0, %1\n\ts_waitcnt lgkmcnt(0)\n\tv_cmp_ne_u32 vcc, 4, %0\n\ts_cbranch_vccnz 1b" : "=&v"(n) : "v"(lds0 + LDS_CNT) : "memory", "vcc");
    };
    auto quiet = [&]() { vth = 15; calm = max(calm, vt + 500 * 16); };
    auto ev_ml = [&]() {
        f32x2 ml[4];
#pragma unroll
        for (int w = 0; w < 4; ++w) ml[w] = *reinterpret_cast<const f32x2*>(smem + LDS_P + ((wave + w) & 3) * (H * 128) + (lane & 31) * 128 + 16);
        const float M = fmaxf(fmaxf(ml[0][0], ml[1][0]), fmaxf(ml[2][0], ml[3][0]));
        const float e0 = __builtin_amdgcn_exp2f(ml[0][0] - M);
        const float L = (e0 * ml[0][1] + __builtin_amdgcn_exp2f(ml[1][0] - M) * ml[1][1]) + (__builtin_amdgcn_exp2f(ml[2][0] - M) * ml[2][1] + __builtin_amdgcn_exp2f(ml[3][0] - M) * ml[3][1]);
        flv = ((__float_as_uint(M + __builtin_amdgcn_logf(L)) + 0x80) & 0xffffff00u) | ((unsigned)(tag >> (8 * (lane & 7))) & 255);
        gw = e0 * __builtin_amdgcn_rcpf(L);
        asm volatile("" : "+v"(flv), "+v"(gw));
    };
    auto ev_area = [&]() {
        u32x4 e = *reinterpret_cast<const u32x4*>(smem + LDS_SK);
        if (__builtin_expect(__any(BAD(e)), 0)) {
#pragma unroll 1
            for (int n = 0; n < (1 << 20) && BAD(e); ++n)
                asm volatile("global_load_dwordx4 %0, %1, %2 sc1\n\ts_waitcnt vmcnt(0)" : "=v"(e) : "v"(psk), "s"(a.part) : "memory");
        }
        area = (((__builtin_amdgcn_readfirstlane(e[0]) >> 13) & (NAREA - 1)) + 1) * AREA + 4096;
        asm volatile("" : "+s"(area));
    };
    auto ev_put = [&]() {
        asm volatile("s_mov_b32 exec_hi, 0\n\tbuffer_store_dword %0, %1, %2, %3 offen sc1\n\ts_mov_b32 exec_hi, -1" :: "v"(flv), "v"(rl - lds0 - LDS_REC), "s"(pr), "s"(area + pfl + sb * LSTR) : "memory");
    };
    auto ev_get = [&]() {
        unsigned l = lane;
        asm volatile("" : "+v"(l));
        fo = area + pfl + 8 * wave * LSTR + (cn & 7) * 16 + (((l & 56) << 2) + (l & 7)) * LSTR;
        asm volatile("s_mov_b32 m0, %2\n\ts_nop 0\n\tglobal_load_lds_dwordx4 %0, %1 sc1" :: "v"(fo), "s"(a.part), "s"(lds0 + LDS_ML + wave * 1024) : "memory");
    };
    auto ev_w1 = [&]() {
        u32x4 y = *reinterpret_cast<const u32x4*>(smem + LDS_ML + wave * 1024 + lane * 16);
        const unsigned tq = (cn & 1) ? t32 : (unsigned)tag;
#define BADW(y) (__builtin_amdgcn_perm(__builtin_amdgcn_perm((y)[3], (y)[2], 0x0400), __builtin_amdgcn_perm((y)[1], (y)[0], 0x0400), 0x05040100) != tq)
        if (__builtin_expect(__any(BADW(y)), 0)) {
#pragma unroll 1
            for (int n = 0; n < (1 << 20) && BADW(y); ++n)
                asm volatile("global_load_dwordx4 %0, %1, %2 sc1\n\ts_waitcnt vmcnt(0)" : "=v"(y) : "v"(fo), "s"(a.part) : "memory");
        }
        const unsigned se = (cn & 8) ? 0x0706050cu : 0x0302010cu;
        const float m = __uint_as_float((cn & 16) ? __builtin_amdgcn_perm(y[3], y[2], se) : __builtin_amdgcn_perm(y[1], y[0], se));
        float u;
        asm volatile("s_nop 1\n\tv_max_f32_dpp %0, %2, %2 row_ror:8 row_mask:0xf bank_mask:0xf\n\ts_nop 1\n\tv_max_f32_dpp %0, %0, %0 row_ror:4 row_mask:0xf bank_mask:0xf\n\ts_nop 1\n\t"
                     "v_max_f32_dpp %0, %0, %0 row_ror:2 row_mask:0xf bank_mask:0xf\n\ts_nop 1\n\tv_max_f32_dpp %0, %0, %0 row_ror:1 row_mask:0xf bank_mask:0xf\n\tv_mov_b32 %1, %0\n\ts_nop 1\n\t"
                     "v_permlane16_swap_b32 %0, %1\n\ts_nop 0\n\tv_max_f32 %0, %0, %1\n\tv_mov_b32 %1, %0\n\ts_nop 1\n\tv_permlane32_swap_b32 %0, %1\n\ts_nop 0\n\tv_max_f32 %0, %0, %1" : "=&v"(mw), "=&v"(u) : "v"(m));
        fs[0][0] = __builtin_amdgcn_exp2f(m - mw);
        asm volatile("" : "+v"(fs[0]));
    };
    auto ev_w2 = [&]() {
        const f32x2 f = fs[0];
        unsigned bp = (lane >> 3) * 4;
        asm volatile("ds_bpermute_b32 %0, %8, %9\n\tds_bpermute_b32 %1, %8, %9 offset:32\n\tds_bpermute_b32 %2, %8, %9 offset:64\n\tds_bpermute_b32 %3, %8, %9 offset:96\n\t"
                     "ds_bpermute_b32 %4, %8, %9 offset:128\n\tds_bpermute_b32 %5, %8, %9 offset:160\n\tds_bpermute_b32 %6, %8, %9 offset:192\n\tds_bpermute_b32 %7, %8, %9 offset:224"
                     : "=&v"(fs[0][0]), "=&v"(fs[0][1]), "=&v"(fs[1][0]), "=&v"(fs[1][1]), "=&v"(fs[2][0]), "=&v"(fs[2][1]), "=&v"(fs[3][0]), "=&v"(fs[3][1]) : "v"(bp), "v"(f[0]) : "memory");
        *reinterpret_cast<f32x2*>(smem + LDS_QRAW + 1024 + wave * 8) = f32x2{mw, xrow_sum(row16_allsum(f[0]))};
        asm volatile("s_waitcnt lgkmcnt(0)" : "+v"(fs[0]), "+v"(fs[1]), "+v"(fs[2]), "+v"(fs[3]) :: "memory");
    };
    constexpr int G1 = 5;
    const int hput = wave == ((b >> 5) & 3) ? G1 + 2 + ((b >> 3) & 3) : -9, hget = cn < NC ? G1 + 7 + ((cn >> 3) & 7) : -9;
    unsigned ev1 = 2 | 1 << G1 | 15 << (G1 + 2) | 9u << (hget & 31) | 1u << (H - 1), ev2 = 3 << G1 | 4u << (hget & 31);
    asm volatile("" : "+s"(ev1), "+s"(ev2));
    float pv = *reinterpret_cast<const float*>(prec);
    f32x2 acc[3][2];
    float y, yn, zn;
    auto prep = [&](int hn) {
        const float s = bflo(sc);
        yn = s * (pv * __uint_as_float(__builtin_amdgcn_readlane(__float_as_uint(gw), hn)));
        float z = yn * dpp<0x55>(s);
        z += dpp<0x124>(z);
        z += dpp<0x128>(z);
        zn = -dpp<0x150>(z);
        asm volatile("" : "+v"(yn), "+v"(zn) :: "memory");
        SB();
        scp += 128;
        sc = *reinterpret_cast<const unsigned short*>(scp);
        prec += 128;
        pv = *reinterpret_cast<const float*>(prec);
        SB();
    };
    prep(0);
#define VCOMP(k) { \
        if ((k) == 0) y = yn; \
        const f32x2 pz = f32x2{dpp<0x150 + 4 * (k)>(y), zn}; \
        _Pragma("unroll") for (int x = 0; x < 3; ++x) \
            _Pragma("unroll") for (int e = 0; e < 2; ++e) { \
                const f32x2 vf = f32x2{(float)buf[k].d[x][2 * e], (float)buf[k].d[x][2 * e + 1]}; \
                if ((k) == 0) asm volatile("v_pk_fma_f32 %0, %1, %2, %1 op_sel:[0,0,1] op_sel_hi:[0,1,1]" : "=v"(acc[x][e]) : "v"(pz), "v"(vf)); \
                else asm volatile("v_pk_fma_f32 %0, %1, %2, %0 op_sel_hi:[0,1,1]" : "+v"(acc[x][e]) : "v"(pz), "v"(vf)); \
            } \
        asm volatile("" ::: "memory"); \
        SB(); }
#define VSTEP(k, hn) { \
        __builtin_amdgcn_s_waitcnt(0xf79); SB();        \
        VCOMP(k) pace(); issue(buf[k], k, vr, nso); SB(); steer(true, k); SB(); \
        if ((k) == 1) { \
            vth = TH; asm volatile("" : "+s"(vth)); \
            if (__builtin_expect(ev1 >> (hn) & 1, 0)) { \
                if ((hn) == 1) { softmax1(); softmax2(); }        \
                if ((hn) == G1) ev_all(); \
                if ((hn) == hput) { ev_put(); quiet(); } \
                if ((hn) == hget) { ev_get(); quiet(); } \
                if ((hn) == hget + 3) ev_w2(); \
                if ((hn) == H - 1) tmin = 0; \
            } \
            SB(); \
        } \
        if ((k) == 2) { \
            if (__builtin_expect(ev2 >> (hn) & 1, 0)) { \
                if ((hn) == G1) ev_ml(); \
                if ((hn) == G1 + 1) ev_area(); \
                if ((hn) == hget + 2) ev_w1(); \
            } \
            SB(); \
            prep(hn); \
        } }
    auto record = [&](unsigned ra, unsigned ca) {
        f32x4 o;
#pragma unroll
        for (int e = 0; e < 4; ++e) {
            const float t0 = sw32(acc[0][e >> 1][e & 1], acc[2][e >> 1][e & 1]);
            const float t1 = sw32(acc[1][e >> 1][e & 1], acc[1][e >> 1][e & 1]);
            o[e] = sw16(t0, t1);
        }
        asm volatile("s_mov_b64 exec, %5\n\tds_write_b128 %1, %2\n\ts_mov_b64 exec, 1\n\tds_add_rtn_u32 %0, %3, %4\n\ts_mov_b64 exec, -1"
                     : "+v"(old) : "v"(ra), "v"(o), "v"(ca), "v"(1u), "s"(0xffffffffffffull) : "memory");
        SB();
    };
#pragma unroll 1
    for (int h = 0; h < H - 1; ++h) {
        nso += hstride;
        VSTEP(0, h + 1)
        asm volatile("s_waitcnt lgkmcnt(0)" : "+v"(old) :: "memory");
        if (__builtin_amdgcn_readfirstlane(old) == 3) *reinterpret_cast<u32x2*>(smem + LDS_REC + (h - 1) * (DH * 4) + lane * 8) = __builtin_expect(h > G1, 1) ? sum4((h - 1) * (DH * 4)) : merge(h - 1);
        SB();
        VSTEP(1, h + 1) VSTEP(2, h + 1) VSTEP(3, h + 1)
        record(lds0 + LDS_REC + h * (DH * 4) + wave * RECW + lane * 16, lds0 + LDS_CNT + h * 4);
    }
    asm volatile("s_branch .Lkv_t%c0\n\t.p2align 12,,3327\n.Lkv_t%c0:" :: "i"(ID));
    VCOMP(0)
    asm volatile("s_waitcnt lgkmcnt(0)\n\ts_barrier" ::: "memory");
    u32x4 ev[4];
#pragma unroll
    for (int p = 0; p < 4; ++p) ev[p] = *reinterpret_cast<const u32x4*>(smem + LDS_REC + (8 * wave + 2 * p + (lane >> 5)) * (DH * 4) + (lane & 31) * 16);
    const f32x4 a0 = *reinterpret_cast<const f32x4*>(smem + LDS_QRAW + 1024), a1 = *reinterpret_cast<const f32x4*>(smem + LDS_QRAW + 1040);
    SB();
    VCOMP(1)
    unsigned eo = (lane >> 5) * (nb * LS) + (lane & 31) * 16, e8 = lane * 8, ra = lds0 + LDS_REC + (H - 1) * (DH * 4) + wave * RECW + lane * 16, ca = lds0 + LDS_CNT + (H - 1) * 4;
    unsigned es[5];
#pragma unroll
    for (int p = 0; p < 4; ++p) es[p] = area + ((8 * wave + 2 * p) * nb + sb) * LS;
    es[4] = area + ((H - 1) * nb + sb) * LS;
    unsigned tx = sched - nb16 - step - stag + EX * 16 + (sb >> 3) * GS * 4;
    if (wave == 3) { const u32x2 t = sum4((H - 2) * (DH * 4)); ev[3][0] = t[0]; ev[3][1] = t[1]; }
    {
        const float M = fmaxf(fmaxf(a0[0], a0[2]), fmaxf(a1[0], a1[2]));
        const float gf = __builtin_amdgcn_exp2f(mw - M) * __builtin_amdgcn_rcpf((__builtin_amdgcn_exp2f(a0[0] - M) * a0[1] + __builtin_amdgcn_exp2f(a0[2] - M) * a0[3]) + (__builtin_amdgcn_exp2f(a1[0] - M) * a1[1] + __builtin_amdgcn_exp2f(a1[2] - M) * a1[3]));
#pragma unroll
        for (int i = 0; i < 4; ++i) fs[i] *= gf;
    }
    rl += (H - 1) * (DH * 4); rh += (H - 1) * (DH * 4);
    asm volatile("" : "+v"(fs[0]), "+v"(fs[1]), "+v"(fs[2]), "+v"(fs[3]), "+v"(ev[0]), "+v"(ev[1]), "+v"(ev[2]), "+v"(ev[3]), "+v"(eo), "+v"(e8), "+v"(ra), "+v"(ca), "+v"(rl), "+v"(rh),
                      "+s"(es[0]), "+s"(es[1]), "+s"(es[2]), "+s"(es[3]), "+s"(es[4]), "+s"(tx));
    unsigned tgt = sched - nb16 - stag + (hc == H - 1 ? HL : HOLD) * 16;
    unsigned rs[8];
#pragma unroll
    for (int i = 0; i < 8; ++i) rs[i] = area + (hc * nb + 32 * i + 8 * wave) * LS + (cn >> 5) * 128;
    const unsigned g = lane & 7, e = (lane >> 3) & 1;
    unsigned xo = (lane >> 3) * LS + g * 16;
    unsigned oo = r == 3 ? 0x80000000u : (hc * DH + (cn >> 5) * 16 + 2 * g + (r == 2 ? 128 + e : r + 64 * e)) * 2;
    unsigned po = lds0 + LDS_QRAW + lane * 4, wo = po + wave * 256;
    unsigned tb = (lane & 1) ? t32 : (unsigned)tag, ta = tb << 16;
    const rsrc_t orr = mkrsrc(a.out, H * DH * 2);
    asm volatile("" : "+s"(rs[0]), "+s"(rs[1]), "+s"(rs[2]), "+s"(rs[3]), "+s"(rs[4]), "+s"(rs[5]), "+s"(rs[6]), "+s"(rs[7]), "+v"(po), "+v"(wo), "+v"(xo), "+v"(oo), "+v"(ta), "+v"(tb), "+s"(tgt) : "s"(orr) : "memory");
    SB();
    VCOMP(2)
    while ((int)((now32() << 4) - tx) < 0) {}
    asm volatile("s_waitcnt vmcnt(0)\n\tbuffer_store_dwordx4 %3, %7, %9, %10 offen sc1\n\tbuffer_store_dwordx4 %4, %7, %9, %11 offen sc1\n\tbuffer_store_dwordx4 %5, %7, %9, %12 offen sc1\n\t"
                 "s_cmp_eq_u32 %14, 3\n\ts_cbranch_scc1 1f\n\tbuffer_store_dwordx4 %6, %7, %9, %13 offen sc1\n\ts_branch 2f\n1:\tbuffer_store_dwordx2 %15, %8, %9, %13 offen sc1\n2:"
                 : "+v"(buf[3].d[0]), "+v"(buf[3].d[1]), "+v"(buf[3].d[2]) : "v"(ev[0]), "v"(ev[1]), "v"(ev[2]), "v"(ev[3]), "v"(eo), "v"(e8), "s"(pr), "s"(es[0]), "s"(es[1]), "s"(es[2]), "s"(es[3]), "s"(wave),
                   "v"(u32x2{ev[3][0], ev[3][1]}) : "memory", "scc");
    if (DBG == 4) tbar = now32();
    SB();
    VCOMP(3)
    asm volatile("s_nop 1" :: "v"(ev[0]), "v"(ev[1]), "v"(ev[2]), "v"(ev[3]));
    SB();
    record(ra, ca);
    asm volatile("s_waitcnt lgkmcnt(0)" : "+v"(old) :: "memory");
    if (__builtin_amdgcn_readfirstlane(old) == 3) __builtin_amdgcn_raw_buffer_store_b64(sum4(0), pr, e8, es[4], 16);
    if (b == 0 && wave == 1) __builtin_amdgcn_raw_buffer_store_b64(u32x2{(unsigned)tag, t32}, pr, lane == 0 ? pep : 0x80000000u, 0, 16);
    unsigned tend = DS ? now32() : 0;
    unsigned tfl = 0, tpl = 0;

    if (cn < NC) {
        i32x4 x[8];
        while ((int)((now32() << 4) - tgt) < 0) {}
        if (DS) tfl = now32();
#define XL(i) x[i] = bld128<16>(pr, xo, rs[i]); SB();
#define XS(i) XL(i) __builtin_amdgcn_s_sleep((SL >> (2 * (i))) & 3); SB();
        XS(0) XS(1) XS(2) XS(3) XS(4) XS(5) XS(6) XL(7)
        if (DBG == 4) th4 = now32();
        SB();
        f32x2 o[3];
#pragma unroll 1
        for (int n = 0; n < (1 << 20); ++n) {
            unsigned bad = 0;
#pragma unroll
            for (int i = 0; i < 8; ++i) {
#pragma unroll
                for (int e = 0; e < 3; ++e) {
                    const f32x2 t = e < 2 ? f32x2{bflo(x[i][2 * e]), bfhi(x[i][2 * e])} : f32x2{bflo(x[i][1]), bflo(x[i][3])};
                    if (i == 0) asm volatile("v_pk_mul_f32 %0, %1, %2 op_sel_hi:[0,1]" : "=v"(o[e]) : "v"(fs[0]), "v"(t));
                    else if (i & 1) asm volatile("v_pk_fma_f32 %0, %1, %2, %0 op_sel:[1,0,0]" : "+v"(o[e]) : "v"(fs[i >> 1]), "v"(t));
                    else asm volatile("v_pk_fma_f32 %0, %1, %2, %0 op_sel_hi:[0,1,1]" : "+v"(o[e]) : "v"(fs[i >> 1]), "v"(t));
                }
                bad = __builtin_amdgcn_bitop3_b32(bad, x[i][1], ta, 0xf6);
                bad = __builtin_amdgcn_bitop3_b32(bad, x[i][3], tb, 0xf6);
                asm volatile("" : "+v"(bad));
                SB();
            }
            if (__builtin_expect(__all(bad < 0x10000u), 1)) break;
            asm volatile("" ::: "memory");
            XL(0) XL(1) XL(2) XL(3) XL(4) XL(5) XL(6) XL(7)
        }
        asm volatile("" : "+v"(o[0]), "+v"(o[1]), "+v"(o[2]));
        if (DS) tpl = now32();
        const float w0 = sw16(sw32(o[0][0], o[2][0]), sw32(o[1][0], o[1][0])), w1 = sw16(sw32(o[0][1], o[2][1]), sw32(o[1][1], o[1][1]));
        asm volatile("ds_write_b32 %0, %1\n\ts_waitcnt lgkmcnt(0)\n\ts_barrier" :: "v"(wo), "v"((c < 8 ? w0 : w1) + dpp<0x128>(c < 8 ? w1 : w0)) : "memory");
        if (wave == 0) {
            f32x2 p0, p1;
            asm volatile("ds_read2st64_b32 %0, %2 offset1:1\n\tds_read2st64_b32 %1, %2 offset0:2 offset1:3\n\ts_waitcnt lgkmcnt(0)" : "=&v"(p0), "=&v"(p1) : "v"(po) : "memory");
            p0 += p1;
            __builtin_amdgcn_raw_buffer_store_b16((unsigned short)pk_bf16(p0[0] + p0[1], 0.f), orr, oo, 0, 0);
        }
        asm volatile(".Lkv_b%c0:" :: "i"(ID));
    }
    if (DBG) {
        const unsigned tc1 = now32();
        if (!DS) tend = tbar = th4 = tfl = tpl = tc1;
        if (DBG == 5) tbar = tdma;
        const unsigned tl16 = sched - nb16 - stag - step;
        u64* stp = reinterpret_cast<u64*>(a.part + STAMP_OFF) + (size_t)((DBG == 4 || DBG == 24 || DBG == 6 ? cn : b) * 4 + wave) * 8;

        if (lane == 0) { stp[0] = tstart; stp[1] = tbar; stp[2] = DBG >= 4 ? tend - (((tend << 4) - tl16) >> 4) : th1; stp[3] = DBG == 4 && cn >= NC ? tend : th4; stp[4] = tend; stp[5] = cn < NC ? tfl : tend; stp[6] = cn < NC ? tpl : tend; stp[7] = tc1; }
        if (DBG == 2) *reinterpret_cast<u32x4*>(a.part + STAMP_OFF + 65536 + (b * 4 + wave) * 1024 + lane * 16) = *reinterpret_cast<const u32x4*>(smem + LDS_TR + 16 + wave * 1024 + lane * 16);
    }
}

template <int TP, int ADV, int PX, int TMIN, int CAP, int HD, int RA, int SKL, int FC, int F, int EX, int GS, int HOLD, int HL, int SL, int ID, int DBG>
static void launch71(void* const* d_in, const int* in_sizes, void* d_out, void* d_ws, hipStream_t stream) {
    typedef const unsigned short* P;
    Args a;
    a.qk = (const int*)d_in[1]; a.qv = (const int*)d_in[4];
    a.k_cos = (P)d_in[9]; a.k_sin = (P)d_in[10];
    a.q = (P)d_in[0]; a.q_cos = (P)d_in[7]; a.q_sin = (P)d_in[8];
    a.sc[0] = (P)d_in[2]; a.sc[1] = (P)d_in[3]; a.sc[2] = (P)d_in[5]; a.sc[3] = (P)d_in[6];
    a.part = (unsigned char*)d_ws;
    a.out = (unsigned short*)d_out;
    kv71_kernel<TP, ADV, PX, TMIN, CAP, HD, RA, SKL, FC, F, EX, GS, HOLD, HL, SL, ID, DBG><<<Skv / RW, 256, 0, stream>>>(a);
}

extern "C" void kernel_launch(void* const* d_in, const int* in_sizes, int n_in, void* d_out, int out_size, void* d_ws, size_t ws_size, hipStream_t stream) {
    launch71<80*16-2,220,40,78*16+8,28,70,240,24,32+256*50+65536*3+(15<<20),1,61,3,80,95,0x2aaa,0,0>(d_in, in_sizes, d_out, d_ws, stream);
}
```

```cpp
#include <hip/hip_runtime.h>
#include <hip/hip_bf16.h>
#include <cstdint>
#define KALIGN 256
#define STAMP_OFF (200 << 20)
using bf16 = __hip_bfloat16;
using i32x4 = __attribute__((ext_vector_type(4))) int;
using u32x4 = __attribute__((ext_vector_type(4))) unsigned;
using u32x2 = __attribute__((ext_vector_type(2))) unsigned;
using f32x2 = __attribute__((ext_vector_type(2))) float;
using f32x4 = __attribute__((ext_vector_type(4))) float;

constexpr int DH = 192;
constexpr int HALF = 96;
constexpr int RW = 64;
constexpr int MAXH = 32;

__device__ __forceinline__ float bflo(unsigned u) { return __uint_as_float(u << 16); }
__device__ __forceinline__ float bfhi(unsigned u) { return __uint_as_float(u & 0xffff0000u); }
__device__ __forceinline__ float rbf(float x) { return __bfloat162float(__float2bfloat16(x)); }

template <int CTRL> __device__ __forceinline__ float dpp(float x) {
    return __builtin_bit_cast(float, __builtin_amdgcn_mov_dpp(__builtin_bit_cast(int, x), CTRL, 0xf, 0xf, true));
}
__device__ __forceinline__ float row16_allsum(float x) {
    x += dpp<0x128>(x);
    x += dpp<0x124>(x);
    x += dpp<0x122>(x);
    x += dpp<0x121>(x);
    return x;
}
__device__ __forceinline__ float row16_allmax(float x) {
    x = fmaxf(x, dpp<0x128>(x));
    x = fmaxf(x, dpp<0x124>(x));
    x = fmaxf(x, dpp<0x122>(x));
    x = fmaxf(x, dpp<0x121>(x));
    return x;
}
__device__ __forceinline__ float xrow_max(float x) {
    auto s = __builtin_amdgcn_permlane16_swap(__float_as_uint(x), __float_as_uint(x), false, false);
    x = fmaxf(__uint_as_float(s[0]), __uint_as_float(s[1]));
    auto t = __builtin_amdgcn_permlane32_swap(__float_as_uint(x), __float_as_uint(x), false, false);
    return fmaxf(__uint_as_float(t[0]), __uint_as_float(t[1]));
}
__device__ __forceinline__ float xrow_sum(float x) {
    auto s = __builtin_amdgcn_permlane16_swap(__float_as_uint(x), __float_as_uint(x), false, false);
    x = __uint_as_float(s[0]) + __uint_as_float(s[1]);
    auto t = __builtin_amdgcn_permlane32_swap(__float_as_uint(x), __float_as_uint(x), false, false);
    return __uint_as_float(t[0]) + __uint_as_float(t[1]);
}
__device__ __forceinline__ float sw32(float a, float b) {
    auto t = __builtin_amdgcn_permlane32_swap(__float_as_uint(a), __float_as_uint(b), false, false);
    return __uint_as_float(t[0]) + __uint_as_float(t[1]);
}
__device__ __forceinline__ float sw16(float a, float b) {
    auto t = __builtin_amdgcn_permlane16_swap(__float_as_uint(a), __float_as_uint(b), false, false);
    return __uint_as_float(t[0]) + __uint_as_float(t[1]);
}

#define SB() __builtin_amdgcn_sched_barrier(0)
typedef __amdgpu_buffer_rsrc_t rsrc_t;
__device__ __forceinline__ rsrc_t mkrsrc(const void* p, unsigned bytes) {
    return __builtin_amdgcn_make_buffer_rsrc(const_cast<void*>(p), 0, bytes, 0x00020000);
}
template <int AUX> __device__ __forceinline__ i32x4 bld128(rsrc_t r, unsigned voff, unsigned soff) {
    return __builtin_bit_cast(i32x4, __builtin_amdgcn_raw_buffer_load_b128(r, voff, soff, AUX));
}
template <int AUX> __device__ __forceinline__ unsigned bld16(rsrc_t r, unsigned voff, unsigned soff) {
    return __builtin_amdgcn_raw_buffer_load_b16(r, voff, soff, AUX);
}

struct HG { i32x4 d[3]; };
typedef unsigned long long u64;
typedef __attribute__((address_space(3))) unsigned char lds_u8;

__device__ __forceinline__ unsigned pk_bf16(float lo, float hi) {
    unsigned r;
    asm("v_cvt_pk_bf16_f32 %0, %1, %2" : "=v"(r) : "v"(lo), "v"(hi));
    return r;
}
__device__ __forceinline__ unsigned now32() { return (unsigned)__builtin_amdgcn_s_memrealtime(); }

constexpr int LDS_REC = 0;
constexpr int RECW = MAXH * DH * 4;
constexpr int LDS_CS = 0;
constexpr int LDS_QT = 12288;
constexpr int LDS_PF = 14336;
constexpr int LDS_QRAW = MAXH * 4 * DH * 4;
constexpr int LDS_Q = LDS_QRAW + MAXH * DH * 2;
constexpr int LDS_P = LDS_Q + 4 * DH * 4;
constexpr int LDS_SC = LDS_P + 4 * MAXH * 128;
constexpr int LDS_CNT = LDS_SC + 4 * MAXH * RW * 2;
constexpr int LDS_SK = LDS_CNT + 256;
constexpr int LDS_TR = LDS_SK + 1024;
constexpr int LDS_BYTES = LDS_TR + 4096 + 64;

struct Args {
    const int* qk; const int* qv;
    const unsigned short* k_cos; const unsigned short* k_sin;
    const unsigned short* q; const unsigned short* q_cos; const unsigned short* q_sin;
    unsigned char* part;
    unsigned short* out;
    const unsigned short* sc[4];
};
constexpr unsigned Skv = 16384;
constexpr float SCALE_LOG2 = 0.10411754627697264f;

template <int TP, int ADV, int PX, int TMIN, int CAP, int HD, int RA, int SKL, int FC, int F, int EX, int GS, int HOLD, int HL, int SL, int ID, int DBG>
__global__ __launch_bounds__(256) __attribute__((aligned(KALIGN))) void kv75_kernel(Args a)
{
    __shared__ __attribute__((aligned(16))) unsigned char smem[LDS_BYTES];
    const unsigned tstart = DBG ? now32() : 0;
    constexpr bool DS = (DBG && DBG < 5) || DBG == 24;
    constexpr int H = MAXH, PFN = 16, SJ = (F >> 9) & 7;
    const int b = blockIdx.x;
    const int tid = threadIdx.x;
    const int lane = tid & 63;
    const int wave = __builtin_amdgcn_readfirstlane(tid >> 6);
    typedef const unsigned short* const __attribute__((address_space(4))) * karg_t;
    const unsigned short* sp = ((karg_t)__builtin_amdgcn_kernarg_segment_ptr())[offsetof(Args, sc) / 8 + wave];
    if (b < PFN) {
        u64 pc; unsigned len, tl;
        asm volatile("s_getpc_b64 %0\n.Lkv_a%c3:\n\ts_mov_b32 %1, .Lkv_b%c3-.Lkv_a%c3-16\n\ts_mov_b32 %2, .Lkv_t%c3-.Lkv_a%c3" : "=s"(pc), "=s"(len), "=s"(tl) : "i"(ID));
        const unsigned o = tid * 16;
        asm volatile("s_mov_b32 m0, %5\n\ts_nop 0\n\tglobal_load_lds_dwordx4 %0, %4\n\tglobal_load_lds_dwordx4 %1, %4\n\tglobal_load_lds_dwordx4 %2, %4\n\tglobal_load_lds_dwordx4 %3, %4"
                     :: "v"(o), "v"(o + 4096), "v"(min(o + 8192, tl)), "v"(min(o + tl, len)), "s"(pc), "s"((unsigned)(size_t)(lds_u8*)smem + LDS_PF) : "memory");
    }
    SB();
    const int r = lane >> 4;
    const int c = lane & 15;
    constexpr unsigned hstride = Skv * (DH * 4);
    constexpr unsigned nb = Skv / RW;
    const unsigned lds0 = (unsigned)(size_t)(lds_u8*)smem;
    const unsigned sb = b;
    const unsigned rowb = (b & 7) * 2048 + (b & 24) * 64 + (b >> 5) * 16;
    const unsigned row0 = rowb + 4 * wave;

    constexpr unsigned LS = 512, psk = 0, pep = 128, AREA = 6u << 20, NAREA = 32, pfl = H * nb * LS;
    const rsrc_t pr = mkrsrc(a.part, (NAREA + 1) * AREA);
    unsigned vg = ((lane >> 3) * Skv + rowb + (lane & 6) * 64 + (lane & 1) * 8) * 2, dg = lds0 + LDS_SC + wave * 4096;
    u64 t64;
    {
        unsigned vs = ((lane >> 3) * Skv + (rowb & ~63u) + (b & 96) * 4 + (lane & 7) * 8) * 2, v1 = vs + 16 * Skv, v2 = vs + 32 * Skv, v3 = vs + 48 * Skv;
        if (!SJ && wave < 2) vs = vg;
        asm volatile("" : "+v"(vs), "+v"(v1), "+v"(v2), "+v"(v3), "+s"(dg));
        SB();
        asm volatile("s_memrealtime %0\n\ts_mov_b32 m0, %6\n\ts_nop 0\n\tglobal_load_lds_dwordx4 %1, %5\n\t"
                     "s_add_u32 m0, m0, 1024\n\ts_nop 0\n\tglobal_load_lds_dwordx4 %2, %5\n\t"
                     "s_add_u32 m0, m0, 1024\n\ts_nop 0\n\tglobal_load_lds_dwordx4 %3, %5\n\t"
                     "s_add_u32 m0, m0, 1024\n\ts_nop 0\n\tglobal_load_lds_dwordx4 %4, %5\n\ts_waitcnt lgkmcnt(0)"
                     : "=&s"(t64) : "v"(vs), "v"(v1), "v"(v2), "v"(v3), "s"(sp), "s"(dg),
                       "s"(a.qk), "s"(a.qv), "s"(a.k_cos), "s"(a.k_sin), "s"(a.q), "s"(a.q_cos), "s"(a.q_sin), "s"(a.part), "s"(a.out) : "memory", "scc");
    }
    auto gather = [&]() {
        if (F & 2) asm volatile("s_mov_b32 m0, %2\n\ts_nop 0\n\tglobal_load_lds_dwordx4 %0, %1 nt" :: "v"(vg), "s"(sp), "s"(dg) : "memory");
        else asm volatile("s_mov_b32 m0, %2\n\ts_nop 0\n\tglobal_load_lds_dwordx4 %0, %1" :: "v"(vg), "s"(sp), "s"(dg) : "memory");
        vg += 16 * Skv; dg += 1024;
    };
    SB();
    const unsigned tdma = (unsigned)t64;
#define DMA3(V, A, B, P) "s_mov_b32 m0, " B "\n\ts_nop 0\n\t" \
                     "global_load_lds_dwordx4 " V ", " A P "\n\t" \
                     "global_load_lds_dwordx4 " V ", " A " offset:1024" P "\n\t" \
                     "global_load_lds_dwordx4 " V ", " A " offset:2048" P "\n\t"
    asm volatile(DMA3("%0", "%1", "%2", "") "s_mov_b32 m0, %4\n\ts_nop 0\n\tglobal_load_lds_dwordx4 %3, off"
                 :: "v"(lane * 16), "s"(a.q + wave * 1536), "s"(lds0 + LDS_QRAW + wave * 3072),
                    "v"(lane < 48 ? (const unsigned char*)((lane < 24 ? a.q_cos : a.q_sin - DH) + 8 * lane) : a.part + pep), "s"(lds0 + LDS_QT + wave * RECW) : "memory");
    if ((b >> 4) == 1 && wave == 3)
        asm volatile("s_mov_b32 m0, %2\n\ts_mov_b64 exec, 15\n\tglobal_load_lds_dword %0, %1\n\ts_mov_b64 exec, -1" :: "v"((((tdma >> 13) & (NAREA - 1)) + 1) * AREA + lane * 0x160000 + b * 128), "s"(a.part), "s"(lds0 + LDS_PF) : "memory");
    SB();
    constexpr int TH = 4, DL = 32, BIG = 70;
    const int stag = (int)(((sb & ~7) + wave * 2 + (b & 1)) * (TP / 2)) / 256 - TP / 4;
    const unsigned nb16 = -(tdma << 4);
    int sched = ADV * 16 + stag - 4 * (TP / 2);
    int vt = 0;
    int calm = 600 * 16, step = TP / 2, tmin = TMIN / 2, vth = TH, pvm = 0;
    int tru = -4, slip = 0;
    int tgt0, tnow, vm, tf;
    auto pace = [&]() {
        if (DBG == 2 && (F & 16)) tf = (now32() << 4) + nb16;
        if (F & 1)
        asm volatile("s_max_i32 %0, %3, %4\n1:\ts_memrealtime vcc\n\ts_waitcnt lgkmcnt(0)\n\ts_lshl4_add_u32 %1, vcc_lo, %5\n\ts_cmp_lt_i32 %1, %0\n\ts_cbranch_scc1 1b\n\t"
                     "s_getreg_b32 %2, hwreg(HW_REG_IB_STS, 0, 4)" : "=&s"(tgt0), "=&s"(tnow), "=s"(vm) : "s"(sched), "s"(vt), "s"(nb16) : "scc", "vcc", "memory");
        else
        asm volatile("s_max_i32 %0, %3, %4\n\ts_branch 2f\n1:\ts_sleep 1\n2:\ts_memrealtime vcc\n\ts_waitcnt lgkmcnt(0)\n\ts_lshl4_add_u32 %1, vcc_lo, %5\n\ts_cmp_lt_i32 %1, %0\n\ts_cbranch_scc1 1b\n\t"
                     "s_getreg_b32 %2, hwreg(HW_REG_IB_STS, 0, 4)" : "=&s"(tgt0), "=&s"(tnow), "=s"(vm) : "s"(sched), "s"(vt), "s"(nb16) : "scc", "vcc", "memory");
    };
    auto steer = [&](bool ctl, int k = 0) {
        sched += step;
        if (!ctl) vt = max(tnow - 128, tgt0) + tmin;
        else {
            vt = tgt0 + tmin;
            if (__builtin_expect(tnow - tgt0 > 128, 0)) {
                asm volatile("");
                vt = tnow - 128 + tmin;
                if (tnow - tgt0 > HD * 16 && tnow > calm) { sched += RA * 16; calm = tnow + 900 * 16; if (DBG == 2) slip += RA * 16; }
            }
            if (__builtin_expect(vm > vth, 0)) {
                asm volatile("" : "+s"(pvm));
                if (vt - sched < CAP * 16 || (pvm > TH && vt - sched > BIG * 16)) vt += (vm - TH) * DL;
            }
            if (FC && ((FC >> 20) >> k & 1)) {
                if (__builtin_expect(vt - sched > ((FC >> 8) & 255) * 16, 0)) {
                    asm volatile("" : "+s"(pvm));
                    if (max(vm, pvm) <= ((FC >> 16) & 15)) vt -= FC & 255;
                }
            }
            pvm = vm;
            if ((F >> 5) & 7) asm volatile(".rept %0\n\ts_nop 7\n\t.endr" :: "i"(3 * ((F >> 5) & 7)));
        }
        if constexpr (DBG == 2) { *reinterpret_cast<unsigned*>(smem + LDS_TR + 16 + wave * 1024 + (tru & 255) * 4) = ((unsigned)(tnow - (sched - slip - step)) >> 4) | ((F & 8) ? vm << 16 : (F & 16) ? ((tnow - tf) >> 4) << 16 : 0); ++tru; }
    };
    {
        unsigned dst = lds0 + LDS_CS + wave * RECW;
        const unsigned v0 = lane * 16 + row0 * (DH * 2), v2 = v0 + 124 * DH * 2, v1 = lane < 32 ? v0 : v2;
#pragma unroll 1
        for (int j = 0; j < 4; ++j) {
            pace();
            asm volatile("s_mov_b32 m0, %4\n\ts_nop 0\n\tglobal_load_lds_dwordx4 %0, %3 nt\n\tglobal_load_lds_dwordx4 %1, %3 offset:1024 nt\n\tglobal_load_lds_dwordx4 %2, %3 offset:2048 nt"
                         :: "v"(v0), "v"(v1), "v"(v2), "s"((j < 2 ? a.k_cos : a.k_sin) + (j & 1) * 256 * DH), "s"(dst) : "memory");
            if (SJ && j == SJ - 1 && wave < 2) gather();
            steer(false);
            sched += PX;
            dst += 3072;
        }
    }
    if (!SJ && wave < 2) { vg += 16 * Skv; dg += 1024; }
    if (tid < MAXH + 4) *reinterpret_cast<unsigned*>(smem + LDS_CNT + tid * 4) = 0;
    const rsrc_t kr = mkrsrc(a.qk, H * hstride);
    const rsrc_t vr = mkrsrc(a.qv, H * hstride);
    unsigned kvo[4][3];
#pragma unroll
    for (int i = 0; i < 12; ++i) { kvo[i / 3][i % 3] = (row0 + r + i / 3 * 128) * (DH * 4) + c * 16 + i % 3 * 256; asm volatile("" : "+v"(kvo[i / 3][i % 3])); }
    auto issue = [&](HG& g, int k, rsrc_t rs, unsigned so) {
#pragma unroll
        for (int x = 0; x < 3; ++x) g.d[x] = bld128<2>(rs, kvo[k][x], so);
    };
    HG buf[4];
#define PRE(g) SB(); pace(); issue(buf[g], g, kr, 0); SB(); steer(false); SB();
    asm volatile("s_waitcnt vmcnt(%0) lgkmcnt(0)\n\ts_barrier" :: "i"(12 - 3 * SJ) : "memory");
    unsigned tbar = DS ? now32() : 0;
    PRE(0)
    const unsigned qoff = (lane < 48 ? lane : lane - 48) * 4;
    const unsigned* qt = reinterpret_cast<const unsigned*>(smem + LDS_QT + wave * RECW + qoff);
    unsigned qc0 = qt[0], qc1 = qt[HALF / 2], qs0 = qt[DH / 2], qs1 = qt[(DH + HALF) / 2];
    u64 tag;
    {
        const u32x2 e = *reinterpret_cast<const u32x2*>(smem + LDS_QT + wave * RECW + 768);
        tag = (((u64)__builtin_amdgcn_readfirstlane(e[1]) << 32) | __builtin_amdgcn_readfirstlane(e[0])) * 6364136223846793005ull + 1442695040888963407ull;
        asm volatile("" : "+s"(tag));
    }
    const unsigned t32 = tag >> 32;
    unsigned qraw = lds0 + LDS_QRAW + qoff;
    unsigned char* qrot = smem + LDS_Q + wave * (DH * 4) + 2 * qoff;
    unsigned q_lo, q_hi;
    auto getq = [&]() {
        asm volatile("" : "+v"(qraw));
        q_lo = *(const __attribute__((address_space(3))) unsigned*)(size_t)qraw, q_hi = *(const __attribute__((address_space(3))) unsigned*)(size_t)(qraw + HALF * 2);
        qraw += DH * 2;
    };
    auto rotq = [&]() {
        const f32x2 lo = f32x2{bflo(q_lo), bfhi(q_lo)}, hi = f32x2{bflo(q_hi), bfhi(q_hi)};
        const f32x2 y1 = lo * f32x2{bflo(qc0), bfhi(qc0)}, y2 = -hi * f32x2{bflo(qs0), bfhi(qs0)};
        const f32x2 y3 = hi * f32x2{bflo(qc1), bfhi(qc1)}, y4 = lo * f32x2{bflo(qs1), bfhi(qs1)};
        const unsigned x1 = pk_bf16(y1[0], y1[1]), x2 = pk_bf16(y2[0], y2[1]), x3 = pk_bf16(y3[0], y3[1]), x4 = pk_bf16(y4[0], y4[1]);
        const f32x2 z1 = f32x2{bflo(x1), bfhi(x1)} + f32x2{bflo(x2), bfhi(x2)}, z2 = f32x2{bflo(x3), bfhi(x3)} + f32x2{bflo(x4), bfhi(x4)};
        const unsigned l2 = pk_bf16(z1[0], z1[1]), h2 = pk_bf16(z2[0], z2[1]);
        *reinterpret_cast<f32x2*>(qrot) = f32x2{bflo(l2), bfhi(l2)};
        *reinterpret_cast<f32x2*>(qrot + HALF * 4) = f32x2{bflo(h2), bfhi(h2)};
        asm volatile("" ::: "memory");
    };
    f32x4 qa[3], qb[3];
    const unsigned char* qh = smem + LDS_Q + wave * (DH * 4) + 16 * c;
    const unsigned char* qh1 = qh + (c < 8 ? 64 + HALF : 64 - HALF) * 4;
    auto loadq = [&]() {
        asm volatile("" ::: "memory");
        qa[0] = *reinterpret_cast<const f32x4*>(qh); SB();
        qb[0] = *reinterpret_cast<const f32x4*>(qh + HALF * 4); SB();
        qa[1] = *reinterpret_cast<const f32x4*>(qh + 256); SB();
        qb[1] = *reinterpret_cast<const f32x4*>(qh1); SB();
        qa[2] = *reinterpret_cast<const f32x4*>(qh + 512); SB();
        qb[2] = *reinterpret_cast<const f32x4*>(qh + (128 - HALF) * 4);
        asm volatile("" ::: "memory");
    };
    getq(); rotq();
    if ((b >> 3) == 4 && wave == 3) __builtin_amdgcn_raw_buffer_store_b128(u32x4{tdma, t32, tdma, (unsigned)tag}, pr, lane == 0 ? psk + (b & 7) * 16 : 0x80000000u, 0, 16);
    PRE(1)
    f32x2 cc[4][3][2], ss[4][3][2];
    const unsigned char* cb = smem + LDS_CS + wave * RECW + r * (DH * 2) + c * 8;
    asm volatile("s_waitcnt vmcnt(12)" ::: "memory");
#pragma unroll
    for (int k = 0; k < 4; ++k)
#pragma unroll
        for (int x = 0; x < 3; ++x) {
            const u32x2 t = *reinterpret_cast<const u32x2*>(cb + k * 1536 + 128 * x);
            cc[k][x][0] = f32x2{bflo(t[0]), bfhi(t[0])}; cc[k][x][1] = f32x2{bflo(t[1]), bfhi(t[1])};
            asm volatile("" : "+v"(cc[k][x][0]), "+v"(cc[k][x][1]));
        }
    PRE(2)
    loadq();
    const unsigned char* scp = smem + LDS_SC + (c & 1) * 4096 + (4 * (c & 12) + 4 * wave + r) * 2;
    unsigned sc = *reinterpret_cast<const unsigned short*>(scp);
    auto loadsin = [&](int k) {
        const unsigned char* sb1 = cb + (c < 8 ? (64 + HALF) * 2 : (64 - HALF) * 2);
        const unsigned sg = c < 8 ? 0u : 0x80008000u;
        u32x2 t[3];
        t[0] = *reinterpret_cast<const u32x2*>(cb + 6144 + k * 1536 + HALF * 2);
        t[1] = *reinterpret_cast<const u32x2*>(sb1 + 6144 + k * 1536) ^ u32x2{sg, sg};
        t[2] = *reinterpret_cast<const u32x2*>(cb + 6144 + k * 1536 + (128 - HALF) * 2) ^ u32x2{0x80008000u, 0x80008000u};
#pragma unroll
        for (int x = 0; x < 3; ++x) {
            ss[k][x][0] = f32x2{bflo(t[x][0]), bfhi(t[x][0])}; ss[k][x][1] = f32x2{bflo(t[x][1]), bfhi(t[x][1])};
            asm volatile("" : "+v"(ss[k][x][0]), "+v"(ss[k][x][1]));
        }
    };
    asm volatile("s_waitcnt vmcnt(12)" ::: "memory");
    loadsin(0); loadsin(1);
    PRE(3)
    asm volatile("s_waitcnt vmcnt(12)" ::: "memory");
    loadsin(2); loadsin(3);
    unsigned th1 = 0, th4 = 0;
    const unsigned char* prec = smem + LDS_P + wave * (H * 128) + r * 32 + (c & 12);
    unsigned pw_ = lds0 + LDS_P + wave * (H * 128) + r * 32 + (c & 12) + (c & 3) * 128;
    float sm, sp_, sacc = 0.f;
    auto softmax1 = [&]() {
        float u;
        asm volatile("v_max_f32_dpp %0, %2, %2 row_ror:4 row_mask:0xf bank_mask:0xf\n\ts_nop 1\n\tv_max_f32_dpp %0, %0, %0 row_ror:8 row_mask:0xf bank_mask:0xf\n\tv_mov_b32 %1, %0\n\ts_nop 1\n\t"
                     "v_permlane16_swap_b32 %0, %1\n\tv_max_f32 %0, %0, %1\n\tv_mov_b32 %1, %0\n\ts_nop 1\n\tv_permlane32_swap_b32 %0, %1\n\tv_max_f32 %0, %0, %1" : "=&v"(sm), "=&v"(u) : "v"(sacc));
        sp_ = __builtin_amdgcn_exp2f(sacc - sm);
        asm volatile("" : "+v"(sp_));
    };
    auto softmax2 = [&]() {
        float l = sp_ + dpp<0x124>(sp_);
        l = xrow_sum(l + dpp<0x128>(l));
        asm volatile("ds_write2_b32 %0, %1, %2 offset1:4" :: "v"(pw_), "v"(sp_), "v"((c & 4) ? l : sm) : "memory");
        pw_ += 512;
    };

    auto skew = [&](int h) {
        if (wave == 1 && h == 1 + ((b >> 3) & 3))
            asm volatile("s_mov_b32 m0, %2\n\ts_nop 0\n\tglobal_load_lds_dwordx4 %0, %1 sc1" :: "v"(psk + (lane & 7) * 16), "s"(a.part), "s"(lds0 + LDS_SK) : "memory");
        if (h == 6) {
            const u32x2 e = *reinterpret_cast<const u32x2*>(smem + LDS_SK + (lane & 7) * 16);
            const int d = (int)(tdma - e[0]);
            if (__all(e[1] == t32 && d > -400 && d < 400)) {
                int sum = d + __builtin_amdgcn_mov_dpp(d, 0xb1, 0xf, 0xf, true), mn = min(d, __builtin_amdgcn_mov_dpp(d, 0xb1, 0xf, 0xf, true));
                sum += __builtin_amdgcn_mov_dpp(sum, 0x4e, 0xf, 0xf, true); mn = min(mn, __builtin_amdgcn_mov_dpp(mn, 0x4e, 0xf, 0xf, true));
                sum += __builtin_amdgcn_mov_dpp(sum, 0x124, 0xf, 0xf, true); mn = min(mn, __builtin_amdgcn_mov_dpp(mn, 0x124, 0xf, 0xf, true));
                sum = __builtin_amdgcn_readfirstlane(min(sum, 8 * (mn + SKL)));
                const int adj = (sum * 73 + 4096) >> 13;
                step -= adj; tmin -= adj;
                sched -= 2 * sum - 224 * adj;
            }
        }
    };

    rsrc_t nx = kr;
    unsigned nso = 0, sm4 = 0x11111110;
    unsigned gm = (wave < 2 ? 0x4081u << 1 : 0x204081u << 4) << (((b >> 3) + 2 * wave) & 3), evk = gm * 5 | 0x5e, gn = 0;
    asm volatile("" : "+s"(sm4), "+s"(gm), "+s"(evk));
#pragma unroll 1
    for (int h = 0; h < H; ++h) {
        float scf = bflo(sc);
        asm volatile("" : "+v"(scf) :: "memory");
        SB();
        nso += hstride;
        scp += 128;
        if (__builtin_expect((h & 7) == 7, 0)) {
            const unsigned fa = lds0 + LDS_CNT + 128 + ((h + 1) >> 5) * 8;
            u32x2 n;
            do asm volatile("ds_read_b64 %0, %1\n\ts_waitcnt lgkmcnt(0)" : "=v"(n) : "v"(fa) : "memory");
            while (__any(min(n[0], n[1]) * 8 <= h));
            if (h == H - 1) { nx = vr; nso = 0; scp += 4096; }
        }
        sc = *reinterpret_cast<const unsigned short*>(scp);
        SB();
        float st[2];
#define KSTEP(k) { \
            __builtin_amdgcn_s_waitcnt(0xf79); SB();        \
            const float kz = dpp<0x151 + 4 * (k)>(scf); \
            const f32x2 kz2 = f32x2{kz, kz}; \
            f32x2 a2 = f32x2{0.f, 0.f}; \
            _Pragma("unroll") for (int x = 0; x < 3; ++x) { \
                _Pragma("unroll") for (int e = 0; e < 2; ++e) { \
                    const f32x2 w = __builtin_elementwise_fma(f32x2{qb[x][2 * e], qb[x][2 * e + 1]}, ss[k][x][e], f32x2{qa[x][2 * e], qa[x][2 * e + 1]} * cc[k][x][e]); \
                    const f32x2 kf = f32x2{(float)buf[k].d[x][2 * e], (float)buf[k].d[x][2 * e + 1]} - kz2; \
                    a2 = __builtin_elementwise_fma(kf, w, a2); \
                } \
            } \
            float sa = a2[0] + a2[1]; \
            asm volatile("" : "+v"(sa) :: "memory"); \
            SB(); pace(); issue(buf[k], k, nx, nso); SB(); steer(true, k); SB(); \
              \
            if ((k) < 2) asm volatile("v_add_f32_dpp %0, %1, %1 row_ror:8 row_mask:0xf bank_mask:0xf" : "=v"(st[(k) & 1]) : "v"(sa)); \
            else asm volatile("v_add_f32_dpp %0, %1, %1 row_ror:8 row_mask:0xf bank_mask:0xc" : "+v"(st[(k) & 1]) : "v"(sa)); \
            if ((k) == 0) { getq(); SB(); if (sm4 >> h & 1) softmax1(); SB(); } \
            if ((k) == 1) { rotq(); SB(); } \
            if ((k) == 2) { \
                asm volatile("" : "+s"(sm4)); \
                if (sm4 >> h & 1) softmax2(); \
                SB(); \
                if (__builtin_expect(evk >> h & 1, 0)) { \
                    if (gm >> h & 1) gather(); \
                    if (gm * 4 >> h & 1) *reinterpret_cast<unsigned*>(smem + LDS_CNT + 128 + wave * 4) = ++gn;        \
                    skew(h); \
                } \
                SB(); \
            } \
            if ((k) == 3) { loadq(); SB(); } }
        KSTEP(0) KSTEP(1) KSTEP(2) KSTEP(3)
        asm volatile("v_add_f32_dpp %0, %0, %0 row_shl:4 row_mask:0xf bank_mask:0x5\n\tv_add_f32_dpp %0, %3, %3 row_shr:4 row_mask:0xf bank_mask:0xa\n\t"
                     "v_mul_f32 %1, %4, %1\n\tv_mov_b32_dpp %2, %2 quad_perm:[1,2,3,3] row_mask:0xf bank_mask:0xf\n\t"
                     "v_add_f32_dpp %0, %0, %0 quad_perm:[1,0,3,2] row_mask:0xf bank_mask:0xf\n\ts_nop 1\n\tv_add_f32_dpp %0, %0, %0 quad_perm:[2,3,0,1] row_mask:0xf bank_mask:0xf\n\t"
                     "v_mul_f32_dpp %0, %1, %0 quad_perm:[0,0,0,0] row_mask:0xf bank_mask:0xf\n\tv_cndmask_b32 %2, %2, %0, %5"
                     : "+v"(st[0]), "+v"(scf), "+v"(sacc) : "v"(st[1]), "s"(SCALE_LOG2), "s"(0x8888888888888888ull));
        if (DS) { if (h == 7) th1 = now32(); }
    }
    if (DS) th4 = now32();

    const unsigned tg = (unsigned)(tag >> (16 * (lane & 3))) << 16;
    unsigned rl = lds0 + LDS_REC + lane * 4, rh = rl + 2 * RECW;
    auto pack = [&](f32x2 t, float u) { return u32x2{pk_bf16(t[0], t[1]), pk_bf16(u, 0.f) | tg}; };
    auto ldrec = [&](unsigned o, f32x2 (&p)[6]) {
        asm volatile("ds_read2st64_b32 %0, %6 offset1:1\n\tds_read2st64_b32 %1, %6 offset0:96 offset1:97\n\tds_read2st64_b32 %2, %7 offset1:1\n\tds_read2st64_b32 %3, %7 offset0:96 offset1:97\n\t"
                     "ds_read2st64_b32 %4, %6 offset0:2 offset1:98\n\tds_read2st64_b32 %5, %7 offset0:2 offset1:98"
                     : "=&v"(p[0]), "=&v"(p[1]), "=&v"(p[2]), "=&v"(p[3]), "=&v"(p[4]), "=&v"(p[5]) : "v"(rl + o), "v"(rh + o) : "memory");
    };
    auto sum4 = [&](unsigned o) {
        f32x2 p[6];
        ldrec(o, p);
        asm volatile("s_waitcnt lgkmcnt(0)" : "+v"(p[0]), "+v"(p[1]), "+v"(p[2]), "+v"(p[3]), "+v"(p[4]), "+v"(p[5]) :: "memory");
        const f32x2 u = p[4] + p[5];
        return pack((p[0] + p[1]) + (p[2] + p[3]), u[0] + u[1]);
    };
    auto merge = [&](int hd) {
        f32x2 p[6], ma, mb;
        f32x4 xa, xb;
        asm volatile("ds_read2st64_b64 %0, %2 offset1:8\n\tds_read2st64_b64 %1, %2 offset0:16 offset1:24" : "=&v"(xa), "=&v"(xb) : "v"(lds0 + LDS_P + hd * 128 + 16) : "memory");
        ldrec(hd * (DH * 4), p);
        asm volatile("s_waitcnt lgkmcnt(6)" : "+v"(xa), "+v"(xb) :: "memory");
        const float M = __builtin_amdgcn_fmed3f(__builtin_amdgcn_fmed3f(xa[0], xa[2], __builtin_inff()), __builtin_amdgcn_fmed3f(xb[0], xb[2], __builtin_inff()), __builtin_inff());
        ma = f32x2{__builtin_amdgcn_exp2f(xa[0] - M), __builtin_amdgcn_exp2f(xa[2] - M)};
        mb = f32x2{__builtin_amdgcn_exp2f(xb[0] - M), __builtin_amdgcn_exp2f(xb[2] - M)};
        const float rL = __builtin_amdgcn_rcpf((ma[0] * xa[1] + ma[1] * xa[3]) + (mb[0] * xb[1] + mb[1] * xb[3]));
        ma *= rL; mb *= rL;
        asm volatile("s_waitcnt lgkmcnt(0)" : "+v"(ma), "+v"(mb), "+v"(p[0]), "+v"(p[1]), "+v"(p[2]), "+v"(p[3]), "+v"(p[4]), "+v"(p[5]) :: "memory");
        const f32x2 u = __builtin_elementwise_fma(mb, p[5], ma * p[4]);
        return pack(ma[0] * p[0] + ma[1] * p[1] + mb[0] * p[2] + mb[1] * p[3], u[0] + u[1]);
    };
    constexpr unsigned LSTR = 128;
    constexpr int NC = 4 * H;
    const int cn = sb;
    const unsigned hc = 4 * (cn & 7) + ((cn >> 3) & 3);
    unsigned old = 0, area = 0, fo = 0;
    unsigned flv = 0;
    float gw = 1.f, mw = 0.f;
    f32x2 fs[4] = {};
    constexpr int LDS_ML = LDS_QRAW + 2048;
#define BAD(y) ((unsigned)(y)[1] != t32 || (unsigned)(y)[3] != (unsigned)tag)
    auto ev_all = [&]() {
        unsigned n;
        asm volatile("1:\tds_read_b32 %0, %1\n\ts_waitcnt lgkmcnt(0)\n\tv_cmp_ne_u32 vcc, 4, %0\n\ts_cbranch_vccnz 1b" : "=&v"(n) : "v"(lds0 + LDS_CNT) : "memory", "vcc");
    };
    auto quiet = [&]() { vth = 15; calm = max(calm, vt + 500 * 16); };
    auto ev_ml = [&]() {
        f32x2 ml[4];
#pragma unroll
        for (int w = 0; w < 4; ++w) ml[w] = *reinterpret_cast<const f32x2*>(smem + LDS_P + ((wave + w) & 3) * (H * 128) + (lane & 31) * 128 + 16);
        const float M = fmaxf(fmaxf(ml[0][0], ml[1][0]), fmaxf(ml[2][0], ml[3][0]));
        const float e0 = __builtin_amdgcn_exp2f(ml[0][0] - M);
        const float L = (e0 * ml[0][1] + __builtin_amdgcn_exp2f(ml[1][0] - M) * ml[1][1]) + (__builtin_amdgcn_exp2f(ml[2][0] - M) * ml[2][1] + __builtin_amdgcn_exp2f(ml[3][0] - M) * ml[3][1]);
        flv = ((__float_as_uint(M + __builtin_amdgcn_logf(L)) + 0x80) & 0xffffff00u) | ((unsigned)(tag >> (8 * (lane & 7))) & 255);
        gw = e0 * __builtin_amdgcn_rcpf(L);
        asm volatile("" : "+v"(flv), "+v"(gw));
    };
    auto ev_area = [&]() {
        u32x4 e = *reinterpret_cast<const u32x4*>(smem + LDS_SK);
        if (__builtin_expect(__any(BAD(e)), 0)) {
#pragma unroll 1
            for (int n = 0; n < (1 << 20) && BAD(e); ++n)
                asm volatile("global_load_dwordx4 %0, %1, %2 sc1\n\ts_waitcnt vmcnt(0)" : "=v"(e) : "v"(psk), "s"(a.part) : "memory");
        }
        area = (((__builtin_amdgcn_readfirstlane(e[0]) >> 13) & (NAREA - 1)) + 1) * AREA + 4096;
        asm volatile("" : "+s"(area));
    };
    auto ev_put = [&]() {
        asm volatile("s_mov_b32 exec_hi, 0\n\tbuffer_store_dword %0, %1, %2, %3 offen sc1\n\ts_mov_b32 exec_hi, -1" :: "v"(flv), "v"(rl - lds0 - LDS_REC), "s"(pr), "s"(area + pfl + sb * LSTR) : "memory");
    };
    auto ev_get = [&]() {
        unsigned l = lane;
        asm volatile("" : "+v"(l));
        fo = area + pfl + 8 * wave * LSTR + (cn & 7) * 16 + (((l & 56) << 2) + (l & 7)) * LSTR;
        asm volatile("s_mov_b32 m0, %2\n\ts_nop 0\n\tglobal_load_lds_dwordx4 %0, %1 sc1" :: "v"(fo), "s"(a.part), "s"(lds0 + LDS_ML + wave * 1024) : "memory");
    };
    auto ev_w1 = [&]() {
        u32x4 y = *reinterpret_cast<const u32x4*>(smem + LDS_ML + wave * 1024 + lane * 16);
        const unsigned tq = (cn & 1) ? t32 : (unsigned)tag;
#define BADW(y) (__builtin_amdgcn_perm(__builtin_amdgcn_perm((y)[3], (y)[2], 0x0400), __builtin_amdgcn_perm((y)[1], (y)[0], 0x0400), 0x05040100) != tq)
        if (__builtin_expect(__any(BADW(y)), 0)) {
#pragma unroll 1
            for (int n = 0; n < (1 << 20) && BADW(y); ++n)
                asm volatile("global_load_dwordx4 %0, %1, %2 sc1\n\ts_waitcnt vmcnt(0)" : "=v"(y) : "v"(fo), "s"(a.part) : "memory");
        }
        const unsigned se = (cn & 8) ? 0x0706050cu : 0x0302010cu;
        const float m = __uint_as_float((cn & 16) ? __builtin_amdgcn_perm(y[3], y[2], se) : __builtin_amdgcn_perm(y[1], y[0], se));
        float u;
        asm volatile("s_nop 1\n\tv_max_f32_dpp %0, %2, %2 row_ror:8 row_mask:0xf bank_mask:0xf\n\ts_nop 1\n\tv_max_f32_dpp %0, %0, %0 row_ror:4 row_mask:0xf bank_mask:0xf\n\ts_nop 1\n\t"
                     "v_max_f32_dpp %0, %0, %0 row_ror:2 row_mask:0xf bank_mask:0xf\n\ts_nop 1\n\tv_max_f32_dpp %0, %0, %0 row_ror:1 row_mask:0xf bank_mask:0xf\n\tv_mov_b32 %1, %0\n\ts_nop 1\n\t"
                     "v_permlane16_swap_b32 %0, %1\n\ts_nop 0\n\tv_max_f32 %0, %0, %1\n\tv_mov_b32 %1, %0\n\ts_nop 1\n\tv_permlane32_swap_b32 %0, %1\n\ts_nop 0\n\tv_max_f32 %0, %0, %1" : "=&v"(mw), "=&v"(u) : "v"(m));
        fs[0][0] = __builtin_amdgcn_exp2f(m - mw);
        asm volatile("" : "+v"(fs[0]));
    };
    auto ev_w2 = [&]() {
        const f32x2 f = fs[0];
        unsigned bp = (lane >> 3) * 4;
        asm volatile("ds_bpermute_b32 %0, %8, %9\n\tds_bpermute_b32 %1, %8, %9 offset:32\n\tds_bpermute_b32 %2, %8, %9 offset:64\n\tds_bpermute_b32 %3, %8, %9 offset:96\n\t"
                     "ds_bpermute_b32 %4, %8, %9 offset:128\n\tds_bpermute_b32 %5, %8, %9 offset:160\n\tds_bpermute_b32 %6, %8, %9 offset:192\n\tds_bpermute_b32 %7, %8, %9 offset:224"
                     : "=&v"(fs[0][0]), "=&v"(fs[0][1]), "=&v"(fs[1][0]), "=&v"(fs[1][1]), "=&v"(fs[2][0]), "=&v"(fs[2][1]), "=&v"(fs[3][0]), "=&v"(fs[3][1]) : "v"(bp), "v"(f[0]) : "memory");
        *reinterpret_cast<f32x2*>(smem + LDS_QRAW + 1024 + wave * 8) = f32x2{mw, xrow_sum(row16_allsum(f[0]))};
        asm volatile("s_waitcnt lgkmcnt(0)" : "+v"(fs[0]), "+v"(fs[1]), "+v"(fs[2]), "+v"(fs[3]) :: "memory");
    };
    constexpr int G1 = 5;
    const int hput = wave == ((b >> 5) & 3) ? G1 + 2 + ((b >> 3) & 3) : -9, hget = cn < NC ? G1 + 7 + ((cn >> 3) & 7) : -9;
    unsigned ev1 = 2 | 1 << G1 | 15 << (G1 + 2) | 9u << (hget & 31) | 1u << (H - 1), ev2 = 3 << G1 | 4u << (hget & 31);
    asm volatile("" : "+s"(ev1), "+s"(ev2));
    float pv = *reinterpret_cast<const float*>(prec);
    f32x2 acc[3][2];
    float y, yn, zn;
    auto prep = [&](int hn) {
        const float s = bflo(sc);
        yn = s * (pv * __uint_as_float(__builtin_amdgcn_readlane(__float_as_uint(gw), hn)));
        float z = yn * dpp<0x55>(s);
        z += dpp<0x124>(z);
        z += dpp<0x128>(z);
        zn = -dpp<0x150>(z);
        asm volatile("" : "+v"(yn), "+v"(zn) :: "memory");
        SB();
        scp += 128;
        sc = *reinterpret_cast<const unsigned short*>(scp);
        prec += 128;
        pv = *reinterpret_cast<const float*>(prec);
        SB();
    };
    prep(0);
#define VCOMP(k) { \
        if ((k) == 0) y = yn; \
        const f32x2 pz = f32x2{dpp<0x150 + 4 * (k)>(y), zn}; \
        _Pragma("unroll") for (int x = 0; x < 3; ++x) \
            _Pragma("unroll") for (int e = 0; e < 2; ++e) { \
                const f32x2 vf = f32x2{(float)buf[k].d[x][2 * e], (float)buf[k].d[x][2 * e + 1]}; \
                if ((k) == 0) asm volatile("v_pk_fma_f32 %0, %1, %2, %1 op_sel:[0,0,1] op_sel_hi:[0,1,1]" : "=v"(acc[x][e]) : "v"(pz), "v"(vf)); \
                else asm volatile("v_pk_fma_f32 %0, %1, %2, %0 op_sel_hi:[0,1,1]" : "+v"(acc[x][e]) : "v"(pz), "v"(vf)); \
            } \
        asm volatile("" ::: "memory"); \
        SB(); }
#define VSTEP(k, hn) { \
        __builtin_amdgcn_s_waitcnt(0xf79); SB();        \
        VCOMP(k) pace(); issue(buf[k], k, vr, nso); SB(); steer(true, k); SB(); \
        if ((k) == 1) { \
            vth = TH; asm volatile("" : "+s"(vth)); \
            if (__builtin_expect(ev1 >> (hn) & 1, 0)) { \
                if ((hn) == 1) { softmax1(); softmax2(); }        \
                if ((hn) == G1) ev_all(); \
                if ((hn) == hput) { ev_put(); quiet(); } \
                if ((hn) == hget) { ev_get(); quiet(); } \
                if ((hn) == hget + 3) ev_w2(); \
                if ((hn) == H - 1) tmin = 0; \
            } \
            SB(); \
        } \
        if ((k) == 2) { \
            if (__builtin_expect(ev2 >> (hn) & 1, 0)) { \
                if ((hn) == G1) ev_ml(); \
                if ((hn) == G1 + 1) ev_area(); \
                if ((hn) == hget + 2) ev_w1(); \
            } \
            SB(); \
            prep(hn); \
        } }
    auto record = [&](unsigned ra, unsigned ca) {
        f32x4 o;
#pragma unroll
        for (int e = 0; e < 4; ++e) {
            const float t0 = sw32(acc[0][e >> 1][e & 1], acc[2][e >> 1][e & 1]);
            const float t1 = sw32(acc[1][e >> 1][e & 1], acc[1][e >> 1][e & 1]);
            o[e] = sw16(t0, t1);
        }
        asm volatile("s_mov_b64 exec, %5\n\tds_write_b128 %1, %2\n\ts_mov_b64 exec, 1\n\tds_add_rtn_u32 %0, %3, %4\n\ts_mov_b64 exec, -1"
                     : "+v"(old) : "v"(ra), "v"(o), "v"(ca), "v"(1u), "s"(0xffffffffffffull) : "memory");
        SB();
    };
#pragma unroll 1
    for (int h = 0; h < H - 1; ++h) {
        nso += hstride;
        VSTEP(0, h + 1)
        asm volatile("s_waitcnt lgkmcnt(0)" : "+v"(old) :: "memory");
        if (__builtin_amdgcn_readfirstlane(old) == 3) *reinterpret_cast<u32x2*>(smem + LDS_REC + (h - 1) * (DH * 4) + lane * 8) = __builtin_expect(h > G1, 1) ? sum4((h - 1) * (DH * 4)) : merge(h - 1);
        SB();
        VSTEP(1, h + 1) VSTEP(2, h + 1) VSTEP(3, h + 1)
        record(lds0 + LDS_REC + h * (DH * 4) + wave * RECW + lane * 16, lds0 + LDS_CNT + h * 4);
    }
    asm volatile("s_branch .Lkv_t%c0\n\t.p2align 12,,3327\n.Lkv_t%c0:" :: "i"(ID));
    VCOMP(0)
    asm volatile("s_waitcnt lgkmcnt(0)\n\ts_barrier" ::: "memory");
    u32x4 ev[4];
#pragma unroll
    for (int p = 0; p < 4; ++p) ev[p] = *reinterpret_cast<const u32x4*>(smem + LDS_REC + (8 * wave + 2 * p + (lane >> 5)) * (DH * 4) + (lane & 31) * 16);
    const f32x4 a0 = *reinterpret_cast<const f32x4*>(smem + LDS_QRAW + 1024), a1 = *reinterpret_cast<const f32x4*>(smem + LDS_QRAW + 1040);
    SB();
    VCOMP(1)
    unsigned eo = (lane >> 5) * (nb * LS) + (lane & 31) * 16, e8 = lane * 8, ra = lds0 + LDS_REC + (H - 1) * (DH * 4) + wave * RECW + lane * 16, ca = lds0 + LDS_CNT + (H - 1) * 4;
    unsigned es[5];
#pragma unroll
    for (int p = 0; p < 4; ++p) es[p] = area + ((8 * wave + 2 * p) * nb + sb) * LS;
    es[4] = area + ((H - 1) * nb + sb) * LS;
    unsigned tx = sched - nb16 - step - stag + EX * 16 + (sb >> 3) * GS * 4;
    if (wave == 3) { const u32x2 t = sum4((H - 2) * (DH * 4)); ev[3][0] = t[0]; ev[3][1] = t[1]; }
    {
        const float M = fmaxf(fmaxf(a0[0], a0[2]), fmaxf(a1[0], a1[2]));
        const float gf = __builtin_amdgcn_exp2f(mw - M) * __builtin_amdgcn_rcpf((__builtin_amdgcn_exp2f(a0[0] - M) * a0[1] + __builtin_amdgcn_exp2f(a0[2] - M) * a0[3]) + (__builtin_amdgcn_exp2f(a1[0] - M) * a1[1] + __builtin_amdgcn_exp2f(a1[2] - M) * a1[3]));
#pragma unroll
        for (int i = 0; i < 4; ++i) fs[i] *= gf;
    }
    rl += (H - 1) * (DH * 4); rh += (H - 1) * (DH * 4);
    asm volatile("" : "+v"(fs[0]), "+v"(fs[1]), "+v"(fs[2]), "+v"(fs[3]), "+v"(ev[0]), "+v"(ev[1]), "+v"(ev[2]), "+v"(ev[3]), "+v"(eo), "+v"(e8), "+v"(ra), "+v"(ca), "+v"(rl), "+v"(rh),
                      "+s"(es[0]), "+s"(es[1]), "+s"(es[2]), "+s"(es[3]), "+s"(es[4]), "+s"(tx));
    unsigned tgt = sched - nb16 - stag + (hc == H - 1 ? HL : HOLD) * 16;
    unsigned rs[8];
#pragma unroll
    for (int i = 0; i < 8; ++i) rs[i] = area + (hc * nb + 32 * i + 8 * wave) * LS + (cn >> 5) * 128;
    const unsigned g = lane & 7, e = (lane >> 3) & 1;
    unsigned xo = (lane >> 3) * LS + g * 16;
    unsigned oo = r == 3 ? 0x80000000u : (hc * DH + (cn >> 5) * 16 + 2 * g + (r == 2 ? 128 + e : r + 64 * e)) * 2;
    unsigned po = lds0 + LDS_QRAW + lane * 4, wo = po + wave * 256;
    unsigned tb = (lane & 1) ? t32 : (unsigned)tag, ta = tb << 16;
    const rsrc_t orr = mkrsrc(a.out, H * DH * 2);
    asm volatile("" : "+s"(rs[0]), "+s"(rs[1]), "+s"(rs[2]), "+s"(rs[3]), "+s"(rs[4]), "+s"(rs[5]), "+s"(rs[6]), "+s"(rs[7]), "+v"(po), "+v"(wo), "+v"(xo), "+v"(oo), "+v"(ta), "+v"(tb), "+s"(tgt) : "s"(orr) : "memory");
    SB();
    VCOMP(2)
    while ((int)((now32() << 4) - tx) < 0) {}
    asm volatile("s_waitcnt vmcnt(0)\n\tbuffer_store_dwordx4 %3, %7, %9, %10 offen sc1\n\tbuffer_store_dwordx4 %4, %7, %9, %11 offen sc1\n\tbuffer_store_dwordx4 %5, %7, %9, %12 offen sc1\n\t"
                 "s_cmp_eq_u32 %14, 3\n\ts_cbranch_scc1 1f\n\tbuffer_store_dwordx4 %6, %7, %9, %13 offen sc1\n\ts_branch 2f\n1:\tbuffer_store_dwordx2 %15, %8, %9, %13 offen sc1\n2:"
                 : "+v"(buf[3].d[0]), "+v"(buf[3].d[1]), "+v"(buf[3].d[2]) : "v"(ev[0]), "v"(ev[1]), "v"(ev[2]), "v"(ev[3]), "v"(eo), "v"(e8), "s"(pr), "s"(es[0]), "s"(es[1]), "s"(es[2]), "s"(es[3]), "s"(wave),
                   "v"(u32x2{ev[3][0], ev[3][1]}) : "memory", "scc");
    if (DBG == 4) tbar = now32();
    SB();
    VCOMP(3)
    asm volatile("s_nop 1" :: "v"(ev[0]), "v"(ev[1]), "v"(ev[2]), "v"(ev[3]));
    SB();
    record(ra, ca);
    asm volatile("s_waitcnt lgkmcnt(0)" : "+v"(old) :: "memory");
    if (__builtin_amdgcn_readfirstlane(old) == 3) __builtin_amdgcn_raw_buffer_store_b64(sum4(0), pr, e8, es[4], 16);
    if (b == 0 && wave == 1) __builtin_amdgcn_raw_buffer_store_b64(u32x2{(unsigned)tag, t32}, pr, lane == 0 ? pep : 0x80000000u, 0, 16);
    unsigned tend = DS ? now32() : 0;
    unsigned tfl = 0, tpl = 0;

    if (cn < NC) {
        i32x4 x[8];
        while ((int)((now32() << 4) - tgt) < 0) {}
        if (DS) tfl = now32();
#define XL(i) x[i] = bld128<16>(pr, xo, rs[i]); SB();
#define XS(i) XL(i) __builtin_amdgcn_s_sleep((SL >> (2 * (i))) & 3); SB();
        XS(0) XS(1) XS(2) XS(3) XS(4) XS(5) XS(6) XL(7)
        if (DBG == 4) th4 = now32();
        SB();
        f32x2 o[3];
#pragma unroll 1
        for (int n = 0; n < (1 << 20); ++n) {
            unsigned bad = 0;
#pragma unroll
            for (int i = 0; i < 8; ++i) {
#pragma unroll
                for (int e = 0; e < 3; ++e) {
                    const f32x2 t = e < 2 ? f32x2{bflo(x[i][2 * e]), bfhi(x[i][2 * e])} : f32x2{bflo(x[i][1]), bflo(x[i][3])};
                    if (i == 0) asm volatile("v_pk_mul_f32 %0, %1, %2 op_sel_hi:[0,1]" : "=v"(o[e]) : "v"(fs[0]), "v"(t));
                    else if (i & 1) asm volatile("v_pk_fma_f32 %0, %1, %2, %0 op_sel:[1,0,0]" : "+v"(o[e]) : "v"(fs[i >> 1]), "v"(t));
                    else asm volatile("v_pk_fma_f32 %0, %1, %2, %0 op_sel_hi:[0,1,1]" : "+v"(o[e]) : "v"(fs[i >> 1]), "v"(t));
                }
                bad = __builtin_amdgcn_bitop3_b32(bad, x[i][1], ta, 0xf6);
                bad = __builtin_amdgcn_bitop3_b32(bad, x[i][3], tb, 0xf6);
                asm volatile("" : "+v"(bad));
                SB();
            }
            if (__builtin_expect(__all(bad < 0x10000u), 1)) break;
            asm volatile("" ::: "memory");
            XL(0) XL(1) XL(2) XL(3) XL(4) XL(5) XL(6) XL(7)
        }
        asm volatile("" : "+v"(o[0]), "+v"(o[1]), "+v"(o[2]));
        if (DS) tpl = now32();
        const float w0 = sw16(sw32(o[0][0], o[2][0]), sw32(o[1][0], o[1][0])), w1 = sw16(sw32(o[0][1], o[2][1]), sw32(o[1][1], o[1][1]));
        asm volatile("ds_write_b32 %0, %1\n\ts_waitcnt lgkmcnt(0)\n\ts_barrier" :: "v"(wo), "v"((c < 8 ? w0 : w1) + dpp<0x128>(c < 8 ? w1 : w0)) : "memory");
        if (wave == 0) {
            f32x2 p0, p1;
            asm volatile("ds_read2st64_b32 %0, %2 offset1:1\n\tds_read2st64_b32 %1, %2 offset0:2 offset1:3\n\ts_waitcnt lgkmcnt(0)" : "=&v"(p0), "=&v"(p1) : "v"(po) : "memory");
            p0 += p1;
            __builtin_amdgcn_raw_buffer_store_b16((unsigned short)pk_bf16(p0[0] + p0[1], 0.f), orr, oo, 0, 0);
        }
        asm volatile(".Lkv_b%c0:" :: "i"(ID));
    }
    if (DBG) {
        const unsigned tc1 = now32();
        if (!DS) tend = tbar = th4 = tfl = tpl = tc1;
        if (DBG == 5) tbar = tdma;
        const unsigned tl16 = sched - nb16 - stag - step;
        u64* stp = reinterpret_cast<u64*>(a.part + STAMP_OFF) + (size_t)((DBG == 4 || DBG == 24 || DBG == 6 ? cn : b) * 4 + wave) * 8;

        if (lane == 0) { stp[0] = tstart; stp[1] = tbar; stp[2] = DBG >= 4 ? tend - (((tend << 4) - tl16) >> 4) : th1; stp[3] = DBG == 4 && cn >= NC ? tend : th4; stp[4] = tend; stp[5] = cn < NC ? tfl : tend; stp[6] = cn < NC ? tpl : tend; stp[7] = tc1; }
        if (DBG == 2) *reinterpret_cast<u32x4*>(a.part + STAMP_OFF + 65536 + (b * 4 + wave) * 1024 + lane * 16) = *reinterpret_cast<const u32x4*>(smem + LDS_TR + 16 + wave * 1024 + lane * 16);
    }
}

template <int TP, int ADV, int PX, int TMIN, int CAP, int HD, int RA, int SKL, int FC, int F, int EX, int GS, int HOLD, int HL, int SL, int ID, int DBG>
static void launch75(void* const* d_in, const int* in_sizes, void* d_out, void* d_ws, hipStream_t stream) {
    typedef const unsigned short* P;
    Args a;
    a.qk = (const int*)d_in[1]; a.qv = (const int*)d_in[4];
    a.k_cos = (P)d_in[9]; a.k_sin = (P)d_in[10];
    a.q = (P)d_in[0]; a.q_cos = (P)d_in[7]; a.q_sin = (P)d_in[8];
    a.sc[0] = (P)d_in[2]; a.sc[1] = (P)d_in[3]; a.sc[2] = (P)d_in[5]; a.sc[3] = (P)d_in[6];
    a.part = (unsigned char*)d_ws;
    a.out = (unsigned short*)d_out;
    kv75_kernel<TP, ADV, PX, TMIN, CAP, HD, RA, SKL, FC, F, EX, GS, HOLD, HL, SL, ID, DBG><<<Skv / RW, 256, 0, stream>>>(a);
}

extern "C" void kernel_launch(void* const* d_in, const int* in_sizes, int n_in, void* d_out, int out_size, void* d_ws, size_t ws_size, hipStream_t stream) {
    launch75<79*16+8,220,40,78*16+2,28,70,240,24,32+256*50+65536*3+(15<<20),1,61,3,80,95,0x2aaa,0,0>(d_in, in_sizes, d_out, d_ws, stream);
}
```

```cpp
#include <hip/hip_runtime.h>
#include <hip/hip_bf16.h>
#include <cstdint>
#define KALIGN 256
#define STAMP_OFF (200 << 20)
using bf16 = __hip_bfloat16;
using i32x4 = __attribute__((ext_vector_type(4))) int;
using u32x4 = __attribute__((ext_vector_type(4))) unsigned;
using u32x2 = __attribute__((ext_vector_type(2))) unsigned;
using f32x2 = __attribute__((ext_vector_type(2))) float;
using f32x4 = __attribute__((ext_vector_type(4))) float;

constexpr int DH = 192;
constexpr int HALF = 96;
constexpr int RW = 64;
constexpr int MAXH = 32;

__device__ __forceinline__ float bflo(unsigned u) { return __uint_as_float(u << 16); }
__device__ __forceinline__ float bfhi(unsigned u) { return __uint_as_float(u & 0xffff0000u); }
__device__ __forceinline__ float rbf(float x) { return __bfloat162float(__float2bfloat16(x)); }

template <int CTRL> __device__ __forceinline__ float dpp(float x) {
    return __builtin_bit_cast(float, __builtin_amdgcn_mov_dpp(__builtin_bit_cast(int, x), CTRL, 0xf, 0xf, true));
}
__device__ __forceinline__ float row16_allsum(float x) {
    x += dpp<0x128>(x);
    x += dpp<0x124>(x);
    x += dpp<0x122>(x);
    x += dpp<0x121>(x);
    return x;
}
__device__ __forceinline__ float row16_allmax(float x) {
    x = fmaxf(x, dpp<0x128>(x));
    x = fmaxf(x, dpp<0x124>(x));
    x = fmaxf(x, dpp<0x122>(x));
    x = fmaxf(x, dpp<0x121>(x));
    return x;
}
__device__ __forceinline__ float xrow_max(float x) {
    auto s = __builtin_amdgcn_permlane16_swap(__float_as_uint(x), __float_as_uint(x), false, false);
    x = fmaxf(__uint_as_float(s[0]), __uint_as_float(s[1]));
    auto t = __builtin_amdgcn_permlane32_swap(__float_as_uint(x), __float_as_uint(x), false, false);
    return fmaxf(__uint_as_float(t[0]), __uint_as_float(t[1]));
}
__device__ __forceinline__ float xrow_sum(float x) {
    auto s = __builtin_amdgcn_permlane16_swap(__float_as_uint(x), __float_as_uint(x), false, false);
    x = __uint_as_float(s[0]) + __uint_as_float(s[1]);
    auto t = __builtin_amdgcn_permlane32_swap(__float_as_uint(x), __float_as_uint(x), false, false);
    return __uint_as_float(t[0]) + __uint_as_float(t[1]);
}
__device__ __forceinline__ float sw32(float a, float b) {
    auto t = __builtin_amdgcn_permlane32_swap(__float_as_uint(a), __float_as_uint(b), false, false);
    return __uint_as_float(t[0]) + __uint_as_float(t[1]);
}
__device__ __forceinline__ float sw16(float a, float b) {
    auto t = __builtin_amdgcn_permlane16_swap(__float_as_uint(a), __float_as_uint(b), false, false);
    return __uint_as_float(t[0]) + __uint_as_float(t[1]);
}

__device__ __forceinline__ void swap32(f32x2& a, f32x2& b) {
    auto s = __builtin_amdgcn_permlane32_swap(__float_as_uint(a[0]), __float_as_uint(b[0]), false, false);
    auto t = __builtin_amdgcn_permlane32_swap(__float_as_uint(a[1]), __float_as_uint(b[1]), false, false);
    a = f32x2{__uint_as_float(s[0]), __uint_as_float(t[0])}; b = f32x2{__uint_as_float(s[1]), __uint_as_float(t[1])};
}
__device__ __forceinline__ void swap16(f32x2& a, f32x2& b) {
    auto s = __builtin_amdgcn_permlane16_swap(__float_as_uint(a[0]), __float_as_uint(b[0]), false, false);
    auto t = __builtin_amdgcn_permlane16_swap(__float_as_uint(a[1]), __float_as_uint(b[1]), false, false);
    a = f32x2{__uint_as_float(s[0]), __uint_as_float(t[0])}; b = f32x2{__uint_as_float(s[1]), __uint_as_float(t[1])};
}
#define SB() __builtin_amdgcn_sched_barrier(0)
typedef __amdgpu_buffer_rsrc_t rsrc_t;
__device__ __forceinline__ rsrc_t mkrsrc(const void* p, unsigned bytes) {
    return __builtin_amdgcn_make_buffer_rsrc(const_cast<void*>(p), 0, bytes, 0x00020000);
}
template <int AUX> __device__ __forceinline__ i32x4 bld128(rsrc_t r, unsigned voff, unsigned soff) {
    return __builtin_bit_cast(i32x4, __builtin_amdgcn_raw_buffer_load_b128(r, voff, soff, AUX));
}
template <int AUX> __device__ __forceinline__ unsigned bld16(rsrc_t r, unsigned voff, unsigned soff) {
    return __builtin_amdgcn_raw_buffer_load_b16(r, voff, soff, AUX);
}

struct HG { i32x4 d[3]; };
typedef unsigned long long u64;
typedef __attribute__((address_space(3))) unsigned char lds_u8;

__device__ __forceinline__ unsigned pk_bf16(float lo, float hi) {
    unsigned r;
    asm("v_cvt_pk_bf16_f32 %0, %1, %2" : "=v"(r) : "v"(lo), "v"(hi));
    return r;
}
__device__ __forceinline__ unsigned now32() { return (unsigned)__builtin_amdgcn_s_memrealtime(); }

constexpr int LDS_REC = 0;
constexpr int RECW = MAXH * DH * 4;
constexpr int LDS_CS = 0;
constexpr int LDS_QT = 12288;
constexpr int LDS_PF = 14336;
constexpr int LDS_QRAW = MAXH * 4 * DH * 4;
constexpr int LDS_Q = LDS_QRAW + MAXH * DH * 2;
constexpr int LDS_P = LDS_Q + 4 * DH * 4;
constexpr int LDS_SC = LDS_P + 4 * MAXH * 128;
constexpr int LDS_CNT = LDS_SC + 4 * MAXH * RW * 2;
constexpr int LDS_SK = LDS_CNT + 256;
constexpr int LDS_TR = LDS_SK + 1024;
constexpr int LDS_BYTES = LDS_TR + 4096 + 64;

struct Args {
    const int* qk; const int* qv;
    const unsigned short* k_cos; const unsigned short* k_sin;
    const unsigned short* q; const unsigned short* q_cos; const unsigned short* q_sin;
    unsigned char* part;
    unsigned short* out;
    const unsigned short* sc[4];
};
constexpr unsigned Skv = 16384;
constexpr float SCALE_LOG2 = 0.10411754627697264f;

template <int TP, int ADV, int PX, int TMIN, int CAP, int HD, int RA, int SKL, int FC, int F, int EX, int GS, int HOLD, int HL, int SL, int ID, int DBG>
__global__ __launch_bounds__(256) __attribute__((aligned(KALIGN))) void kv87_kernel(Args a)
{
    __shared__ __attribute__((aligned(16))) unsigned char smem[LDS_BYTES];
    const unsigned tstart = DBG ? now32() : 0;
    constexpr bool DS = (DBG && DBG < 5) || DBG == 24;
    constexpr int H = MAXH, PFN = 16, SJ = (F >> 9) & 7;
    const int b = blockIdx.x;
    const int tid = threadIdx.x;
    const int lane = tid & 63;
    const int wave = __builtin_amdgcn_readfirstlane(tid >> 6);
    typedef const unsigned short* const __attribute__((address_space(4))) * karg_t;
    const unsigned short* sp = ((karg_t)__builtin_amdgcn_kernarg_segment_ptr())[offsetof(Args, sc) / 8 + wave];
    if (b < PFN) {
        u64 pc; unsigned len, tl;
        asm volatile("s_getpc_b64 %0\n.Lkv_a%c3:\n\ts_mov_b32 %1, .Lkv_b%c3-.Lkv_a%c3-16\n\ts_mov_b32 %2, .Lkv_t%c3-.Lkv_a%c3" : "=s"(pc), "=s"(len), "=s"(tl) : "i"(ID));
        const unsigned o = tid * 16;
        asm volatile("s_mov_b32 m0, %5\n\ts_nop 0\n\tglobal_load_lds_dwordx4 %0, %4\n\tglobal_load_lds_dwordx4 %1, %4\n\tglobal_load_lds_dwordx4 %2, %4\n\tglobal_load_lds_dwordx4 %3, %4"
                     :: "v"(o), "v"(o + 4096), "v"(min(o + 8192, tl)), "v"(min(o + tl, len)), "s"(pc), "s"((unsigned)(size_t)(lds_u8*)smem + LDS_PF) : "memory");
    }
    SB();
    const int r = lane >> 4;
    const int c = lane & 15;
    constexpr unsigned hstride = Skv * (DH * 4);
    constexpr unsigned nb = Skv / RW;
    const unsigned lds0 = (unsigned)(size_t)(lds_u8*)smem;
    const unsigned sb = b;
    const unsigned rowb = (b & 7) * 2048 + (b & 24) * 64 + (b >> 5) * 16;
    const unsigned row0 = rowb + 4 * wave;

    constexpr unsigned LS = 512, psk = 0, pep = 128, AREA = 6u << 20, NAREA = 32, pfl = H * nb * LS;
    const rsrc_t pr = mkrsrc(a.part, (NAREA + 1) * AREA);
    unsigned vg = ((lane >> 3) * Skv + rowb + (lane & 6) * 64 + (lane & 1) * 8) * 2, dg = lds0 + LDS_SC + wave * 4096;
    u64 t64;
    {
        unsigned vs = ((lane >> 3) * Skv + (rowb & ~63u) + (b & 96) * 4 + (lane & 7) * 8) * 2, v1 = vs + 16 * Skv, v2 = vs + 32 * Skv, v3 = vs + 48 * Skv;
        if (!SJ && wave < 2) vs = vg;
        asm volatile("" : "+v"(vs), "+v"(v1), "+v"(v2), "+v"(v3), "+s"(dg));
        SB();
        asm volatile("s_memrealtime %0\n\ts_mov_b32 m0, %6\n\ts_nop 0\n\tglobal_load_lds_dwordx4 %1, %5\n\t"
                     "s_add_u32 m0, m0, 1024\n\ts_nop 0\n\tglobal_load_lds_dwordx4 %2, %5\n\t"
                     "s_add_u32 m0, m0, 1024\n\ts_nop 0\n\tglobal_load_lds_dwordx4 %3, %5\n\t"
                     "s_add_u32 m0, m0, 1024\n\ts_nop 0\n\tglobal_load_lds_dwordx4 %4, %5\n\ts_waitcnt lgkmcnt(0)"
                     : "=&s"(t64) : "v"(vs), "v"(v1), "v"(v2), "v"(v3), "s"(sp), "s"(dg),
                       "s"(a.qk), "s"(a.qv), "s"(a.k_cos), "s"(a.k_sin), "s"(a.q), "s"(a.q_cos), "s"(a.q_sin), "s"(a.part), "s"(a.out) : "memory", "scc");
    }
    auto gather = [&]() {
        if (F & 2) asm volatile("s_mov_b32 m0, %2\n\ts_nop 0\n\tglobal_load_lds_dwordx4 %0, %1 nt" :: "v"(vg), "s"(sp), "s"(dg) : "memory");
        else asm volatile("s_mov_b32 m0, %2\n\ts_nop 0\n\tglobal_load_lds_dwordx4 %0, %1" :: "v"(vg), "s"(sp), "s"(dg) : "memory");
        vg += 16 * Skv; dg += 1024;
    };
    SB();
    const unsigned tdma = (unsigned)t64;
#define DMA3(V, A, B, P) "s_mov_b32 m0, " B "\n\ts_nop 0\n\t" \
                     "global_load_lds_dwordx4 " V ", " A P "\n\t" \
                     "global_load_lds_dwordx4 " V ", " A " offset:1024" P "\n\t" \
                     "global_load_lds_dwordx4 " V ", " A " offset:2048" P "\n\t"
    asm volatile(DMA3("%0", "%1", "%2", "") "s_mov_b32 m0, %4\n\ts_nop 0\n\tglobal_load_lds_dwordx4 %3, off"
                 :: "v"(lane * 16), "s"(a.q + wave * 1536), "s"(lds0 + LDS_QRAW + wave * 3072),
                    "v"(lane < 48 ? (const unsigned char*)((lane < 24 ? a.q_cos : a.q_sin - DH) + 8 * lane) : a.part + pep), "s"(lds0 + LDS_QT + wave * RECW) : "memory");
    if ((b >> 4) == 1 && wave == 3)
        asm volatile("s_mov_b32 m0, %2\n\ts_mov_b64 exec, 15\n\tglobal_load_lds_dword %0, %1\n\ts_mov_b64 exec, -1" :: "v"((((tdma >> 13) & (NAREA - 1)) + 1) * AREA + lane * 0x160000 + b * 128), "s"(a.part), "s"(lds0 + LDS_PF) : "memory");
    SB();
    constexpr int TH = 4, DL = 32, BIG = 70;
    const int stag = (int)(((sb & ~7) + wave * 2 + (b & 1)) * (TP / 2)) / 256 - TP / 4;
    const unsigned nb16 = -(tdma << 4);
    int sched = ADV * 16 + stag - 4 * (TP / 2);
    int vt = 0;
    int calm = 600 * 16, step = TP / 2, tmin = TMIN / 2, vth = TH, pvm = 0;
    int tru = -4, slip = 0;
    int tgt0, tnow, vm, tf;
    auto pace = [&]() {
        if (DBG == 2 && (F & 16)) tf = (now32() << 4) + nb16;
        if (F & 1)
        asm volatile("s_max_i32 %0, %3, %4\n1:\ts_memrealtime vcc\n\ts_waitcnt lgkmcnt(0)\n\ts_lshl4_add_u32 %1, vcc_lo, %5\n\ts_cmp_lt_i32 %1, %0\n\ts_cbranch_scc1 1b\n\t"
                     "s_getreg_b32 %2, hwreg(HW_REG_IB_STS, 0, 4)" : "=&s"(tgt0), "=&s"(tnow), "=s"(vm) : "s"(sched), "s"(vt), "s"(nb16) : "scc", "vcc", "memory");
        else
        asm volatile("s_max_i32 %0, %3, %4\n\ts_branch 2f\n1:\ts_sleep 1\n2:\ts_memrealtime vcc\n\ts_waitcnt lgkmcnt(0)\n\ts_lshl4_add_u32 %1, vcc_lo, %5\n\ts_cmp_lt_i32 %1, %0\n\ts_cbranch_scc1 1b\n\t"
                     "s_getreg_b32 %2, hwreg(HW_REG_IB_STS, 0, 4)" : "=&s"(tgt0), "=&s"(tnow), "=s"(vm) : "s"(sched), "s"(vt), "s"(nb16) : "scc", "vcc", "memory");
    };
    auto steer = [&](bool ctl, int k = 0) {
        sched += step;
        if (!ctl) vt = max(tnow - 128, tgt0) + tmin;
        else {
            vt = tgt0 + tmin;
            if (__builtin_expect(tnow - tgt0 > 128, 0)) {
                asm volatile("");
                vt = tnow - 128 + tmin;
                if (tnow - tgt0 > HD * 16 && tnow > calm) { sched += RA * 16; calm = tnow + 900 * 16; if (DBG == 2) slip += RA * 16; }
            }
            if (__builtin_expect(vm > vth, 0)) {
                asm volatile("" : "+s"(pvm));
                if (vt - sched < CAP * 16 || (pvm > TH && vt - sched > BIG * 16)) vt += (vm - TH) * DL;
            }
            if (FC && ((FC >> 20) >> k & 1)) {
                if (__builtin_expect(vt - sched > ((FC >> 8) & 255) * 16, 0)) {
                    asm volatile("" : "+s"(pvm));
                    if (max(vm, pvm) <= ((FC >> 16) & 15)) vt -= FC & 255;
                }
            }
            pvm = vm;
            if ((F >> 5) & 7) asm volatile(".rept %0\n\ts_nop 7\n\t.endr" :: "i"(3 * ((F >> 5) & 7)));
        }
        if constexpr (DBG == 2) { *reinterpret_cast<unsigned*>(smem + LDS_TR + 16 + wave * 1024 + (tru & 255) * 4) = ((unsigned)(tnow - (sched - slip - step)) >> 4) | ((F & 8) ? vm << 16 : (F & 16) ? ((tnow - tf) >> 4) << 16 : 0); ++tru; }
    };
    {
        unsigned dst = lds0 + LDS_CS + wave * RECW;
        const unsigned v0 = lane * 16 + row0 * (DH * 2), v2 = v0 + 124 * DH * 2, v1 = lane < 32 ? v0 : v2;
#pragma unroll 1
        for (int j = 0; j < 4; ++j) {
            pace();
            asm volatile("s_mov_b32 m0, %4\n\ts_nop 0\n\tglobal_load_lds_dwordx4 %0, %3 nt\n\tglobal_load_lds_dwordx4 %1, %3 offset:1024 nt\n\tglobal_load_lds_dwordx4 %2, %3 offset:2048 nt"
                         :: "v"(v0), "v"(v1), "v"(v2), "s"((j < 2 ? a.k_cos : a.k_sin) + (j & 1) * 256 * DH), "s"(dst) : "memory");
            if (SJ && j == SJ - 1 && wave < 2) gather();
            steer(false);
            sched += PX;
            dst += 3072;
        }
    }
    if (!SJ && wave < 2) { vg += 16 * Skv; dg += 1024; }
    if (tid < MAXH + 5) *reinterpret_cast<unsigned*>(smem + LDS_CNT + tid * 4) = 0;
    const rsrc_t kr = mkrsrc(a.qk, H * hstride);
    const rsrc_t vr = mkrsrc(a.qv, H * hstride);
    unsigned kvo[4][3];
#pragma unroll
    for (int i = 0; i < 12; ++i) { kvo[i / 3][i % 3] = (row0 + r + i / 3 * 128) * (DH * 4) + c * 16 + i % 3 * 256; asm volatile("" : "+v"(kvo[i / 3][i % 3])); }
    auto issue = [&](HG& g, int k, rsrc_t rs, unsigned so) {
#pragma unroll
        for (int x = 0; x < 3; ++x) g.d[x] = bld128<2>(rs, kvo[k][x], so);
    };
    HG buf[4];
#define PRE(g) SB(); pace(); issue(buf[g], g, kr, 0); SB(); steer(false); SB();
    asm volatile("s_waitcnt vmcnt(%0) lgkmcnt(0)\n\ts_barrier" :: "i"(12 - 3 * SJ) : "memory");
    unsigned tbar = DS ? now32() : 0;
    PRE(0)
    const unsigned qoff = (lane < 48 ? lane : lane - 48) * 4;
    const unsigned* qt = reinterpret_cast<const unsigned*>(smem + LDS_QT + wave * RECW + qoff);
    unsigned qc0 = qt[0], qc1 = qt[HALF / 2], qs0 = qt[DH / 2], qs1 = qt[(DH + HALF) / 2];
    u64 tag;
    {
        const u32x2 e = *reinterpret_cast<const u32x2*>(smem + LDS_QT + wave * RECW + 768);
        tag = (((u64)__builtin_amdgcn_readfirstlane(e[1]) << 32) | __builtin_amdgcn_readfirstlane(e[0])) * 6364136223846793005ull + 1442695040888963407ull;
        asm volatile("" : "+s"(tag));
    }
    const unsigned t32 = tag >> 32;
    unsigned qraw = lds0 + LDS_QRAW + qoff;
    unsigned char* qrot = smem + LDS_Q + wave * (DH * 4) + 2 * qoff;
    unsigned q_lo, q_hi;
    auto getq = [&]() {
        asm volatile("" : "+v"(qraw));
        q_lo = *(const __attribute__((address_space(3))) unsigned*)(size_t)qraw, q_hi = *(const __attribute__((address_space(3))) unsigned*)(size_t)(qraw + HALF * 2);
        qraw += DH * 2;
    };
    auto rotq = [&]() {
        const f32x2 lo = f32x2{bflo(q_lo), bfhi(q_lo)}, hi = f32x2{bflo(q_hi), bfhi(q_hi)};
        const f32x2 y1 = lo * f32x2{bflo(qc0), bfhi(qc0)}, y2 = -hi * f32x2{bflo(qs0), bfhi(qs0)};
        const f32x2 y3 = hi * f32x2{bflo(qc1), bfhi(qc1)}, y4 = lo * f32x2{bflo(qs1), bfhi(qs1)};
        const unsigned x1 = pk_bf16(y1[0], y1[1]), x2 = pk_bf16(y2[0], y2[1]), x3 = pk_bf16(y3[0], y3[1]), x4 = pk_bf16(y4[0], y4[1]);
        const f32x2 z1 = f32x2{bflo(x1), bfhi(x1)} + f32x2{bflo(x2), bfhi(x2)}, z2 = f32x2{bflo(x3), bfhi(x3)} + f32x2{bflo(x4), bfhi(x4)};
        const unsigned l2 = pk_bf16(z1[0], z1[1]), h2 = pk_bf16(z2[0], z2[1]);
        *reinterpret_cast<f32x2*>(qrot) = f32x2{bflo(l2), bfhi(l2)};
        *reinterpret_cast<f32x2*>(qrot + HALF * 4) = f32x2{bflo(h2), bfhi(h2)};
        asm volatile("" ::: "memory");
    };
    f32x4 qa[3], qb[3];
    const unsigned char* qh = smem + LDS_Q + wave * (DH * 4) + 16 * c;
    const unsigned char* qh1 = qh + (c < 8 ? 64 + HALF : 64 - HALF) * 4;
    auto loadq = [&]() {
        asm volatile("" ::: "memory");
        qa[0] = *reinterpret_cast<const f32x4*>(qh); SB();
        qb[0] = *reinterpret_cast<const f32x4*>(qh + HALF * 4); SB();
        qa[1] = *reinterpret_cast<const f32x4*>(qh + 256); SB();
        qb[1] = *reinterpret_cast<const f32x4*>(qh1); SB();
        qa[2] = *reinterpret_cast<const f32x4*>(qh + 512); SB();
        qb[2] = *reinterpret_cast<const f32x4*>(qh + (128 - HALF) * 4);
        asm volatile("" ::: "memory");
    };
    getq(); rotq();
    if ((b >> 3) == 4 && wave == 3) __builtin_amdgcn_raw_buffer_store_b128(u32x4{tdma, t32, tdma, (unsigned)tag}, pr, lane == 0 ? psk + (b & 7) * 16 : 0x80000000u, 0, 16);
    PRE(1)
    f32x2 cc[4][3][2], ss[4][3][2];
    const unsigned char* cb = smem + LDS_CS + wave * RECW + r * (DH * 2) + c * 8;
    asm volatile("s_waitcnt vmcnt(12)" ::: "memory");
#pragma unroll
    for (int k = 0; k < 4; ++k)
#pragma unroll
        for (int x = 0; x < 3; ++x) {
            const u32x2 t = *reinterpret_cast<const u32x2*>(cb + k * 1536 + 128 * x);
            cc[k][x][0] = f32x2{bflo(t[0]), bfhi(t[0])}; cc[k][x][1] = f32x2{bflo(t[1]), bfhi(t[1])};
            asm volatile("" : "+v"(cc[k][x][0]), "+v"(cc[k][x][1]));
        }
    PRE(2)
    loadq();
    const unsigned char* scp = smem + LDS_SC + (c & 1) * 4096 + (4 * (c & 12) + 4 * wave + r) * 2;
    unsigned sc = *reinterpret_cast<const unsigned short*>(scp);
    auto loadsin = [&](int k) {
        const unsigned char* sb1 = cb + (c < 8 ? (64 + HALF) * 2 : (64 - HALF) * 2);
        const unsigned sg = c < 8 ? 0u : 0x80008000u;
        u32x2 t[3];
        t[0] = *reinterpret_cast<const u32x2*>(cb + 6144 + k * 1536 + HALF * 2);
        t[1] = *reinterpret_cast<const u32x2*>(sb1 + 6144 + k * 1536) ^ u32x2{sg, sg};
        t[2] = *reinterpret_cast<const u32x2*>(cb + 6144 + k * 1536 + (128 - HALF) * 2) ^ u32x2{0x80008000u, 0x80008000u};
#pragma unroll
        for (int x = 0; x < 3; ++x) {
            ss[k][x][0] = f32x2{bflo(t[x][0]), bfhi(t[x][0])}; ss[k][x][1] = f32x2{bflo(t[x][1]), bfhi(t[x][1])};
            asm volatile("" : "+v"(ss[k][x][0]), "+v"(ss[k][x][1]));
        }
    };
    asm volatile("s_waitcnt vmcnt(12)" ::: "memory");
    loadsin(0); loadsin(1);
    PRE(3)
    asm volatile("s_waitcnt vmcnt(12)" ::: "memory");
    loadsin(2); loadsin(3);
    unsigned th1 = 0, th4 = 0;
    const unsigned char* prec = smem + LDS_P + wave * (H * 128) + r * 32 + (c & 12);
    unsigned pw_ = lds0 + LDS_P + wave * (H * 128) + r * 32 + (c & 12) + (c & 3) * 128;
    float sm, sp_, sacc = 0.f;
    auto softmax1 = [&]() {
        float u;
        asm volatile("v_max_f32_dpp %0, %2, %2 row_ror:4 row_mask:0xf bank_mask:0xf\n\ts_nop 1\n\tv_max_f32_dpp %0, %0, %0 row_ror:8 row_mask:0xf bank_mask:0xf\n\tv_mov_b32 %1, %0\n\ts_nop 1\n\t"
                     "v_permlane16_swap_b32 %0, %1\n\tv_max_f32 %0, %0, %1\n\tv_mov_b32 %1, %0\n\ts_nop 1\n\tv_permlane32_swap_b32 %0, %1\n\tv_max_f32 %0, %0, %1" : "=&v"(sm), "=&v"(u) : "v"(sacc));
        sp_ = __builtin_amdgcn_exp2f(sacc - sm);
        asm volatile("" : "+v"(sp_));
    };
    auto softmax2 = [&]() {
        float l = sp_ + dpp<0x124>(sp_);
        l = xrow_sum(l + dpp<0x128>(l));
        asm volatile("ds_write2_b32 %0, %1, %2 offset1:4" :: "v"(pw_), "v"(sp_), "v"((c & 4) ? l : sm) : "memory");
        pw_ += 512;
    };

    auto skew = [&](int h) {
        if (wave == 1 && h == 1 + ((b >> 3) & 3))
            asm volatile("s_mov_b32 m0, %2\n\ts_nop 0\n\tglobal_load_lds_dwordx4 %0, %1 sc1" :: "v"(psk + (lane & 7) * 16), "s"(a.part), "s"(lds0 + LDS_SK) : "memory");
        if (h == 6) {
            const u32x2 e = *reinterpret_cast<const u32x2*>(smem + LDS_SK + (lane & 7) * 16);
            const int d = (int)(tdma - e[0]);
            if (__all(e[1] == t32 && d > -400 && d < 400)) {
                int sum = d + __builtin_amdgcn_mov_dpp(d, 0xb1, 0xf, 0xf, true), mn = min(d, __builtin_amdgcn_mov_dpp(d, 0xb1, 0xf, 0xf, true));
                sum += __builtin_amdgcn_mov_dpp(sum, 0x4e, 0xf, 0xf, true); mn = min(mn, __builtin_amdgcn_mov_dpp(mn, 0x4e, 0xf, 0xf, true));
                sum += __builtin_amdgcn_mov_dpp(sum, 0x124, 0xf, 0xf, true); mn = min(mn, __builtin_amdgcn_mov_dpp(mn, 0x124, 0xf, 0xf, true));
                sum = __builtin_amdgcn_readfirstlane(min(sum, 8 * (mn + SKL)));
                const int adj = (sum * 73 + 4096) >> 13;
                step -= adj; tmin -= adj;
                sched -= 2 * sum - 224 * adj;
            }
        }
    };

    rsrc_t nx = kr;
    unsigned nso = 0, sm4 = 0x11111110;
    unsigned gm = (wave < 2 ? 0x4081u << 1 : 0x204081u << 4) << (((b >> 3) + 2 * wave) & 3), evk = gm * 5 | 0x5e, gn = 0;
    asm volatile("" : "+s"(sm4), "+s"(gm), "+s"(evk));
#pragma unroll 1
    for (int h = 0; h < H; ++h) {
        float scf = bflo(sc);
        asm volatile("" : "+v"(scf) :: "memory");
        SB();
        nso += hstride;
        scp += 128;
        if (__builtin_expect((h & 7) == 7, 0)) {
            const unsigned fa = lds0 + LDS_CNT + 128 + ((h + 1) >> 5) * 8;
            u32x2 n;
            do asm volatile("ds_read_b64 %0, %1\n\ts_waitcnt lgkmcnt(0)" : "=v"(n) : "v"(fa) : "memory");
            while (__any(min(n[0], n[1]) * 8 <= h));
            if (h == H - 1) { nx = vr; nso = 0; scp += 4096; }
        }
        sc = *reinterpret_cast<const unsigned short*>(scp);
        SB();
        float st[2];
#define KSTEP(k) { \
            __builtin_amdgcn_s_waitcnt(0xf79); SB();        \
            const float kz = dpp<0x151 + 4 * (k)>(scf); \
            const f32x2 kz2 = f32x2{kz, kz}; \
            f32x2 a2 = f32x2{0.f, 0.f}; \
            _Pragma("unroll") for (int x = 0; x < 3; ++x) { \
                _Pragma("unroll") for (int e = 0; e < 2; ++e) { \
                    const f32x2 w = __builtin_elementwise_fma(f32x2{qb[x][2 * e], qb[x][2 * e + 1]}, ss[k][x][e], f32x2{qa[x][2 * e], qa[x][2 * e + 1]} * cc[k][x][e]); \
                    const f32x2 kf = f32x2{(float)buf[k].d[x][2 * e], (float)buf[k].d[x][2 * e + 1]} - kz2; \
                    a2 = __builtin_elementwise_fma(kf, w, a2); \
                } \
            } \
            float sa = a2[0] + a2[1]; \
            asm volatile("" : "+v"(sa) :: "memory"); \
            SB(); pace(); issue(buf[k], k, nx, nso); SB(); steer(true, k); SB(); \
              \
            if ((k) < 2) asm volatile("v_add_f32_dpp %0, %1, %1 row_ror:8 row_mask:0xf bank_mask:0xf" : "=v"(st[(k) & 1]) : "v"(sa)); \
            else asm volatile("v_add_f32_dpp %0, %1, %1 row_ror:8 row_mask:0xf bank_mask:0xc" : "+v"(st[(k) & 1]) : "v"(sa)); \
            if ((k) == 0) { getq(); SB(); if (sm4 >> h & 1) softmax1(); SB(); } \
            if ((k) == 1) { rotq(); SB(); } \
            if ((k) == 2) { \
                asm volatile("" : "+s"(sm4)); \
                if (sm4 >> h & 1) softmax2(); \
                SB(); \
                if (__builtin_expect(evk >> h & 1, 0)) { \
                    if (gm >> h & 1) gather(); \
                    if (gm * 4 >> h & 1) *reinterpret_cast<unsigned*>(smem + LDS_CNT + 128 + wave * 4) = ++gn;        \
                    skew(h); \
                } \
                SB(); \
            } \
            if ((k) == 3) { loadq(); SB(); } }
        KSTEP(0) KSTEP(1) KSTEP(2) KSTEP(3)
        asm volatile("v_add_f32_dpp %0, %0, %0 row_shl:4 row_mask:0xf bank_mask:0x5\n\tv_add_f32_dpp %0, %3, %3 row_shr:4 row_mask:0xf bank_mask:0xa\n\t"
                     "v_mul_f32 %1, %4, %1\n\tv_mov_b32_dpp %2, %2 quad_perm:[1,2,3,3] row_mask:0xf bank_mask:0xf\n\t"
                     "v_add_f32_dpp %0, %0, %0 quad_perm:[1,0,3,2] row_mask:0xf bank_mask:0xf\n\ts_nop 1\n\tv_add_f32_dpp %0, %0, %0 quad_perm:[2,3,0,1] row_mask:0xf bank_mask:0xf\n\t"
                     "v_mul_f32_dpp %0, %1, %0 quad_perm:[0,0,0,0] row_mask:0xf bank_mask:0xf\n\tv_cndmask_b32 %2, %2, %0, %5"
                     : "+v"(st[0]), "+v"(scf), "+v"(sacc) : "v"(st[1]), "s"(SCALE_LOG2), "s"(0x8888888888888888ull));
        if (DS) { if (h == 7) th1 = now32(); }
    }
    if (DS) th4 = now32();

    unsigned rl = lds0 + LDS_REC + lane * 16, rh = rl + 2 * RECW;
    unsigned s0, s1, k1;
    asm volatile("" : "=v"(s0), "=v"(s1), "=v"(k1));
    unsigned c7 = 0x70007, c8 = 0xfff8fff8u, c9 = 0x06000000;
    asm volatile("" : "+s"(c7), "+s"(c8), "+s"(c9));
    auto ev_s = [&]() {
        unsigned l = lane;
        asm volatile("" : "+v"(l));
        const unsigned t = tag >> (3 * (l & 14) + 12 * (l & 1));
        s0 = (t & 63) * 0x2001 & 0x70007;
        s1 = (l & 1) ? 0 : (t >> 6 & 63) * 0x2001 & 0x70007;
        k1 = (l & 1) * 0x2001;
        asm volatile("" : "+v"(s0), "+v"(s1), "+v"(k1));
    };
    auto pack = [&](f32x4 v) {
        unsigned h0, h1, m, z;
        asm("v_max3_f32 %2, |%4|, |%5|, |%6|\n\tv_max3_f32 %2, %2, |%7|, %13\n\ts_nop 1\n\tv_max_u32_dpp %2, %2, %2 quad_perm:[1,0,3,2] row_mask:0xf bank_mask:0xf\n\t"
            "v_and_b32 %3, 0x7e000000, %2\n\tv_lshrrev_b32 %2, 25, %2\n\tv_sub_u32 %3, 0x84800000, %3\n\tv_mul_u32_u24 %2, %2, %8\n\t"
            "v_fma_mixlo_f16 %0, %4, %3, 0\n\tv_fma_mixlo_f16 %1, %6, %3, 0\n\tv_fma_mixhi_f16 %0, %5, %3, 0\n\tv_fma_mixhi_f16 %1, %7, %3, 0\n\tv_and_or_b32 %2, %2, %11, %10\n\t"
            "v_pk_add_u16 %0, %0, 4 op_sel_hi:[1,0]\n\tv_pk_add_u16 %1, %1, 4 op_sel_hi:[1,0]\n\tv_and_or_b32 %0, %0, %12, %9\n\tv_and_or_b32 %1, %1, %12, %2"
            : "=&v"(h0), "=&v"(h1), "=&v"(m), "=&v"(z) : "v"(v[0]), "v"(v[1]), "v"(v[2]), "v"(v[3]), "v"(k1), "v"(s0), "v"(s1), "s"(c7), "s"(c8), "s"(c9));
        return u32x2{h0, h1};
    };
#define LDREC(hd) "ds_read_b128 %0, %4 offset:768*" #hd "\n\tds_read_b128 %1, %4 offset:768*" #hd "+24576\n\tds_read_b128 %2, %5 offset:768*" #hd "\n\tds_read_b128 %3, %5 offset:768*" #hd "+24576"
    auto ldrec = [&](unsigned o, f32x4 (&p)[4]) {
        asm volatile(LDREC(0) : "=&v"(p[0]), "=&v"(p[1]), "=&v"(p[2]), "=&v"(p[3]) : "v"(rl + o), "v"(rh + o) : "memory");
    };
    auto add4 = [&](f32x4 (&p)[4]) { return pack((p[0] + p[1]) + (p[2] + p[3])); };
    auto sum4 = [&](unsigned o) {
        f32x4 p[4];
        ldrec(o, p);
        asm volatile("s_waitcnt lgkmcnt(0)" : "+v"(p[0]), "+v"(p[1]), "+v"(p[2]), "+v"(p[3]) :: "memory");
        return add4(p);
    };
    auto wide = [&](u32x2 t) {
        return u32x4{t[0], t[1], (unsigned)__builtin_amdgcn_mov_dpp(t[0], 0xb1, 0xf, 0xf, true), (unsigned)__builtin_amdgcn_mov_dpp(t[1], 0xb1, 0xf, 0xf, true)};
    };
    auto merge = [&](int hd) {
        f32x4 p[4];
        f32x2 ma, mb;
        f32x4 xa, xb;
        asm volatile("ds_read2st64_b64 %0, %2 offset1:8\n\tds_read2st64_b64 %1, %2 offset0:16 offset1:24" : "=&v"(xa), "=&v"(xb) : "v"(lds0 + LDS_P + hd * 128 + 16) : "memory");
        ldrec(hd * (DH * 4), p);
        asm volatile("s_waitcnt lgkmcnt(4)" : "+v"(xa), "+v"(xb) :: "memory");
        const float M = __builtin_amdgcn_fmed3f(__builtin_amdgcn_fmed3f(xa[0], xa[2], __builtin_inff()), __builtin_amdgcn_fmed3f(xb[0], xb[2], __builtin_inff()), __builtin_inff());
        ma = f32x2{__builtin_amdgcn_exp2f(xa[0] - M), __builtin_amdgcn_exp2f(xa[2] - M)};
        mb = f32x2{__builtin_amdgcn_exp2f(xb[0] - M), __builtin_amdgcn_exp2f(xb[2] - M)};
        const float rL = __builtin_amdgcn_rcpf((ma[0] * xa[1] + ma[1] * xa[3]) + (mb[0] * xb[1] + mb[1] * xb[3]));
        ma *= rL; mb *= rL;
        asm volatile("s_waitcnt lgkmcnt(0)" : "+v"(ma), "+v"(mb), "+v"(p[0]), "+v"(p[1]), "+v"(p[2]), "+v"(p[3]) :: "memory");
        return pack(ma[0] * p[0] + ma[1] * p[1] + mb[0] * p[2] + mb[1] * p[3]);
    };
    constexpr unsigned LSTR = 128;
    constexpr int NC = 3 * H;
    const int cn = sb;
    const unsigned hc = 4 * (cn & 7) + ((cn >> 3) & 3);
    unsigned old = 0, area = 0, fo = 0;
    unsigned flv = 0;
    float gw = 1.f, mw = 0.f;
    f32x2 fs[4] = {};
    constexpr int LDS_ML = LDS_QRAW + 2048;
#define BAD(y) ((unsigned)(y)[1] != t32 || (unsigned)(y)[3] != (unsigned)tag)
    auto ev_all = [&](int hd) {
        unsigned n;
        asm volatile("1:\tds_read_b32 %0, %1\n\ts_waitcnt lgkmcnt(0)\n\tv_cmp_ne_u32 vcc, 4, %0\n\ts_cbranch_vccnz 1b" : "=&v"(n) : "v"(lds0 + LDS_CNT + hd * 4) : "memory", "vcc");
    };
    auto quiet = [&]() { vth = 15; calm = max(calm, vt + 500 * 16); };
    auto ev_ml = [&]() {
        f32x2 ml[4];
#pragma unroll
        for (int w = 0; w < 4; ++w) ml[w] = *reinterpret_cast<const f32x2*>(smem + LDS_P + ((wave + w) & 3) * (H * 128) + (lane & 31) * 128 + 16);
        const float M = fmaxf(fmaxf(ml[0][0], ml[1][0]), fmaxf(ml[2][0], ml[3][0]));
        const float e0 = __builtin_amdgcn_exp2f(ml[0][0] - M);
        const float L = (e0 * ml[0][1] + __builtin_amdgcn_exp2f(ml[1][0] - M) * ml[1][1]) + (__builtin_amdgcn_exp2f(ml[2][0] - M) * ml[2][1] + __builtin_amdgcn_exp2f(ml[3][0] - M) * ml[3][1]);
        flv = ((__float_as_uint(M + __builtin_amdgcn_logf(L)) + 0x80) & 0xffffff00u) | ((unsigned)(tag >> (8 * (lane & 7))) & 255);
        gw = e0 * __builtin_amdgcn_rcpf(L);
        asm volatile("" : "+v"(flv), "+v"(gw));
    };
    auto ev_area = [&]() {
        u32x4 e = *reinterpret_cast<const u32x4*>(smem + LDS_SK);
        if (__builtin_expect(__any(BAD(e)), 0)) {
#pragma unroll 1
            for (int n = 0; n < (1 << 20) && BAD(e); ++n)
                asm volatile("global_load_dwordx4 %0, %1, %2 sc1\n\ts_waitcnt vmcnt(0)" : "=v"(e) : "v"(psk), "s"(a.part) : "memory");
        }
        area = (((__builtin_amdgcn_readfirstlane(e[0]) >> 13) & (NAREA - 1)) + 1) * AREA + 4096;
        asm volatile("" : "+s"(area));
    };
    unsigned eo[5], xo[8], oo, po, wo, ta, tb, tc, ea, txb, tgb;
    u32x4 ev[4];
    f32x4 a0, a1;
    asm volatile("" : "=v"(eo[0]), "=v"(eo[1]), "=v"(eo[2]), "=v"(eo[3]), "=v"(eo[4]), "=v"(xo[0]), "=v"(xo[1]), "=v"(xo[2]), "=v"(xo[3]), "=v"(xo[4]), "=v"(xo[5]), "=v"(xo[6]), "=v"(xo[7]),
                      "=v"(oo), "=v"(po), "=v"(wo), "=v"(ta), "=v"(tb), "=v"(tc), "=v"(ea), "=v"(ev[0]), "=v"(ev[1]), "=v"(ev[2]), "=v"(a0), "=v"(a1));
    txb = nb16 + step + stag - EX * 16 - (sb >> 3) * GS * 4;
    tgb = nb16 + stag - (hc == H - 1 ? HL : HOLD) * 16;
    asm volatile("" : "+s"(txb), "+s"(tgb));
    auto ev_put = [&]() {
        asm volatile("s_mov_b32 exec_hi, 0\n\tbuffer_store_dword %0, %1, %2, %3 offen sc1\n\ts_mov_b32 exec_hi, -1" :: "v"(flv), "v"(lane * 4), "s"(pr), "s"(area + pfl + sb * LSTR) : "memory");
    };
    auto ev_get = [&]() {
        unsigned l = lane;
        asm volatile("" : "+v"(l));
        asm volatile("" : "+s"(area));
        fo = area + pfl + 8 * wave * LSTR + (cn & 7) * 16 + (((l & 56) << 2) + (l & 7)) * LSTR;
        asm volatile("s_mov_b32 m0, %2\n\ts_nop 0\n\tglobal_load_lds_dwordx4 %0, %1 sc1" :: "v"(fo), "s"(a.part), "s"(lds0 + LDS_ML + wave * 1024) : "memory");
    };
    auto ev_w1 = [&]() {
        u32x4 y = *reinterpret_cast<const u32x4*>(smem + LDS_ML + wave * 1024 + lane * 16);
        const unsigned tq = (cn & 1) ? t32 : (unsigned)tag;
#define BADW(y) (__builtin_amdgcn_perm(__builtin_amdgcn_perm((y)[3], (y)[2], 0x0400), __builtin_amdgcn_perm((y)[1], (y)[0], 0x0400), 0x05040100) != tq)
        if (__builtin_expect(__any(BADW(y)), 0)) {
#pragma unroll 1
            for (int n = 0; n < (1 << 20) && BADW(y); ++n)
                asm volatile("global_load_dwordx4 %0, %1, %2 sc1\n\ts_waitcnt vmcnt(0)" : "=v"(y) : "v"(fo), "s"(a.part) : "memory");
        }
        const unsigned se = (cn & 8) ? 0x0706050cu : 0x0302010cu;
        const float m = __uint_as_float((cn & 16) ? __builtin_amdgcn_perm(y[3], y[2], se) : __builtin_amdgcn_perm(y[1], y[0], se));
        float u;
        asm volatile("s_nop 1\n\tv_max_f32_dpp %0, %2, %2 row_ror:8 row_mask:0xf bank_mask:0xf\n\ts_nop 1\n\tv_max_f32_dpp %0, %0, %0 row_ror:4 row_mask:0xf bank_mask:0xf\n\ts_nop 1\n\t"
                     "v_max_f32_dpp %0, %0, %0 row_ror:2 row_mask:0xf bank_mask:0xf\n\ts_nop 1\n\tv_max_f32_dpp %0, %0, %0 row_ror:1 row_mask:0xf bank_mask:0xf\n\tv_mov_b32 %1, %0\n\ts_nop 1\n\t"
                     "v_permlane16_swap_b32 %0, %1\n\ts_nop 0\n\tv_max_f32 %0, %0, %1\n\tv_mov_b32 %1, %0\n\ts_nop 1\n\tv_permlane32_swap_b32 %0, %1\n\ts_nop 0\n\tv_max_f32 %0, %0, %1" : "=&v"(mw), "=&v"(u) : "v"(m));
        fs[0][0] = __builtin_amdgcn_exp2f(m - mw);
        asm volatile("" : "+v"(fs[0]));
    };
    auto ev_w2 = [&]() {
        const f32x2 f = fs[0];
        unsigned bp = (lane >> 3) * 4;
        asm volatile("ds_bpermute_b32 %0, %8, %9\n\tds_bpermute_b32 %1, %8, %9 offset:32\n\tds_bpermute_b32 %2, %8, %9 offset:64\n\tds_bpermute_b32 %3, %8, %9 offset:96\n\t"
                     "ds_bpermute_b32 %4, %8, %9 offset:128\n\tds_bpermute_b32 %5, %8, %9 offset:160\n\tds_bpermute_b32 %6, %8, %9 offset:192\n\tds_bpermute_b32 %7, %8, %9 offset:224"
                     : "=&v"(fs[0][0]), "=&v"(fs[0][1]), "=&v"(fs[1][0]), "=&v"(fs[1][1]), "=&v"(fs[2][0]), "=&v"(fs[2][1]), "=&v"(fs[3][0]), "=&v"(fs[3][1]) : "v"(bp), "v"(f[0]) : "memory");
        *reinterpret_cast<f32x2*>(smem + LDS_QRAW + 1024 + wave * 8) = f32x2{mw, xrow_sum(row16_allsum(f[0]))};
        asm volatile("s_waitcnt lgkmcnt(0)" : "+v"(fs[0]), "+v"(fs[1]), "+v"(fs[2]), "+v"(fs[3]) :: "memory");
    };
#define HERE unsigned l = lane, w = wave, n = sb; asm volatile("" : "+v"(l)); asm volatile("" : "+s"(w), "+s"(n));
    auto ev_a = [&]() {
        HERE
        const unsigned t = (((l >> 5) * nb + n) * LS + (l & 31) * 16) | ((l & 31) + 8) << 26 & 0x80000000u;
        eo[0] = t + 6 * w * nb * LS; eo[1] = eo[0] + 2 * nb * LS; eo[2] = eo[0] + 4 * nb * LS;
        eo[3] = t + (24 + 2 * w) * nb * LS;
        asm volatile("" : "+v"(eo[0]), "+v"(eo[1]), "+v"(eo[2]), "+v"(eo[3]));
    };
    auto ev_a2 = [&]() {
        HERE
        eo[4] = (((H - 1) * nb + n) * LS + l * 8) | l << 31 | (l + 16) << 25 & 0x80000000u;
        eo[3] = w == 3 ? eo[4] - nb * LS : eo[3];
        ea = lds0 + LDS_REC + (6 * w + (l >> 5)) * (DH * 4) + (l & 31) * 16;
        asm volatile("" : "+v"(eo[3]), "+v"(eo[4]), "+v"(ea));
    };
    auto ev_b = [&]() {
        HERE
        xo[0] = (hc * nb + 8 * w + (l >> 3)) * LS + (n >> 5) * 128 + (l & 7) * 16;
#pragma unroll
        for (int i = 1; i < 8; ++i) xo[i] = xo[0] + 32 * i * LS;
        asm volatile("" : "+v"(xo[0]), "+v"(xo[1]), "+v"(xo[2]), "+v"(xo[3]), "+v"(xo[4]), "+v"(xo[5]), "+v"(xo[6]), "+v"(xo[7]));
    };
    auto ev_c = [&]() {
        HERE
        oo = (hc * DH + (n >> 5) * 64 + 8 * (l & 7) + (l >> 3)) * 2;
        po = lds0 + LDS_QRAW + l * 4; wo = po + w * 256;
        asm volatile("" : "+v"(oo), "+v"(po), "+v"(wo));
    };
    auto ev_c2 = [&]() {
        HERE
        const unsigned t = tag >> 6 * (l & 7);
        ta = (t & 63) * 0x2001 & c7; tb = (t >> 6 & 63) * 0x2001 & c7; tc = (t >> 12 & 63) * 0x2001 & c7;
        asm volatile("" : "+v"(ta), "+v"(tb), "+v"(tc));
    };
    auto ev_d = [&]() {
        HERE
        ev_all(24);
        asm volatile("ds_read_b128 %0, %5\n\tds_read_b128 %1, %5 offset:1536\n\tds_read_b128 %2, %5 offset:3072\n\tds_read_b128 %3, %6\n\tds_read_b128 %4, %6 offset:16\n\ts_waitcnt lgkmcnt(0)"
                     : "+v"(ev[0]), "+v"(ev[1]), "+v"(ev[2]), "+v"(a0), "+v"(a1) : "v"(ea), "v"(lds0 + LDS_QRAW + 1024) : "memory");
        ea += (24 - 4 * w) * (DH * 4);
        asm volatile("" : "+v"(ea));
        if (4 * n + w == 1) { asm volatile("s_mov_b64 exec, 1\n\tbuffer_store_dwordx2 %0, off, %1, 0 offset:%2 sc1\n\ts_mov_b64 exec, -1" :: "v"(u32x2{(unsigned)tag, t32}), "s"(pr), "i"(pep) : "memory"); quiet(); }
    };
    auto ev_e = [&]() {
        const float M = fmaxf(fmaxf(a0[0], a0[2]), fmaxf(a1[0], a1[2]));
        const float gf = __builtin_amdgcn_exp2f(mw - M) * __builtin_amdgcn_rcpf((__builtin_amdgcn_exp2f(a0[0] - M) * a0[1] + __builtin_amdgcn_exp2f(a0[2] - M) * a0[3]) + (__builtin_amdgcn_exp2f(a1[0] - M) * a1[1] + __builtin_amdgcn_exp2f(a1[2] - M) * a1[3]));
#pragma unroll
        for (int i = 0; i < 4; ++i) fs[i] *= gf;
        asm volatile("" : "+v"(fs[0]), "+v"(fs[1]), "+v"(fs[2]), "+v"(fs[3]));
    };
    constexpr int G1 = 5, G2 = 27;
    const int hput = wave == ((b >> 5) & 3) ? G1 + 2 + ((b >> 3) & 3) : -9, hget = cn < NC ? G1 + 7 + ((cn >> 3) & 7) : -9;
    unsigned ev1 = 2 | 1 << G1 | 15 << (G1 + 2) | 9u << (hget & 31) | 1u << (H - 1), ev2 = 2 | 3 << G1 | 4u << (hget & 31);
    asm volatile("" : "+s"(ev1), "+s"(ev2), "+s"(area));
    float pv = *reinterpret_cast<const float*>(prec);
    f32x2 acc[3][2];
    float y, yn, zn;
    auto prep = [&](int hn) {
        const float s = bflo(sc);
        yn = s * (pv * __uint_as_float(__builtin_amdgcn_readlane(__float_as_uint(gw), hn)));
        float z = yn * dpp<0x55>(s);
        z += dpp<0x124>(z);
        z += dpp<0x128>(z);
        zn = -dpp<0x150>(z);
        asm volatile("" : "+v"(yn), "+v"(zn) :: "memory");
        SB();
        scp += 128;
        sc = *reinterpret_cast<const unsigned short*>(scp);
        prec += 128;
        pv = *reinterpret_cast<const float*>(prec);
        SB();
    };
    prep(0);
#define VCOMP(k) { \
        if ((k) == 0) y = yn; \
        const f32x2 pz = f32x2{dpp<0x150 + 4 * (k)>(y), zn}; \
        _Pragma("unroll") for (int x = 0; x < 3; ++x) \
            _Pragma("unroll") for (int e = 0; e < 2; ++e) { \
                const f32x2 vf = f32x2{(float)buf[k].d[x][2 * e], (float)buf[k].d[x][2 * e + 1]}; \
                if ((k) == 0) asm volatile("v_pk_fma_f32 %0, %1, %2, %1 op_sel:[0,0,1] op_sel_hi:[0,1,1]" : "=v"(acc[x][e]) : "v"(pz), "v"(vf)); \
                else asm volatile("v_pk_fma_f32 %0, %1, %2, %0 op_sel_hi:[0,1,1]" : "+v"(acc[x][e]) : "v"(pz), "v"(vf)); \
            } \
        asm volatile("" ::: "memory"); \
        SB(); }
#define ON(hn, x, f) { int q = (hn) - (x); asm volatile("" : "+s"(q)); if (__builtin_expect(q == 0, 0)) f(); }
#define VSTEP(k, hn) { \
        __builtin_amdgcn_s_waitcnt(0xf79); SB();        \
        VCOMP(k) pace(); issue(buf[k], k, vr, nso); SB(); steer(true, k); SB(); \
        if ((k) == 1) { \
            vth = TH; asm volatile("" : "+s"(vth)); \
            if (__builtin_expect(ev1 >> (hn) & 1, 0)) { \
                if ((hn) == 1) { softmax1(); softmax2(); }        \
                if ((hn) == G1) ev_all(0);        \
                if ((hn) == hput) { ev_put(); quiet(); } \
                if ((hn) == hget) { ev_get(); quiet(); } \
                if ((hn) == hget + 3) ev_w2(); \
                if ((hn) == H - 1) tmin = 0; \
            } \
            SB(); \
            if (__builtin_expect((hn) >= G2 - 4, 0)) { ON(hn, G2 - 4, ev_a) ON(hn, G2 - 3, ev_a2) ON(hn, G2 - 2, ev_b) ON(hn, G2 - 1, ev_c) ON(hn, G2, ev_d) ON(hn, G2 + 1, ev_e) ON(hn, G2 + 2, ev_c2) } \
            SB(); \
        } \
        if ((k) == 2) { \
            if (__builtin_expect(ev2 >> (hn) & 1, 0)) { \
                ON(hn, 1, ev_s) \
                if ((hn) == G1) ev_ml(); \
                if ((hn) == G1 + 1) ev_area(); \
                if ((hn) == hget + 2) ev_w1(); \
            } \
            SB(); \
            prep(hn); \
        } }
    unsigned ra = lds0 + LDS_REC + wave * RECW + (4 * c + r) * 4, ca = lds0 + LDS_CNT, one = 1;
    asm volatile("" : "+v"(ra), "+v"(ca), "+v"(one));
    auto record = [&]() {
        float w[3];
        f32x2 u[3];
#pragma unroll
        for (int x = 0; x < 3; ++x) { swap32(acc[x][0], acc[x][1]); SB(); }
#pragma unroll
        for (int x = 0; x < 3; ++x) { u[x] = acc[x][0] + acc[x][1]; SB(); }
#pragma unroll
        for (int x = 0; x < 3; ++x) {
            auto t = __builtin_amdgcn_permlane16_swap(__float_as_uint(u[x][0]), __float_as_uint(u[x][1]), false, false);
            u[x] = f32x2{__uint_as_float(t[0]), __uint_as_float(t[1])};
            SB();
        }
#pragma unroll
        for (int x = 0; x < 3; ++x) w[x] = u[x][0] + u[x][1];
        asm volatile("ds_write2st64_b32 %1, %2, %3 offset1:1\n\tds_write_b32 %1, %4 offset:512\n\ts_mov_b64 exec, 1\n\tds_add_rtn_u32 %0, %5, %6\n\ts_mov_b64 exec, -1"
                     : "+v"(old) : "v"(ra), "v"(w[0]), "v"(w[1]), "v"(w[2]), "v"(ca), "v"(one) : "memory");
        SB();
    };
#pragma unroll 1
    for (int h = 0; h < H - 1; ++h) {
        nso += hstride;
        VSTEP(0, h + 1)
        asm volatile("s_waitcnt lgkmcnt(0)" : "+v"(old) :: "memory");
        if (__builtin_amdgcn_readfirstlane(old) == 3) *reinterpret_cast<u32x2*>(smem + LDS_REC + (h - 1) * (DH * 4) + lane * 8) = __builtin_expect(h > G1, 1) ? sum4((h - 1) * (DH * 4)) : merge(h - 1);
        SB();
        VSTEP(1, h + 1) VSTEP(2, h + 1) VSTEP(3, h + 1)
        record();
        ra += DH * 4; ca += 4;
        asm volatile("" : "+v"(ra), "+v"(ca));
    }
    if (DBG == 4 && (F >> 13) == 5) { tbar = now32(); asm volatile("" : "+s"(tbar)); }
    asm volatile("s_branch .Lkv_t%c0\n\t.p2align 12,,3327\n.Lkv_t%c0:" :: "i"(ID));
    if (DBG == 4 && (F >> 13) == 4) { tbar = now32(); asm volatile("" : "+s"(tbar)); }
    VCOMP(0)
    asm volatile("s_waitcnt lgkmcnt(0)\n\ts_barrier" ::: "memory");
    if (DBG == 4 && (F >> 13) == 3) { tbar = now32(); asm volatile("" : "+s"(tbar)); }
    f32x4 p[4];
    asm volatile(LDREC(30) "\n\tds_read_b128 %6, %7" : "=&v"(p[0]), "=&v"(p[1]), "=&v"(p[2]), "=&v"(p[3]), "+v"(rl), "+v"(rh), "=&v"(ev[3]) : "v"(ea) : "memory");
    SB();
    VCOMP(1)
    asm volatile("s_waitcnt lgkmcnt(0)" : "+v"(p[0]), "+v"(p[1]), "+v"(p[2]), "+v"(p[3]), "+v"(ev[3]) :: "memory");
    if (wave == 3) ev[3] = wide(add4(p));
    int lx = hc == H - 1 ? ((SL >> 24) & 15) * ((SL >> 16) & 255) : 0;
    asm volatile("" : "+s"(lx));
    unsigned tx = txb - sched, tgt = tgb - sched;
    asm volatile("" : "+v"(ev[3]));
    asm volatile("" : "+s"(tx), "+s"(tgt), "+s"(area));
    SB();
    VCOMP(2)
    if (DBG == 4 && (F >> 13) == 1) { tbar = now32(); asm volatile("" : "+s"(tbar)); }
    auto until = [&](unsigned t) {
        int d;
        asm volatile("1:\ts_memrealtime vcc\n\ts_waitcnt lgkmcnt(0)\n\ts_lshl4_add_u32 %0, vcc_lo, %1\n\ts_cmp_lt_i32 %0, 0\n\ts_cbranch_scc1 1b" : "=&s"(d) : "s"(t) : "memory", "vcc", "scc");
        return d;
    };
    const rsrc_t orr = mkrsrc(a.out, H * DH * 2);
    asm volatile("" :: "s"(orr), "s"(pr));
    until(tx);
    if (F & 4096) asm volatile("" : "+v"(buf[3].d[0]));
    if (F & 256) asm volatile("" : "+v"(buf[3].d[2]));
    SB();
#pragma unroll
    for (int i = 0; i < 4; ++i) { __builtin_amdgcn_raw_buffer_store_b128(ev[i], pr, eo[i], area, 16); SB(); }
    if (DBG == 4 && !(F >> 13)) tbar = now32();
    SB();
    VCOMP(3)
    if (DBG == 4 && (F >> 13) == 6) { tbar = now32(); asm volatile("" : "+s"(tbar)); }
    record();
    asm volatile(LDREC(31) "\n\ts_waitcnt lgkmcnt(4)" : "=&v"(p[0]), "=&v"(p[1]), "=&v"(p[2]), "=&v"(p[3]), "+v"(rl), "+v"(rh), "+v"(old) :: "memory");
    const bool last = __builtin_amdgcn_readfirstlane(old) == 3;
    if (__builtin_expect(last, 1)) {
        asm volatile("s_waitcnt lgkmcnt(0)" : "+v"(p[0]), "+v"(p[1]), "+v"(p[2]), "+v"(p[3]) :: "memory");
        __builtin_amdgcn_raw_buffer_store_b128(wide(add4(p)), pr, eo[4], area, 16);
    }
    asm volatile("s_waitcnt lgkmcnt(0)" :: "v"(p[0]), "v"(p[1]), "v"(p[2]), "v"(p[3]) : "memory");
    unsigned tend = DS ? now32() : 0;
    unsigned tfl = 0, tpl = 0;

    if (cn < NC) {
        i32x4 x[8];
        float o[8];
        int cm = -138, kd = 0x200004;
        unsigned bad, mk;
        asm volatile("" : "+v"(cm));
        asm volatile("" : "+s"(kd));
        int late = until(tgt) + lx;
        if (DS) tfl = now32();
        if (DBG == 4) th4 = 0;
#define XL(i) x[i] = bld128<16 + (SL >> 28)>(pr, xo[i], area); SB();
#define XS(i) XL(i) asm volatile("s_cmp_lt_i32 %0, %1\n\ts_cbranch_scc0 1f\n\ts_sleep %2\n1:" :: "s"(late), "i"(((SL >> 16) & 255) * ((i) + 1)), "i"((SL >> (2 * (i))) & 3) : "scc"); SB();
#pragma unroll 1
        do {
            XS(0) XS(1) XS(2) XS(3) XS(4) XS(5) XS(6) XL(7)
            if (DBG == 4 && !th4) th4 = now32();
            late += 1024;
            mk = late < (1 << 30) ? 0x70007 : 0;
            asm volatile("" : "+s"(mk), "+s"(late));
            SB();
#pragma unroll
            for (int i = 0; i < 8; ++i) {
                float w;
                int x0, x1, x2, x3;
#define MIX(a, b, x, z) "\n\tv_fma_mix_f32 %" #a ", %9, %" #x ", " z(a) " op_sel_hi:[0,1,0]\n\tv_fma_mix_f32 %" #b ", %9, %" #x ", " z(b) " op_sel:[0,1,0] op_sel_hi:[0,1,0]"
#define ROW(z) "v_and_b32 %9, 0x70007, %23\n\tv_xor_b32 %13, %23, %9\n\tv_dot2_i32_i16 %9, %9, %18, %19\n\tv_xor_b32 %10, %20, %15\n\tv_xor_b32 %11, %21, %16\n\tv_xor_b32 %12, %22, %17\n\t" \
               "v_ldexp_f32 %9, %14, %9\n\tv_or3_b32 %8, " z(8) ", %10, %11" MIX(0, 1, 10, z) MIX(2, 3, 11, z) "\n\tv_or_b32 %8, %8, %12" MIX(4, 5, 12, z) MIX(6, 7, 13, z)
#define ZERO(a) "0"
#define SAME(a) "%" #a
                if (i == 0) asm volatile(ROW(ZERO) : "=&v"(o[0]), "=&v"(o[1]), "=&v"(o[2]), "=&v"(o[3]), "=&v"(o[4]), "=&v"(o[5]), "=&v"(o[6]), "=&v"(o[7]), "=&v"(bad), "=&v"(w),
                                           "=&v"(x0), "=&v"(x1), "=&v"(x2), "=&v"(x3) : "v"(fs[i >> 1][i & 1]), "v"(ta), "v"(tb), "v"(tc), "s"(kd), "v"(cm),
                                    "v"(x[i][0]), "v"(x[i][1]), "v"(x[i][2]), "v"(x[i][3]));
                else asm volatile(ROW(SAME) : "+v"(o[0]), "+v"(o[1]), "+v"(o[2]), "+v"(o[3]), "+v"(o[4]), "+v"(o[5]), "+v"(o[6]), "+v"(o[7]), "+v"(bad), "=&v"(w),
                                    "=&v"(x0), "=&v"(x1), "=&v"(x2), "=&v"(x3) : "v"(fs[i >> 1][i & 1]), "v"(ta), "v"(tb), "v"(tc), "s"(kd), "v"(cm),
                                    "v"(x[i][0]), "v"(x[i][1]), "v"(x[i][2]), "v"(x[i][3]));
                if (DBG == 6 && __any(((x0 | x1 | x2) & 0x70007) != 0) && !th1) th4 |= 1 << i;
                SB();
            }
            if (DBG == 6) th1 = 1;
        } while (__builtin_expect(__builtin_amdgcn_ballot_w64((bad & mk) != 0) != 0, 0));
        if (DS) tpl = now32();
        f32x2 u0 = f32x2{o[0], o[1]}, u1 = f32x2{o[2], o[3]}, u2 = f32x2{o[4], o[5]}, u3 = f32x2{o[6], o[7]};
        swap32(u0, u2); swap32(u1, u3);
        SB();
        u0 += u2; u1 += u3;
        swap16(u0, u1);
        u0 += u1;
        f32x2 p0, p1;
        asm volatile("s_nop 0\n\ts_nop 0\n\tv_add_f32_dpp %3, %3, %3 row_ror:8 row_mask:0xf bank_mask:0x3\n\tv_add_f32_dpp %3, %4, %4 row_ror:8 row_mask:0xf bank_mask:0xc\n\t"
                     "ds_write_b32 %5, %3\n\ts_mov_b64 exec, 1\n\tds_add_rtn_u32 %2, %7, %8 offset:20\n\ts_mov_b64 exec, -1\n\t"
                     "ds_read2st64_b32 %0, %6 offset1:1\n\tds_read2st64_b32 %1, %6 offset0:2 offset1:3\n\ts_waitcnt lgkmcnt(2)"
                     : "=&v"(p0), "=&v"(p1), "+v"(old), "+v"(u0[0]) : "v"(u0[1]), "v"(wo), "v"(po), "v"(ca), "v"(one) : "memory");
        if (__builtin_expect(__builtin_amdgcn_readfirstlane(old) == 3, 1)) {
            asm volatile("s_waitcnt lgkmcnt(0)" : "+v"(p0), "+v"(p1) :: "memory");
            p0 += p1;
            const float t = p0[0] + p0[1];
            __builtin_amdgcn_raw_buffer_store_b16((unsigned short)pk_bf16(t, t), orr, oo, 0, 0);
        }
        asm volatile(".Lkv_b%c0:" :: "i"(ID));
    }
    if (DBG) {
        const unsigned tc1 = now32();
        const unsigned fm = th4;
        if (!DS) tend = tbar = th4 = tfl = tpl = tc1;
        if (DBG == 6) tfl = tpl = fm;
        if (DBG == 5) tbar = tdma;
        const unsigned tl16 = sched - nb16 - stag - step;
        u64* stp = reinterpret_cast<u64*>(a.part + STAMP_OFF) + (size_t)((DBG == 4 || DBG == 24 || DBG == 6 ? cn : b) * 4 + wave) * 8;

        if (lane == 0) { stp[0] = tstart; stp[1] = tbar; stp[2] = DBG >= 4 ? tend - (((tend << 4) - tl16) >> 4) : th1; stp[3] = DBG == 4 && cn >= NC ? tend : th4; stp[4] = tend; stp[5] = cn < NC || DBG == 6 ? tfl : tend; stp[6] = cn < NC || DBG == 6 ? tpl : tend; stp[7] = tc1; }
        if (DBG == 2) *reinterpret_cast<u32x4*>(a.part + STAMP_OFF + 65536 + (b * 4 + wave) * 1024 + lane * 16) = *reinterpret_cast<const u32x4*>(smem + LDS_TR + 16 + wave * 1024 + lane * 16);
    }
}

template <int TP, int ADV, int PX, int TMIN, int CAP, int HD, int RA, int SKL, int FC, int F, int EX, int GS, int HOLD, int HL, int SL, int ID, int DBG>
static void launch87(void* const* d_in, const int* in_sizes, void* d_out, void* d_ws, hipStream_t stream) {
    typedef const unsigned short* P;
    Args a;
    a.qk = (const int*)d_in[1]; a.qv = (const int*)d_in[4];
    a.k_cos = (P)d_in[9]; a.k_sin = (P)d_in[10];
    a.q = (P)d_in[0]; a.q_cos = (P)d_in[7]; a.q_sin = (P)d_in[8];
    a.sc[0] = (P)d_in[2]; a.sc[1] = (P)d_in[3]; a.sc[2] = (P)d_in[5]; a.sc[3] = (P)d_in[6];
    a.part = (unsigned char*)d_ws;
    a.out = (unsigned short*)d_out;
    kv87_kernel<TP, ADV, PX, TMIN, CAP, HD, RA, SKL, FC, F, EX, GS, HOLD, HL, SL, ID, DBG><<<Skv / RW, 256, 0, stream>>>(a);
}

extern "C" void kernel_launch(void* const* d_in, const int* in_sizes, int n_in, void* d_out, int out_size, void* d_ws, size_t ws_size, hipStream_t stream) {
    launch87<79*16+8,220,40,78*16+2,28,70,240,24,32+256*50+65536*3+(15<<20),1+256,56,2,66,84,0x2aaa+(90<<16)+(2<<24),0,0>(d_in, in_sizes, d_out, d_ws, stream);
}
```

```cpp
#include <hip/hip_runtime.h>
#include <hip/hip_bf16.h>
#include <cstdint>
#define KALIGN 256
#define STAMP_OFF (200 << 20)
using bf16 = __hip_bfloat16;
using i32x4 = __attribute__((ext_vector_type(4))) int;
using u32x4 = __attribute__((ext_vector_type(4))) unsigned;
using u32x2 = __attribute__((ext_vector_type(2))) unsigned;
using f32x2 = __attribute__((ext_vector_type(2))) float;
using f32x4 = __attribute__((ext_vector_type(4))) float;

constexpr int DH = 192;
constexpr int HALF = 96;
constexpr int RW = 64;
constexpr int MAXH = 32;

__device__ __forceinline__ float bflo(unsigned u) { return __uint_as_float(u << 16); }
__device__ __forceinline__ float bfhi(unsigned u) { return __uint_as_float(u & 0xffff0000u); }
__device__ __forceinline__ float rbf(float x) { return __bfloat162float(__float2bfloat16(x)); }

template <int CTRL> __device__ __forceinline__ float dpp(float x) {
    return __builtin_bit_cast(float, __builtin_amdgcn_mov_dpp(__builtin_bit_cast(int, x), CTRL, 0xf, 0xf, true));
}
__device__ __forceinline__ float row16_allsum(float x) {
    x += dpp<0x128>(x);
    x += dpp<0x124>(x);
    x += dpp<0x122>(x);
    x += dpp<0x121>(x);
    return x;
}
__device__ __forceinline__ float row16_allmax(float x) {
    x = fmaxf(x, dpp<0x128>(x));
    x = fmaxf(x, dpp<0x124>(x));
    x = fmaxf(x, dpp<0x122>(x));
    x = fmaxf(x, dpp<0x121>(x));
    return x;
}
__device__ __forceinline__ float xrow_max(float x) {
    auto s = __builtin_amdgcn_permlane16_swap(__float_as_uint(x), __float_as_uint(x), false, false);
    x = fmaxf(__uint_as_float(s[0]), __uint_as_float(s[1]));
    auto t = __builtin_amdgcn_permlane32_swap(__float_as_uint(x), __float_as_uint(x), false, false);
    return fmaxf(__uint_as_float(t[0]), __uint_as_float(t[1]));
}
__device__ __forceinline__ float xrow_sum(float x) {
    auto s = __builtin_amdgcn_permlane16_swap(__float_as_uint(x), __float_as_uint(x), false, false);
    x = __uint_as_float(s[0]) + __uint_as_float(s[1]);
    auto t = __builtin_amdgcn_permlane32_swap(__float_as_uint(x), __float_as_uint(x), false, false);
    return __uint_as_float(t[0]) + __uint_as_float(t[1]);
}
__device__ __forceinline__ float sw32(float a, float b) {
    auto t = __builtin_amdgcn_permlane32_swap(__float_as_uint(a), __float_as_uint(b), false, false);
    return __uint_as_float(t[0]) + __uint_as_float(t[1]);
}
__device__ __forceinline__ float sw16(float a, float b) {
    auto t = __builtin_amdgcn_permlane16_swap(__float_as_uint(a), __float_as_uint(b), false, false);
    return __uint_as_float(t[0]) + __uint_as_float(t[1]);
}

__device__ __forceinline__ void swap32(f32x2& a, f32x2& b) {
    auto s = __builtin_amdgcn_permlane32_swap(__float_as_uint(a[0]), __float_as_uint(b[0]), false, false);
    auto t = __builtin_amdgcn_permlane32_swap(__float_as_uint(a[1]), __float_as_uint(b[1]), false, false);
    a = f32x2{__uint_as_float(s[0]), __uint_as_float(t[0])}; b = f32x2{__uint_as_float(s[1]), __uint_as_float(t[1])};
}
__device__ __forceinline__ void swap16(f32x2& a, f32x2& b) {
    auto s = __builtin_amdgcn_permlane16_swap(__float_as_uint(a[0]), __float_as_uint(b[0]), false, false);
    auto t = __builtin_amdgcn_permlane16_swap(__float_as_uint(a[1]), __float_as_uint(b[1]), false, false);
    a = f32x2{__uint_as_float(s[0]), __uint_as_float(t[0])}; b = f32x2{__uint_as_float(s[1]), __uint_as_float(t[1])};
}
#define SB() __builtin_amdgcn_sched_barrier(0)
typedef __amdgpu_buffer_rsrc_t rsrc_t;
__device__ __forceinline__ rsrc_t mkrsrc(const void* p, unsigned bytes) {
    return __builtin_amdgcn_make_buffer_rsrc(const_cast<void*>(p), 0, bytes, 0x00020000);
}
template <int AUX> __device__ __forceinline__ i32x4 bld128(rsrc_t r, unsigned voff, unsigned soff) {
    return __builtin_bit_cast(i32x4, __builtin_amdgcn_raw_buffer_load_b128(r, voff, soff, AUX));
}
template <int AUX> __device__ __forceinline__ unsigned bld16(rsrc_t r, unsigned voff, unsigned soff) {
    return __builtin_amdgcn_raw_buffer_load_b16(r, voff, soff, AUX);
}

struct HG { i32x4 d[3]; };
typedef unsigned long long u64;
typedef __attribute__((address_space(3))) unsigned char lds_u8;

__device__ __forceinline__ unsigned pk_bf16(float lo, float hi) {
    unsigned r;
    asm("v_cvt_pk_bf16_f32 %0, %1, %2" : "=v"(r) : "v"(lo), "v"(hi));
    return r;
}
__device__ __forceinline__ unsigned now32() { return (unsigned)__builtin_amdgcn_s_memrealtime(); }

constexpr int LDS_REC = 0;
constexpr int RECW = MAXH * DH * 4;
constexpr int LDS_CS = 0;
constexpr int LDS_QT = 12288;
constexpr int LDS_PF = 14336;
constexpr int LDS_QRAW = MAXH * 4 * DH * 4;
constexpr int LDS_Q = LDS_QRAW + MAXH * DH * 2;
constexpr int LDS_P = LDS_Q + 4 * DH * 4;
constexpr int LDS_SC = LDS_P + 4 * MAXH * 128;
constexpr int LDS_CNT = LDS_SC + 4 * MAXH * RW * 2;
constexpr int LDS_SK = LDS_CNT + 256;
constexpr int LDS_TR = LDS_SK + 1024;
constexpr int LDS_BYTES = LDS_TR + 4096 + 64;

struct Args {
    const int* qk; const int* qv;
    const unsigned short* k_cos; const unsigned short* k_sin;
    const unsigned short* q; const unsigned short* q_cos; const unsigned short* q_sin;
    unsigned char* part;
    unsigned short* out;
    const unsigned short* sc[4];
};
constexpr unsigned Skv = 16384;
constexpr float SCALE_LOG2 = 0.10411754627697264f;

template <int TP, int ADV, int PX, int TMIN, int CAP, int HD, int RA, int SKL, int FC, int F, int EX, int GS, int HOLD, int HL, int SL, int ID, int DBG>
__global__ __launch_bounds__(256) __attribute__((aligned(KALIGN))) void kv87_kernel(Args a)
{
    __shared__ __attribute__((aligned(16))) unsigned char smem[LDS_BYTES];
    const unsigned tstart = DBG ? now32() : 0;
    constexpr bool DS = (DBG && DBG < 5) || DBG == 24;
    constexpr int H = MAXH, PFN = 16, SJ = (F >> 9) & 3;
    const int b = blockIdx.x;
    const int tid = threadIdx.x;
    const int lane = tid & 63;
    const int wave = __builtin_amdgcn_readfirstlane(tid >> 6);
    typedef const unsigned short* const __attribute__((address_space(4))) * karg_t;
    const unsigned short* sp = ((karg_t)__builtin_amdgcn_kernarg_segment_ptr())[offsetof(Args, sc) / 8 + wave];
    if (b < PFN) {
        u64 pc; unsigned len, tl;
        asm volatile("s_getpc_b64 %0\n.Lkv_a%c3:\n\ts_mov_b32 %1, .Lkv_b%c3-.Lkv_a%c3-16\n\ts_mov_b32 %2, .Lkv_t%c3-.Lkv_a%c3" : "=s"(pc), "=s"(len), "=s"(tl) : "i"(ID));
        const unsigned o = tid * 16;
        asm volatile("s_mov_b32 m0, %5\n\ts_nop 0\n\tglobal_load_lds_dwordx4 %0, %4\n\tglobal_load_lds_dwordx4 %1, %4\n\tglobal_load_lds_dwordx4 %2, %4\n\tglobal_load_lds_dwordx4 %3, %4"
                     :: "v"(o), "v"(o + 4096), "v"(min(o + 8192, tl)), "v"(min(o + tl, len)), "s"(pc), "s"((unsigned)(size_t)(lds_u8*)smem + LDS_PF) : "memory");
    }
    SB();
    const int r = lane >> 4;
    const int c = lane & 15;
    constexpr unsigned hstride = Skv * (DH * 4);
    constexpr unsigned nb = Skv / RW;
    const unsigned lds0 = (unsigned)(size_t)(lds_u8*)smem;
    const unsigned sb = b;
    const unsigned rowb = (b & 7) * 2048 + (b & 24) * 64 + (b >> 5) * 16;
    const unsigned row0 = rowb + 4 * wave;

    constexpr unsigned LS = 512, psk = 0, pep = 128, AREA = 6u << 20, NAREA = 32, pfl = H * nb * LS;
    const rsrc_t pr = mkrsrc(a.part, (NAREA + 1) * AREA);
    unsigned vg = ((lane >> 3) * Skv + rowb + (lane & 6) * 64 + (lane & 1) * 8) * 2, dg = lds0 + LDS_SC + wave * 4096;
    u64 t64;
    {
        unsigned vs = ((lane >> 3) * Skv + (rowb & ~63u) + (b & 96) * 4 + (lane & 7) * 8) * 2, v1 = vs + 16 * Skv, v2 = vs + 32 * Skv, v3 = vs + 48 * Skv;
        if (!SJ && wave < 2) vs = vg;
        asm volatile("" : "+v"(vs), "+v"(v1), "+v"(v2), "+v"(v3), "+s"(dg));
        SB();
        asm volatile("s_memrealtime %0\n\ts_mov_b32 m0, %6\n\ts_nop 0\n\tglobal_load_lds_dwordx4 %1, %5\n\t"
                     "s_add_u32 m0, m0, 1024\n\ts_nop 0\n\tglobal_load_lds_dwordx4 %2, %5\n\t"
                     "s_add_u32 m0, m0, 1024\n\ts_nop 0\n\tglobal_load_lds_dwordx4 %3, %5\n\t"
                     "s_add_u32 m0, m0, 1024\n\ts_nop 0\n\tglobal_load_lds_dwordx4 %4, %5\n\ts_waitcnt lgkmcnt(0)"
                     : "=&s"(t64) : "v"(vs), "v"(v1), "v"(v2), "v"(v3), "s"(sp), "s"(dg),
                       "s"(a.qk), "s"(a.qv), "s"(a.k_cos), "s"(a.k_sin), "s"(a.q), "s"(a.q_cos), "s"(a.q_sin), "s"(a.part), "s"(a.out) : "memory", "scc");
    }
    auto gather = [&]() {
        if (F & 2) asm volatile("s_mov_b32 m0, %2\n\ts_nop 0\n\tglobal_load_lds_dwordx4 %0, %1 nt" :: "v"(vg), "s"(sp), "s"(dg) : "memory");
        else asm volatile("s_mov_b32 m0, %2\n\ts_nop 0\n\tglobal_load_lds_dwordx4 %0, %1" :: "v"(vg), "s"(sp), "s"(dg) : "memory");
        vg += 16 * Skv; dg += 1024;
    };
    SB();
    const unsigned tdma = (unsigned)t64;
#define DMA3(V, A, B, P) "s_mov_b32 m0, " B "\n\ts_nop 0\n\t" \
                     "global_load_lds_dwordx4 " V ", " A P "\n\t" \
                     "global_load_lds_dwordx4 " V ", " A " offset:1024" P "\n\t" \
                     "global_load_lds_dwordx4 " V ", " A " offset:2048" P "\n\t"
    asm volatile(DMA3("%0", "%1", "%2", "") "s_mov_b32 m0, %4\n\ts_nop 0\n\tglobal_load_lds_dwordx4 %3, off"
                 :: "v"(lane * 16), "s"(a.q + wave * 1536), "s"(lds0 + LDS_QRAW + wave * 3072),
                    "v"(lane < 48 ? (const unsigned char*)((lane < 24 ? a.q_cos : a.q_sin - DH) + 8 * lane) : a.part + pep), "s"(lds0 + LDS_QT + wave * RECW) : "memory");
    if ((b >> 4) == 1 && wave == 3)
        asm volatile("s_mov_b32 m0, %2\n\ts_mov_b64 exec, 15\n\tglobal_load_lds_dword %0, %1\n\ts_mov_b64 exec, -1" :: "v"((((tdma >> 13) & (NAREA - 1)) + 1) * AREA + lane * 0x160000 + b * 128), "s"(a.part), "s"(lds0 + LDS_PF) : "memory");
    SB();
    constexpr int TH = 4, DL = 32, BIG = 70;
    const int stag = (int)(((sb & ~7) + wave * 2 + (b & 1)) * (TP / 2)) / 256 - TP / 4;
    const unsigned nb16 = -(tdma << 4);
    int sched = ADV * 16 + stag - 4 * (TP / 2);
    int vt = 0;
    int calm = 600 * 16, step = TP / 2, tmin = TMIN / 2, vth = TH, pvm = 0;
    int tru = -4, slip = 0;
    int tgt0, tnow, vm, tf;
    auto pace = [&]() {
        if (DBG == 2 && (F & 16)) tf = (now32() << 4) + nb16;
        if (F & 1)
        asm volatile("s_max_i32 %0, %3, %4\n1:\ts_memrealtime vcc\n\ts_waitcnt lgkmcnt(0)\n\ts_lshl4_add_u32 %1, vcc_lo, %5\n\ts_cmp_lt_i32 %1, %0\n\ts_cbranch_scc1 1b\n\t"
                     "s_getreg_b32 %2, hwreg(HW_REG_IB_STS, 0, 4)" : "=&s"(tgt0), "=&s"(tnow), "=s"(vm) : "s"(sched), "s"(vt), "s"(nb16) : "scc", "vcc", "memory");
        else
        asm volatile("s_max_i32 %0, %3, %4\n\ts_branch 2f\n1:\ts_sleep 1\n2:\ts_memrealtime vcc\n\ts_waitcnt lgkmcnt(0)\n\ts_lshl4_add_u32 %1, vcc_lo, %5\n\ts_cmp_lt_i32 %1, %0\n\ts_cbranch_scc1 1b\n\t"
                     "s_getreg_b32 %2, hwreg(HW_REG_IB_STS, 0, 4)" : "=&s"(tgt0), "=&s"(tnow), "=s"(vm) : "s"(sched), "s"(vt), "s"(nb16) : "scc", "vcc", "memory");
    };
    auto steer = [&](bool ctl, int k = 0) {
        sched += step;
        if (!ctl) vt = max(tnow - 128, tgt0) + tmin;
        else {
            vt = tgt0 + tmin;
            if (__builtin_expect(tnow - tgt0 > 128, 0)) {
                asm volatile("");
                vt = tnow - 128 + tmin;
                if (tnow - tgt0 > HD * 16 && tnow > calm) { sched += RA * 16; calm = tnow + 900 * 16; if (DBG == 2) slip += RA * 16; }
            }
            if (__builtin_expect(vm > vth, 0)) {
                asm volatile("" : "+s"(pvm));
                if (vt - sched < CAP * 16 || (pvm > TH && vt - sched > BIG * 16)) vt += (vm - TH) * DL;
            }
            if (FC && ((FC >> 20) >> k & 1)) {
                if (__builtin_expect(vt - sched > ((FC >> 8) & 255) * 16, 0)) {
                    asm volatile("" : "+s"(pvm));
                    if (max(vm, pvm) <= ((FC >> 16) & 15)) vt -= FC & 255;
                }
            }
            pvm = vm;
            if ((F >> 5) & 7) asm volatile(".rept %0\n\ts_nop 7\n\t.endr" :: "i"(3 * ((F >> 5) & 7)));
        }
        if constexpr (DBG == 2) { *reinterpret_cast<unsigned*>(smem + LDS_TR + 16 + wave * 1024 + (tru & 255) * 4) = ((unsigned)(tnow - (sched - slip - step)) >> 4) | ((F & 8) ? vm << 16 : (F & 16) ? ((tnow - tf) >> 4) << 16 : 0); ++tru; }
    };
    {
        unsigned dst = lds0 + LDS_CS + wave * RECW;
        const unsigned v0 = lane * 16 + row0 * (DH * 2), v2 = v0 + 124 * DH * 2, v1 = lane < 32 ? v0 : v2;
#pragma unroll 1
        for (int j = 0; j < 4; ++j) {
            pace();
            asm volatile("s_mov_b32 m0, %4\n\ts_nop 0\n\tglobal_load_lds_dwordx4 %0, %3 nt\n\tglobal_load_lds_dwordx4 %1, %3 offset:1024 nt\n\tglobal_load_lds_dwordx4 %2, %3 offset:2048 nt"
                         :: "v"(v0), "v"(v1), "v"(v2), "s"((j < 2 ? a.k_cos : a.k_sin) + (j & 1) * 256 * DH), "s"(dst) : "memory");
            if (SJ && j == SJ - 1 && wave < 2) gather();
            steer(false);
            sched += PX;
            dst += 3072;
        }
    }
    if (!SJ && wave < 2) { vg += 16 * Skv; dg += 1024; }
    if (tid < MAXH + 5) *reinterpret_cast<unsigned*>(smem + LDS_CNT + tid * 4) = 0;
    const rsrc_t kr = mkrsrc(a.qk, H * hstride);
    const rsrc_t vr = mkrsrc(a.qv, H * hstride);
    unsigned kvo[4][3];
#pragma unroll
    for (int i = 0; i < 12; ++i) { kvo[i / 3][i % 3] = (row0 + r + i / 3 * 128) * (DH * 4) + c * 16 + i % 3 * 256; asm volatile("" : "+v"(kvo[i / 3][i % 3])); }
    auto issue = [&](HG& g, int k, rsrc_t rs, unsigned so) {
#pragma unroll
        for (int x = 0; x < 3; ++x) g.d[x] = bld128<2>(rs, kvo[k][x], so);
    };
    HG buf[4];
#define PRE(g) SB(); pace(); issue(buf[g], g, kr, 0); SB(); steer(false); SB();
    asm volatile("s_waitcnt vmcnt(%0) lgkmcnt(0)\n\ts_barrier" :: "i"(12 - 3 * SJ) : "memory");
    unsigned tbar = DS ? now32() : 0;
    PRE(0)
    const unsigned qoff = (lane < 48 ? lane : lane - 48) * 4;
    const unsigned* qt = reinterpret_cast<const unsigned*>(smem + LDS_QT + wave * RECW + qoff);
    unsigned qc0 = qt[0], qc1 = qt[HALF / 2], qs0 = qt[DH / 2], qs1 = qt[(DH + HALF) / 2];
    u64 tag;
    {
        const u32x2 e = *reinterpret_cast<const u32x2*>(smem + LDS_QT + wave * RECW + 768);
        tag = (((u64)__builtin_amdgcn_readfirstlane(e[1]) << 32) | __builtin_amdgcn_readfirstlane(e[0])) * 6364136223846793005ull + 1442695040888963407ull;
        asm volatile("" : "+s"(tag));
    }
    const unsigned t32 = tag >> 32;
    unsigned qraw = lds0 + LDS_QRAW + qoff;
    unsigned char* qrot = smem + LDS_Q + wave * (DH * 4) + 2 * qoff;
    unsigned q_lo, q_hi;
    auto getq = [&]() {
        asm volatile("" : "+v"(qraw));
        q_lo = *(const __attribute__((address_space(3))) unsigned*)(size_t)qraw, q_hi = *(const __attribute__((address_space(3))) unsigned*)(size_t)(qraw + HALF * 2);
        qraw += DH * 2;
    };
    auto rotq = [&]() {
        const f32x2 lo = f32x2{bflo(q_lo), bfhi(q_lo)}, hi = f32x2{bflo(q_hi), bfhi(q_hi)};
        const f32x2 y1 = lo * f32x2{bflo(qc0), bfhi(qc0)}, y2 = -hi * f32x2{bflo(qs0), bfhi(qs0)};
        const f32x2 y3 = hi * f32x2{bflo(qc1), bfhi(qc1)}, y4 = lo * f32x2{bflo(qs1), bfhi(qs1)};
        const unsigned x1 = pk_bf16(y1[0], y1[1]), x2 = pk_bf16(y2[0], y2[1]), x3 = pk_bf16(y3[0], y3[1]), x4 = pk_bf16(y4[0], y4[1]);
        const f32x2 z1 = f32x2{bflo(x1), bfhi(x1)} + f32x2{bflo(x2), bfhi(x2)}, z2 = f32x2{bflo(x3), bfhi(x3)} + f32x2{bflo(x4), bfhi(x4)};
        const unsigned l2 = pk_bf16(z1[0], z1[1]), h2 = pk_bf16(z2[0], z2[1]);
        *reinterpret_cast<f32x2*>(qrot) = f32x2{bflo(l2), bfhi(l2)};
        *reinterpret_cast<f32x2*>(qrot + HALF * 4) = f32x2{bflo(h2), bfhi(h2)};
        asm volatile("" ::: "memory");
    };
    f32x4 qa[3], qb[3];
    const unsigned char* qh = smem + LDS_Q + wave * (DH * 4) + 16 * c;
    const unsigned char* qh1 = qh + (c < 8 ? 64 + HALF : 64 - HALF) * 4;
    auto loadq = [&]() {
        asm volatile("" ::: "memory");
        qa[0] = *reinterpret_cast<const f32x4*>(qh); SB();
        qb[0] = *reinterpret_cast<const f32x4*>(qh + HALF * 4); SB();
        qa[1] = *reinterpret_cast<const f32x4*>(qh + 256); SB();
        qb[1] = *reinterpret_cast<const f32x4*>(qh1); SB();
        qa[2] = *reinterpret_cast<const f32x4*>(qh + 512); SB();
        qb[2] = *reinterpret_cast<const f32x4*>(qh + (128 - HALF) * 4);
        asm volatile("" ::: "memory");
    };
    getq(); rotq();
    if ((b >> 3) == 4 && wave == 3) __builtin_amdgcn_raw_buffer_store_b128(u32x4{tdma, t32, tdma, (unsigned)tag}, pr, lane == 0 ? psk + (b & 7) * 16 : 0x80000000u, 0, 16);
    PRE(1)
    f32x2 cc[4][3][2], ss[4][3][2];
    const unsigned char* cb = smem + LDS_CS + wave * RECW + r * (DH * 2) + c * 8;
    asm volatile("s_waitcnt vmcnt(12)" ::: "memory");
#pragma unroll
    for (int k = 0; k < 4; ++k)
#pragma unroll
        for (int x = 0; x < 3; ++x) {
            const u32x2 t = *reinterpret_cast<const u32x2*>(cb + k * 1536 + 128 * x);
            cc[k][x][0] = f32x2{bflo(t[0]), bfhi(t[0])}; cc[k][x][1] = f32x2{bflo(t[1]), bfhi(t[1])};
            asm volatile("" : "+v"(cc[k][x][0]), "+v"(cc[k][x][1]));
        }
    PRE(2)
    loadq();
    const unsigned char* scp = smem + LDS_SC + (c & 1) * 4096 + (4 * (c & 12) + 4 * wave + r) * 2;
    unsigned sc = *reinterpret_cast<const unsigned short*>(scp);
    auto loadsin = [&](int k) {
        const unsigned char* sb1 = cb + (c < 8 ? (64 + HALF) * 2 : (64 - HALF) * 2);
        const unsigned sg = c < 8 ? 0u : 0x80008000u;
        u32x2 t[3];
        t[0] = *reinterpret_cast<const u32x2*>(cb + 6144 + k * 1536 + HALF * 2);
        t[1] = *reinterpret_cast<const u32x2*>(sb1 + 6144 + k * 1536) ^ u32x2{sg, sg};
        t[2] = *reinterpret_cast<const u32x2*>(cb + 6144 + k * 1536 + (128 - HALF) * 2) ^ u32x2{0x80008000u, 0x80008000u};
#pragma unroll
        for (int x = 0; x < 3; ++x) {
            ss[k][x][0] = f32x2{bflo(t[x][0]), bfhi(t[x][0])}; ss[k][x][1] = f32x2{bflo(t[x][1]), bfhi(t[x][1])};
            asm volatile("" : "+v"(ss[k][x][0]), "+v"(ss[k][x][1]));
        }
    };
    asm volatile("s_waitcnt vmcnt(12)" ::: "memory");
    loadsin(0); loadsin(1);
    PRE(3)
    asm volatile("s_waitcnt vmcnt(12)" ::: "memory");
    loadsin(2); loadsin(3);
    unsigned th1 = 0, th4 = 0;
    const unsigned char* prec = smem + LDS_P + wave * (H * 128) + r * 32 + (c & 12);
    unsigned pw_ = lds0 + LDS_P + wave * (H * 128) + r * 32 + (c & 12) + (c & 3) * 128;
    float sm, sp_, sacc = 0.f;
    auto softmax1 = [&]() {
        float u;
        asm volatile("v_max_f32_dpp %0, %2, %2 row_ror:4 row_mask:0xf bank_mask:0xf\n\ts_nop 1\n\tv_max_f32_dpp %0, %0, %0 row_ror:8 row_mask:0xf bank_mask:0xf\n\tv_mov_b32 %1, %0\n\ts_nop 1\n\t"
                     "v_permlane16_swap_b32 %0, %1\n\tv_max_f32 %0, %0, %1\n\tv_mov_b32 %1, %0\n\ts_nop 1\n\tv_permlane32_swap_b32 %0, %1\n\tv_max_f32 %0, %0, %1" : "=&v"(sm), "=&v"(u) : "v"(sacc));
        sp_ = __builtin_amdgcn_exp2f(sacc - sm);
        asm volatile("" : "+v"(sp_));
    };
    auto softmax2 = [&]() {
        float l = sp_ + dpp<0x124>(sp_);
        l = xrow_sum(l + dpp<0x128>(l));
        asm volatile("ds_write2_b32 %0, %1, %2 offset1:4" :: "v"(pw_), "v"(sp_), "v"((c & 4) ? l : sm) : "memory");
        pw_ += 512;
    };

    auto skew = [&](int h) {
        if (wave == 1 && h == 1 + ((b >> 3) & 3))
            asm volatile("s_mov_b32 m0, %2\n\ts_nop 0\n\tglobal_load_lds_dwordx4 %0, %1 sc1" :: "v"(psk + (lane & 7) * 16), "s"(a.part), "s"(lds0 + LDS_SK) : "memory");
        if (h == 6) {
            const u32x2 e = *reinterpret_cast<const u32x2*>(smem + LDS_SK + (lane & 7) * 16);
            const int d = (int)(tdma - e[0]);
            if (__all(e[1] == t32 && d > -400 && d < 400)) {
                int sum = d + __builtin_amdgcn_mov_dpp(d, 0xb1, 0xf, 0xf, true), mn = min(d, __builtin_amdgcn_mov_dpp(d, 0xb1, 0xf, 0xf, true));
                sum += __builtin_amdgcn_mov_dpp(sum, 0x4e, 0xf, 0xf, true); mn = min(mn, __builtin_amdgcn_mov_dpp(mn, 0x4e, 0xf, 0xf, true));
                sum += __builtin_amdgcn_mov_dpp(sum, 0x124, 0xf, 0xf, true); mn = min(mn, __builtin_amdgcn_mov_dpp(mn, 0x124, 0xf, 0xf, true));
                sum = __builtin_amdgcn_readfirstlane(min(sum, 8 * (mn + SKL)));
                const int adj = (sum * 73 + 4096) >> 13;
                step -= adj; tmin -= adj;
                sched -= 2 * sum - 224 * adj;
            }
        }
    };

    rsrc_t nx = kr;
    unsigned nso = 0, sm4 = 0x11111110;
    unsigned gm = (wave < 2 ? 0x4081u << 1 : 0x204081u << 4) << (((b >> 3) + 2 * wave) & 3), evk = gm * 5 | 0x5e, gn = 0;
    asm volatile("" : "+s"(sm4), "+s"(gm), "+s"(evk));
#pragma unroll 1
    for (int h = 0; h < H; ++h) {
        float scf = bflo(sc);
        asm volatile("" : "+v"(scf) :: "memory");
        SB();
        nso += hstride;
        scp += 128;
        if (__builtin_expect((h & 7) == 7, 0)) {
            const unsigned fa = lds0 + LDS_CNT + 128 + ((h + 1) >> 5) * 8;
            u32x2 n;
            do asm volatile("ds_read_b64 %0, %1\n\ts_waitcnt lgkmcnt(0)" : "=v"(n) : "v"(fa) : "memory");
            while (__any(min(n[0], n[1]) * 8 <= h));
            if (h == H - 1) { nx = vr; nso = 0; scp += 4096; }
        }
        sc = *reinterpret_cast<const unsigned short*>(scp);
        SB();
        float st[2];
#define KSTEP(k) { \
            __builtin_amdgcn_s_waitcnt(0xf79); SB();        \
            const float kz = dpp<0x151 + 4 * (k)>(scf); \
            const f32x2 kz2 = f32x2{kz, kz}; \
            f32x2 a2 = f32x2{0.f, 0.f}; \
            _Pragma("unroll") for (int x = 0; x < 3; ++x) { \
                _Pragma("unroll") for (int e = 0; e < 2; ++e) { \
                    const f32x2 w = __builtin_elementwise_fma(f32x2{qb[x][2 * e], qb[x][2 * e + 1]}, ss[k][x][e], f32x2{qa[x][2 * e], qa[x][2 * e + 1]} * cc[k][x][e]); \
                    const f32x2 kf = f32x2{(float)buf[k].d[x][2 * e], (float)buf[k].d[x][2 * e + 1]} - kz2; \
                    a2 = __builtin_elementwise_fma(kf, w, a2); \
                } \
            } \
            float sa = a2[0] + a2[1]; \
            asm volatile("" : "+v"(sa) :: "memory"); \
            SB(); pace(); issue(buf[k], k, nx, nso); SB(); steer(true, k); SB(); \
              \
            if ((k) < 2) asm volatile("v_add_f32_dpp %0, %1, %1 row_ror:8 row_mask:0xf bank_mask:0xf" : "=v"(st[(k) & 1]) : "v"(sa)); \
            else asm volatile("v_add_f32_dpp %0, %1, %1 row_ror:8 row_mask:0xf bank_mask:0xc" : "+v"(st[(k) & 1]) : "v"(sa)); \
            if ((k) == 0) { getq(); SB(); if (sm4 >> h & 1) softmax1(); SB(); } \
            if ((k) == 1) { rotq(); SB(); } \
            if ((k) == 2) { \
                asm volatile("" : "+s"(sm4)); \
                if (sm4 >> h & 1) softmax2(); \
                SB(); \
                if (__builtin_expect(evk >> h & 1, 0)) { \
                    if (gm >> h & 1) gather(); \
                    if (gm * 4 >> h & 1) *reinterpret_cast<unsigned*>(smem + LDS_CNT + 128 + wave * 4) = ++gn;        \
                    skew(h); \
                } \
                SB(); \
            } \
            if ((k) == 3) { loadq(); SB(); } }
        KSTEP(0) KSTEP(1) KSTEP(2) KSTEP(3)
        asm volatile("v_add_f32_dpp %0, %0, %0 row_shl:4 row_mask:0xf bank_mask:0x5\n\tv_add_f32_dpp %0, %3, %3 row_shr:4 row_mask:0xf bank_mask:0xa\n\t"
                     "v_mul_f32 %1, %4, %1\n\tv_mov_b32_dpp %2, %2 quad_perm:[1,2,3,3] row_mask:0xf bank_mask:0xf\n\t"
                     "v_add_f32_dpp %0, %0, %0 quad_perm:[1,0,3,2] row_mask:0xf bank_mask:0xf\n\ts_nop 1\n\tv_add_f32_dpp %0, %0, %0 quad_perm:[2,3,0,1] row_mask:0xf bank_mask:0xf\n\t"
                     "v_mul_f32_dpp %0, %1, %0 quad_perm:[0,0,0,0] row_mask:0xf bank_mask:0xf\n\tv_cndmask_b32 %2, %2, %0, %5"
                     : "+v"(st[0]), "+v"(scf), "+v"(sacc) : "v"(st[1]), "s"(SCALE_LOG2), "s"(0x8888888888888888ull));
        if (DS) { if (h == 7) th1 = now32(); }
    }
    if (DS) th4 = now32();

    unsigned rl = lds0 + LDS_REC + lane * 16, rh = rl + 2 * RECW;
    unsigned s0, s1, k1;
    asm volatile("" : "=v"(s0), "=v"(s1), "=v"(k1));
    unsigned c7 = 0x70007, c8 = 0xfff8fff8u, c9 = 0x06000000;
    asm volatile("" : "+s"(c7), "+s"(c8), "+s"(c9));
    auto ev_s = [&]() {
        unsigned l = lane;
        asm volatile("" : "+v"(l));
        const unsigned t = tag >> (3 * (l & 14) + 12 * (l & 1));
        s0 = (t & 63) * 0x2001 & 0x70007;
        s1 = (l & 1) ? 0 : (t >> 6 & 63) * 0x2001 & 0x70007;
        k1 = (l & 1) * 0x2001;
        asm volatile("" : "+v"(s0), "+v"(s1), "+v"(k1));
    };
    auto pack = [&](f32x4 v) {
        unsigned h0, h1, m, z;
        asm("v_max3_f32 %2, |%4|, |%5|, |%6|\n\tv_max3_f32 %2, %2, |%7|, %13\n\ts_nop 1\n\tv_max_u32_dpp %2, %2, %2 quad_perm:[1,0,3,2] row_mask:0xf bank_mask:0xf\n\t"
            "v_and_b32 %3, 0x7e000000, %2\n\tv_lshrrev_b32 %2, 25, %2\n\tv_sub_u32 %3, 0x84800000, %3\n\tv_mul_u32_u24 %2, %2, %8\n\t"
            "v_fma_mixlo_f16 %0, %4, %3, 0\n\tv_fma_mixlo_f16 %1, %6, %3, 0\n\tv_fma_mixhi_f16 %0, %5, %3, 0\n\tv_fma_mixhi_f16 %1, %7, %3, 0\n\tv_and_or_b32 %2, %2, %11, %10\n\t"
            "v_pk_add_u16 %0, %0, 4 op_sel_hi:[1,0]\n\tv_pk_add_u16 %1, %1, 4 op_sel_hi:[1,0]\n\tv_and_or_b32 %0, %0, %12, %9\n\tv_and_or_b32 %1, %1, %12, %2"
            : "=&v"(h0), "=&v"(h1), "=&v"(m), "=&v"(z) : "v"(v[0]), "v"(v[1]), "v"(v[2]), "v"(v[3]), "v"(k1), "v"(s0), "v"(s1), "s"(c7), "s"(c8), "s"(c9));
        return u32x2{h0, h1};
    };
#define LDREC(hd) "ds_read_b128 %0, %4 offset:768*" #hd "\n\tds_read_b128 %1, %4 offset:768*" #hd "+24576\n\tds_read_b128 %2, %5 offset:768*" #hd "\n\tds_read_b128 %3, %5 offset:768*" #hd "+24576"
    auto ldrec = [&](unsigned o, f32x4 (&p)[4]) {
        asm volatile(LDREC(0) : "=&v"(p[0]), "=&v"(p[1]), "=&v"(p[2]), "=&v"(p[3]) : "v"(rl + o), "v"(rh + o) : "memory");
    };
    auto add4 = [&](f32x4 (&p)[4]) { return pack((p[0] + p[1]) + (p[2] + p[3])); };
    auto sum4 = [&](unsigned o) {
        f32x4 p[4];
        ldrec(o, p);
        asm volatile("s_waitcnt lgkmcnt(0)" : "+v"(p[0]), "+v"(p[1]), "+v"(p[2]), "+v"(p[3]) :: "memory");
        return add4(p);
    };
    auto wide = [&](u32x2 t) {
        return u32x4{t[0], t[1], (unsigned)__builtin_amdgcn_mov_dpp(t[0], 0xb1, 0xf, 0xf, true), (unsigned)__builtin_amdgcn_mov_dpp(t[1], 0xb1, 0xf, 0xf, true)};
    };
    auto merge = [&](int hd) {
        f32x4 p[4];
        f32x2 ma, mb;
        f32x4 xa, xb;
        asm volatile("ds_read2st64_b64 %0, %2 offset1:8\n\tds_read2st64_b64 %1, %2 offset0:16 offset1:24" : "=&v"(xa), "=&v"(xb) : "v"(lds0 + LDS_P + hd * 128 + 16) : "memory");
        ldrec(hd * (DH * 4), p);
        asm volatile("s_waitcnt lgkmcnt(4)" : "+v"(xa), "+v"(xb) :: "memory");
        const float M = __builtin_amdgcn_fmed3f(__builtin_amdgcn_fmed3f(xa[0], xa[2], __builtin_inff()), __builtin_amdgcn_fmed3f(xb[0], xb[2], __builtin_inff()), __builtin_inff());
        ma = f32x2{__builtin_amdgcn_exp2f(xa[0] - M), __builtin_amdgcn_exp2f(xa[2] - M)};
        mb = f32x2{__builtin_amdgcn_exp2f(xb[0] - M), __builtin_amdgcn_exp2f(xb[2] - M)};
        const float rL = __builtin_amdgcn_rcpf((ma[0] * xa[1] + ma[1] * xa[3]) + (mb[0] * xb[1] + mb[1] * xb[3]));
        ma *= rL; mb *= rL;
        asm volatile("s_waitcnt lgkmcnt(0)" : "+v"(ma), "+v"(mb), "+v"(p[0]), "+v"(p[1]), "+v"(p[2]), "+v"(p[3]) :: "memory");
        return pack(ma[0] * p[0] + ma[1] * p[1] + mb[0] * p[2] + mb[1] * p[3]);
    };
    constexpr unsigned LSTR = 128;
    constexpr int NC = 3 * H;
    const int cn = sb;
    const unsigned hc = 4 * (cn & 7) + ((cn >> 3) & 3);
    unsigned old = 0, area = 0, fo = 0;
    unsigned flv = 0;
    float gw = 1.f, mw = 0.f;
    f32x2 fs[4] = {};
    constexpr int LDS_ML = LDS_QRAW + 2048;
#define BAD(y) ((unsigned)(y)[1] != t32 || (unsigned)(y)[3] != (unsigned)tag)
    auto ev_all = [&](int hd) {
        unsigned n;
        asm volatile("1:\tds_read_b32 %0, %1\n\ts_waitcnt lgkmcnt(0)\n\tv_cmp_ne_u32 vcc, 4, %0\n\ts_cbranch_vccnz 1b" : "=&v"(n) : "v"(lds0 + LDS_CNT + hd * 4) : "memory", "vcc");
    };
    auto quiet = [&]() { vth = 15; calm = max(calm, vt + 500 * 16); };
    auto ev_ml = [&]() {
        f32x2 ml[4];
#pragma unroll
        for (int w = 0; w < 4; ++w) ml[w] = *reinterpret_cast<const f32x2*>(smem + LDS_P + ((wave + w) & 3) * (H * 128) + (lane & 31) * 128 + 16);
        const float M = fmaxf(fmaxf(ml[0][0], ml[1][0]), fmaxf(ml[2][0], ml[3][0]));
        const float e0 = __builtin_amdgcn_exp2f(ml[0][0] - M);
        const float L = (e0 * ml[0][1] + __builtin_amdgcn_exp2f(ml[1][0] - M) * ml[1][1]) + (__builtin_amdgcn_exp2f(ml[2][0] - M) * ml[2][1] + __builtin_amdgcn_exp2f(ml[3][0] - M) * ml[3][1]);
        flv = ((__float_as_uint(M + __builtin_amdgcn_logf(L)) + 0x80) & 0xffffff00u) | ((unsigned)(tag >> (8 * (lane & 7))) & 255);
        gw = e0 * __builtin_amdgcn_rcpf(L);
        asm volatile("" : "+v"(flv), "+v"(gw));
    };
    auto ev_area = [&]() {
        u32x4 e = *reinterpret_cast<const u32x4*>(smem + LDS_SK);
        if (__builtin_expect(__any(BAD(e)), 0)) {
#pragma unroll 1
            for (int n = 0; n < (1 << 20) && BAD(e); ++n)
                asm volatile("global_load_dwordx4 %0, %1, %2 sc1\n\ts_waitcnt vmcnt(0)" : "=v"(e) : "v"(psk), "s"(a.part) : "memory");
        }
        area = (((__builtin_amdgcn_readfirstlane(e[0]) >> 13) & (NAREA - 1)) + 1) * AREA + 4096;
        asm volatile("" : "+s"(area));
    };
    unsigned eo[5], xo[8], oo, po, wo, ta, tb, tc, ea, txb, tgb;
    u32x4 ev[4];
    f32x4 a0, a1;
    asm volatile("" : "=v"(eo[0]), "=v"(eo[1]), "=v"(eo[2]), "=v"(eo[3]), "=v"(eo[4]), "=v"(xo[0]), "=v"(xo[1]), "=v"(xo[2]), "=v"(xo[3]), "=v"(xo[4]), "=v"(xo[5]), "=v"(xo[6]), "=v"(xo[7]),
                      "=v"(oo), "=v"(po), "=v"(wo), "=v"(ta), "=v"(tb), "=v"(tc), "=v"(ea), "=v"(ev[0]), "=v"(ev[1]), "=v"(ev[2]), "=v"(a0), "=v"(a1));
    txb = nb16 + step + stag - EX * 16 - (sb >> 3) * GS * 4;
    tgb = nb16 + stag - (hc == H - 1 ? HL : HOLD) * 16;
    asm volatile("" : "+s"(txb), "+s"(tgb));
    auto ev_put = [&]() {
        asm volatile("s_mov_b32 exec_hi, 0\n\tbuffer_store_dword %0, %1, %2, %3 offen sc1\n\ts_mov_b32 exec_hi, -1" :: "v"(flv), "v"(lane * 4), "s"(pr), "s"(area + pfl + sb * LSTR) : "memory");
    };
    auto ev_get = [&]() {
        unsigned l = lane;
        asm volatile("" : "+v"(l));
        asm volatile("" : "+s"(area));
        fo = area + pfl + 8 * wave * LSTR + (cn & 7) * 16 + (((l & 56) << 2) + (l & 7)) * LSTR;
        asm volatile("s_mov_b32 m0, %2\n\ts_nop 0\n\tglobal_load_lds_dwordx4 %0, %1 sc1" :: "v"(fo), "s"(a.part), "s"(lds0 + LDS_ML + wave * 1024) : "memory");
    };
    auto ev_w1 = [&]() {
        u32x4 y = *reinterpret_cast<const u32x4*>(smem + LDS_ML + wave * 1024 + lane * 16);
        const unsigned tq = (cn & 1) ? t32 : (unsigned)tag;
#define BADW(y) (__builtin_amdgcn_perm(__builtin_amdgcn_perm((y)[3], (y)[2], 0x0400), __builtin_amdgcn_perm((y)[1], (y)[0], 0x0400), 0x05040100) != tq)
        if (__builtin_expect(__any(BADW(y)), 0)) {
#pragma unroll 1
            for (int n = 0; n < (1 << 20) && BADW(y); ++n)
                asm volatile("global_load_dwordx4 %0, %1, %2 sc1\n\ts_waitcnt vmcnt(0)" : "=v"(y) : "v"(fo), "s"(a.part) : "memory");
        }
        const unsigned se = (cn & 8) ? 0x0706050cu : 0x0302010cu;
        const float m = __uint_as_float((cn & 16) ? __builtin_amdgcn_perm(y[3], y[2], se) : __builtin_amdgcn_perm(y[1], y[0], se));
        float u;
        asm volatile("s_nop 1\n\tv_max_f32_dpp %0, %2, %2 row_ror:8 row_mask:0xf bank_mask:0xf\n\ts_nop 1\n\tv_max_f32_dpp %0, %0, %0 row_ror:4 row_mask:0xf bank_mask:0xf\n\ts_nop 1\n\t"
                     "v_max_f32_dpp %0, %0, %0 row_ror:2 row_mask:0xf bank_mask:0xf\n\ts_nop 1\n\tv_max_f32_dpp %0, %0, %0 row_ror:1 row_mask:0xf bank_mask:0xf\n\tv_mov_b32 %1, %0\n\ts_nop 1\n\t"
                     "v_permlane16_swap_b32 %0, %1\n\ts_nop 0\n\tv_max_f32 %0, %0, %1\n\tv_mov_b32 %1, %0\n\ts_nop 1\n\tv_permlane32_swap_b32 %0, %1\n\ts_nop 0\n\tv_max_f32 %0, %0, %1" : "=&v"(mw), "=&v"(u) : "v"(m));
        fs[0][0] = __builtin_amdgcn_exp2f(m - mw);
        asm volatile("" : "+v"(fs[0]));
    };
    auto ev_w2 = [&]() {
        const f32x2 f = fs[0];
        unsigned bp = (lane >> 3) * 4;
        asm volatile("ds_bpermute_b32 %0, %8, %9\n\tds_bpermute_b32 %1, %8, %9 offset:32\n\tds_bpermute_b32 %2, %8, %9 offset:64\n\tds_bpermute_b32 %3, %8, %9 offset:96\n\t"
                     "ds_bpermute_b32 %4, %8, %9 offset:128\n\tds_bpermute_b32 %5, %8, %9 offset:160\n\tds_bpermute_b32 %6, %8, %9 offset:192\n\tds_bpermute_b32 %7, %8, %9 offset:224"
                     : "=&v"(fs[0][0]), "=&v"(fs[0][1]), "=&v"(fs[1][0]), "=&v"(fs[1][1]), "=&v"(fs[2][0]), "=&v"(fs[2][1]), "=&v"(fs[3][0]), "=&v"(fs[3][1]) : "v"(bp), "v"(f[0]) : "memory");
        *reinterpret_cast<f32x2*>(smem + LDS_QRAW + 1024 + wave * 8) = f32x2{mw, xrow_sum(row16_allsum(f[0]))};
        asm volatile("s_waitcnt lgkmcnt(0)" : "+v"(fs[0]), "+v"(fs[1]), "+v"(fs[2]), "+v"(fs[3]) :: "memory");
    };
#define HERE unsigned l = lane, w = wave, n = sb; asm volatile("" : "+v"(l)); asm volatile("" : "+s"(w), "+s"(n));
    auto ev_a = [&]() {
        HERE
        const unsigned t = (((l >> 5) * nb + n) * LS + (l & 31) * 16) | ((l & 31) + 8) << 26 & 0x80000000u;
        eo[0] = t + 6 * w * nb * LS; eo[1] = eo[0] + 2 * nb * LS; eo[2] = eo[0] + 4 * nb * LS;
        eo[3] = t + (24 + 2 * w) * nb * LS;
        asm volatile("" : "+v"(eo[0]), "+v"(eo[1]), "+v"(eo[2]), "+v"(eo[3]));
    };
    auto ev_a2 = [&]() {
        HERE
        eo[4] = (((H - 1) * nb + n) * LS + l * 8) | l << 31 | (l + 16) << 25 & 0x80000000u;
        eo[3] = w == 3 ? eo[4] - nb * LS : eo[3];
        ea = lds0 + LDS_REC + (6 * w + (l >> 5)) * (DH * 4) + (l & 31) * 16;
        asm volatile("" : "+v"(eo[3]), "+v"(eo[4]), "+v"(ea));
    };
    auto ev_b = [&]() {
        HERE
        xo[0] = (hc * nb + 8 * w + (l >> 3)) * LS + (n >> 5) * 128 + (l & 7) * 16;
#pragma unroll
        for (int i = 1; i < 8; ++i) xo[i] = xo[0] + 32 * i * LS;
        asm volatile("" : "+v"(xo[0]), "+v"(xo[1]), "+v"(xo[2]), "+v"(xo[3]), "+v"(xo[4]), "+v"(xo[5]), "+v"(xo[6]), "+v"(xo[7]));
    };
    auto ev_c = [&]() {
        HERE
        oo = (hc * DH + (n >> 5) * 64 + 8 * (l & 7) + (l >> 3)) * 2;
        po = lds0 + LDS_QRAW + l * 4; wo = po + w * 256;
        asm volatile("" : "+v"(oo), "+v"(po), "+v"(wo));
    };
    auto ev_c2 = [&]() {
        HERE
        const unsigned t = tag >> 6 * (l & 7);
        ta = (t & 63) * 0x2001 & c7; tb = (t >> 6 & 63) * 0x2001 & c7; tc = (t >> 12 & 63) * 0x2001 & c7;
        asm volatile("" : "+v"(ta), "+v"(tb), "+v"(tc));
    };
    auto ev_d = [&]() {
        HERE
        ev_all(24);
        asm volatile("ds_read_b128 %0, %5\n\tds_read_b128 %1, %5 offset:1536\n\tds_read_b128 %2, %5 offset:3072\n\tds_read_b128 %3, %6\n\tds_read_b128 %4, %6 offset:16\n\ts_waitcnt lgkmcnt(0)"
                     : "+v"(ev[0]), "+v"(ev[1]), "+v"(ev[2]), "+v"(a0), "+v"(a1) : "v"(ea), "v"(lds0 + LDS_QRAW + 1024) : "memory");
        ea += (24 - 4 * w) * (DH * 4);
        asm volatile("" : "+v"(ea));
        if (4 * n + w == 1) { asm volatile("s_mov_b64 exec, 1\n\tbuffer_store_dwordx2 %0, off, %1, 0 offset:%2 sc1\n\ts_mov_b64 exec, -1" :: "v"(u32x2{(unsigned)tag, t32}), "s"(pr), "i"(pep) : "memory"); quiet(); }
    };
    auto ev_e = [&]() {
        const float M = fmaxf(fmaxf(a0[0], a0[2]), fmaxf(a1[0], a1[2]));
        const float gf = __builtin_amdgcn_exp2f(mw - M) * __builtin_amdgcn_rcpf((__builtin_amdgcn_exp2f(a0[0] - M) * a0[1] + __builtin_amdgcn_exp2f(a0[2] - M) * a0[3]) + (__builtin_amdgcn_exp2f(a1[0] - M) * a1[1] + __builtin_amdgcn_exp2f(a1[2] - M) * a1[3]));
#pragma unroll
        for (int i = 0; i < 4; ++i) fs[i] *= gf;
        asm volatile("" : "+v"(fs[0]), "+v"(fs[1]), "+v"(fs[2]), "+v"(fs[3]));
    };
    constexpr int G1 = 5, G2 = 27;
    const int hput = wave == ((b >> 5) & 3) ? G1 + 2 + ((b >> 3) & 3) : -9, hget = cn < NC ? G1 + 7 + ((cn >> 3) & 7) : -9;
    unsigned ev1 = 2 | 1 << G1 | 15 << (G1 + 2) | 9u << (hget & 31) | 1u << (H - 1), ev2 = 2 | 3 << G1 | 4u << (hget & 31);
    asm volatile("" : "+s"(ev1), "+s"(ev2), "+s"(area));
    float pv = *reinterpret_cast<const float*>(prec);
    f32x2 acc[3][2];
    float y, yn, zn;
    auto prep = [&](int hn) {
        const float s = bflo(sc);
        yn = s * (pv * __uint_as_float(__builtin_amdgcn_readlane(__float_as_uint(gw), hn)));
        float z = yn * dpp<0x55>(s);
        z += dpp<0x124>(z);
        z += dpp<0x128>(z);
        zn = -dpp<0x150>(z);
        asm volatile("" : "+v"(yn), "+v"(zn) :: "memory");
        SB();
        scp += 128;
        sc = *reinterpret_cast<const unsigned short*>(scp);
        prec += 128;
        pv = *reinterpret_cast<const float*>(prec);
        SB();
    };
    prep(0);
#define VCOMP(k) VPART(k, 0, 3)
#define VPART(k, x0, x1) { \
        if ((k) == 0) y = yn; \
        const f32x2 pz = f32x2{dpp<0x150 + 4 * (k)>(y), zn}; \
        _Pragma("unroll") for (int x = x0; x < x1; ++x) \
            _Pragma("unroll") for (int e = 0; e < 2; ++e) { \
                const f32x2 vf = f32x2{(float)buf[k].d[x][2 * e], (float)buf[k].d[x][2 * e + 1]}; \
                if ((k) == 0) asm volatile("v_pk_fma_f32 %0, %1, %2, %1 op_sel:[0,0,1] op_sel_hi:[0,1,1]" : "=v"(acc[x][e]) : "v"(pz), "v"(vf)); \
                else asm volatile("v_pk_fma_f32 %0, %1, %2, %0 op_sel_hi:[0,1,1]" : "+v"(acc[x][e]) : "v"(pz), "v"(vf)); \
            } \
        asm volatile("" ::: "memory"); \
        SB(); }
#define ON(hn, x, f) { int q = (hn) - (x); asm volatile("" : "+s"(q)); if (__builtin_expect(q == 0, 0)) f(); }
#define VSTEP(k, hn) { \
        __builtin_amdgcn_s_waitcnt(0xf79); SB();        \
        VCOMP(k) pace(); issue(buf[k], k, vr, nso); SB(); steer(true, k); SB(); \
        if ((k) == 1) { \
            vth = TH; asm volatile("" : "+s"(vth)); \
            if (__builtin_expect(ev1 >> (hn) & 1, 0)) { \
                if ((hn) == 1) { softmax1(); softmax2(); }        \
                if ((hn) == G1) ev_all(0);        \
                if ((hn) == hput) { ev_put(); quiet(); } \
                if ((hn) == hget) { ev_get(); quiet(); } \
                if ((hn) == hget + 3) ev_w2(); \
                if ((hn) == H - 1) tmin = 0; \
            } \
            SB(); \
            if (__builtin_expect((hn) >= G2 - 4, 0)) { ON(hn, G2 - 4, ev_a) ON(hn, G2 - 3, ev_a2) ON(hn, G2 - 2, ev_b) ON(hn, G2 - 1, ev_c) ON(hn, G2, ev_d) ON(hn, G2 + 1, ev_e) ON(hn, G2 + 2, ev_c2) } \
            SB(); \
        } \
        if ((k) == 2) { \
            if (__builtin_expect(ev2 >> (hn) & 1, 0)) { \
                ON(hn, 1, ev_s) \
                if ((hn) == G1) ev_ml(); \
                if ((hn) == G1 + 1) ev_area(); \
                if ((hn) == hget + 2) ev_w1(); \
            } \
            SB(); \
            prep(hn); \
        } }
    unsigned ra = lds0 + LDS_REC + wave * RECW + (4 * c + r) * 4, ca = lds0 + LDS_CNT, one = 1;
    asm volatile("" : "+v"(ra), "+v"(ca), "+v"(one));
    auto record = [&]() {
        float w[3];
        f32x2 u[3];
#pragma unroll
        for (int x = 0; x < 3; ++x) { swap32(acc[x][0], acc[x][1]); SB(); }
#pragma unroll
        for (int x = 0; x < 3; ++x) { u[x] = acc[x][0] + acc[x][1]; SB(); }
#pragma unroll
        for (int x = 0; x < 3; ++x) {
            auto t = __builtin_amdgcn_permlane16_swap(__float_as_uint(u[x][0]), __float_as_uint(u[x][1]), false, false);
            u[x] = f32x2{__uint_as_float(t[0]), __uint_as_float(t[1])};
            SB();
        }
#pragma unroll
        for (int x = 0; x < 3; ++x) w[x] = u[x][0] + u[x][1];
        asm volatile("ds_write2st64_b32 %1, %2, %3 offset1:1\n\tds_write_b32 %1, %4 offset:512\n\ts_mov_b64 exec, 1\n\tds_add_rtn_u32 %0, %5, %6\n\ts_mov_b64 exec, -1"
                     : "+v"(old) : "v"(ra), "v"(w[0]), "v"(w[1]), "v"(w[2]), "v"(ca), "v"(one) : "memory");
        SB();
    };
#pragma unroll 1
    for (int h = 0; h < H - 1; ++h) {
        nso += hstride;
        VSTEP(0, h + 1)
        asm volatile("s_waitcnt lgkmcnt(0)" : "+v"(old) :: "memory");
        if (__builtin_amdgcn_readfirstlane(old) == 3) *reinterpret_cast<u32x2*>(smem + LDS_REC + (h - 1) * (DH * 4) + lane * 8) = __builtin_expect(h > G1, 1) ? sum4((h - 1) * (DH * 4)) : merge(h - 1);
        SB();
        VSTEP(1, h + 1) VSTEP(2, h + 1) VSTEP(3, h + 1)
        record();
        ra += DH * 4; ca += 4;
        asm volatile("" : "+v"(ra), "+v"(ca));
    }
    if (DBG == 4 && (F >> 13) == 5) { tbar = now32(); asm volatile("" : "+s"(tbar)); }
    asm volatile("s_branch .Lkv_t%c0\n\t.p2align 12,,3327\n.Lkv_t%c0:" :: "i"(ID));
    if (DBG == 4 && (F >> 13) == 4) { tbar = now32(); asm volatile("" : "+s"(tbar)); }
    VCOMP(0)
    asm volatile("s_waitcnt lgkmcnt(0)\n\ts_barrier" ::: "memory");
    if (DBG == 4 && (F >> 13) == 3) { tbar = now32(); asm volatile("" : "+s"(tbar)); }
    f32x4 p[4];
    asm volatile(LDREC(30) "\n\tds_read_b128 %6, %7" : "=&v"(p[0]), "=&v"(p[1]), "=&v"(p[2]), "=&v"(p[3]), "+v"(rl), "+v"(rh), "=&v"(ev[3]) : "v"(ea) : "memory");
    SB();
    VCOMP(1)
    asm volatile("s_waitcnt lgkmcnt(0)" : "+v"(p[0]), "+v"(p[1]), "+v"(p[2]), "+v"(p[3]), "+v"(ev[3]) :: "memory");
    if (wave == 3) ev[3] = wide(add4(p));
    int lx = hc == H - 1 ? ((SL >> 24) & 15) * ((SL >> 16) & 255) : 0;
    asm volatile("" : "+s"(lx));
    unsigned tx = txb - sched, tgt = tgb - sched;
    asm volatile("" : "+v"(ev[3]));
    asm volatile("" : "+s"(tx), "+s"(tgt), "+s"(area));
    SB();
    VCOMP(2)
    if (DBG == 4 && (F >> 13) == 1) { tbar = now32(); asm volatile("" : "+s"(tbar)); }
    auto until = [&](unsigned t) {
        int d;
        asm volatile("1:\ts_memrealtime vcc\n\ts_waitcnt lgkmcnt(0)\n\ts_lshl4_add_u32 %0, vcc_lo, %1\n\ts_cmp_lt_i32 %0, 0\n\ts_cbranch_scc1 1b" : "=&s"(d) : "s"(t) : "memory", "vcc", "scc");
        return d;
    };
    const rsrc_t orr = mkrsrc(a.out, H * DH * 2);
    asm volatile("" :: "s"(orr), "s"(pr));
    until(tx);
    constexpr int XP = (F & 4096) ? ((F & 4) ? 3 : 2) : 0;
    VPART(3, 0, XP)
    if ((F & 256) && XP < 3) asm volatile("" : "+v"(buf[3].d[2]));
    SB();
#pragma unroll
    for (int i = 0; i < 4; ++i) { __builtin_amdgcn_raw_buffer_store_b128(ev[i], pr, eo[i], area, 16); SB(); }
    if (DBG == 4 && !(F >> 13)) tbar = now32();
    SB();
    VPART(3, XP, 3)
    if (DBG == 4 && (F >> 13) == 6) { tbar = now32(); asm volatile("" : "+s"(tbar)); }
    record();
    asm volatile(LDREC(31) "\n\ts_waitcnt lgkmcnt(4)" : "=&v"(p[0]), "=&v"(p[1]), "=&v"(p[2]), "=&v"(p[3]), "+v"(rl), "+v"(rh), "+v"(old) :: "memory");
    if (F & 2048) {
        int last = __builtin_amdgcn_readfirstlane(old);
        asm volatile("" : "+s"(last));
        asm volatile("s_waitcnt lgkmcnt(0)" : "+v"(p[0]), "+v"(p[1]), "+v"(p[2]), "+v"(p[3]) :: "memory");
        const u32x4 t = wide(add4(p));
        asm volatile("s_cmp_eq_u32 %4, 3\n\ts_cbranch_scc0 1f\n\tbuffer_store_dwordx4 %0, %1, %2, %3 offen sc1\n1:\ts_nop 1" :: "v"(t), "v"(eo[4]), "s"(pr), "s"(area), "s"(last) : "memory", "scc");
    } else {
    const bool last = __builtin_amdgcn_readfirstlane(old) == 3;
    if (__builtin_expect(last, 1)) {
        asm volatile("s_waitcnt lgkmcnt(0)" : "+v"(p[0]), "+v"(p[1]), "+v"(p[2]), "+v"(p[3]) :: "memory");
        __builtin_amdgcn_raw_buffer_store_b128(wide(add4(p)), pr, eo[4], area, 16);
    }
    asm volatile("s_waitcnt lgkmcnt(0)" :: "v"(p[0]), "v"(p[1]), "v"(p[2]), "v"(p[3]) : "memory");
    }
    unsigned tend = DS ? now32() : 0;
    unsigned tfl = 0, tpl = 0;

    if (cn < NC) {
        i32x4 x[8];
        float o[8];
        int cm = -138, kd = 0x200004, rounds = 0;
        unsigned bad, mk;
        asm volatile("" : "+v"(cm));
        asm volatile("" : "+s"(kd), "+s"(rounds));
        int late = until(tgt) + lx;
        if (DS) tfl = now32();
        if (DBG == 4) th4 = 0;
#define XL(i) x[i] = bld128<16 + (SL >> 28)>(pr, xo[i], area); SB();
#define XS(i) XL(i) asm volatile("s_cmp_lt_i32 %0, %1\n\ts_cbranch_scc0 1f\n\ts_sleep %2\n1:" :: "s"(late), "i"(((SL >> 16) & 255) * ((i) + 1)), "i"((SL >> (2 * (i))) & 3) : "scc"); SB();
#pragma unroll 1
        do {
            XS(0) XS(1) XS(2) XS(3) XS(4) XS(5) XS(6) XL(7)
            if (DBG == 4 && !th4) th4 = now32();
            late = 1 << 30;
            mk = ++rounds < (1 << 20) ? 0x70007 : 0;
            asm volatile("" : "+s"(mk), "+s"(late), "+s"(rounds));
            SB();
#pragma unroll
            for (int i = 0; i < 8; ++i) {
                float w;
                int x0, x1, x2, x3;
#define MIX(a, b, x, z) "\n\tv_fma_mix_f32 %" #a ", %9, %" #x ", " z(a) " op_sel_hi:[0,1,0]\n\tv_fma_mix_f32 %" #b ", %9, %" #x ", " z(b) " op_sel:[0,1,0] op_sel_hi:[0,1,0]"
#define ROW(z) "v_and_b32 %9, 0x70007, %23\n\tv_xor_b32 %13, %23, %9\n\tv_dot2_i32_i16 %9, %9, %18, %19\n\tv_xor_b32 %10, %20, %15\n\tv_xor_b32 %11, %21, %16\n\tv_xor_b32 %12, %22, %17\n\t" \
               "v_ldexp_f32 %9, %14, %9\n\tv_or3_b32 %8, " z(8) ", %10, %11" MIX(0, 1, 10, z) MIX(2, 3, 11, z) "\n\tv_or_b32 %8, %8, %12" MIX(4, 5, 12, z) MIX(6, 7, 13, z)
#define ZERO(a) "0"
#define SAME(a) "%" #a
                if (i == 0) asm volatile(ROW(ZERO) : "=&v"(o[0]), "=&v"(o[1]), "=&v"(o[2]), "=&v"(o[3]), "=&v"(o[4]), "=&v"(o[5]), "=&v"(o[6]), "=&v"(o[7]), "=&v"(bad), "=&v"(w),
                                           "=&v"(x0), "=&v"(x1), "=&v"(x2), "=&v"(x3) : "v"(fs[i >> 1][i & 1]), "v"(ta), "v"(tb), "v"(tc), "s"(kd), "v"(cm),
                                    "v"(x[i][0]), "v"(x[i][1]), "v"(x[i][2]), "v"(x[i][3]));
                else asm volatile(ROW(SAME) : "+v"(o[0]), "+v"(o[1]), "+v"(o[2]), "+v"(o[3]), "+v"(o[4]), "+v"(o[5]), "+v"(o[6]), "+v"(o[7]), "+v"(bad), "=&v"(w),
                                    "=&v"(x0), "=&v"(x1), "=&v"(x2), "=&v"(x3) : "v"(fs[i >> 1][i & 1]), "v"(ta), "v"(tb), "v"(tc), "s"(kd), "v"(cm),
                                    "v"(x[i][0]), "v"(x[i][1]), "v"(x[i][2]), "v"(x[i][3]));
                if (DBG == 6 && __any(((x0 | x1 | x2) & 0x70007) != 0) && !th1) th4 |= 1 << i;
                SB();
            }
            if (DBG == 6) th1 = 1;
        } while (__builtin_expect(__builtin_amdgcn_ballot_w64((bad & mk) != 0) != 0, 0));
        if (DS) tpl = now32();
        f32x2 u0 = f32x2{o[0], o[1]}, u1 = f32x2{o[2], o[3]}, u2 = f32x2{o[4], o[5]}, u3 = f32x2{o[6], o[7]};
        swap32(u0, u2); swap32(u1, u3);
        SB();
        u0 += u2; u1 += u3;
        swap16(u0, u1);
        u0 += u1;
        f32x2 p0, p1;
        asm volatile("s_nop 0\n\ts_nop 0\n\tv_add_f32_dpp %3, %3, %3 row_ror:8 row_mask:0xf bank_mask:0x3\n\tv_add_f32_dpp %3, %4, %4 row_ror:8 row_mask:0xf bank_mask:0xc\n\t"
                     "ds_write_b32 %5, %3\n\ts_mov_b64 exec, 1\n\tds_add_rtn_u32 %2, %7, %8 offset:20\n\ts_mov_b64 exec, -1\n\t"
                     "ds_read2st64_b32 %0, %6 offset1:1\n\tds_read2st64_b32 %1, %6 offset0:2 offset1:3\n\ts_waitcnt lgkmcnt(2)"
                     : "=&v"(p0), "=&v"(p1), "+v"(old), "+v"(u0[0]) : "v"(u0[1]), "v"(wo), "v"(po), "v"(ca), "v"(one) : "memory");
        if (F & 2048) {
            int last = __builtin_amdgcn_readfirstlane(old);
            asm volatile("" : "+s"(last));
            asm volatile("s_waitcnt lgkmcnt(0)" : "+v"(p0), "+v"(p1) :: "memory");
            p0 += p1;
            const float t = p0[0] + p0[1];
            asm volatile("s_cmp_eq_u32 %3, 3\n\ts_cbranch_scc0 1f\n\tbuffer_store_short %0, %1, %2, 0 offen\n1:" :: "v"(pk_bf16(t, t)), "v"(oo), "s"(orr), "s"(last) : "memory", "scc");
        } else
        if (__builtin_expect(__builtin_amdgcn_readfirstlane(old) == 3, 1)) {
            asm volatile("s_waitcnt lgkmcnt(0)" : "+v"(p0), "+v"(p1) :: "memory");
            p0 += p1;
            const float t = p0[0] + p0[1];
            __builtin_amdgcn_raw_buffer_store_b16((unsigned short)pk_bf16(t, t), orr, oo, 0, 0);
        }
        asm volatile(".Lkv_b%c0:" :: "i"(ID));
    }
    if (DBG) {
        const unsigned tc1 = now32();
        const unsigned fm = th4;
        if (!DS) tend = tbar = th4 = tfl = tpl = tc1;
        if (DBG == 6) tfl = tpl = fm;
        if (DBG == 5) tbar = tdma;
        const unsigned tl16 = sched - nb16 - stag - step;
        u64* stp = reinterpret_cast<u64*>(a.part + STAMP_OFF) + (size_t)((DBG == 4 || DBG == 24 || DBG == 6 ? cn : b) * 4 + wave) * 8;

        if (lane == 0) { stp[0] = tstart; stp[1] = tbar; stp[2] = DBG >= 4 ? tend - (((tend << 4) - tl16) >> 4) : th1; stp[3] = DBG == 4 && cn >= NC ? tend : th4; stp[4] = tend; stp[5] = cn < NC || DBG == 6 ? tfl : tend; stp[6] = cn < NC || DBG == 6 ? tpl : tend; stp[7] = tc1; }
        if (DBG == 2) *reinterpret_cast<u32x4*>(a.part + STAMP_OFF + 65536 + (b * 4 + wave) * 1024 + lane * 16) = *reinterpret_cast<const u32x4*>(smem + LDS_TR + 16 + wave * 1024 + lane * 16);
    }
}

template <int TP, int ADV, int PX, int TMIN, int CAP, int HD, int RA, int SKL, int FC, int F, int EX, int GS, int HOLD, int HL, int SL, int ID, int DBG>
static void launch87(void* const* d_in, const int* in_sizes, void* d_out, void* d_ws, hipStream_t stream) {
    typedef const unsigned short* P;
    Args a;
    a.qk = (const int*)d_in[1]; a.qv = (const int*)d_in[4];
    a.k_cos = (P)d_in[9]; a.k_sin = (P)d_in[10];
    a.q = (P)d_in[0]; a.q_cos = (P)d_in[7]; a.q_sin = (P)d_in[8];
    a.sc[0] = (P)d_in[2]; a.sc[1] = (P)d_in[3]; a.sc[2] = (P)d_in[5]; a.sc[3] = (P)d_in[6];
    a.part = (unsigned char*)d_ws;
    a.out = (unsigned short*)d_out;
    kv87_kernel<TP, ADV, PX, TMIN, CAP, HD, RA, SKL, FC, F, EX, GS, HOLD, HL, SL, ID, DBG><<<Skv / RW, 256, 0, stream>>>(a);
}

extern "C" void kernel_launch(void* const* d_in, const int* in_sizes, int n_in, void* d_out, int out_size, void* d_ws, size_t ws_size, hipStream_t stream) {
    launch87<79*16+8,220,40,78*16+2,28,70,240,24,32+256*50+65536*3+(15<<20),257,56,2,66,84,0x2aaa+(90<<16)+(2<<24),0,0>(d_in, in_sizes, d_out, d_ws, stream);
}
```

```cpp
#include <hip/hip_runtime.h>
#include <hip/hip_bf16.h>
#include <cstdint>
#define KALIGN 256
#define STAMP_OFF (200 << 20)
using bf16 = __hip_bfloat16;
using i32x4 = __attribute__((ext_vector_type(4))) int;
using u32x4 = __attribute__((ext_vector_type(4))) unsigned;
using u32x2 = __attribute__((ext_vector_type(2))) unsigned;
using f32x2 = __attribute__((ext_vector_type(2))) float;
using f32x4 = __attribute__((ext_vector_type(4))) float;

constexpr int DH = 192;
constexpr int HALF = 96;
constexpr int RW = 64;
constexpr int MAXH = 32;

__device__ __forceinline__ float bflo(unsigned u) { return __uint_as_float(u << 16); }
__device__ __forceinline__ float bfhi(unsigned u) { return __uint_as_float(u & 0xffff0000u); }
__device__ __forceinline__ float rbf(float x) { return __bfloat162float(__float2bfloat16(x)); }

template <int CTRL> __device__ __forceinline__ float dpp(float x) {
    return __builtin_bit_cast(float, __builtin_amdgcn_mov_dpp(__builtin_bit_cast(int, x), CTRL, 0xf, 0xf, true));
}
__device__ __forceinline__ float row16_allsum(float x) {
    x += dpp<0x128>(x);
    x += dpp<0x124>(x);
    x += dpp<0x122>(x);
    x += dpp<0x121>(x);
    return x;
}
__device__ __forceinline__ float row16_allmax(float x) {
    x = fmaxf(x, dpp<0x128>(x));
    x = fmaxf(x, dpp<0x124>(x));
    x = fmaxf(x, dpp<0x122>(x));
    x = fmaxf(x, dpp<0x121>(x));
    return x;
}
__device__ __forceinline__ float xrow_max(float x) {
    auto s = __builtin_amdgcn_permlane16_swap(__float_as_uint(x), __float_as_uint(x), false, false);
    x = fmaxf(__uint_as_float(s[0]), __uint_as_float(s[1]));
    auto t = __builtin_amdgcn_permlane32_swap(__float_as_uint(x), __float_as_uint(x), false, false);
    return fmaxf(__uint_as_float(t[0]), __uint_as_float(t[1]));
}
__device__ __forceinline__ float xrow_sum(float x) {
    auto s = __builtin_amdgcn_permlane16_swap(__float_as_uint(x), __float_as_uint(x), false, false);
    x = __uint_as_float(s[0]) + __uint_as_float(s[1]);
    auto t = __builtin_amdgcn_permlane32_swap(__float_as_uint(x), __float_as_uint(x), false, false);
    return __uint_as_float(t[0]) + __uint_as_float(t[1]);
}
__device__ __forceinline__ float sw32(float a, float b) {
    auto t = __builtin_amdgcn_permlane32_swap(__float_as_uint(a), __float_as_uint(b), false, false);
    return __uint_as_float(t[0]) + __uint_as_float(t[1]);
}
__device__ __forceinline__ float sw16(float a, float b) {
    auto t = __builtin_amdgcn_permlane16_swap(__float_as_uint(a), __float_as_uint(b), false, false);
    return __uint_as_float(t[0]) + __uint_as_float(t[1]);
}

__device__ __forceinline__ void swap32(f32x2& a, f32x2& b) {
    auto s = __builtin_amdgcn_permlane32_swap(__float_as_uint(a[0]), __float_as_uint(b[0]), false, false);
    auto t = __builtin_amdgcn_permlane32_swap(__float_as_uint(a[1]), __float_as_uint(b[1]), false, false);
    a = f32x2{__uint_as_float(s[0]), __uint_as_float(t[0])}; b = f32x2{__uint_as_float(s[1]), __uint_as_float(t[1])};
}
__device__ __forceinline__ void swap16(f32x2& a, f32x2& b) {
    auto s = __builtin_amdgcn_permlane16_swap(__float_as_uint(a[0]), __float_as_uint(b[0]), false, false);
    auto t = __builtin_amdgcn_permlane16_swap(__float_as_uint(a[1]), __float_as_uint(b[1]), false, false);
    a = f32x2{__uint_as_float(s[0]), __uint_as_float(t[0])}; b = f32x2{__uint_as_float(s[1]), __uint_as_float(t[1])};
}
#define SB() __builtin_amdgcn_sched_barrier(0)
typedef __amdgpu_buffer_rsrc_t rsrc_t;
__device__ __forceinline__ rsrc_t mkrsrc(const void* p, unsigned bytes) {
    return __builtin_amdgcn_make_buffer_rsrc(const_cast<void*>(p), 0, bytes, 0x00020000);
}
template <int AUX> __device__ __forceinline__ i32x4 bld128(rsrc_t r, unsigned voff, unsigned soff) {
    return __builtin_bit_cast(i32x4, __builtin_amdgcn_raw_buffer_load_b128(r, voff, soff, AUX));
}
template <int AUX> __device__ __forceinline__ unsigned bld16(rsrc_t r, unsigned voff, unsigned soff) {
    return __builtin_amdgcn_raw_buffer_load_b16(r, voff, soff, AUX);
}

struct HG { i32x4 d[3]; };
typedef unsigned long long u64;
typedef __attribute__((address_space(3))) unsigned char lds_u8;

__device__ __forceinline__ unsigned pk_bf16(float lo, float hi) {
    unsigned r;
    asm("v_cvt_pk_bf16_f32 %0, %1, %2" : "=v"(r) : "v"(lo), "v"(hi));
    return r;
}
__device__ __forceinline__ unsigned now32() { return (unsigned)__builtin_amdgcn_s_memrealtime(); }

constexpr int LDS_REC = 0;
constexpr int RECW = MAXH * DH * 4;
constexpr int LDS_CS = 0;
constexpr int LDS_QT = 12288;
constexpr int LDS_PF = 14336;
constexpr int LDS_QRAW = MAXH * 4 * DH * 4;
constexpr int LDS_Q = LDS_QRAW + MAXH * DH * 2;
constexpr int LDS_P = LDS_Q + 4 * DH * 4;
constexpr int LDS_SC = LDS_P + 4 * MAXH * 128;
constexpr int LDS_CNT = LDS_SC + 4 * MAXH * RW * 2;
constexpr int LDS_SK = LDS_CNT + 256;
constexpr int LDS_TR = LDS_SK + 1024;
constexpr int LDS_BYTES = LDS_TR + 4096 + 64;

struct Args {
    const int* qk; const int* qv;
    const unsigned short* k_cos; const unsigned short* k_sin;
    const unsigned short* q; const unsigned short* q_cos; const unsigned short* q_sin;
    unsigned char* part;
    unsigned short* out;
    const unsigned short* sc[4];
};
constexpr unsigned Skv = 16384;
constexpr float SCALE_LOG2 = 0.10411754627697264f;

template <int TP, int ADV, int PX, int TMIN, int CAP, int HD, int RA, int SKL, int FC, int F, int EX, int GS, int HOLD, int HL, int SL, int ID, int DBG>
__global__ __launch_bounds__(256) __attribute__((aligned(KALIGN))) void kv90_kernel(Args a)
{
    __shared__ __attribute__((aligned(16))) unsigned char smem[LDS_BYTES];
    const unsigned tstart = DBG ? now32() : 0;
    constexpr bool DS = (DBG && DBG < 5) || DBG == 24;
    constexpr int H = MAXH, PFN = 16, SJ = (F >> 9) & 3;
    const int b = blockIdx.x;
    const int tid = threadIdx.x;
    const int lane = tid & 63;
    const int wave = __builtin_amdgcn_readfirstlane(tid >> 6);
    typedef const unsigned short* const __attribute__((address_space(4))) * karg_t;
    const unsigned short* sp = ((karg_t)__builtin_amdgcn_kernarg_segment_ptr())[offsetof(Args, sc) / 8 + wave];
    if (b < PFN) {
        u64 pc; unsigned len, tl;
        asm volatile("s_getpc_b64 %0\n.Lkv_a%c3:\n\ts_mov_b32 %1, .Lkv_b%c3-.Lkv_a%c3-16\n\ts_mov_b32 %2, .Lkv_t%c3-.Lkv_a%c3" : "=s"(pc), "=s"(len), "=s"(tl) : "i"(ID));
        const unsigned o = tid * 16;
        asm volatile("s_mov_b32 m0, %5\n\ts_nop 0\n\tglobal_load_lds_dwordx4 %0, %4\n\tglobal_load_lds_dwordx4 %1, %4\n\tglobal_load_lds_dwordx4 %2, %4\n\tglobal_load_lds_dwordx4 %3, %4"
                     :: "v"(o), "v"(o + 4096), "v"(min(o + 8192, tl)), "v"(min(o + min(tl, 12288u), len)), "s"(pc), "s"((unsigned)(size_t)(lds_u8*)smem + LDS_PF) : "memory");
    }
    SB();
    const int r = lane >> 4;
    const int c = lane & 15;
    constexpr unsigned hstride = Skv * (DH * 4);
    constexpr unsigned nb = Skv / RW;
    const unsigned lds0 = (unsigned)(size_t)(lds_u8*)smem;
    const unsigned sb = b;
    const unsigned rowb = (b & 7) * 2048 + (b & 24) * 64 + (b >> 5) * 16;
    const unsigned row0 = rowb + 4 * wave;

    constexpr unsigned LS = 512, psk = 0, pep = 128, AREA = 6u << 20, NAREA = 32, pfl = H * nb * LS;
    const rsrc_t pr = mkrsrc(a.part, (NAREA + 1) * AREA);
    unsigned vg = ((lane >> 3) * Skv + rowb + (lane & 6) * 64 + (lane & 1) * 8) * 2, dg = lds0 + LDS_SC + wave * 4096;
    u64 t64;
    {
        unsigned vs = ((lane >> 3) * Skv + (rowb & ~63u) + (b & 96) * 4 + (lane & 7) * 8) * 2, v1 = vs + 16 * Skv, v2 = vs + 32 * Skv, v3 = vs + 48 * Skv;
        if (!SJ && wave < 2) vs = vg;
        asm volatile("" : "+v"(vs), "+v"(v1), "+v"(v2), "+v"(v3), "+s"(dg));
        SB();
        asm volatile("s_memrealtime %0\n\ts_mov_b32 m0, %6\n\ts_nop 0\n\tglobal_load_lds_dwordx4 %1, %5\n\t"
                     "s_add_u32 m0, m0, 1024\n\ts_nop 0\n\tglobal_load_lds_dwordx4 %2, %5\n\t"
                     "s_add_u32 m0, m0, 1024\n\ts_nop 0\n\tglobal_load_lds_dwordx4 %3, %5\n\t"
                     "s_add_u32 m0, m0, 1024\n\ts_nop 0\n\tglobal_load_lds_dwordx4 %4, %5\n\ts_waitcnt lgkmcnt(0)"
                     : "=&s"(t64) : "v"(vs), "v"(v1), "v"(v2), "v"(v3), "s"(sp), "s"(dg),
                       "s"(a.qk), "s"(a.qv), "s"(a.k_cos), "s"(a.k_sin), "s"(a.q), "s"(a.q_cos), "s"(a.q_sin), "s"(a.part), "s"(a.out) : "memory", "scc");
    }
    auto gather = [&]() {
        if (F & 2) asm volatile("s_mov_b32 m0, %2\n\ts_nop 0\n\tglobal_load_lds_dwordx4 %0, %1 nt" :: "v"(vg), "s"(sp), "s"(dg) : "memory");
        else asm volatile("s_mov_b32 m0, %2\n\ts_nop 0\n\tglobal_load_lds_dwordx4 %0, %1" :: "v"(vg), "s"(sp), "s"(dg) : "memory");
        vg += 16 * Skv; dg += 1024;
    };
    SB();
    const unsigned tdma = (unsigned)t64;
#define DMA3(V, A, B, P) "s_mov_b32 m0, " B "\n\ts_nop 0\n\t" \
                     "global_load_lds_dwordx4 " V ", " A P "\n\t" \
                     "global_load_lds_dwordx4 " V ", " A " offset:1024" P "\n\t" \
                     "global_load_lds_dwordx4 " V ", " A " offset:2048" P "\n\t"
    asm volatile(DMA3("%0", "%1", "%2", "") "s_mov_b32 m0, %4\n\ts_nop 0\n\tglobal_load_lds_dwordx4 %3, off"
                 :: "v"(lane * 16), "s"(a.q + wave * 1536), "s"(lds0 + LDS_QRAW + wave * 3072),
                    "v"(lane < 48 ? (const unsigned char*)((lane < 24 ? a.q_cos : a.q_sin - DH) + 8 * lane) : a.part + pep), "s"(lds0 + LDS_QT + wave * RECW) : "memory");
    if ((b >> 4) == 1 && wave == 3)
        asm volatile("s_mov_b32 m0, %2\n\ts_mov_b64 exec, 15\n\tglobal_load_lds_dword %0, %1\n\ts_mov_b64 exec, -1" :: "v"((((tdma >> 13) & (NAREA - 1)) + 1) * AREA + lane * 0x160000 + b * 128), "s"(a.part), "s"(lds0 + LDS_PF) : "memory");
    SB();
    constexpr int TH = 4, DL = 32, BIG = 70;
    const int stag = (int)(((sb & ~7) + wave * 2 + (b & 1)) * (TP / 2)) / 256 - TP / 4;
    const unsigned nb16 = -(tdma << 4);
    int sched = ADV * 16 + stag - 4 * (TP / 2);
    int vt = 0;
    int calm = 600 * 16, hush = 0, step = TP / 2, tmin = TMIN / 2, vth = TH, pvm = 0;
    int tru = -4, slip = 0;
    int tgt0, tnow, vm, tf;
    auto pace = [&]() {
        if (DBG == 2 && (F & 16)) tf = (now32() << 4) + nb16;
        if (F & 1)
        asm volatile("s_max_i32 %0, %3, %4\n1:\ts_memrealtime vcc\n\ts_waitcnt lgkmcnt(0)\n\ts_lshl4_add_u32 %1, vcc_lo, %5\n\ts_cmp_lt_i32 %1, %0\n\ts_cbranch_scc1 1b\n\t"
                     "s_getreg_b32 %2, hwreg(HW_REG_IB_STS, 0, 4)" : "=&s"(tgt0), "=&s"(tnow), "=s"(vm) : "s"(sched), "s"(vt), "s"(nb16) : "scc", "vcc", "memory");
        else
        asm volatile("s_max_i32 %0, %3, %4\n\ts_branch 2f\n1:\ts_sleep 1\n2:\ts_memrealtime vcc\n\ts_waitcnt lgkmcnt(0)\n\ts_lshl4_add_u32 %1, vcc_lo, %5\n\ts_cmp_lt_i32 %1, %0\n\ts_cbranch_scc1 1b\n\t"
                     "s_getreg_b32 %2, hwreg(HW_REG_IB_STS, 0, 4)" : "=&s"(tgt0), "=&s"(tnow), "=s"(vm) : "s"(sched), "s"(vt), "s"(nb16) : "scc", "vcc", "memory");
    };
    int nre = 0;
    auto rebase = [&]() { sched += (RA & 1023) * 16; step -= RA >> 10; calm = tnow + 900 * 16; ++nre; if (DBG == 2) slip += (RA & 1023) * 16; };
    auto steer = [&](bool ctl, int k = 0) {
        sched += step;
        if (!ctl) vt = max(tnow - 128, tgt0) + tmin;
        else {
            vt = tgt0 + tmin;
            if (__builtin_expect(tnow - tgt0 > 128, 0)) {
                asm volatile("");
                vt = tnow - 128 + tmin;
                if (tnow - tgt0 > (tnow > hush ? HD & 255 : HD >> 8) * 16 && tnow > calm) {
                    asm volatile("");
                    rebase();
                    if (FC >> 24) asm volatile("ds_write_b32 %0, %1" :: "v"(lds0 + LDS_CNT + 148), "v"(nre) : "memory");
                }
            }
            if (__builtin_expect(vm > vth, 0)) {
                asm volatile("" : "+s"(pvm));
                if (vt - sched < CAP * 16 || (pvm > TH && vt - sched > BIG * 16)) vt += (vm - TH) * DL;
            }
            if (FC && (((FC >> 20) & 15) >> k & 1)) {
                if (__builtin_expect(vt - sched > ((FC >> 8) & 255) * 16, 0)) {
                    asm volatile("" : "+s"(pvm));
                    if (max(vm, pvm) <= ((FC >> 16) & 15)) vt -= FC & 255;
                    if ((FC >> 24) >> k & 1) {
                        int n;
                        asm volatile("ds_read_b32 %0, %1\n\ts_waitcnt lgkmcnt(0)" : "=v"(n) : "v"(lds0 + LDS_CNT + 148) : "memory");
                        if (__builtin_amdgcn_readfirstlane(n) > nre) rebase();
                    }
                }
            }
            pvm = vm;
            if ((F >> 5) & 7) asm volatile(".rept %0\n\ts_nop 7\n\t.endr" :: "i"(3 * ((F >> 5) & 7)));
        }
        if constexpr (DBG == 2) { *reinterpret_cast<unsigned*>(smem + LDS_TR + 16 + wave * 1024 + (tru & 255) * 4) = ((unsigned)(tnow - (sched - slip - step)) >> 4) | ((F & 8) ? vm << 16 : (F & 16) ? ((tnow - tf) >> 4) << 16 : 0); ++tru; }
    };
    {
        unsigned dst = lds0 + LDS_CS + wave * RECW;
        const unsigned v0 = lane * 16 + row0 * (DH * 2), v2 = v0 + 124 * DH * 2, v1 = lane < 32 ? v0 : v2;
#pragma unroll 1
        for (int j = 0; j < 4; ++j) {
            pace();
            asm volatile("s_mov_b32 m0, %4\n\ts_nop 0\n\tglobal_load_lds_dwordx4 %0, %3 nt\n\tglobal_load_lds_dwordx4 %1, %3 offset:1024 nt\n\tglobal_load_lds_dwordx4 %2, %3 offset:2048 nt"
                         :: "v"(v0), "v"(v1), "v"(v2), "s"((j < 2 ? a.k_cos : a.k_sin) + (j & 1) * 256 * DH), "s"(dst) : "memory");
            if (SJ && j == SJ - 1 && wave < 2) gather();
            steer(false);
            sched += PX;
            dst += 3072;
        }
    }
    if (!SJ && wave < 2) { vg += 16 * Skv; dg += 1024; }
    if (tid < MAXH + 6) *reinterpret_cast<unsigned*>(smem + LDS_CNT + tid * 4) = 0;
    const rsrc_t kr = mkrsrc(a.qk, H * hstride);
    const rsrc_t vr = mkrsrc(a.qv, H * hstride);
    unsigned kvo[4][3];
#pragma unroll
    for (int i = 0; i < 12; ++i) { kvo[i / 3][i % 3] = (row0 + r + i / 3 * 128) * (DH * 4) + c * 16 + i % 3 * 256; asm volatile("" : "+v"(kvo[i / 3][i % 3])); }
    auto issue = [&](HG& g, int k, rsrc_t rs, unsigned so) {
#pragma unroll
        for (int x = 0; x < 3; ++x) g.d[x] = bld128<2>(rs, kvo[k][x], so);
    };
    HG buf[4];
#define PRE(g) SB(); pace(); issue(buf[g], g, kr, 0); SB(); steer(false); SB();
    asm volatile("s_waitcnt vmcnt(%0) lgkmcnt(0)\n\ts_barrier" :: "i"(12 - 3 * SJ) : "memory");
    unsigned tbar = DS ? now32() : 0;
    PRE(0)
    const unsigned qoff = (lane < 48 ? lane : lane - 48) * 4;
    const unsigned* qt = reinterpret_cast<const unsigned*>(smem + LDS_QT + wave * RECW + qoff);
    unsigned qc0 = qt[0], qc1 = qt[HALF / 2], qs0 = qt[DH / 2], qs1 = qt[(DH + HALF) / 2];
    u64 tag;
    {
        const u32x2 e = *reinterpret_cast<const u32x2*>(smem + LDS_QT + wave * RECW + 768);
        tag = (((u64)__builtin_amdgcn_readfirstlane(e[1]) << 32) | __builtin_amdgcn_readfirstlane(e[0])) * 6364136223846793005ull + 1442695040888963407ull;
        asm volatile("" : "+s"(tag));
    }
    const unsigned t32 = tag >> 32;
    unsigned qraw = lds0 + LDS_QRAW + qoff;
    unsigned char* qrot = smem + LDS_Q + wave * (DH * 4) + 2 * qoff;
    unsigned q_lo, q_hi;
    auto getq = [&]() {
        asm volatile("" : "+v"(qraw));
        q_lo = *(const __attribute__((address_space(3))) unsigned*)(size_t)qraw, q_hi = *(const __attribute__((address_space(3))) unsigned*)(size_t)(qraw + HALF * 2);
        qraw += DH * 2;
    };
    auto rotq = [&]() {
        const f32x2 lo = f32x2{bflo(q_lo), bfhi(q_lo)}, hi = f32x2{bflo(q_hi), bfhi(q_hi)};
        const f32x2 y1 = lo * f32x2{bflo(qc0), bfhi(qc0)}, y2 = -hi * f32x2{bflo(qs0), bfhi(qs0)};
        const f32x2 y3 = hi * f32x2{bflo(qc1), bfhi(qc1)}, y4 = lo * f32x2{bflo(qs1), bfhi(qs1)};
        const unsigned x1 = pk_bf16(y1[0], y1[1]), x2 = pk_bf16(y2[0], y2[1]), x3 = pk_bf16(y3[0], y3[1]), x4 = pk_bf16(y4[0], y4[1]);
        const f32x2 z1 = f32x2{bflo(x1), bfhi(x1)} + f32x2{bflo(x2), bfhi(x2)}, z2 = f32x2{bflo(x3), bfhi(x3)} + f32x2{bflo(x4), bfhi(x4)};
        const unsigned l2 = pk_bf16(z1[0], z1[1]), h2 = pk_bf16(z2[0], z2[1]);
        *reinterpret_cast<f32x2*>(qrot) = f32x2{bflo(l2), bfhi(l2)};
        *reinterpret_cast<f32x2*>(qrot + HALF * 4) = f32x2{bflo(h2), bfhi(h2)};
        asm volatile("" ::: "memory");
    };
    f32x4 qa[3], qb[3];
    const unsigned char* qh = smem + LDS_Q + wave * (DH * 4) + 16 * c;
    const unsigned char* qh1 = qh + (c < 8 ? 64 + HALF : 64 - HALF) * 4;
    auto loadq = [&]() {
        asm volatile("" ::: "memory");
        qa[0] = *reinterpret_cast<const f32x4*>(qh); SB();
        qb[0] = *reinterpret_cast<const f32x4*>(qh + HALF * 4); SB();
        qa[1] = *reinterpret_cast<const f32x4*>(qh + 256); SB();
        qb[1] = *reinterpret_cast<const f32x4*>(qh1); SB();
        qa[2] = *reinterpret_cast<const f32x4*>(qh + 512); SB();
        qb[2] = *reinterpret_cast<const f32x4*>(qh + (128 - HALF) * 4);
        asm volatile("" ::: "memory");
    };
    getq(); rotq();
    if ((b >> 3) == 4 && wave == 3) __builtin_amdgcn_raw_buffer_store_b128(u32x4{tdma, t32, tdma, (unsigned)tag}, pr, lane == 0 ? psk + (b & 7) * 16 : 0x80000000u, 0, 16);
    PRE(1)
    f32x2 cc[4][3][2], ss[4][3][2];
    const unsigned char* cb = smem + LDS_CS + wave * RECW + r * (DH * 2) + c * 8;
    asm volatile("s_waitcnt vmcnt(12)" ::: "memory");
#pragma unroll
    for (int k = 0; k < 4; ++k)
#pragma unroll
        for (int x = 0; x < 3; ++x) {
            const u32x2 t = *reinterpret_cast<const u32x2*>(cb + k * 1536 + 128 * x);
            cc[k][x][0] = f32x2{bflo(t[0]), bfhi(t[0])}; cc[k][x][1] = f32x2{bflo(t[1]), bfhi(t[1])};
            asm volatile("" : "+v"(cc[k][x][0]), "+v"(cc[k][x][1]));
        }
    PRE(2)
    loadq();
    const unsigned char* scp = smem + LDS_SC + (c & 1) * 4096 + (4 * (c & 12) + 4 * wave + r) * 2;
    unsigned sc = *reinterpret_cast<const unsigned short*>(scp);
    auto loadsin = [&](int k) {
        const unsigned char* sb1 = cb + (c < 8 ? (64 + HALF) * 2 : (64 - HALF) * 2);
        const unsigned sg = c < 8 ? 0u : 0x80008000u;
        u32x2 t[3];
        t[0] = *reinterpret_cast<const u32x2*>(cb + 6144 + k * 1536 + HALF * 2);
        t[1] = *reinterpret_cast<const u32x2*>(sb1 + 6144 + k * 1536) ^ u32x2{sg, sg};
        t[2] = *reinterpret_cast<const u32x2*>(cb + 6144 + k * 1536 + (128 - HALF) * 2) ^ u32x2{0x80008000u, 0x80008000u};
#pragma unroll
        for (int x = 0; x < 3; ++x) {
            ss[k][x][0] = f32x2{bflo(t[x][0]), bfhi(t[x][0])}; ss[k][x][1] = f32x2{bflo(t[x][1]), bfhi(t[x][1])};
            asm volatile("" : "+v"(ss[k][x][0]), "+v"(ss[k][x][1]));
        }
    };
    asm volatile("s_waitcnt vmcnt(12)" ::: "memory");
    loadsin(0); loadsin(1);
    PRE(3)
    asm volatile("s_waitcnt vmcnt(12)" ::: "memory");
    loadsin(2); loadsin(3);
    unsigned th1 = 0, th4 = 0;
    const unsigned char* prec = smem + LDS_P + wave * (H * 128) + r * 32 + (c & 12);
    unsigned pw_ = lds0 + LDS_P + wave * (H * 128) + r * 32 + (c & 12) + (c & 3) * 128;
    float sm, sp_, sacc = 0.f;
    auto softmax1 = [&]() {
        float u;
        asm volatile("v_max_f32_dpp %0, %2, %2 row_ror:4 row_mask:0xf bank_mask:0xf\n\ts_nop 1\n\tv_max_f32_dpp %0, %0, %0 row_ror:8 row_mask:0xf bank_mask:0xf\n\tv_mov_b32 %1, %0\n\ts_nop 1\n\t"
                     "v_permlane16_swap_b32 %0, %1\n\tv_max_f32 %0, %0, %1\n\tv_mov_b32 %1, %0\n\ts_nop 1\n\tv_permlane32_swap_b32 %0, %1\n\tv_max_f32 %0, %0, %1" : "=&v"(sm), "=&v"(u) : "v"(sacc));
        sp_ = __builtin_amdgcn_exp2f(sacc - sm);
        asm volatile("" : "+v"(sp_));
    };
    auto softmax2 = [&]() {
        float l = sp_ + dpp<0x124>(sp_);
        l = xrow_sum(l + dpp<0x128>(l));
        asm volatile("ds_write2_b32 %0, %1, %2 offset1:4" :: "v"(pw_), "v"(sp_), "v"((c & 4) ? l : sm) : "memory");
        pw_ += 512;
    };

    auto skew = [&](int h) {
        if (wave == 1 && h == 1 + ((b >> 3) & 3))
            asm volatile("s_mov_b32 m0, %2\n\ts_nop 0\n\tglobal_load_lds_dwordx4 %0, %1 sc1" :: "v"(psk + (lane & 7) * 16), "s"(a.part), "s"(lds0 + LDS_SK) : "memory");
        if (h == 6) {
            const u32x2 e = *reinterpret_cast<const u32x2*>(smem + LDS_SK + (lane & 7) * 16);
            const int d = (int)(tdma - e[0]);
            if (__all(e[1] == t32 && d > -400 && d < 400)) {
                int sum = d + __builtin_amdgcn_mov_dpp(d, 0xb1, 0xf, 0xf, true), mn = min(d, __builtin_amdgcn_mov_dpp(d, 0xb1, 0xf, 0xf, true));
                sum += __builtin_amdgcn_mov_dpp(sum, 0x4e, 0xf, 0xf, true); mn = min(mn, __builtin_amdgcn_mov_dpp(mn, 0x4e, 0xf, 0xf, true));
                sum += __builtin_amdgcn_mov_dpp(sum, 0x124, 0xf, 0xf, true); mn = min(mn, __builtin_amdgcn_mov_dpp(mn, 0x124, 0xf, 0xf, true));
                sum = __builtin_amdgcn_readfirstlane(min(sum, 8 * (mn + SKL)));
                const int adj = (sum * 73 + 4096) >> 13;
                step -= adj; tmin -= adj;
                sched -= 2 * sum - 224 * adj;
            }
        }
    };

    rsrc_t nx = kr;
    unsigned nso = 0, sm4 = 0x11111110;
    unsigned gm = (wave < 2 ? 0x4081u << 1 : 0x204081u << 4) << (((b >> 3) + 2 * wave) & 3), evk = gm * 5 | 0x5e, gn = 0;
    asm volatile("" : "+s"(sm4), "+s"(gm), "+s"(evk));
#pragma unroll 1
    for (int h = 0; h < H; ++h) {
        float scf = bflo(sc);
        asm volatile("" : "+v"(scf) :: "memory");
        SB();
        nso += hstride;
        scp += 128;
        if (__builtin_expect((h & 7) == 7, 0)) {
            const unsigned fa = lds0 + LDS_CNT + 128 + ((h + 1) >> 5) * 8;
            u32x2 n;
            do asm volatile("ds_read_b64 %0, %1\n\ts_waitcnt lgkmcnt(0)" : "=v"(n) : "v"(fa) : "memory");
            while (__any(min(n[0], n[1]) * 8 <= h));
            if (h == H - 1) { nx = vr; nso = 0; scp += 4096; }
        }
        sc = *reinterpret_cast<const unsigned short*>(scp);
        SB();
        float st[2];
#define KSTEP(k) { \
            __builtin_amdgcn_s_waitcnt(0xf79); SB();        \
            const float kz = dpp<0x151 + 4 * (k)>(scf); \
            const f32x2 kz2 = f32x2{kz, kz}; \
            f32x2 a2 = f32x2{0.f, 0.f}; \
            _Pragma("unroll") for (int x = 0; x < 3; ++x) { \
                _Pragma("unroll") for (int e = 0; e < 2; ++e) { \
                    const f32x2 w = __builtin_elementwise_fma(f32x2{qb[x][2 * e], qb[x][2 * e + 1]}, ss[k][x][e], f32x2{qa[x][2 * e], qa[x][2 * e + 1]} * cc[k][x][e]); \
                    const f32x2 kf = f32x2{(float)buf[k].d[x][2 * e], (float)buf[k].d[x][2 * e + 1]} - kz2; \
                    a2 = __builtin_elementwise_fma(kf, w, a2); \
                } \
            } \
            float sa = a2[0] + a2[1]; \
            asm volatile("" : "+v"(sa) :: "memory"); \
            SB(); pace(); issue(buf[k], k, nx, nso); SB(); steer(true, k); SB(); \
              \
            if ((k) < 2) asm volatile("v_add_f32_dpp %0, %1, %1 row_ror:8 row_mask:0xf bank_mask:0xf" : "=v"(st[(k) & 1]) : "v"(sa)); \
            else asm volatile("v_add_f32_dpp %0, %1, %1 row_ror:8 row_mask:0xf bank_mask:0xc" : "+v"(st[(k) & 1]) : "v"(sa)); \
            if ((k) == 0) { getq(); SB(); if (sm4 >> h & 1) softmax1(); SB(); } \
            if ((k) == 1) { rotq(); SB(); } \
            if ((k) == 2) { \
                asm volatile("" : "+s"(sm4)); \
                if (sm4 >> h & 1) softmax2(); \
                SB(); \
                if (__builtin_expect(evk >> h & 1, 0)) { \
                    if (gm >> h & 1) gather(); \
                    if (gm * 4 >> h & 1) *reinterpret_cast<unsigned*>(smem + LDS_CNT + 128 + wave * 4) = ++gn;        \
                    skew(h); \
                } \
                SB(); \
            } \
            if ((k) == 3) { loadq(); SB(); } }
        KSTEP(0) KSTEP(1) KSTEP(2) KSTEP(3)
        asm volatile("v_add_f32_dpp %0, %0, %0 row_shl:4 row_mask:0xf bank_mask:0x5\n\tv_add_f32_dpp %0, %3, %3 row_shr:4 row_mask:0xf bank_mask:0xa\n\t"
                     "v_mul_f32 %1, %4, %1\n\tv_mov_b32_dpp %2, %2 quad_perm:[1,2,3,3] row_mask:0xf bank_mask:0xf\n\t"
                     "v_add_f32_dpp %0, %0, %0 quad_perm:[1,0,3,2] row_mask:0xf bank_mask:0xf\n\ts_nop 1\n\tv_add_f32_dpp %0, %0, %0 quad_perm:[2,3,0,1] row_mask:0xf bank_mask:0xf\n\t"
                     "v_mul_f32_dpp %0, %1, %0 quad_perm:[0,0,0,0] row_mask:0xf bank_mask:0xf\n\tv_cndmask_b32 %2, %2, %0, %5"
                     : "+v"(st[0]), "+v"(scf), "+v"(sacc) : "v"(st[1]), "s"(SCALE_LOG2), "s"(0x8888888888888888ull));
        if (DS) { if (h == 7) th1 = now32(); }
    }
    if (DS) th4 = now32();

    unsigned rl = lds0 + LDS_REC + lane * 16, rh = rl + 2 * RECW;
    unsigned s0, s1, k1;
    asm volatile("" : "=v"(s0), "=v"(s1), "=v"(k1));
    unsigned c7 = 0x70007, c8 = 0xfff8fff8u, c9 = 0x06000000;
    asm volatile("" : "+s"(c7), "+s"(c8), "+s"(c9));
    auto ev_s = [&]() {
        unsigned l = lane;
        asm volatile("" : "+v"(l));
        const unsigned t = tag >> (3 * (l & 14) + 12 * (l & 1));
        s0 = (t & 63) * 0x2001 & 0x70007;
        s1 = (l & 1) ? 0 : (t >> 6 & 63) * 0x2001 & 0x70007;
        k1 = (l & 1) * 0x2001;
        asm volatile("" : "+v"(s0), "+v"(s1), "+v"(k1));
    };
    auto pack = [&](f32x4 v) {
        unsigned h0, h1, m, z;
        asm("v_max3_f32 %2, |%4|, |%5|, |%6|\n\tv_max3_f32 %2, %2, |%7|, %13\n\ts_nop 1\n\tv_max_u32_dpp %2, %2, %2 quad_perm:[1,0,3,2] row_mask:0xf bank_mask:0xf\n\t"
            "v_and_b32 %3, 0x7e000000, %2\n\tv_lshrrev_b32 %2, 25, %2\n\tv_sub_u32 %3, 0x84800000, %3\n\tv_mul_u32_u24 %2, %2, %8\n\t"
            "v_fma_mixlo_f16 %0, %4, %3, 0\n\tv_fma_mixlo_f16 %1, %6, %3, 0\n\tv_fma_mixhi_f16 %0, %5, %3, 0\n\tv_fma_mixhi_f16 %1, %7, %3, 0\n\tv_and_or_b32 %2, %2, %11, %10\n\t"
            "v_pk_add_u16 %0, %0, 4 op_sel_hi:[1,0]\n\tv_pk_add_u16 %1, %1, 4 op_sel_hi:[1,0]\n\tv_and_or_b32 %0, %0, %12, %9\n\tv_and_or_b32 %1, %1, %12, %2"
            : "=&v"(h0), "=&v"(h1), "=&v"(m), "=&v"(z) : "v"(v[0]), "v"(v[1]), "v"(v[2]), "v"(v[3]), "v"(k1), "v"(s0), "v"(s1), "s"(c7), "s"(c8), "s"(c9));
        return u32x2{h0, h1};
    };
#define LDREC(hd) "ds_read_b128 %0, %4 offset:768*" #hd "\n\tds_read_b128 %1, %4 offset:768*" #hd "+24576\n\tds_read_b128 %2, %5 offset:768*" #hd "\n\tds_read_b128 %3, %5 offset:768*" #hd "+24576"
    auto ldrec = [&](unsigned o, f32x4 (&p)[4]) {
        asm volatile(LDREC(0) : "=&v"(p[0]), "=&v"(p[1]), "=&v"(p[2]), "=&v"(p[3]) : "v"(rl + o), "v"(rh + o) : "memory");
    };
    auto add4 = [&](f32x4 (&p)[4]) { return pack((p[0] + p[1]) + (p[2] + p[3])); };
    auto sum4 = [&](unsigned o) {
        f32x4 p[4];
        ldrec(o, p);
        asm volatile("s_waitcnt lgkmcnt(0)" : "+v"(p[0]), "+v"(p[1]), "+v"(p[2]), "+v"(p[3]) :: "memory");
        return add4(p);
    };
    auto wide = [&](u32x2 t) {
        return u32x4{t[0], t[1], (unsigned)__builtin_amdgcn_mov_dpp(t[0], 0xb1, 0xf, 0xf, true), (unsigned)__builtin_amdgcn_mov_dpp(t[1], 0xb1, 0xf, 0xf, true)};
    };
    auto merge = [&](int hd) {
        f32x4 p[4];
        f32x2 ma, mb;
        f32x4 xa, xb;
        asm volatile("ds_read2st64_b64 %0, %2 offset1:8\n\tds_read2st64_b64 %1, %2 offset0:16 offset1:24" : "=&v"(xa), "=&v"(xb) : "v"(lds0 + LDS_P + hd * 128 + 16) : "memory");
        ldrec(hd * (DH * 4), p);
        asm volatile("s_waitcnt lgkmcnt(4)" : "+v"(xa), "+v"(xb) :: "memory");
        const float M = __builtin_amdgcn_fmed3f(__builtin_amdgcn_fmed3f(xa[0], xa[2], __builtin_inff()), __builtin_amdgcn_fmed3f(xb[0], xb[2], __builtin_inff()), __builtin_inff());
        ma = f32x2{__builtin_amdgcn_exp2f(xa[0] - M), __builtin_amdgcn_exp2f(xa[2] - M)};
        mb = f32x2{__builtin_amdgcn_exp2f(xb[0] - M), __builtin_amdgcn_exp2f(xb[2] - M)};
        const float rL = __builtin_amdgcn_rcpf((ma[0] * xa[1] + ma[1] * xa[3]) + (mb[0] * xb[1] + mb[1] * xb[3]));
        ma *= rL; mb *= rL;
        asm volatile("s_waitcnt lgkmcnt(0)" : "+v"(ma), "+v"(mb), "+v"(p[0]), "+v"(p[1]), "+v"(p[2]), "+v"(p[3]) :: "memory");
        return pack(ma[0] * p[0] + ma[1] * p[1] + mb[0] * p[2] + mb[1] * p[3]);
    };
    constexpr unsigned LSTR = 128;
    constexpr int NC = 3 * H;
    const int cn = sb;
    const unsigned hc = 4 * (cn & 7) + ((cn >> 3) & 3);
    unsigned old = 0, area = 0, fo = 0;
    unsigned flv = 0;
    float gw = 1.f, mw = 0.f;
    f32x2 fs[4] = {};
    constexpr int LDS_ML = LDS_QRAW + 2048;
#define BAD(y) ((unsigned)(y)[1] != t32 || (unsigned)(y)[3] != (unsigned)tag)
    auto ev_all = [&](int hd) {
        unsigned n;
        asm volatile("1:\tds_read_b32 %0, %1\n\ts_waitcnt lgkmcnt(0)\n\tv_cmp_ne_u32 vcc, 4, %0\n\ts_cbranch_vccnz 1b" : "=&v"(n) : "v"(lds0 + LDS_CNT + hd * 4) : "memory", "vcc");
    };
    auto quiet = [&]() { vth = 15; hush = vt + 500 * 16; };
    auto ev_ml = [&]() {
        f32x2 ml[4];
#pragma unroll
        for (int w = 0; w < 4; ++w) ml[w] = *reinterpret_cast<const f32x2*>(smem + LDS_P + ((wave + w) & 3) * (H * 128) + (lane & 31) * 128 + 16);
        const float M = fmaxf(fmaxf(ml[0][0], ml[1][0]), fmaxf(ml[2][0], ml[3][0]));
        const float e0 = __builtin_amdgcn_exp2f(ml[0][0] - M);
        const float L = (e0 * ml[0][1] + __builtin_amdgcn_exp2f(ml[1][0] - M) * ml[1][1]) + (__builtin_amdgcn_exp2f(ml[2][0] - M) * ml[2][1] + __builtin_amdgcn_exp2f(ml[3][0] - M) * ml[3][1]);
        flv = ((__float_as_uint(M + __builtin_amdgcn_logf(L)) + 0x80) & 0xffffff00u) | ((unsigned)(tag >> (8 * (lane & 7))) & 255);
        gw = e0 * __builtin_amdgcn_rcpf(L);
        asm volatile("" : "+v"(flv), "+v"(gw));
    };
    auto ev_area = [&]() {
        u32x4 e = *reinterpret_cast<const u32x4*>(smem + LDS_SK);
        if (__builtin_expect(__any(BAD(e)), 0)) {
#pragma unroll 1
            for (int n = 0; n < (1 << 20) && BAD(e); ++n)
                asm volatile("global_load_dwordx4 %0, %1, %2 sc1\n\ts_waitcnt vmcnt(0)" : "=v"(e) : "v"(psk), "s"(a.part) : "memory");
        }
        area = (((__builtin_amdgcn_readfirstlane(e[0]) >> 13) & (NAREA - 1)) + 1) * AREA + 4096;
        asm volatile("" : "+s"(area));
    };
    unsigned eo[5], xo[8], oo, po, wo, ta, tb, tc, ea, txb, tgb;
    u32x4 ev[4];
    f32x4 a0, a1;
    asm volatile("" : "=v"(eo[0]), "=v"(eo[1]), "=v"(eo[2]), "=v"(eo[3]), "=v"(eo[4]), "=v"(xo[0]), "=v"(xo[1]), "=v"(xo[2]), "=v"(xo[3]), "=v"(xo[4]), "=v"(xo[5]), "=v"(xo[6]), "=v"(xo[7]),
                      "=v"(oo), "=v"(po), "=v"(wo), "=v"(ta), "=v"(tb), "=v"(tc), "=v"(ea), "=v"(ev[0]), "=v"(ev[1]), "=v"(ev[2]), "=v"(a0), "=v"(a1));
    txb = nb16 + step + stag - EX * 16 - (sb >> 3) * GS * 4;
    tgb = nb16 + stag - (hc == H - 1 ? HL : HOLD) * 16;
    asm volatile("" : "+s"(txb), "+s"(tgb));
    auto ev_put = [&]() {
        asm volatile("s_mov_b32 exec_hi, 0\n\tbuffer_store_dword %0, %1, %2, %3 offen sc1\n\ts_mov_b32 exec_hi, -1" :: "v"(flv), "v"(lane * 4), "s"(pr), "s"(area + pfl + sb * LSTR) : "memory");
    };
    auto ev_get = [&]() {
        unsigned l = lane;
        asm volatile("" : "+v"(l));
        asm volatile("" : "+s"(area));
        fo = area + pfl + 8 * wave * LSTR + (cn & 7) * 16 + (((l & 56) << 2) + (l & 7)) * LSTR;
        asm volatile("s_mov_b32 m0, %2\n\ts_nop 0\n\tglobal_load_lds_dwordx4 %0, %1 sc1" :: "v"(fo), "s"(a.part), "s"(lds0 + LDS_ML + wave * 1024) : "memory");
    };
    auto ev_w1 = [&]() {
        u32x4 y = *reinterpret_cast<const u32x4*>(smem + LDS_ML + wave * 1024 + lane * 16);
        const unsigned tq = (cn & 1) ? t32 : (unsigned)tag;
#define BADW(y) (__builtin_amdgcn_perm(__builtin_amdgcn_perm((y)[3], (y)[2], 0x0400), __builtin_amdgcn_perm((y)[1], (y)[0], 0x0400), 0x05040100) != tq)
        if (__builtin_expect(__any(BADW(y)), 0)) {
#pragma unroll 1
            for (int n = 0; n < (1 << 20) && BADW(y); ++n)
                asm volatile("global_load_dwordx4 %0, %1, %2 sc1\n\ts_waitcnt vmcnt(0)" : "=v"(y) : "v"(fo), "s"(a.part) : "memory");
        }
        const unsigned se = (cn & 8) ? 0x0706050cu : 0x0302010cu;
        const float m = __uint_as_float((cn & 16) ? __builtin_amdgcn_perm(y[3], y[2], se) : __builtin_amdgcn_perm(y[1], y[0], se));
        float u;
        asm volatile("s_nop 1\n\tv_max_f32_dpp %0, %2, %2 row_ror:8 row_mask:0xf bank_mask:0xf\n\ts_nop 1\n\tv_max_f32_dpp %0, %0, %0 row_ror:4 row_mask:0xf bank_mask:0xf\n\ts_nop 1\n\t"
                     "v_max_f32_dpp %0, %0, %0 row_ror:2 row_mask:0xf bank_mask:0xf\n\ts_nop 1\n\tv_max_f32_dpp %0, %0, %0 row_ror:1 row_mask:0xf bank_mask:0xf\n\tv_mov_b32 %1, %0\n\ts_nop 1\n\t"
                     "v_permlane16_swap_b32 %0, %1\n\ts_nop 0\n\tv_max_f32 %0, %0, %1\n\tv_mov_b32 %1, %0\n\ts_nop 1\n\tv_permlane32_swap_b32 %0, %1\n\ts_nop 0\n\tv_max_f32 %0, %0, %1" : "=&v"(mw), "=&v"(u) : "v"(m));
        fs[0][0] = __builtin_amdgcn_exp2f(m - mw);
        asm volatile("" : "+v"(fs[0]));
    };
    auto ev_w2 = [&]() {
        const f32x2 f = fs[0];
        unsigned bp = (lane >> 3) * 4;
        asm volatile("ds_bpermute_b32 %0, %8, %9\n\tds_bpermute_b32 %1, %8, %9 offset:32\n\tds_bpermute_b32 %2, %8, %9 offset:64\n\tds_bpermute_b32 %3, %8, %9 offset:96\n\t"
                     "ds_bpermute_b32 %4, %8, %9 offset:128\n\tds_bpermute_b32 %5, %8, %9 offset:160\n\tds_bpermute_b32 %6, %8, %9 offset:192\n\tds_bpermute_b32 %7, %8, %9 offset:224"
                     : "=&v"(fs[0][0]), "=&v"(fs[0][1]), "=&v"(fs[1][0]), "=&v"(fs[1][1]), "=&v"(fs[2][0]), "=&v"(fs[2][1]), "=&v"(fs[3][0]), "=&v"(fs[3][1]) : "v"(bp), "v"(f[0]) : "memory");
        *reinterpret_cast<f32x2*>(smem + LDS_QRAW + 1024 + wave * 8) = f32x2{mw, xrow_sum(row16_allsum(f[0]))};
        asm volatile("s_waitcnt lgkmcnt(0)" : "+v"(fs[0]), "+v"(fs[1]), "+v"(fs[2]), "+v"(fs[3]) :: "memory");
    };
#define HERE unsigned l = lane, w = wave, n = sb; asm volatile("" : "+v"(l)); asm volatile("" : "+s"(w), "+s"(n));
    auto ev_a = [&]() {
        HERE
        const unsigned t = (((l >> 5) * nb + n) * LS + (l & 31) * 16) | ((l & 31) + 8) << 26 & 0x80000000u;
        eo[0] = t + 6 * w * nb * LS; eo[1] = eo[0] + 2 * nb * LS; eo[2] = eo[0] + 4 * nb * LS;
        eo[3] = t + (24 + 2 * w) * nb * LS;
        asm volatile("" : "+v"(eo[0]), "+v"(eo[1]), "+v"(eo[2]), "+v"(eo[3]));
    };
    auto ev_a2 = [&]() {
        HERE
        eo[4] = (((H - 1) * nb + n) * LS + l * 8) | l << 31 | (l + 16) << 25 & 0x80000000u;
        eo[3] = w == 3 ? eo[4] - nb * LS : eo[3];
        ea = lds0 + LDS_REC + (6 * w + (l >> 5)) * (DH * 4) + (l & 31) * 16;
        asm volatile("" : "+v"(eo[3]), "+v"(eo[4]), "+v"(ea));
    };
    auto ev_b = [&]() {
        HERE
        xo[0] = (hc * nb + 8 * w + (l >> 3)) * LS + (n >> 5) * 128 + (l & 7) * 16;
#pragma unroll
        for (int i = 1; i < 8; ++i) xo[i] = xo[0] + 32 * i * LS;
        asm volatile("" : "+v"(xo[0]), "+v"(xo[1]), "+v"(xo[2]), "+v"(xo[3]), "+v"(xo[4]), "+v"(xo[5]), "+v"(xo[6]), "+v"(xo[7]));
    };
    auto ev_c = [&]() {
        HERE
        oo = (hc * DH + (n >> 5) * 64 + 8 * (l & 7) + (l >> 3)) * 2;
        po = lds0 + LDS_QRAW + l * 4; wo = po + w * 256;
        asm volatile("" : "+v"(oo), "+v"(po), "+v"(wo));
    };
    auto ev_c2 = [&]() {
        HERE
        const unsigned t = tag >> 6 * (l & 7);
        ta = (t & 63) * 0x2001 & c7; tb = (t >> 6 & 63) * 0x2001 & c7; tc = (t >> 12 & 63) * 0x2001 & c7;
        asm volatile("" : "+v"(ta), "+v"(tb), "+v"(tc));
    };
    auto ev_d = [&]() {
        HERE
        ev_all(24);
        asm volatile("ds_read_b128 %0, %5\n\tds_read_b128 %1, %5 offset:1536\n\tds_read_b128 %2, %5 offset:3072\n\tds_read_b128 %3, %6\n\tds_read_b128 %4, %6 offset:16\n\ts_waitcnt lgkmcnt(0)"
                     : "+v"(ev[0]), "+v"(ev[1]), "+v"(ev[2]), "+v"(a0), "+v"(a1) : "v"(ea), "v"(lds0 + LDS_QRAW + 1024) : "memory");
        ea += (24 - 4 * w) * (DH * 4);
        asm volatile("" : "+v"(ea));
        if (4 * n + w == 1) { asm volatile("s_mov_b64 exec, 1\n\tbuffer_store_dwordx2 %0, off, %1, 0 offset:%2 sc1\n\ts_mov_b64 exec, -1" :: "v"(u32x2{(unsigned)tag, t32}), "s"(pr), "i"(pep) : "memory"); quiet(); }
    };
    auto ev_e = [&]() {
        const float M = fmaxf(fmaxf(a0[0], a0[2]), fmaxf(a1[0], a1[2]));
        const float gf = __builtin_amdgcn_exp2f(mw - M) * __builtin_amdgcn_rcpf((__builtin_amdgcn_exp2f(a0[0] - M) * a0[1] + __builtin_amdgcn_exp2f(a0[2] - M) * a0[3]) + (__builtin_amdgcn_exp2f(a1[0] - M) * a1[1] + __builtin_amdgcn_exp2f(a1[2] - M) * a1[3]));
#pragma unroll
        for (int i = 0; i < 4; ++i) fs[i] *= gf;
        asm volatile("" : "+v"(fs[0]), "+v"(fs[1]), "+v"(fs[2]), "+v"(fs[3]));
    };
    constexpr int G1 = 5, G2 = 27;
    const int hput = wave == ((b >> 5) & 3) ? G1 + 2 + ((b >> 3) & 3) : -9, hget = cn < NC ? G1 + 7 + ((cn >> 3) & 7) : -9;
    unsigned ev1 = 2 | 1 << G1 | 15 << (G1 + 2) | 9u << (hget & 31) | 1u << (H - 1), ev2 = 2 | 3 << G1 | 4u << (hget & 31);
    asm volatile("" : "+s"(ev1), "+s"(ev2), "+s"(area));
    float pv = *reinterpret_cast<const float*>(prec);
    f32x2 acc[3][2];
    float y, yn, zn;
    auto prep = [&](int hn) {
        const float s = bflo(sc);
        yn = s * (pv * __uint_as_float(__builtin_amdgcn_readlane(__float_as_uint(gw), hn)));
        float z = yn * dpp<0x55>(s);
        z += dpp<0x124>(z);
        z += dpp<0x128>(z);
        zn = -dpp<0x150>(z);
        asm volatile("" : "+v"(yn), "+v"(zn) :: "memory");
        SB();
        scp += 128;
        sc = *reinterpret_cast<const unsigned short*>(scp);
        prec += 128;
        pv = *reinterpret_cast<const float*>(prec);
        SB();
    };
    prep(0);
#define VCOMP(k) VPART(k, 0, 3)
#define VPART(k, x0, x1) { \
        if ((k) == 0) y = yn; \
        const f32x2 pz = f32x2{dpp<0x150 + 4 * (k)>(y), zn}; \
        _Pragma("unroll") for (int x = x0; x < x1; ++x) \
            _Pragma("unroll") for (int e = 0; e < 2; ++e) { \
                const f32x2 vf = f32x2{(float)buf[k].d[x][2 * e], (float)buf[k].d[x][2 * e + 1]}; \
                if ((k) == 0) asm volatile("v_pk_fma_f32 %0, %1, %2, %1 op_sel:[0,0,1] op_sel_hi:[0,1,1]" : "=v"(acc[x][e]) : "v"(pz), "v"(vf)); \
                else asm volatile("v_pk_fma_f32 %0, %1, %2, %0 op_sel_hi:[0,1,1]" : "+v"(acc[x][e]) : "v"(pz), "v"(vf)); \
            } \
        asm volatile("" ::: "memory"); \
        SB(); }
#define ON(hn, x, f) { int q = (hn) - (x); asm volatile("" : "+s"(q)); if (__builtin_expect(q == 0, 0)) f(); }
#define VSTEP(k, hn) { \
        __builtin_amdgcn_s_waitcnt(0xf79); SB();        \
        VCOMP(k) pace(); issue(buf[k], k, vr, nso); SB(); steer(true, k); SB(); \
        if ((k) == 1) { \
            vth = TH; asm volatile("" : "+s"(vth)); \
            if (__builtin_expect(ev1 >> (hn) & 1, 0)) { \
                if ((hn) == 1) { softmax1(); softmax2(); }        \
                if ((hn) == G1) ev_all(0);        \
                if ((hn) == hput) { ev_put(); quiet(); } \
                if ((hn) == hget) { ev_get(); quiet(); } \
                if ((hn) == hget + 3) ev_w2(); \
                if ((hn) == H - 1) tmin = 0; \
            } \
            SB(); \
            if (__builtin_expect((hn) >= G2 - 4, 0)) { ON(hn, G2 - 4, ev_a) ON(hn, G2 - 3, ev_a2) ON(hn, G2 - 2, ev_b) ON(hn, G2 - 1, ev_c) ON(hn, G2, ev_d) ON(hn, G2 + 1, ev_e) ON(hn, G2 + 2, ev_c2) } \
            SB(); \
        } \
        if ((k) == 2) { \
            if (__builtin_expect(ev2 >> (hn) & 1, 0)) { \
                ON(hn, 1, ev_s) \
                if ((hn) == G1) ev_ml(); \
                if ((hn) == G1 + 1) ev_area(); \
                if ((hn) == hget + 2) ev_w1(); \
            } \
            SB(); \
            prep(hn); \
        } }
    unsigned ra = lds0 + LDS_REC + wave * RECW + (4 * c + r) * 4, ca = lds0 + LDS_CNT, one = 1;
    asm volatile("" : "+v"(ra), "+v"(ca), "+v"(one));
    auto record = [&]() {
        float w[3];
        f32x2 u[3];
#pragma unroll
        for (int x = 0; x < 3; ++x) { swap32(acc[x][0], acc[x][1]); SB(); }
#pragma unroll
        for (int x = 0; x < 3; ++x) { u[x] = acc[x][0] + acc[x][1]; SB(); }
#pragma unroll
        for (int x = 0; x < 3; ++x) {
            auto t = __builtin_amdgcn_permlane16_swap(__float_as_uint(u[x][0]), __float_as_uint(u[x][1]), false, false);
            u[x] = f32x2{__uint_as_float(t[0]), __uint_as_float(t[1])};
            SB();
        }
#pragma unroll
        for (int x = 0; x < 3; ++x) w[x] = u[x][0] + u[x][1];
        asm volatile("ds_write2st64_b32 %1, %2, %3 offset1:1\n\tds_write_b32 %1, %4 offset:512\n\ts_mov_b64 exec, 1\n\tds_add_rtn_u32 %0, %5, %6\n\ts_mov_b64 exec, -1"
                     : "+v"(old) : "v"(ra), "v"(w[0]), "v"(w[1]), "v"(w[2]), "v"(ca), "v"(one) : "memory");
        SB();
    };
#pragma unroll 1
    for (int h = 0; h < H - 1; ++h) {
        nso += hstride;
        VSTEP(0, h + 1)
        asm volatile("s_waitcnt lgkmcnt(0)" : "+v"(old) :: "memory");
        if (__builtin_amdgcn_readfirstlane(old) == 3) *reinterpret_cast<u32x2*>(smem + LDS_REC + (h - 1) * (DH * 4) + lane * 8) = __builtin_expect(h > G1, 1) ? sum4((h - 1) * (DH * 4)) : merge(h - 1);
        SB();
        VSTEP(1, h + 1) VSTEP(2, h + 1) VSTEP(3, h + 1)
        record();
        ra += DH * 4; ca += 4;
        asm volatile("" : "+v"(ra), "+v"(ca));
    }
    if (DBG == 4 && (F >> 13) == 5) { tbar = now32(); asm volatile("" : "+s"(tbar)); }
    asm volatile("s_branch .Lkv_t%c0\n\t.p2align 12,,3327\n.Lkv_t%c0:" :: "i"(ID));
    if (DBG == 4 && (F >> 13) == 4) { tbar = now32(); asm volatile("" : "+s"(tbar)); }
    unsigned tx = txb - sched, tgt = tgb - sched;
    asm volatile("ds_write_b32 %0, %1 offset:36" :: "v"(ca + 4 * wave), "v"(tx) : "memory");
    VCOMP(0)
    asm volatile("s_waitcnt lgkmcnt(0)\n\ts_barrier" ::: "memory");
    if (DBG == 4 && (F >> 13) == 3) { tbar = now32(); asm volatile("" : "+s"(tbar)); }
    f32x4 p[4];
    i32x4 tw;
    asm volatile(LDREC(30) "\n\tds_read_b128 %6, %8\n\tds_read_b128 %7, %9 offset:36" : "=&v"(p[0]), "=&v"(p[1]), "=&v"(p[2]), "=&v"(p[3]), "+v"(rl), "+v"(rh), "=&v"(ev[3]), "=&v"(tw) : "v"(ea), "v"(ca) : "memory");
    SB();
    VCOMP(1)
    asm volatile("s_waitcnt lgkmcnt(0)" : "+v"(p[0]), "+v"(p[1]), "+v"(p[2]), "+v"(p[3]), "+v"(ev[3]), "+v"(tw) :: "memory");
    if (!(F & 4)) {
        tw -= (int)tx;
        const int d = __builtin_amdgcn_readfirstlane(min(min(tw[0], tw[1]), min(tw[2], tw[3])));
        tx += d; tgt += d;
    }
    if (wave == 3) ev[3] = wide(add4(p));
    int lx = hc == H - 1 ? ((SL >> 24) & 15) * ((SL >> 16) & 255) : 0;
    asm volatile("" : "+s"(lx));
    asm volatile("" : "+v"(ev[3]));
    asm volatile("" : "+s"(tx), "+s"(tgt), "+s"(area));
    SB();
    VCOMP(2)
    if (DBG == 4 && (F >> 13) == 1) { tbar = now32(); asm volatile("" : "+s"(tbar)); }
    auto until = [&](unsigned t) {
        int d;
        asm volatile("1:\ts_memrealtime vcc\n\ts_waitcnt lgkmcnt(0)\n\ts_lshl4_add_u32 %0, vcc_lo, %1\n\ts_cmp_lt_i32 %0, 0\n\ts_cbranch_scc1 1b" : "=&s"(d) : "s"(t) : "memory", "vcc", "scc");
        return d;
    };
    const rsrc_t orr = mkrsrc(a.out, H * DH * 2);
    asm volatile("" :: "s"(orr), "s"(pr));
    until(tx);
    constexpr int XP = (F & 4096) ? ((F & 4) ? 3 : 2) : 0;
    VPART(3, 0, XP)
    if ((F & 256) && XP < 3) asm volatile("" : "+v"(buf[3].d[2]));
    SB();
#pragma unroll
    for (int i = 0; i < 4; ++i) { __builtin_amdgcn_raw_buffer_store_b128(ev[i], pr, eo[i], area, 16); SB(); }
    if (DBG == 4 && !(F >> 13)) tbar = now32();
    SB();
    VPART(3, XP, 3)
    if (DBG == 4 && (F >> 13) == 6) { tbar = now32(); asm volatile("" : "+s"(tbar)); }
    record();
    asm volatile(LDREC(31) "\n\ts_waitcnt lgkmcnt(4)" : "=&v"(p[0]), "=&v"(p[1]), "=&v"(p[2]), "=&v"(p[3]), "+v"(rl), "+v"(rh), "+v"(old) :: "memory");
    if (F & 2048) {
        int last = __builtin_amdgcn_readfirstlane(old);
        asm volatile("" : "+s"(last));
        asm volatile("s_waitcnt lgkmcnt(0)" : "+v"(p[0]), "+v"(p[1]), "+v"(p[2]), "+v"(p[3]) :: "memory");
        const u32x4 t = wide(add4(p));
        asm volatile("s_cmp_eq_u32 %4, 3\n\ts_cbranch_scc0 1f\n\tbuffer_store_dwordx4 %0, %1, %2, %3 offen sc1\n1:\ts_nop 1" :: "v"(t), "v"(eo[4]), "s"(pr), "s"(area), "s"(last) : "memory", "scc");
    } else {
    const bool last = __builtin_amdgcn_readfirstlane(old) == 3;
    if (__builtin_expect(last, 1)) {
        asm volatile("s_waitcnt lgkmcnt(0)" : "+v"(p[0]), "+v"(p[1]), "+v"(p[2]), "+v"(p[3]) :: "memory");
        __builtin_amdgcn_raw_buffer_store_b128(wide(add4(p)), pr, eo[4], area, 16);
    }
    asm volatile("s_waitcnt lgkmcnt(0)" :: "v"(p[0]), "v"(p[1]), "v"(p[2]), "v"(p[3]) : "memory");
    }
    unsigned tend = DS ? now32() : 0;
    unsigned tfl = 0, tpl = 0;

    if (cn < NC) {
        i32x4 x[8];
        float o[8];
        int cm = -138, kd = 0x200004, rounds = 0;
        unsigned bad, mk;
        asm volatile("" : "+v"(cm));
        asm volatile("" : "+s"(kd), "+s"(rounds));
        int late = until(tgt) + lx;
        if (DS) tfl = now32();
        if (DBG == 4) th4 = 0;
#define XL(i) x[i] = bld128<16 + (SL >> 28)>(pr, xo[i], area); SB();
#define XS(i) XL(i) asm volatile("s_cmp_lt_i32 %0, %1\n\ts_cbranch_scc0 1f\n\ts_sleep %2\n1:" :: "s"(late), "i"(((SL >> 16) & 255) * ((i) + 1)), "i"((SL >> (2 * (i))) & 3) : "scc"); SB();
#pragma unroll 1
        do {
            XS(0) XS(1) XS(2) XS(3) XS(4) XS(5) XS(6) XL(7)
            if (DBG == 4 && !th4) th4 = now32();
            late = 1 << 30;
            mk = ++rounds < (1 << 20) ? 0x70007 : 0;
            asm volatile("" : "+s"(mk), "+s"(late), "+s"(rounds));
            SB();
#pragma unroll
            for (int i = 0; i < 8; ++i) {
                float w;
                int x0, x1, x2, x3;
#define MIX(a, b, x, z) "\n\tv_fma_mix_f32 %" #a ", %9, %" #x ", " z(a) " op_sel_hi:[0,1,0]\n\tv_fma_mix_f32 %" #b ", %9, %" #x ", " z(b) " op_sel:[0,1,0] op_sel_hi:[0,1,0]"
#define ROW(z) "v_and_b32 %9, 0x70007, %23\n\tv_xor_b32 %13, %23, %9\n\tv_dot2_i32_i16 %9, %9, %18, %19\n\tv_xor_b32 %10, %20, %15\n\tv_xor_b32 %11, %21, %16\n\tv_xor_b32 %12, %22, %17\n\t" \
               "v_ldexp_f32 %9, %14, %9\n\tv_or3_b32 %8, " z(8) ", %10, %11" MIX(0, 1, 10, z) MIX(2, 3, 11, z) "\n\tv_or_b32 %8, %8, %12" MIX(4, 5, 12, z) MIX(6, 7, 13, z)
#define ZERO(a) "0"
#define SAME(a) "%" #a
                if (i == 0) asm volatile(ROW(ZERO) : "=&v"(o[0]), "=&v"(o[1]), "=&v"(o[2]), "=&v"(o[3]), "=&v"(o[4]), "=&v"(o[5]), "=&v"(o[6]), "=&v"(o[7]), "=&v"(bad), "=&v"(w),
                                           "=&v"(x0), "=&v"(x1), "=&v"(x2), "=&v"(x3) : "v"(fs[i >> 1][i & 1]), "v"(ta), "v"(tb), "v"(tc), "s"(kd), "v"(cm),
                                    "v"(x[i][0]), "v"(x[i][1]), "v"(x[i][2]), "v"(x[i][3]));
                else asm volatile(ROW(SAME) : "+v"(o[0]), "+v"(o[1]), "+v"(o[2]), "+v"(o[3]), "+v"(o[4]), "+v"(o[5]), "+v"(o[6]), "+v"(o[7]), "+v"(bad), "=&v"(w),
                                    "=&v"(x0), "=&v"(x1), "=&v"(x2), "=&v"(x3) : "v"(fs[i >> 1][i & 1]), "v"(ta), "v"(tb), "v"(tc), "s"(kd), "v"(cm),
                                    "v"(x[i][0]), "v"(x[i][1]), "v"(x[i][2]), "v"(x[i][3]));
                if (DBG == 6 && __any(((x0 | x1 | x2) & 0x70007) != 0) && !th1) th4 |= 1 << i;
                SB();
            }
            if (DBG == 6) th1 = 1;
        } while (__builtin_expect(__builtin_amdgcn_ballot_w64((bad & mk) != 0) != 0, 0));
        if (DS) tpl = now32();
        f32x2 u0 = f32x2{o[0], o[1]}, u1 = f32x2{o[2], o[3]}, u2 = f32x2{o[4], o[5]}, u3 = f32x2{o[6], o[7]};
        swap32(u0, u2); swap32(u1, u3);
        SB();
        u0 += u2; u1 += u3;
        swap16(u0, u1);
        u0 += u1;
        f32x2 p0, p1;
        asm volatile("s_nop 0\n\ts_nop 0\n\tv_add_f32_dpp %3, %3, %3 row_ror:8 row_mask:0xf bank_mask:0x3\n\tv_add_f32_dpp %3, %4, %4 row_ror:8 row_mask:0xf bank_mask:0xc\n\t"
                     "ds_write_b32 %5, %3\n\ts_mov_b64 exec, 1\n\tds_add_rtn_u32 %2, %7, %8 offset:20\n\ts_mov_b64 exec, -1\n\t"
                     "ds_read2st64_b32 %0, %6 offset1:1\n\tds_read2st64_b32 %1, %6 offset0:2 offset1:3\n\ts_waitcnt lgkmcnt(2)"
                     : "=&v"(p0), "=&v"(p1), "+v"(old), "+v"(u0[0]) : "v"(u0[1]), "v"(wo), "v"(po), "v"(ca), "v"(one) : "memory");
        if (F & 2048) {
            int last = __builtin_amdgcn_readfirstlane(old);
            asm volatile("" : "+s"(last));
            asm volatile("s_waitcnt lgkmcnt(0)" : "+v"(p0), "+v"(p1) :: "memory");
            p0 += p1;
            const float t = p0[0] + p0[1];
            asm volatile("s_cmp_eq_u32 %3, 3\n\ts_cbranch_scc0 1f\n\tbuffer_store_short %0, %1, %2, 0 offen\n1:" :: "v"(pk_bf16(t, t)), "v"(oo), "s"(orr), "s"(last) : "memory", "scc");
        } else
        if (__builtin_expect(__builtin_amdgcn_readfirstlane(old) == 3, 1)) {
            asm volatile("s_waitcnt lgkmcnt(0)" : "+v"(p0), "+v"(p1) :: "memory");
            p0 += p1;
            const float t = p0[0] + p0[1];
            __builtin_amdgcn_raw_buffer_store_b16((unsigned short)pk_bf16(t, t), orr, oo, 0, 0);
        }
        asm volatile(".Lkv_b%c0:" :: "i"(ID));
    }
    if (DBG) {
        const unsigned tc1 = now32();
        const unsigned fm = th4;
        if (!DS) tend = tbar = th4 = tfl = tpl = tc1;
        if (DBG == 6) tfl = tpl = fm;
        if (DBG == 5) tbar = tdma;
        const unsigned tl16 = sched - nb16 - stag - step;
        u64* stp = reinterpret_cast<u64*>(a.part + STAMP_OFF) + (size_t)((DBG == 4 || DBG == 24 || DBG == 6 ? cn : b) * 4 + wave) * 8;

        if (lane == 0) { stp[0] = tstart; stp[1] = tbar; stp[2] = DBG >= 4 ? tend - (((tend << 4) - tl16) >> 4) : th1; stp[3] = DBG == 4 && cn >= NC ? tend : th4; stp[4] = tend; stp[5] = cn < NC || DBG == 6 ? tfl : tend; stp[6] = cn < NC || DBG == 6 ? tpl : tend; stp[7] = tc1; }
        if (DBG == 2) *reinterpret_cast<u32x4*>(a.part + STAMP_OFF + 65536 + (b * 4 + wave) * 1024 + lane * 16) = *reinterpret_cast<const u32x4*>(smem + LDS_TR + 16 + wave * 1024 + lane * 16);
    }
}

template <int TP, int ADV, int PX, int TMIN, int CAP, int HD, int RA, int SKL, int FC, int F, int EX, int GS, int HOLD, int HL, int SL, int ID, int DBG>
static void launch90(void* const* d_in, const int* in_sizes, void* d_out, void* d_ws, hipStream_t stream) {
    typedef const unsigned short* P;
    Args a;
    a.qk = (const int*)d_in[1]; a.qv = (const int*)d_in[4];
    a.k_cos = (P)d_in[9]; a.k_sin = (P)d_in[10];
    a.q = (P)d_in[0]; a.q_cos = (P)d_in[7]; a.q_sin = (P)d_in[8];
    a.sc[0] = (P)d_in[2]; a.sc[1] = (P)d_in[3]; a.sc[2] = (P)d_in[5]; a.sc[3] = (P)d_in[6];
    a.part = (unsigned char*)d_ws;
    a.out = (unsigned short*)d_out;
    kv90_kernel<TP, ADV, PX, TMIN, CAP, HD, RA, SKL, FC, F, EX, GS, HOLD, HL, SL, ID, DBG><<<Skv / RW, 256, 0, stream>>>(a);
}

extern "C" void kernel_launch(void* const* d_in, const int* in_sizes, int n_in, void* d_out, int out_size, void* d_ws, size_t ws_size, hipStream_t stream) {
    launch90<79*16+8,220,40,78*16+2,28,70+256*110,240,24,32+256*50+65536*3+(15<<20)+(2<<24),257,56,2,66,84,0x2aaa+(90<<16)+(2<<24),0,0>(d_in, in_sizes, d_out, d_ws, stream);
}
```

```cpp
#include <hip/hip_runtime.h>
#include <hip/hip_bf16.h>
#include <cstdint>
#define KALIGN 256
#define STAMP_OFF (200 << 20)
using bf16 = __hip_bfloat16;
using i32x4 = __attribute__((ext_vector_type(4))) int;
using u32x4 = __attribute__((ext_vector_type(4))) unsigned;
using u32x2 = __attribute__((ext_vector_type(2))) unsigned;
using f32x2 = __attribute__((ext_vector_type(2))) float;
using f32x4 = __attribute__((ext_vector_type(4))) float;

constexpr int DH = 192;
constexpr int HALF = 96;
constexpr int RW = 64;
constexpr int MAXH = 32;

__device__ __forceinline__ float bflo(unsigned u) { return __uint_as_float(u << 16); }
__device__ __forceinline__ float bfhi(unsigned u) { return __uint_as_float(u & 0xffff0000u); }
__device__ __forceinline__ float rbf(float x) { return __bfloat162float(__float2bfloat16(x)); }

template <int CTRL> __device__ __forceinline__ float dpp(float x) {
    return __builtin_bit_cast(float, __builtin_amdgcn_mov_dpp(__builtin_bit_cast(int, x), CTRL, 0xf, 0xf, true));
}
__device__ __forceinline__ float row16_allsum(float x) {
    x += dpp<0x128>(x);
    x += dpp<0x124>(x);
    x += dpp<0x122>(x);
    x += dpp<0x121>(x);
    return x;
}
__device__ __forceinline__ float row16_allmax(float x) {
    x = fmaxf(x, dpp<0x128>(x));
    x = fmaxf(x, dpp<0x124>(x));
    x = fmaxf(x, dpp<0x122>(x));
    x = fmaxf(x, dpp<0x121>(x));
    return x;
}
__device__ __forceinline__ float xrow_max(float x) {
    auto s = __builtin_amdgcn_permlane16_swap(__float_as_uint(x), __float_as_uint(x), false, false);
    x = fmaxf(__uint_as_float(s[0]), __uint_as_float(s[1]));
    auto t = __builtin_amdgcn_permlane32_swap(__float_as_uint(x), __float_as_uint(x), false, false);
    return fmaxf(__uint_as_float(t[0]), __uint_as_float(t[1]));
}
__device__ __forceinline__ float xrow_sum(float x) {
    auto s = __builtin_amdgcn_permlane16_swap(__float_as_uint(x), __float_as_uint(x), false, false);
    x = __uint_as_float(s[0]) + __uint_as_float(s[1]);
    auto t = __builtin_amdgcn_permlane32_swap(__float_as_uint(x), __float_as_uint(x), false, false);
    return __uint_as_float(t[0]) + __uint_as_float(t[1]);
}
__device__ __forceinline__ float sw32(float a, float b) {
    auto t = __builtin_amdgcn_permlane32_swap(__float_as_uint(a), __float_as_uint(b), false, false);
    return __uint_as_float(t[0]) + __uint_as_float(t[1]);
}
__device__ __forceinline__ float sw16(float a, float b) {
    auto t = __builtin_amdgcn_permlane16_swap(__float_as_uint(a), __float_as_uint(b), false, false);
    return __uint_as_float(t[0]) + __uint_as_float(t[1]);
}

__device__ __forceinline__ void swap32(f32x2& a, f32x2& b) {
    auto s = __builtin_amdgcn_permlane32_swap(__float_as_uint(a[0]), __float_as_uint(b[0]), false, false);
    auto t = __builtin_amdgcn_permlane32_swap(__float_as_uint(a[1]), __float_as_uint(b[1]), false, false);
    a = f32x2{__uint_as_float(s[0]), __uint_as_float(t[0])}; b = f32x2{__uint_as_float(s[1]), __uint_as_float(t[1])};
}
__device__ __forceinline__ void swap16(f32x2& a, f32x2& b) {
    auto s = __builtin_amdgcn_permlane16_swap(__float_as_uint(a[0]), __float_as_uint(b[0]), false, false);
    auto t = __builtin_amdgcn_permlane16_swap(__float_as_uint(a[1]), __float_as_uint(b[1]), false, false);
    a = f32x2{__uint_as_float(s[0]), __uint_as_float(t[0])}; b = f32x2{__uint_as_float(s[1]), __uint_as_float(t[1])};
}
#define SB() __builtin_amdgcn_sched_barrier(0)
typedef __amdgpu_buffer_rsrc_t rsrc_t;
__device__ __forceinline__ rsrc_t mkrsrc(const void* p, unsigned bytes) {
    return __builtin_amdgcn_make_buffer_rsrc(const_cast<void*>(p), 0, bytes, 0x00020000);
}
template <int AUX> __device__ __forceinline__ i32x4 bld128(rsrc_t r, unsigned voff, unsigned soff) {
    return __builtin_bit_cast(i32x4, __builtin_amdgcn_raw_buffer_load_b128(r, voff, soff, AUX));
}
template <int AUX> __device__ __forceinline__ unsigned bld16(rsrc_t r, unsigned voff, unsigned soff) {
    return __builtin_amdgcn_raw_buffer_load_b16(r, voff, soff, AUX);
}

struct HG { i32x4 d[3]; };
typedef unsigned long long u64;
typedef __attribute__((address_space(3))) unsigned char lds_u8;

__device__ __forceinline__ unsigned pk_bf16(float lo, float hi) {
    unsigned r;
    asm("v_cvt_pk_bf16_f32 %0, %1, %2" : "=v"(r) : "v"(lo), "v"(hi));
    return r;
}
__device__ __forceinline__ unsigned now32() { return (unsigned)__builtin_amdgcn_s_memrealtime(); }

constexpr int LDS_REC = 0;
constexpr int RECW = MAXH * DH * 4;
constexpr int LDS_CS = 0;
constexpr int LDS_QT = 12288;
constexpr int LDS_PF = 14336;
constexpr int LDS_QRAW = MAXH * 4 * DH * 4;
constexpr int LDS_Q = LDS_QRAW + MAXH * DH * 2;
constexpr int LDS_P = LDS_Q + 4 * DH * 4;
constexpr int LDS_SC = LDS_P + 4 * MAXH * 128;
constexpr int LDS_CNT = LDS_SC + 4 * MAXH * RW * 2;
constexpr int LDS_SK = LDS_CNT + 256;
constexpr int LDS_TR = LDS_SK + 1024;
constexpr int LDS_BYTES = LDS_TR + 4096 + 64;

struct Args {
    const int* qk; const int* qv;
    const unsigned short* k_cos; const unsigned short* k_sin;
    const unsigned short* q; const unsigned short* q_cos; const unsigned short* q_sin;
    unsigned char* part;
    unsigned short* out;
    const unsigned short* sc[4];
};
constexpr unsigned Skv = 16384;
constexpr float SCALE_LOG2 = 0.10411754627697264f;

template <int TP, int ADV, int PX, int TMIN, int CAP, int HD, int RA, int SKL, int FC, int F, int EX, int GS, int HOLD, int HL, int SL, int ID, int DBG>
__global__ __launch_bounds__(256) __attribute__((aligned(KALIGN))) void kv91_kernel(Args a)
{
    __shared__ __attribute__((aligned(16))) unsigned char smem[LDS_BYTES];
    const unsigned tstart = DBG ? now32() : 0;
    constexpr bool DS = (DBG && DBG < 5) || DBG == 24;
    constexpr int H = MAXH, PFN = 16, SJ = (F >> 9) & 3;
    const int b = blockIdx.x;
    const int tid = threadIdx.x;
    const int lane = tid & 63;
    const int wave = __builtin_amdgcn_readfirstlane(tid >> 6);
    typedef const unsigned short* const __attribute__((address_space(4))) * karg_t;
    const unsigned short* sp = ((karg_t)__builtin_amdgcn_kernarg_segment_ptr())[offsetof(Args, sc) / 8 + wave];
    if (b < PFN) {
        u64 pc; unsigned len, tl;
        asm volatile("s_getpc_b64 %0\n.Lkv_a%c3:\n\ts_mov_b32 %1, .Lkv_b%c3-.Lkv_a%c3-16\n\ts_mov_b32 %2, .Lkv_t%c3-.Lkv_a%c3" : "=s"(pc), "=s"(len), "=s"(tl) : "i"(ID));
        const unsigned o = tid * 16;
        asm volatile("s_mov_b32 m0, %5\n\ts_nop 0\n\tglobal_load_lds_dwordx4 %0, %4\n\tglobal_load_lds_dwordx4 %1, %4\n\tglobal_load_lds_dwordx4 %2, %4\n\tglobal_load_lds_dwordx4 %3, %4"
                     :: "v"(o), "v"(o + 4096), "v"(min(o + 8192, tl)), "v"(min(o + min(tl, 12288u), len)), "s"(pc), "s"((unsigned)(size_t)(lds_u8*)smem + LDS_PF) : "memory");
    }
    SB();
    const int r = lane >> 4;
    const int c = lane & 15;
    constexpr unsigned hstride = Skv * (DH * 4);
    constexpr unsigned nb = Skv / RW;
    const unsigned lds0 = (unsigned)(size_t)(lds_u8*)smem;
    const unsigned sb = b;
    const unsigned rowb = (b & 7) * 2048 + (b & 24) * 64 + (b >> 5) * 16;
    const unsigned row0 = rowb + 4 * wave;

    constexpr unsigned LS = 512, psk = 0, pep = 128, AREA = 6u << 20, NAREA = 32, pfl = H * nb * LS;
    const rsrc_t pr = mkrsrc(a.part, (NAREA + 1) * AREA);
    unsigned vg = ((lane >> 3) * Skv + rowb + (lane & 6) * 64 + (lane & 1) * 8) * 2, dg = lds0 + LDS_SC + wave * 4096;
    u64 t64;
    {
        unsigned vs = ((lane >> 3) * Skv + (rowb & ~63u) + (b & 96) * 4 + (lane & 7) * 8) * 2, v1 = vs + 16 * Skv, v2 = vs + 32 * Skv, v3 = vs + 48 * Skv;
        if (!SJ && wave < 2) vs = vg;
        asm volatile("" : "+v"(vs), "+v"(v1), "+v"(v2), "+v"(v3), "+s"(dg));
        SB();
        asm volatile("s_memrealtime %0\n\ts_mov_b32 m0, %6\n\ts_nop 0\n\tglobal_load_lds_dwordx4 %1, %5\n\t"
                     "s_add_u32 m0, m0, 1024\n\ts_nop 0\n\tglobal_load_lds_dwordx4 %2, %5\n\t"
                     "s_add_u32 m0, m0, 1024\n\ts_nop 0\n\tglobal_load_lds_dwordx4 %3, %5\n\t"
                     "s_add_u32 m0, m0, 1024\n\ts_nop 0\n\tglobal_load_lds_dwordx4 %4, %5\n\ts_waitcnt lgkmcnt(0)"
                     : "=&s"(t64) : "v"(vs), "v"(v1), "v"(v2), "v"(v3), "s"(sp), "s"(dg),
                       "s"(a.qk), "s"(a.qv), "s"(a.k_cos), "s"(a.k_sin), "s"(a.q), "s"(a.q_cos), "s"(a.q_sin), "s"(a.part), "s"(a.out) : "memory", "scc");
    }
    auto gather = [&]() {
        if (F & 2) asm volatile("s_mov_b32 m0, %2\n\ts_nop 0\n\tglobal_load_lds_dwordx4 %0, %1 nt" :: "v"(vg), "s"(sp), "s"(dg) : "memory");
        else asm volatile("s_mov_b32 m0, %2\n\ts_nop 0\n\tglobal_load_lds_dwordx4 %0, %1" :: "v"(vg), "s"(sp), "s"(dg) : "memory");
        vg += 16 * Skv; dg += 1024;
    };
    SB();
    const unsigned tdma = (unsigned)t64;
#define DMA3(V, A, B, P) "s_mov_b32 m0, " B "\n\ts_nop 0\n\t" \
                     "global_load_lds_dwordx4 " V ", " A P "\n\t" \
                     "global_load_lds_dwordx4 " V ", " A " offset:1024" P "\n\t" \
                     "global_load_lds_dwordx4 " V ", " A " offset:2048" P "\n\t"
    asm volatile(DMA3("%0", "%1", "%2", "") "s_mov_b32 m0, %4\n\ts_nop 0\n\tglobal_load_lds_dwordx4 %3, off"
                 :: "v"(lane * 16), "s"(a.q + wave * 1536), "s"(lds0 + LDS_QRAW + wave * 3072),
                    "v"(lane < 48 ? (const unsigned char*)((lane < 24 ? a.q_cos : a.q_sin - DH) + 8 * lane) : a.part + pep), "s"(lds0 + LDS_QT + wave * RECW) : "memory");
    if ((b >> 4) == 1 && wave == 3)
        asm volatile("s_mov_b32 m0, %2\n\ts_mov_b64 exec, 15\n\tglobal_load_lds_dword %0, %1\n\ts_mov_b64 exec, -1" :: "v"((((tdma >> 13) & (NAREA - 1)) + 1) * AREA + lane * 0x160000 + b * 128), "s"(a.part), "s"(lds0 + LDS_PF) : "memory");
    SB();
    constexpr int TH = 4, DL = 32, BIG = 70;
    const int stag = (int)(((sb & ~7) + wave * 2 + (b & 1)) * (TP / 2)) / 256 - TP / 4;
    const unsigned nb16 = -(tdma << 4);
    int sched = ADV * 16 + stag - 4 * (TP / 2);
    int vt = 0;
    int calm = 600 * 16, hush = 0, step = TP / 2, tmin = TMIN / 2, vth = TH, pvm = 0;
    int tru = -4, slip = 0;
    int tgt0, tnow, vm, tf;
    auto pace = [&]() {
        if (DBG == 2 && (F & 16)) tf = (now32() << 4) + nb16;
        if (F & 1)
        asm volatile("s_max_i32 %0, %3, %4\n1:\ts_memrealtime vcc\n\ts_waitcnt lgkmcnt(0)\n\ts_lshl4_add_u32 %1, vcc_lo, %5\n\ts_cmp_lt_i32 %1, %0\n\ts_cbranch_scc1 1b\n\t"
                     "s_getreg_b32 %2, hwreg(HW_REG_IB_STS, 0, 4)" : "=&s"(tgt0), "=&s"(tnow), "=s"(vm) : "s"(sched), "s"(vt), "s"(nb16) : "scc", "vcc", "memory");
        else
        asm volatile("s_max_i32 %0, %3, %4\n\ts_branch 2f\n1:\ts_sleep 1\n2:\ts_memrealtime vcc\n\ts_waitcnt lgkmcnt(0)\n\ts_lshl4_add_u32 %1, vcc_lo, %5\n\ts_cmp_lt_i32 %1, %0\n\ts_cbranch_scc1 1b\n\t"
                     "s_getreg_b32 %2, hwreg(HW_REG_IB_STS, 0, 4)" : "=&s"(tgt0), "=&s"(tnow), "=s"(vm) : "s"(sched), "s"(vt), "s"(nb16) : "scc", "vcc", "memory");
    };
    int nre = 0;
    auto rebase = [&]() { sched += (RA & 1023) * 16; step -= RA >> 10; calm = tnow + 900 * 16; ++nre; if (DBG == 2) slip += (RA & 1023) * 16; };
    auto steer = [&](bool ctl, int k = 0) {
        sched += step;
        if (!ctl) vt = max(tnow - 128, tgt0) + tmin;
        else {
            vt = tgt0 + tmin;
            if (__builtin_expect(tnow - tgt0 > 128, 0)) {
                asm volatile("");
                vt = tnow - 128 + tmin;
                if (tnow - tgt0 > (tnow > hush ? HD & 255 : HD >> 8) * 16 && tnow > calm) {
                    asm volatile("");
                    rebase();
                    if (FC >> 24) asm volatile("ds_write_b32 %0, %1" :: "v"(lds0 + LDS_CNT + 148), "v"(nre) : "memory");
                }
            }
            if (__builtin_expect(vm > vth, 0)) {
                asm volatile("" : "+s"(pvm));
                if (vt - sched < CAP * 16 || (pvm > TH && vt - sched > BIG * 16)) vt += (vm - TH) * DL;
            }
            if (FC && (((FC >> 20) & 15) >> k & 1)) {
                if (__builtin_expect(vt - sched > ((FC >> 8) & 255) * 16, 0)) {
                    asm volatile("" : "+s"(pvm));
                    if (max(vm, pvm) <= ((FC >> 16) & 15)) vt -= FC & 255;
                    if ((FC >> 24) >> k & 1) {
                        int n;
                        asm volatile("ds_read_b32 %0, %1\n\ts_waitcnt lgkmcnt(0)" : "=v"(n) : "v"(lds0 + LDS_CNT + 148) : "memory");
                        if (__builtin_amdgcn_readfirstlane(n) > nre) rebase();
                    }
                }
            }
            pvm = vm;
            if ((F >> 5) & 7) asm volatile(".rept %0\n\ts_nop 7\n\t.endr" :: "i"(3 * ((F >> 5) & 7)));
        }
        if constexpr (DBG == 2) { *reinterpret_cast<unsigned*>(smem + LDS_TR + 16 + wave * 1024 + (tru & 255) * 4) = ((unsigned)(tnow - (sched - slip - step)) >> 4) | ((F & 8) ? vm << 16 : (F & 16) ? ((tnow - tf) >> 4) << 16 : 0); ++tru; }
    };
    {
        unsigned dst = lds0 + LDS_CS + wave * RECW;
        const unsigned v0 = lane * 16 + row0 * (DH * 2), v2 = v0 + 124 * DH * 2, v1 = lane < 32 ? v0 : v2;
#pragma unroll 1
        for (int j = 0; j < 4; ++j) {
            pace();
            asm volatile("s_mov_b32 m0, %4\n\ts_nop 0\n\tglobal_load_lds_dwordx4 %0, %3 nt\n\tglobal_load_lds_dwordx4 %1, %3 offset:1024 nt\n\tglobal_load_lds_dwordx4 %2, %3 offset:2048 nt"
                         :: "v"(v0), "v"(v1), "v"(v2), "s"((j < 2 ? a.k_cos : a.k_sin) + (j & 1) * 256 * DH), "s"(dst) : "memory");
            if (SJ && j == SJ - 1 && wave < 2) gather();
            steer(false);
            sched += PX & 255;
            dst += 3072;
        }
    }
    if (!SJ && wave < 2) { vg += 16 * Skv; dg += 1024; }
    if (tid < MAXH + 6) *reinterpret_cast<unsigned*>(smem + LDS_CNT + tid * 4) = 0;
    const rsrc_t kr = mkrsrc(a.qk, H * hstride);
    const rsrc_t vr = mkrsrc(a.qv, H * hstride);
    unsigned kvo[4][3];
#pragma unroll
    for (int i = 0; i < 12; ++i) { kvo[i / 3][i % 3] = (row0 + r + i / 3 * 128) * (DH * 4) + c * 16 + i % 3 * 256; asm volatile("" : "+v"(kvo[i / 3][i % 3])); }
    auto issue = [&](HG& g, int k, rsrc_t rs, unsigned so) {
#pragma unroll
        for (int x = 0; x < 3; ++x) g.d[x] = bld128<2>(rs, kvo[k][x], so);
    };
    HG buf[4];
#define PRE(g) SB(); pace(); issue(buf[g], g, kr, 0); SB(); steer(false); SB();
    asm volatile("s_waitcnt vmcnt(%0) lgkmcnt(0)\n\ts_barrier" :: "i"(12 - 3 * SJ) : "memory");
    unsigned tbar = DS ? now32() : 0;
    PRE(0)
    const unsigned qoff = (lane < 48 ? lane : lane - 48) * 4;
    const unsigned* qt = reinterpret_cast<const unsigned*>(smem + LDS_QT + wave * RECW + qoff);
    unsigned qc0 = qt[0], qc1 = qt[HALF / 2], qs0 = qt[DH / 2], qs1 = qt[(DH + HALF) / 2];
    u64 tag;
    {
        const u32x2 e = *reinterpret_cast<const u32x2*>(smem + LDS_QT + wave * RECW + 768);
        tag = (((u64)__builtin_amdgcn_readfirstlane(e[1]) << 32) | __builtin_amdgcn_readfirstlane(e[0])) * 6364136223846793005ull + 1442695040888963407ull;
        asm volatile("" : "+s"(tag));
    }
    const unsigned t32 = tag >> 32;
    unsigned qraw = lds0 + LDS_QRAW + qoff;
    unsigned char* qrot = smem + LDS_Q + wave * (DH * 4) + 2 * qoff;
    unsigned q_lo, q_hi;
    auto getq = [&]() {
        asm volatile("" : "+v"(qraw));
        q_lo = *(const __attribute__((address_space(3))) unsigned*)(size_t)qraw, q_hi = *(const __attribute__((address_space(3))) unsigned*)(size_t)(qraw + HALF * 2);
        qraw += DH * 2;
    };
    auto rotq = [&]() {
        const f32x2 lo = f32x2{bflo(q_lo), bfhi(q_lo)}, hi = f32x2{bflo(q_hi), bfhi(q_hi)};
        const f32x2 y1 = lo * f32x2{bflo(qc0), bfhi(qc0)}, y2 = -hi * f32x2{bflo(qs0), bfhi(qs0)};
        const f32x2 y3 = hi * f32x2{bflo(qc1), bfhi(qc1)}, y4 = lo * f32x2{bflo(qs1), bfhi(qs1)};
        const unsigned x1 = pk_bf16(y1[0], y1[1]), x2 = pk_bf16(y2[0], y2[1]), x3 = pk_bf16(y3[0], y3[1]), x4 = pk_bf16(y4[0], y4[1]);
        const f32x2 z1 = f32x2{bflo(x1), bfhi(x1)} + f32x2{bflo(x2), bfhi(x2)}, z2 = f32x2{bflo(x3), bfhi(x3)} + f32x2{bflo(x4), bfhi(x4)};
        const unsigned l2 = pk_bf16(z1[0], z1[1]), h2 = pk_bf16(z2[0], z2[1]);
        *reinterpret_cast<f32x2*>(qrot) = f32x2{bflo(l2), bfhi(l2)};
        *reinterpret_cast<f32x2*>(qrot + HALF * 4) = f32x2{bflo(h2), bfhi(h2)};
        asm volatile("" ::: "memory");
    };
    f32x4 qa[3], qb[3];
    const unsigned char* qh = smem + LDS_Q + wave * (DH * 4) + 16 * c;
    const unsigned char* qh1 = qh + (c < 8 ? 64 + HALF : 64 - HALF) * 4;
    auto loadq = [&]() {
        asm volatile("" ::: "memory");
        qa[0] = *reinterpret_cast<const f32x4*>(qh); SB();
        qb[0] = *reinterpret_cast<const f32x4*>(qh + HALF * 4); SB();
        qa[1] = *reinterpret_cast<const f32x4*>(qh + 256); SB();
        qb[1] = *reinterpret_cast<const f32x4*>(qh1); SB();
        qa[2] = *reinterpret_cast<const f32x4*>(qh + 512); SB();
        qb[2] = *reinterpret_cast<const f32x4*>(qh + (128 - HALF) * 4);
        asm volatile("" ::: "memory");
    };
    getq(); rotq();
    if ((b >> 3) == 4 && wave == 3) __builtin_amdgcn_raw_buffer_store_b128(u32x4{tdma, t32, tdma, (unsigned)tag}, pr, lane == 0 ? psk + (b & 7) * 16 : 0x80000000u, 0, 16);
    PRE(1)
    f32x2 cc[4][3][2], ss[4][3][2];
    const unsigned char* cb = smem + LDS_CS + wave * RECW + r * (DH * 2) + c * 8;
    asm volatile("s_waitcnt vmcnt(12)" ::: "memory");
#pragma unroll
    for (int k = 0; k < 4; ++k)
#pragma unroll
        for (int x = 0; x < 3; ++x) {
            const u32x2 t = *reinterpret_cast<const u32x2*>(cb + k * 1536 + 128 * x);
            cc[k][x][0] = f32x2{bflo(t[0]), bfhi(t[0])}; cc[k][x][1] = f32x2{bflo(t[1]), bfhi(t[1])};
            asm volatile("" : "+v"(cc[k][x][0]), "+v"(cc[k][x][1]));
        }
    PRE(2)
    loadq();
    const unsigned char* scp = smem + LDS_SC + (c & 1) * 4096 + (4 * (c & 12) + 4 * wave + r) * 2;
    unsigned sc = *reinterpret_cast<const unsigned short*>(scp);
    auto loadsin = [&](int k) {
        const unsigned char* sb1 = cb + (c < 8 ? (64 + HALF) * 2 : (64 - HALF) * 2);
        const unsigned sg = c < 8 ? 0u : 0x80008000u;
        u32x2 t[3];
        t[0] = *reinterpret_cast<const u32x2*>(cb + 6144 + k * 1536 + HALF * 2);
        t[1] = *reinterpret_cast<const u32x2*>(sb1 + 6144 + k * 1536) ^ u32x2{sg, sg};
        t[2] = *reinterpret_cast<const u32x2*>(cb + 6144 + k * 1536 + (128 - HALF) * 2) ^ u32x2{0x80008000u, 0x80008000u};
#pragma unroll
        for (int x = 0; x < 3; ++x) {
            ss[k][x][0] = f32x2{bflo(t[x][0]), bfhi(t[x][0])}; ss[k][x][1] = f32x2{bflo(t[x][1]), bfhi(t[x][1])};
            asm volatile("" : "+v"(ss[k][x][0]), "+v"(ss[k][x][1]));
        }
    };
    asm volatile("s_waitcnt vmcnt(12)" ::: "memory");
    loadsin(0); loadsin(1);
    PRE(3)
    asm volatile("s_waitcnt vmcnt(12)" ::: "memory");
    loadsin(2); loadsin(3);
    unsigned th1 = 0, th4 = 0;
    const unsigned char* prec = smem + LDS_P + wave * (H * 128) + r * 32 + (c & 12);
    unsigned pw_ = lds0 + LDS_P + wave * (H * 128) + r * 32 + (c & 12) + (c & 3) * 128;
    float sm, sp_, sacc = 0.f;
    auto softmax1 = [&]() {
        float u;
        asm volatile("v_max_f32_dpp %0, %2, %2 row_ror:4 row_mask:0xf bank_mask:0xf\n\ts_nop 1\n\tv_max_f32_dpp %0, %0, %0 row_ror:8 row_mask:0xf bank_mask:0xf\n\tv_mov_b32 %1, %0\n\ts_nop 1\n\t"
                     "v_permlane16_swap_b32 %0, %1\n\tv_max_f32 %0, %0, %1\n\tv_mov_b32 %1, %0\n\ts_nop 1\n\tv_permlane32_swap_b32 %0, %1\n\tv_max_f32 %0, %0, %1" : "=&v"(sm), "=&v"(u) : "v"(sacc));
        sp_ = __builtin_amdgcn_exp2f(sacc - sm);
        asm volatile("" : "+v"(sp_));
    };
    auto softmax2 = [&]() {
        float l = sp_ + dpp<0x124>(sp_);
        l = xrow_sum(l + dpp<0x128>(l));
        asm volatile("ds_write2_b32 %0, %1, %2 offset1:4" :: "v"(pw_), "v"(sp_), "v"((c & 4) ? l : sm) : "memory");
        pw_ += 512;
    };

    auto skew = [&](int h) {
        if (wave == 1 && h == 1 + ((b >> 3) & 3))
            asm volatile("s_mov_b32 m0, %2\n\ts_nop 0\n\tglobal_load_lds_dwordx4 %0, %1 sc1" :: "v"(psk + (lane & 7) * 16), "s"(a.part), "s"(lds0 + LDS_SK) : "memory");
        if (h == 6) {
            const u32x2 e = *reinterpret_cast<const u32x2*>(smem + LDS_SK + (lane & 7) * 16);
            const int d = (int)(tdma - e[0]);
            if (__all(e[1] == t32 && d > -400 && d < 400)) {
                int sum = d + __builtin_amdgcn_mov_dpp(d, 0xb1, 0xf, 0xf, true), mn = min(d, __builtin_amdgcn_mov_dpp(d, 0xb1, 0xf, 0xf, true));
                sum += __builtin_amdgcn_mov_dpp(sum, 0x4e, 0xf, 0xf, true); mn = min(mn, __builtin_amdgcn_mov_dpp(mn, 0x4e, 0xf, 0xf, true));
                sum += __builtin_amdgcn_mov_dpp(sum, 0x124, 0xf, 0xf, true); mn = min(mn, __builtin_amdgcn_mov_dpp(mn, 0x124, 0xf, 0xf, true));
                sum = __builtin_amdgcn_readfirstlane(min(sum, 8 * (mn + (SKL & 255))));
                const int adj = (sum * 73 + 4096) >> 13;
                step -= adj; tmin -= adj;
                sched -= 2 * sum - 224 * adj;
            }
        }
    };

    rsrc_t nx = kr;
    unsigned nso = 0, sm4 = 0x11111110;
    unsigned gm = (wave < 2 ? 0x4081u << 1 : 0x204081u << 4) << (((b >> 3) + 2 * wave) & 3), evk = gm * 5 | 0x5e, gn = 0;
    asm volatile("" : "+s"(sm4), "+s"(gm), "+s"(evk));
#pragma unroll 1
    for (int h = 0; h < H; ++h) {
        float scf = bflo(sc);
        asm volatile("" : "+v"(scf) :: "memory");
        SB();
        nso += hstride;
        scp += 128;
        if (__builtin_expect((h & 7) == 7, 0)) {
            const unsigned fa = lds0 + LDS_CNT + 128 + ((h + 1) >> 5) * 8;
            u32x2 n;
            do asm volatile("ds_read_b64 %0, %1\n\ts_waitcnt lgkmcnt(0)" : "=v"(n) : "v"(fa) : "memory");
            while (__any(min(n[0], n[1]) * 8 <= h));
            if (h == H - 1) { nx = vr; nso = 0; scp += 4096; }
        }
        sc = *reinterpret_cast<const unsigned short*>(scp);
        SB();
        float st[2];
#define KSTEP(k) { \
            __builtin_amdgcn_s_waitcnt(0xf79); SB();        \
            const float kz = dpp<0x151 + 4 * (k)>(scf); \
            const f32x2 kz2 = f32x2{kz, kz}; \
            f32x2 a2 = f32x2{0.f, 0.f}; \
            _Pragma("unroll") for (int x = 0; x < 3; ++x) { \
                _Pragma("unroll") for (int e = 0; e < 2; ++e) { \
                    const f32x2 w = __builtin_elementwise_fma(f32x2{qb[x][2 * e], qb[x][2 * e + 1]}, ss[k][x][e], f32x2{qa[x][2 * e], qa[x][2 * e + 1]} * cc[k][x][e]); \
                    const f32x2 kf = f32x2{(float)buf[k].d[x][2 * e], (float)buf[k].d[x][2 * e + 1]} - kz2; \
                    a2 = __builtin_elementwise_fma(kf, w, a2); \
                } \
            } \
            float sa = a2[0] + a2[1]; \
            asm volatile("" : "+v"(sa) :: "memory"); \
            SB(); pace(); issue(buf[k], k, nx, nso); SB(); steer(true, k); SB(); \
              \
            if ((k) < 2) asm volatile("v_add_f32_dpp %0, %1, %1 row_ror:8 row_mask:0xf bank_mask:0xf" : "=v"(st[(k) & 1]) : "v"(sa)); \
            else asm volatile("v_add_f32_dpp %0, %1, %1 row_ror:8 row_mask:0xf bank_mask:0xc" : "+v"(st[(k) & 1]) : "v"(sa)); \
            if ((k) == 0) { getq(); SB(); if (sm4 >> h & 1) softmax1(); SB(); } \
            if ((k) == 1) { rotq(); SB(); } \
            if ((k) == 2) { \
                asm volatile("" : "+s"(sm4)); \
                if (sm4 >> h & 1) softmax2(); \
                SB(); \
                if (__builtin_expect(evk >> h & 1, 0)) { \
                    if (gm >> h & 1) gather(); \
                    if (gm * 4 >> h & 1) *reinterpret_cast<unsigned*>(smem + LDS_CNT + 128 + wave * 4) = ++gn;        \
                    skew(h); \
                } \
                SB(); \
            } \
            if ((k) == 3) { loadq(); SB(); } }
        KSTEP(0) KSTEP(1) KSTEP(2) KSTEP(3)
        asm volatile("v_add_f32_dpp %0, %0, %0 row_shl:4 row_mask:0xf bank_mask:0x5\n\tv_add_f32_dpp %0, %3, %3 row_shr:4 row_mask:0xf bank_mask:0xa\n\t"
                     "v_mul_f32 %1, %4, %1\n\tv_mov_b32_dpp %2, %2 quad_perm:[1,2,3,3] row_mask:0xf bank_mask:0xf\n\t"
                     "v_add_f32_dpp %0, %0, %0 quad_perm:[1,0,3,2] row_mask:0xf bank_mask:0xf\n\ts_nop 1\n\tv_add_f32_dpp %0, %0, %0 quad_perm:[2,3,0,1] row_mask:0xf bank_mask:0xf\n\t"
                     "v_mul_f32_dpp %0, %1, %0 quad_perm:[0,0,0,0] row_mask:0xf bank_mask:0xf\n\tv_cndmask_b32 %2, %2, %0, %5"
                     : "+v"(st[0]), "+v"(scf), "+v"(sacc) : "v"(st[1]), "s"(SCALE_LOG2), "s"(0x8888888888888888ull));
        if (DS) { if (h == 7) th1 = now32(); }
    }
    if (DS) th4 = now32();

    unsigned rl = lds0 + LDS_REC + lane * 16, rh = rl + 2 * RECW;
    unsigned s0, s1, k1;
    asm volatile("" : "=v"(s0), "=v"(s1), "=v"(k1));
    unsigned c7 = 0x70007, c8 = 0xfff8fff8u, c9 = 0x06000000;
    asm volatile("" : "+s"(c7), "+s"(c8), "+s"(c9));
    auto ev_s = [&]() {
        unsigned l = lane;
        asm volatile("" : "+v"(l));
        const unsigned t = tag >> (3 * (l & 14) + 12 * (l & 1));
        s0 = (t & 63) * 0x2001 & 0x70007;
        s1 = (l & 1) ? 0 : (t >> 6 & 63) * 0x2001 & 0x70007;
        k1 = (l & 1) * 0x2001;
        asm volatile("" : "+v"(s0), "+v"(s1), "+v"(k1));
    };
    auto pack = [&](f32x4 v) {
        unsigned h0, h1, m, z;
        asm("v_max3_f32 %2, |%4|, |%5|, |%6|\n\tv_max3_f32 %2, %2, |%7|, %13\n\ts_nop 1\n\tv_max_u32_dpp %2, %2, %2 quad_perm:[1,0,3,2] row_mask:0xf bank_mask:0xf\n\t"
            "v_and_b32 %3, 0x7e000000, %2\n\tv_lshrrev_b32 %2, 25, %2\n\tv_sub_u32 %3, 0x84800000, %3\n\tv_mul_u32_u24 %2, %2, %8\n\t"
            "v_fma_mixlo_f16 %0, %4, %3, 0\n\tv_fma_mixlo_f16 %1, %6, %3, 0\n\tv_fma_mixhi_f16 %0, %5, %3, 0\n\tv_fma_mixhi_f16 %1, %7, %3, 0\n\tv_and_or_b32 %2, %2, %11, %10\n\t"
            "v_pk_add_u16 %0, %0, 4 op_sel_hi:[1,0]\n\tv_pk_add_u16 %1, %1, 4 op_sel_hi:[1,0]\n\tv_and_or_b32 %0, %0, %12, %9\n\tv_and_or_b32 %1, %1, %12, %2"
            : "=&v"(h0), "=&v"(h1), "=&v"(m), "=&v"(z) : "v"(v[0]), "v"(v[1]), "v"(v[2]), "v"(v[3]), "v"(k1), "v"(s0), "v"(s1), "s"(c7), "s"(c8), "s"(c9));
        return u32x2{h0, h1};
    };
#define LDREC(hd) "ds_read_b128 %0, %4 offset:768*" #hd "\n\tds_read_b128 %1, %4 offset:768*" #hd "+24576\n\tds_read_b128 %2, %5 offset:768*" #hd "\n\tds_read_b128 %3, %5 offset:768*" #hd "+24576"
    auto ldrec = [&](unsigned o, f32x4 (&p)[4]) {
        asm volatile(LDREC(0) : "=&v"(p[0]), "=&v"(p[1]), "=&v"(p[2]), "=&v"(p[3]) : "v"(rl + o), "v"(rh + o) : "memory");
    };
    auto add4 = [&](f32x4 (&p)[4]) { return pack((p[0] + p[1]) + (p[2] + p[3])); };
    auto sum4 = [&](unsigned o) {
        f32x4 p[4];
        ldrec(o, p);
        asm volatile("s_waitcnt lgkmcnt(0)" : "+v"(p[0]), "+v"(p[1]), "+v"(p[2]), "+v"(p[3]) :: "memory");
        return add4(p);
    };
    auto wide = [&](u32x2 t) {
        return u32x4{t[0], t[1], (unsigned)__builtin_amdgcn_mov_dpp(t[0], 0xb1, 0xf, 0xf, true), (unsigned)__builtin_amdgcn_mov_dpp(t[1], 0xb1, 0xf, 0xf, true)};
    };
    auto merge = [&](int hd) {
        f32x4 p[4];
        f32x2 ma, mb;
        f32x4 xa, xb;
        asm volatile("ds_read2st64_b64 %0, %2 offset1:8\n\tds_read2st64_b64 %1, %2 offset0:16 offset1:24" : "=&v"(xa), "=&v"(xb) : "v"(lds0 + LDS_P + hd * 128 + 16) : "memory");
        ldrec(hd * (DH * 4), p);
        asm volatile("s_waitcnt lgkmcnt(4)" : "+v"(xa), "+v"(xb) :: "memory");
        const float M = __builtin_amdgcn_fmed3f(__builtin_amdgcn_fmed3f(xa[0], xa[2], __builtin_inff()), __builtin_amdgcn_fmed3f(xb[0], xb[2], __builtin_inff()), __builtin_inff());
        ma = f32x2{__builtin_amdgcn_exp2f(xa[0] - M), __builtin_amdgcn_exp2f(xa[2] - M)};
        mb = f32x2{__builtin_amdgcn_exp2f(xb[0] - M), __builtin_amdgcn_exp2f(xb[2] - M)};
        const float rL = __builtin_amdgcn_rcpf((ma[0] * xa[1] + ma[1] * xa[3]) + (mb[0] * xb[1] + mb[1] * xb[3]));
        ma *= rL; mb *= rL;
        asm volatile("s_waitcnt lgkmcnt(0)" : "+v"(ma), "+v"(mb), "+v"(p[0]), "+v"(p[1]), "+v"(p[2]), "+v"(p[3]) :: "memory");
        return pack(ma[0] * p[0] + ma[1] * p[1] + mb[0] * p[2] + mb[1] * p[3]);
    };
    constexpr unsigned LSTR = 128;
    constexpr int NC = 3 * H;
    const int cn = sb;
    const unsigned hc = 4 * (cn & 7) + ((cn >> 3) & 3);
    unsigned old = 0, area = 0, fo = 0;
    unsigned flv = 0;
    float gw = 1.f, mw = 0.f;
    f32x2 fs[4] = {};
    constexpr int LDS_ML = LDS_QRAW + 2048;
#define BAD(y) ((unsigned)(y)[1] != t32 || (unsigned)(y)[3] != (unsigned)tag)
    auto ev_all = [&](int hd) {
        unsigned n;
        asm volatile("1:\tds_read_b32 %0, %1\n\ts_waitcnt lgkmcnt(0)\n\tv_cmp_ne_u32 vcc, 4, %0\n\ts_cbranch_vccnz 1b" : "=&v"(n) : "v"(lds0 + LDS_CNT + hd * 4) : "memory", "vcc");
    };
    auto quiet = [&]() { vth = 15; hush = vt + 500 * 16; };
    auto ev_ml = [&]() {
        f32x2 ml[4];
#pragma unroll
        for (int w = 0; w < 4; ++w) ml[w] = *reinterpret_cast<const f32x2*>(smem + LDS_P + ((wave + w) & 3) * (H * 128) + (lane & 31) * 128 + 16);
        const float M = fmaxf(fmaxf(ml[0][0], ml[1][0]), fmaxf(ml[2][0], ml[3][0]));
        const float e0 = __builtin_amdgcn_exp2f(ml[0][0] - M);
        const float L = (e0 * ml[0][1] + __builtin_amdgcn_exp2f(ml[1][0] - M) * ml[1][1]) + (__builtin_amdgcn_exp2f(ml[2][0] - M) * ml[2][1] + __builtin_amdgcn_exp2f(ml[3][0] - M) * ml[3][1]);
        flv = ((__float_as_uint(M + __builtin_amdgcn_logf(L)) + 0x80) & 0xffffff00u) | ((unsigned)(tag >> (8 * (lane & 7))) & 255);
        gw = e0 * __builtin_amdgcn_rcpf(L);
        asm volatile("" : "+v"(flv), "+v"(gw));
    };
    auto ev_area = [&]() {
        u32x4 e = *reinterpret_cast<const u32x4*>(smem + LDS_SK);
        if (__builtin_expect(__any(BAD(e)), 0)) {
#pragma unroll 1
            for (int n = 0; n < (1 << 20) && BAD(e); ++n)
                asm volatile("global_load_dwordx4 %0, %1, %2 sc1\n\ts_waitcnt vmcnt(0)" : "=v"(e) : "v"(psk), "s"(a.part) : "memory");
        }
        area = (((__builtin_amdgcn_readfirstlane(e[0]) >> 13) & (NAREA - 1)) + 1) * AREA + 4096;
        asm volatile("" : "+s"(area));
    };
    unsigned eo[5], xo[8], oo, po, wo, ta, tb, tc, ea, txb, tgb;
    u32x4 ev[4];
    f32x4 a0, a1;
    asm volatile("" : "=v"(eo[0]), "=v"(eo[1]), "=v"(eo[2]), "=v"(eo[3]), "=v"(eo[4]), "=v"(xo[0]), "=v"(xo[1]), "=v"(xo[2]), "=v"(xo[3]), "=v"(xo[4]), "=v"(xo[5]), "=v"(xo[6]), "=v"(xo[7]),
                      "=v"(oo), "=v"(po), "=v"(wo), "=v"(ta), "=v"(tb), "=v"(tc), "=v"(ea), "=v"(ev[0]), "=v"(ev[1]), "=v"(ev[2]), "=v"(a0), "=v"(a1));
    txb = nb16 + step + stag - EX * 16 - (sb >> 3) * GS * 4;
    tgb = nb16 + stag - (hc == H - 1 ? HL : HOLD & 255) * 16;
    asm volatile("" : "+s"(txb), "+s"(tgb));
    auto ev_put = [&]() {
        asm volatile("s_mov_b32 exec_hi, 0\n\tbuffer_store_dword %0, %1, %2, %3 offen sc1\n\ts_mov_b32 exec_hi, -1" :: "v"(flv), "v"(lane * 4), "s"(pr), "s"(area + pfl + sb * LSTR) : "memory");
    };
    auto ev_get = [&]() {
        unsigned l = lane;
        asm volatile("" : "+v"(l));
        asm volatile("" : "+s"(area));
        fo = area + pfl + 8 * wave * LSTR + (cn & 7) * 16 + (((l & 56) << 2) + (l & 7)) * LSTR;
        asm volatile("s_mov_b32 m0, %2\n\ts_nop 0\n\tglobal_load_lds_dwordx4 %0, %1 sc1" :: "v"(fo), "s"(a.part), "s"(lds0 + LDS_ML + wave * 1024) : "memory");
    };
    auto ev_w1 = [&]() {
        u32x4 y = *reinterpret_cast<const u32x4*>(smem + LDS_ML + wave * 1024 + lane * 16);
        const unsigned tq = (cn & 1) ? t32 : (unsigned)tag;
#define BADW(y) (__builtin_amdgcn_perm(__builtin_amdgcn_perm((y)[3], (y)[2], 0x0400), __builtin_amdgcn_perm((y)[1], (y)[0], 0x0400), 0x05040100) != tq)
        if (__builtin_expect(__any(BADW(y)), 0)) {
#pragma unroll 1
            for (int n = 0; n < (1 << 20) && BADW(y); ++n)
                asm volatile("global_load_dwordx4 %0, %1, %2 sc1\n\ts_waitcnt vmcnt(0)" : "=v"(y) : "v"(fo), "s"(a.part) : "memory");
        }
        const unsigned se = (cn & 8) ? 0x0706050cu : 0x0302010cu;
        const float m = __uint_as_float((cn & 16) ? __builtin_amdgcn_perm(y[3], y[2], se) : __builtin_amdgcn_perm(y[1], y[0], se));
        float u;
        asm volatile("s_nop 1\n\tv_max_f32_dpp %0, %2, %2 row_ror:8 row_mask:0xf bank_mask:0xf\n\ts_nop 1\n\tv_max_f32_dpp %0, %0, %0 row_ror:4 row_mask:0xf bank_mask:0xf\n\ts_nop 1\n\t"
                     "v_max_f32_dpp %0, %0, %0 row_ror:2 row_mask:0xf bank_mask:0xf\n\ts_nop 1\n\tv_max_f32_dpp %0, %0, %0 row_ror:1 row_mask:0xf bank_mask:0xf\n\tv_mov_b32 %1, %0\n\ts_nop 1\n\t"
                     "v_permlane16_swap_b32 %0, %1\n\ts_nop 0\n\tv_max_f32 %0, %0, %1\n\tv_mov_b32 %1, %0\n\ts_nop 1\n\tv_permlane32_swap_b32 %0, %1\n\ts_nop 0\n\tv_max_f32 %0, %0, %1" : "=&v"(mw), "=&v"(u) : "v"(m));
        fs[0][0] = __builtin_amdgcn_exp2f(m - mw);
        asm volatile("" : "+v"(fs[0]));
    };
    auto ev_w2 = [&]() {
        const f32x2 f = fs[0];
        unsigned bp = (lane >> 3) * 4;
        asm volatile("ds_bpermute_b32 %0, %8, %9\n\tds_bpermute_b32 %1, %8, %9 offset:32\n\tds_bpermute_b32 %2, %8, %9 offset:64\n\tds_bpermute_b32 %3, %8, %9 offset:96\n\t"
                     "ds_bpermute_b32 %4, %8, %9 offset:128\n\tds_bpermute_b32 %5, %8, %9 offset:160\n\tds_bpermute_b32 %6, %8, %9 offset:192\n\tds_bpermute_b32 %7, %8, %9 offset:224"
                     : "=&v"(fs[0][0]), "=&v"(fs[0][1]), "=&v"(fs[1][0]), "=&v"(fs[1][1]), "=&v"(fs[2][0]), "=&v"(fs[2][1]), "=&v"(fs[3][0]), "=&v"(fs[3][1]) : "v"(bp), "v"(f[0]) : "memory");
        *reinterpret_cast<f32x2*>(smem + LDS_QRAW + 1024 + wave * 8) = f32x2{mw, xrow_sum(row16_allsum(f[0]))};
        asm volatile("s_waitcnt lgkmcnt(0)" : "+v"(fs[0]), "+v"(fs[1]), "+v"(fs[2]), "+v"(fs[3]) :: "memory");
    };
#define HERE unsigned l = lane, w = wave, n = sb; asm volatile("" : "+v"(l)); asm volatile("" : "+s"(w), "+s"(n));
    auto ev_a = [&]() {
        HERE
        const unsigned t = (((l >> 5) * nb + n) * LS + (l & 31) * 16) | ((l & 31) + 8) << 26 & 0x80000000u;
        eo[0] = t + 6 * w * nb * LS; eo[1] = eo[0] + 2 * nb * LS; eo[2] = eo[0] + 4 * nb * LS;
        eo[3] = t + (24 + 2 * w) * nb * LS;
        asm volatile("" : "+v"(eo[0]), "+v"(eo[1]), "+v"(eo[2]), "+v"(eo[3]));
    };
    auto ev_a2 = [&]() {
        HERE
        eo[4] = (((H - 1) * nb + n) * LS + l * 8) | l << 31 | (l + 16) << 25 & 0x80000000u;
        eo[3] = w == 3 ? eo[4] - nb * LS : eo[3];
        ea = lds0 + LDS_REC + (6 * w + (l >> 5)) * (DH * 4) + (l & 31) * 16;
        asm volatile("" : "+v"(eo[3]), "+v"(eo[4]), "+v"(ea));
    };
    auto ev_b = [&]() {
        HERE
        xo[0] = (hc * nb + 8 * w + (l >> 3)) * LS + (n >> 5) * 128 + (l & 7) * 16;
#pragma unroll
        for (int i = 1; i < 8; ++i) xo[i] = xo[0] + 32 * i * LS;
        asm volatile("" : "+v"(xo[0]), "+v"(xo[1]), "+v"(xo[2]), "+v"(xo[3]), "+v"(xo[4]), "+v"(xo[5]), "+v"(xo[6]), "+v"(xo[7]));
    };
    auto ev_c = [&]() {
        HERE
        oo = (hc * DH + (n >> 5) * 64 + 8 * (l & 7) + (l >> 3)) * 2;
        po = lds0 + LDS_QRAW + l * 4; wo = po + w * 256;
        asm volatile("" : "+v"(oo), "+v"(po), "+v"(wo));
    };
    auto ev_c2 = [&]() {
        HERE
        const unsigned t = tag >> 6 * (l & 7);
        ta = (t & 63) * 0x2001 & c7; tb = (t >> 6 & 63) * 0x2001 & c7; tc = (t >> 12 & 63) * 0x2001 & c7;
        asm volatile("" : "+v"(ta), "+v"(tb), "+v"(tc));
    };
    auto ev_d = [&]() {
        HERE
        ev_all(24);
        asm volatile("ds_read_b128 %0, %5\n\tds_read_b128 %1, %5 offset:1536\n\tds_read_b128 %2, %5 offset:3072\n\tds_read_b128 %3, %6\n\tds_read_b128 %4, %6 offset:16\n\ts_waitcnt lgkmcnt(0)"
                     : "+v"(ev[0]), "+v"(ev[1]), "+v"(ev[2]), "+v"(a0), "+v"(a1) : "v"(ea), "v"(lds0 + LDS_QRAW + 1024) : "memory");
        ea += (24 - 4 * w) * (DH * 4);
        asm volatile("" : "+v"(ea));
        if (4 * n + w == 1) { asm volatile("s_mov_b64 exec, 1\n\tbuffer_store_dwordx2 %0, off, %1, 0 offset:%2 sc1\n\ts_mov_b64 exec, -1" :: "v"(u32x2{(unsigned)tag, t32}), "s"(pr), "i"(pep) : "memory"); quiet(); }
    };
    auto ev_t = [&]() {
        HERE
        if (w == 1 && (n & (HOLD >> 8 & 255)) == 0) {
            asm volatile("" : "+s"(area));
            asm volatile("s_mov_b32 m0, %2\n\ts_mov_b64 exec, 3\n\tglobal_load_lds_dword %0, %1\n\ts_mov_b64 exec, -1" :: "v"(area + (l << 21) + n * LS + 384), "s"(a.part), "s"(lds0 + LDS_SK) : "memory");
            quiet();
        }
    };
    auto ev_e = [&]() {
        const float M = fmaxf(fmaxf(a0[0], a0[2]), fmaxf(a1[0], a1[2]));
        const float gf = __builtin_amdgcn_exp2f(mw - M) * __builtin_amdgcn_rcpf((__builtin_amdgcn_exp2f(a0[0] - M) * a0[1] + __builtin_amdgcn_exp2f(a0[2] - M) * a0[3]) + (__builtin_amdgcn_exp2f(a1[0] - M) * a1[1] + __builtin_amdgcn_exp2f(a1[2] - M) * a1[3]));
#pragma unroll
        for (int i = 0; i < 4; ++i) fs[i] *= gf;
        asm volatile("" : "+v"(fs[0]), "+v"(fs[1]), "+v"(fs[2]), "+v"(fs[3]));
    };
    constexpr int G1 = 5, G2 = 27;
    const int hput = wave == ((b >> 5) & 3) ? G1 + 2 + ((b >> 3) & 3) : -9, hget = cn < NC ? G1 + 7 + ((cn >> 3) & 7) : -9;
    unsigned ev1 = 2 | 1 << G1 | 15 << (G1 + 2) | 9u << (hget & 31) | 1u << (H - 1), ev2 = 2 | 3 << G1 | 4u << (hget & 31);
    asm volatile("" : "+s"(ev1), "+s"(ev2), "+s"(area));
    float pv = *reinterpret_cast<const float*>(prec);
    f32x2 acc[3][2];
    float y, yn, zn;
    auto prep = [&](int hn) {
        const float s = bflo(sc);
        yn = s * (pv * __uint_as_float(__builtin_amdgcn_readlane(__float_as_uint(gw), hn)));
        float z = yn * dpp<0x55>(s);
        z += dpp<0x124>(z);
        z += dpp<0x128>(z);
        zn = -dpp<0x150>(z);
        asm volatile("" : "+v"(yn), "+v"(zn) :: "memory");
        SB();
        scp += 128;
        sc = *reinterpret_cast<const unsigned short*>(scp);
        prec += 128;
        pv = *reinterpret_cast<const float*>(prec);
        SB();
    };
    prep(0);
#define VCOMP(k) VPART(k, 0, 3)
#define VPART(k, x0, x1) { \
        if ((k) == 0) y = yn; \
        const f32x2 pz = f32x2{dpp<0x150 + 4 * (k)>(y), zn}; \
        _Pragma("unroll") for (int x = x0; x < x1; ++x) \
            _Pragma("unroll") for (int e = 0; e < 2; ++e) { \
                const f32x2 vf = f32x2{(float)buf[k].d[x][2 * e], (float)buf[k].d[x][2 * e + 1]}; \
                if ((k) == 0) asm volatile("v_pk_fma_f32 %0, %1, %2, %1 op_sel:[0,0,1] op_sel_hi:[0,1,1]" : "=v"(acc[x][e]) : "v"(pz), "v"(vf)); \
                else asm volatile("v_pk_fma_f32 %0, %1, %2, %0 op_sel_hi:[0,1,1]" : "+v"(acc[x][e]) : "v"(pz), "v"(vf)); \
            } \
        asm volatile("" ::: "memory"); \
        SB(); }
#define ON(hn, x, f) { int q = (hn) - (x); asm volatile("" : "+s"(q)); if (__builtin_expect(q == 0, 0)) f(); }
#define VSTEP(k, hn) { \
        __builtin_amdgcn_s_waitcnt(0xf79); SB();        \
        VCOMP(k) pace(); issue(buf[k], k, vr, nso); SB(); steer(true, k); SB(); \
        if ((k) == 1) { \
            vth = TH; asm volatile("" : "+s"(vth)); \
            if (__builtin_expect(ev1 >> (hn) & 1, 0)) { \
                if ((hn) == 1) { softmax1(); softmax2(); }        \
                if ((hn) == G1) ev_all(0);        \
                if ((hn) == hput) { ev_put(); quiet(); } \
                if ((hn) == hget) { ev_get(); quiet(); } \
                if ((hn) == hget + 3) ev_w2(); \
                if ((hn) == H - 1) tmin = 0; \
            } \
            SB(); \
            if (__builtin_expect((hn) >= G2 - 4, 0)) { ON(hn, G2 - 4, ev_a) ON(hn, G2 - 3, ev_a2) ON(hn, G2 - 2, ev_b) ON(hn, G2 - 1, ev_c) ON(hn, G2, ev_d) ON(hn, G2 + 1, ev_e) ON(hn, G2 + 2, ev_c2) if (HOLD >> 16) ON(hn, HOLD >> 16, ev_t) \
                if ((hn) == (PX >> 16)) tmin -= (PX >> 8) & 255;        \
                if ((SKL >> 8) && (hn) >= (SKL >> 8)) vth = 15; \
                asm volatile("" : "+s"(tmin), "+s"(vth)); } \
            SB(); \
        } \
        if ((k) == 2) { \
            if (__builtin_expect(ev2 >> (hn) & 1, 0)) { \
                ON(hn, 1, ev_s) \
                if ((hn) == G1) ev_ml(); \
                if ((hn) == G1 + 1) ev_area(); \
                if ((hn) == hget + 2) ev_w1(); \
            } \
            SB(); \
            prep(hn); \
        } }
    unsigned ra = lds0 + LDS_REC + wave * RECW + (4 * c + r) * 4, ca = lds0 + LDS_CNT, one = 1;
    asm volatile("" : "+v"(ra), "+v"(ca), "+v"(one));
    auto record = [&]() {
        float w[3];
        f32x2 u[3];
#pragma unroll
        for (int x = 0; x < 3; ++x) { swap32(acc[x][0], acc[x][1]); SB(); }
#pragma unroll
        for (int x = 0; x < 3; ++x) { u[x] = acc[x][0] + acc[x][1]; SB(); }
#pragma unroll
        for (int x = 0; x < 3; ++x) {
            auto t = __builtin_amdgcn_permlane16_swap(__float_as_uint(u[x][0]), __float_as_uint(u[x][1]), false, false);
            u[x] = f32x2{__uint_as_float(t[0]), __uint_as_float(t[1])};
            SB();
        }
#pragma unroll
        for (int x = 0; x < 3; ++x) w[x] = u[x][0] + u[x][1];
        asm volatile("ds_write2st64_b32 %1, %2, %3 offset1:1\n\tds_write_b32 %1, %4 offset:512\n\ts_mov_b64 exec, 1\n\tds_add_rtn_u32 %0, %5, %6\n\ts_mov_b64 exec, -1"
                     : "+v"(old) : "v"(ra), "v"(w[0]), "v"(w[1]), "v"(w[2]), "v"(ca), "v"(one) : "memory");
        SB();
    };
#pragma unroll 1
    for (int h = 0; h < H - 1; ++h) {
        nso += hstride;
        VSTEP(0, h + 1)
        asm volatile("s_waitcnt lgkmcnt(0)" : "+v"(old) :: "memory");
        if (__builtin_amdgcn_readfirstlane(old) == 3) *reinterpret_cast<u32x2*>(smem + LDS_REC + (h - 1) * (DH * 4) + lane * 8) = __builtin_expect(h > G1, 1) ? sum4((h - 1) * (DH * 4)) : merge(h - 1);
        SB();
        VSTEP(1, h + 1) VSTEP(2, h + 1) VSTEP(3, h + 1)
        record();
        ra += DH * 4; ca += 4;
        asm volatile("" : "+v"(ra), "+v"(ca));
    }
    if (DBG == 4 && (F >> 13) == 5) { tbar = now32(); asm volatile("" : "+s"(tbar)); }
    asm volatile("s_branch .Lkv_t%c0\n\t.p2align 12,,3327\n.Lkv_t%c0:" :: "i"(ID));
    if (DBG == 4 && (F >> 13) == 4) { tbar = now32(); asm volatile("" : "+s"(tbar)); }
    unsigned tx = txb - sched, tgt = tgb - sched;
    asm volatile("ds_write_b32 %0, %1 offset:36" :: "v"(ca + 4 * wave), "v"(tx) : "memory");
    VCOMP(0)
    asm volatile("s_waitcnt lgkmcnt(0)\n\ts_barrier" ::: "memory");
    if (DBG == 4 && (F >> 13) == 3) { tbar = now32(); asm volatile("" : "+s"(tbar)); }
    f32x4 p[4];
    i32x4 tw;
    asm volatile(LDREC(30) "\n\tds_read_b128 %6, %8\n\tds_read_b128 %7, %9 offset:36" : "=&v"(p[0]), "=&v"(p[1]), "=&v"(p[2]), "=&v"(p[3]), "+v"(rl), "+v"(rh), "=&v"(ev[3]), "=&v"(tw) : "v"(ea), "v"(ca) : "memory");
    SB();
    VCOMP(1)
    asm volatile("s_waitcnt lgkmcnt(0)" : "+v"(p[0]), "+v"(p[1]), "+v"(p[2]), "+v"(p[3]), "+v"(ev[3]), "+v"(tw) :: "memory");
    if (!(F & 4)) {
        tw -= (int)tx;
        const int d = __builtin_amdgcn_readfirstlane(min(min(tw[0], tw[1]), min(tw[2], tw[3])));
        tx += d; tgt += d;
    }
    if (wave == 3) ev[3] = wide(add4(p));
    int lx = hc == H - 1 ? ((SL >> 24) & 15) * ((SL >> 16) & 255) : 0;
    asm volatile("" : "+s"(lx));
    asm volatile("" : "+v"(ev[3]));
    asm volatile("" : "+s"(tx), "+s"(tgt), "+s"(area));
    SB();
    VCOMP(2)
    if (DBG == 4 && (F >> 13) == 1) { tbar = now32(); asm volatile("" : "+s"(tbar)); }
    auto until = [&](unsigned t) {
        int d;
        asm volatile("1:\ts_memrealtime vcc\n\ts_waitcnt lgkmcnt(0)\n\ts_lshl4_add_u32 %0, vcc_lo, %1\n\ts_cmp_lt_i32 %0, 0\n\ts_cbranch_scc1 1b" : "=&s"(d) : "s"(t) : "memory", "vcc", "scc");
        return d;
    };
    const rsrc_t orr = mkrsrc(a.out, H * DH * 2);
    asm volatile("" :: "s"(orr), "s"(pr));
    until(tx);
    constexpr int XP = (F & 4096) ? ((F & 4) ? 3 : 2) : 0;
    VPART(3, 0, XP)
    if ((F & 256) && XP < 3) asm volatile("" : "+v"(buf[3].d[2]));
    SB();
#pragma unroll
    for (int i = 0; i < 4; ++i) { __builtin_amdgcn_raw_buffer_store_b128(ev[i], pr, eo[i], area, 16); SB(); }
    if (DBG == 4 && !(F >> 13)) tbar = now32();
    SB();
    VPART(3, XP, 3)
    if (DBG == 4 && (F >> 13) == 6) { tbar = now32(); asm volatile("" : "+s"(tbar)); }
    record();
    asm volatile(LDREC(31) "\n\ts_waitcnt lgkmcnt(4)" : "=&v"(p[0]), "=&v"(p[1]), "=&v"(p[2]), "=&v"(p[3]), "+v"(rl), "+v"(rh), "+v"(old) :: "memory");
    if (F & 2048) {
        int last = __builtin_amdgcn_readfirstlane(old);
        asm volatile("" : "+s"(last));
        asm volatile("s_waitcnt lgkmcnt(0)" : "+v"(p[0]), "+v"(p[1]), "+v"(p[2]), "+v"(p[3]) :: "memory");
        const u32x4 t = wide(add4(p));
        asm volatile("s_cmp_eq_u32 %4, 3\n\ts_cbranch_scc0 1f\n\tbuffer_store_dwordx4 %0, %1, %2, %3 offen sc1\n1:\ts_nop 1" :: "v"(t), "v"(eo[4]), "s"(pr), "s"(area), "s"(last) : "memory", "scc");
    } else {
    const bool last = __builtin_amdgcn_readfirstlane(old) == 3;
    if (__builtin_expect(last, 1)) {
        asm volatile("s_waitcnt lgkmcnt(0)" : "+v"(p[0]), "+v"(p[1]), "+v"(p[2]), "+v"(p[3]) :: "memory");
        __builtin_amdgcn_raw_buffer_store_b128(wide(add4(p)), pr, eo[4], area, 16);
    }
    asm volatile("s_waitcnt lgkmcnt(0)" :: "v"(p[0]), "v"(p[1]), "v"(p[2]), "v"(p[3]) : "memory");
    }
    unsigned tend = DS ? now32() : 0;
    unsigned tfl = 0, tpl = 0;

    if (cn < NC) {
        i32x4 x[8];
        float o[8];
        int cm = -138, kd = 0x200004, rounds = 0;
        unsigned bad, mk;
        asm volatile("" : "+v"(cm));
        asm volatile("" : "+s"(kd), "+s"(rounds));
        int late = until(tgt) + lx;
        if (DS) tfl = now32();
        if (DBG == 4) th4 = 0;
#define XL(i) x[i] = bld128<16 + (SL >> 28)>(pr, xo[i], area); SB();
#define XS(i) XL(i) asm volatile("s_cmp_lt_i32 %0, %1\n\ts_cbranch_scc0 1f\n\ts_sleep %2\n1:" :: "s"(late), "i"(((SL >> 16) & 255) * ((i) + 1)), "i"((SL >> (2 * (i))) & 3) : "scc"); SB();
#pragma unroll 1
        do {
            XS(0) XS(1) XS(2) XS(3) XS(4) XS(5) XS(6) XL(7)
            if (DBG == 4 && !th4) th4 = now32();
            late = 1 << 30;
            mk = ++rounds < (1 << 20) ? 0x70007 : 0;
            asm volatile("" : "+s"(mk), "+s"(late), "+s"(rounds));
            SB();
#pragma unroll
            for (int i = 0; i < 8; ++i) {
                float w;
                int x0, x1, x2, x3;
#define MIX(a, b, x, z) "\n\tv_fma_mix_f32 %" #a ", %9, %" #x ", " z(a) " op_sel_hi:[0,1,0]\n\tv_fma_mix_f32 %" #b ", %9, %" #x ", " z(b) " op_sel:[0,1,0] op_sel_hi:[0,1,0]"
#define ROW(z) "v_and_b32 %9, 0x70007, %23\n\tv_xor_b32 %13, %23, %9\n\tv_dot2_i32_i16 %9, %9, %18, %19\n\tv_xor_b32 %10, %20, %15\n\tv_xor_b32 %11, %21, %16\n\tv_xor_b32 %12, %22, %17\n\t" \
               "v_ldexp_f32 %9, %14, %9\n\tv_or3_b32 %8, " z(8) ", %10, %11" MIX(0, 1, 10, z) MIX(2, 3, 11, z) "\n\tv_or_b32 %8, %8, %12" MIX(4, 5, 12, z) MIX(6, 7, 13, z)
#define ZERO(a) "0"
#define SAME(a) "%" #a
                if (i == 0) asm volatile(ROW(ZERO) : "=&v"(o[0]), "=&v"(o[1]), "=&v"(o[2]), "=&v"(o[3]), "=&v"(o[4]), "=&v"(o[5]), "=&v"(o[6]), "=&v"(o[7]), "=&v"(bad), "=&v"(w),
                                           "=&v"(x0), "=&v"(x1), "=&v"(x2), "=&v"(x3) : "v"(fs[i >> 1][i & 1]), "v"(ta), "v"(tb), "v"(tc), "s"(kd), "v"(cm),
                                    "v"(x[i][0]), "v"(x[i][1]), "v"(x[i][2]), "v"(x[i][3]));
                else asm volatile(ROW(SAME) : "+v"(o[0]), "+v"(o[1]), "+v"(o[2]), "+v"(o[3]), "+v"(o[4]), "+v"(o[5]), "+v"(o[6]), "+v"(o[7]), "+v"(bad), "=&v"(w),
                                    "=&v"(x0), "=&v"(x1), "=&v"(x2), "=&v"(x3) : "v"(fs[i >> 1][i & 1]), "v"(ta), "v"(tb), "v"(tc), "s"(kd), "v"(cm),
                                    "v"(x[i][0]), "v"(x[i][1]), "v"(x[i][2]), "v"(x[i][3]));
                if (DBG == 6 && __any(((x0 | x1 | x2) & 0x70007) != 0) && !th1) th4 |= 1 << i;
                SB();
            }
            if (DBG == 6) th1 = 1;
        } while (__builtin_expect(__builtin_amdgcn_ballot_w64((bad & mk) != 0) != 0, 0));
        if (DS) tpl = now32();
        f32x2 u0 = f32x2{o[0], o[1]}, u1 = f32x2{o[2], o[3]}, u2 = f32x2{o[4], o[5]}, u3 = f32x2{o[6], o[7]};
        swap32(u0, u2); swap32(u1, u3);
        SB();
        u0 += u2; u1 += u3;
        swap16(u0, u1);
        u0 += u1;
        f32x2 p0, p1;
        asm volatile("s_nop 0\n\ts_nop 0\n\tv_add_f32_dpp %3, %3, %3 row_ror:8 row_mask:0xf bank_mask:0x3\n\tv_add_f32_dpp %3, %4, %4 row_ror:8 row_mask:0xf bank_mask:0xc\n\t"
                     "ds_write_b32 %5, %3\n\ts_mov_b64 exec, 1\n\tds_add_rtn_u32 %2, %7, %8 offset:20\n\ts_mov_b64 exec, -1\n\t"
                     "ds_read2st64_b32 %0, %6 offset1:1\n\tds_read2st64_b32 %1, %6 offset0:2 offset1:3\n\ts_waitcnt lgkmcnt(2)"
                     : "=&v"(p0), "=&v"(p1), "+v"(old), "+v"(u0[0]) : "v"(u0[1]), "v"(wo), "v"(po), "v"(ca), "v"(one) : "memory");
        if (F & 2048) {
            int last = __builtin_amdgcn_readfirstlane(old);
            asm volatile("" : "+s"(last));
            asm volatile("s_waitcnt lgkmcnt(0)" : "+v"(p0), "+v"(p1) :: "memory");
            p0 += p1;
            const float t = p0[0] + p0[1];
            asm volatile("s_cmp_eq_u32 %3, 3\n\ts_cbranch_scc0 1f\n\tbuffer_store_short %0, %1, %2, 0 offen\n1:" :: "v"(pk_bf16(t, t)), "v"(oo), "s"(orr), "s"(last) : "memory", "scc");
        } else
        if (__builtin_expect(__builtin_amdgcn_readfirstlane(old) == 3, 1)) {
            asm volatile("s_waitcnt lgkmcnt(0)" : "+v"(p0), "+v"(p1) :: "memory");
            p0 += p1;
            const float t = p0[0] + p0[1];
            __builtin_amdgcn_raw_buffer_store_b16((unsigned short)pk_bf16(t, t), orr, oo, 0, 0);
        }
        asm volatile(".Lkv_b%c0:" :: "i"(ID));
    }
    if (DBG) {
        const unsigned tc1 = now32();
        const unsigned fm = th4;
        if (!DS) tend = tbar = th4 = tfl = tpl = tc1;
        if (DBG == 6) tfl = tpl = fm;
        if (DBG == 5) tbar = tdma;
        const unsigned tl16 = sched - nb16 - stag - step;
        u64* stp = reinterpret_cast<u64*>(a.part + STAMP_OFF) + (size_t)((DBG == 4 || DBG == 24 || DBG == 6 ? cn : b) * 4 + wave) * 8;

        if (lane == 0) { stp[0] = tstart; stp[1] = tbar; stp[2] = DBG >= 4 ? tend - (((tend << 4) - tl16) >> 4) : th1; stp[3] = DBG == 4 && cn >= NC ? tend : th4; stp[4] = tend; stp[5] = cn < NC || DBG == 6 ? tfl : tend; stp[6] = cn < NC || DBG == 6 ? tpl : tend; stp[7] = tc1; }
        if (DBG == 2) *reinterpret_cast<u32x4*>(a.part + STAMP_OFF + 65536 + (b * 4 + wave) * 1024 + lane * 16) = *reinterpret_cast<const u32x4*>(smem + LDS_TR + 16 + wave * 1024 + lane * 16);
    }
}

template <int TP, int ADV, int PX, int TMIN, int CAP, int HD, int RA, int SKL, int FC, int F, int EX, int GS, int HOLD, int HL, int SL, int ID, int DBG>
static void launch91(void* const* d_in, const int* in_sizes, void* d_out, void* d_ws, hipStream_t stream) {
    typedef const unsigned short* P;
    Args a;
    a.qk = (const int*)d_in[1]; a.qv = (const int*)d_in[4];
    a.k_cos = (P)d_in[9]; a.k_sin = (P)d_in[10];
    a.q = (P)d_in[0]; a.q_cos = (P)d_in[7]; a.q_sin = (P)d_in[8];
    a.sc[0] = (P)d_in[2]; a.sc[1] = (P)d_in[3]; a.sc[2] = (P)d_in[5]; a.sc[3] = (P)d_in[6];
    a.part = (unsigned char*)d_ws;
    a.out = (unsigned short*)d_out;
    kv91_kernel<TP, ADV, PX, TMIN, CAP, HD, RA, SKL, FC, F, EX, GS, HOLD, HL, SL, ID, DBG><<<Skv / RW, 256, 0, stream>>>(a);
}

extern "C" void kernel_launch(void* const* d_in, const int* in_sizes, int n_in, void* d_out, int out_size, void* d_ws, size_t ws_size, hipStream_t stream) {
    launch91<79*16+8,220,40+256*24+65536*24,78*16+2,28,70+256*110,240,24,32+256*50+65536*3+(15<<20)+(2<<24),257,56,2,78,86,0x2999+(70<<16)+(1<<24),0,0>(d_in, in_sizes, d_out, d_ws, stream);
}
```

```cpp
#include <hip/hip_runtime.h>
#include <hip/hip_bf16.h>
#include <cstdint>
#define KALIGN 256
#define STAMP_OFF (200 << 20)
using bf16 = __hip_bfloat16;
using i32x4 = __attribute__((ext_vector_type(4))) int;
using u32x4 = __attribute__((ext_vector_type(4))) unsigned;
using u32x2 = __attribute__((ext_vector_type(2))) unsigned;
using f32x2 = __attribute__((ext_vector_type(2))) float;
using f32x4 = __attribute__((ext_vector_type(4))) float;

constexpr int DH = 192;
constexpr int HALF = 96;
constexpr int RW = 64;
constexpr int MAXH = 32;

__device__ __forceinline__ float bflo(unsigned u) { return __uint_as_float(u << 16); }
__device__ __forceinline__ float bfhi(unsigned u) { return __uint_as_float(u & 0xffff0000u); }
__device__ __forceinline__ float rbf(float x) { return __bfloat162float(__float2bfloat16(x)); }

template <int CTRL> __device__ __forceinline__ float dpp(float x) {
    return __builtin_bit_cast(float, __builtin_amdgcn_mov_dpp(__builtin_bit_cast(int, x), CTRL, 0xf, 0xf, true));
}
__device__ __forceinline__ float row16_allsum(float x) {
    x += dpp<0x128>(x);
    x += dpp<0x124>(x);
    x += dpp<0x122>(x);
    x += dpp<0x121>(x);
    return x;
}
__device__ __forceinline__ float row16_allmax(float x) {
    x = fmaxf(x, dpp<0x128>(x));
    x = fmaxf(x, dpp<0x124>(x));
    x = fmaxf(x, dpp<0x122>(x));
    x = fmaxf(x, dpp<0x121>(x));
    return x;
}
__device__ __forceinline__ float xrow_max(float x) {
    auto s = __builtin_amdgcn_permlane16_swap(__float_as_uint(x), __float_as_uint(x), false, false);
    x = fmaxf(__uint_as_float(s[0]), __uint_as_float(s[1]));
    auto t = __builtin_amdgcn_permlane32_swap(__float_as_uint(x), __float_as_uint(x), false, false);
    return fmaxf(__uint_as_float(t[0]), __uint_as_float(t[1]));
}
__device__ __forceinline__ float xrow_sum(float x) {
    auto s = __builtin_amdgcn_permlane16_swap(__float_as_uint(x), __float_as_uint(x), false, false);
    x = __uint_as_float(s[0]) + __uint_as_float(s[1]);
    auto t = __builtin_amdgcn_permlane32_swap(__float_as_uint(x), __float_as_uint(x), false, false);
    return __uint_as_float(t[0]) + __uint_as_float(t[1]);
}
__device__ __forceinline__ float sw32(float a, float b) {
    auto t = __builtin_amdgcn_permlane32_swap(__float_as_uint(a), __float_as_uint(b), false, false);
    return __uint_as_float(t[0]) + __uint_as_float(t[1]);
}
__device__ __forceinline__ float sw16(float a, float b) {
    auto t = __builtin_amdgcn_permlane16_swap(__float_as_uint(a), __float_as_uint(b), false, false);
    return __uint_as_float(t[0]) + __uint_as_float(t[1]);
}

__device__ __forceinline__ void swap32(f32x2& a, f32x2& b) {
    auto s = __builtin_amdgcn_permlane32_swap(__float_as_uint(a[0]), __float_as_uint(b[0]), false, false);
    auto t = __builtin_amdgcn_permlane32_swap(__float_as_uint(a[1]), __float_as_uint(b[1]), false, false);
    a = f32x2{__uint_as_float(s[0]), __uint_as_float(t[0])}; b = f32x2{__uint_as_float(s[1]), __uint_as_float(t[1])};
}
__device__ __forceinline__ void swap16(f32x2& a, f32x2& b) {
    auto s = __builtin_amdgcn_permlane16_swap(__float_as_uint(a[0]), __float_as_uint(b[0]), false, false);
    auto t = __builtin_amdgcn_permlane16_swap(__float_as_uint(a[1]), __float_as_uint(b[1]), false, false);
    a = f32x2{__uint_as_float(s[0]), __uint_as_float(t[0])}; b = f32x2{__uint_as_float(s[1]), __uint_as_float(t[1])};
}
#define SB() __builtin_amdgcn_sched_barrier(0)
typedef __amdgpu_buffer_rsrc_t rsrc_t;
__device__ __forceinline__ rsrc_t mkrsrc(const void* p, unsigned bytes) {
    return __builtin_amdgcn_make_buffer_rsrc(const_cast<void*>(p), 0, bytes, 0x00020000);
}
template <int AUX> __device__ __forceinline__ i32x4 bld128(rsrc_t r, unsigned voff, unsigned soff) {
    return __builtin_bit_cast(i32x4, __builtin_amdgcn_raw_buffer_load_b128(r, voff, soff, AUX));
}
template <int AUX> __device__ __forceinline__ unsigned bld16(rsrc_t r, unsigned voff, unsigned soff) {
    return __builtin_amdgcn_raw_buffer_load_b16(r, voff, soff, AUX);
}

struct HG { i32x4 d[3]; };
typedef unsigned long long u64;
typedef __attribute__((address_space(3))) unsigned char lds_u8;

__device__ __forceinline__ unsigned pk_bf16(float lo, float hi) {
    unsigned r;
    asm("v_cvt_pk_bf16_f32 %0, %1, %2" : "=v"(r) : "v"(lo), "v"(hi));
    return r;
}
__device__ __forceinline__ unsigned now32() { return (unsigned)__builtin_amdgcn_s_memrealtime(); }

constexpr int LDS_REC = 0;
constexpr int RECW = MAXH * DH * 4;
constexpr int LDS_CS = 0;
constexpr int LDS_QT = 12288;
constexpr int LDS_PF = 14336;
constexpr int LDS_QRAW = MAXH * 4 * DH * 4;
constexpr int LDS_Q = LDS_QRAW + MAXH * DH * 2;
constexpr int LDS_P = LDS_Q + 4 * DH * 4;
constexpr int LDS_SC = LDS_P + 4 * MAXH * 128;
constexpr int LDS_CNT = LDS_SC + 4 * MAXH * RW * 2;
constexpr int LDS_SK = LDS_CNT + 256;
constexpr int LDS_TR = LDS_SK + 1024;
constexpr int LDS_BYTES = LDS_TR + 4096 + 64;

struct Args {
    const int* qk; const int* qv;
    const unsigned short* k_cos; const unsigned short* k_sin;
    const unsigned short* q; const unsigned short* q_cos; const unsigned short* q_sin;
    unsigned char* part;
    unsigned short* out;
    const unsigned short* sc[4];
};
constexpr unsigned Skv = 16384;
constexpr float SCALE_LOG2 = 0.10411754627697264f;

template <int TP, int ADV, int PX, int TMIN, int CAP, int HD, int RA, int SKL, int FC, int F, int EX, int GS, int HOLD, int HL, int SL, int ID, int DBG>
__global__ __launch_bounds__(256) __attribute__((aligned(KALIGN))) void kv91_kernel(Args a)
{
    __shared__ __attribute__((aligned(16))) unsigned char smem[LDS_BYTES];
    const unsigned tstart = DBG ? now32() : 0;
    constexpr bool DS = (DBG && DBG < 5) || DBG == 24;
    constexpr int H = MAXH, PFN = 16, SJ = (F >> 9) & 3;
    const int b = blockIdx.x;
    const int tid = threadIdx.x;
    const int lane = tid & 63;
    const int wave = __builtin_amdgcn_readfirstlane(tid >> 6);
    typedef const unsigned short* const __attribute__((address_space(4))) * karg_t;
    const unsigned short* sp = ((karg_t)__builtin_amdgcn_kernarg_segment_ptr())[offsetof(Args, sc) / 8 + wave];
    if (b < PFN) {
        u64 pc; unsigned len, tl;
        asm volatile("s_getpc_b64 %0\n.Lkv_a%c3:\n\ts_mov_b32 %1, .Lkv_b%c3-.Lkv_a%c3-16\n\ts_mov_b32 %2, .Lkv_t%c3-.Lkv_a%c3" : "=s"(pc), "=s"(len), "=s"(tl) : "i"(ID));
        const unsigned o = tid * 16;
        asm volatile("s_mov_b32 m0, %5\n\ts_nop 0\n\tglobal_load_lds_dwordx4 %0, %4\n\tglobal_load_lds_dwordx4 %1, %4\n\tglobal_load_lds_dwordx4 %2, %4\n\tglobal_load_lds_dwordx4 %3, %4"
                     :: "v"(o), "v"(o + 4096), "v"(min(o + 8192, tl)), "v"(min(o + min(tl, 12288u), len)), "s"(pc), "s"((unsigned)(size_t)(lds_u8*)smem + LDS_PF) : "memory");
    }
    SB();
    const int r = lane >> 4;
    const int c = lane & 15;
    constexpr unsigned hstride = Skv * (DH * 4);
    constexpr unsigned nb = Skv / RW;
    const unsigned lds0 = (unsigned)(size_t)(lds_u8*)smem;
    const unsigned sb = b;
    const unsigned rowb = (b & 7) * 2048 + (b & 24) * 64 + (b >> 5) * 16;
    const unsigned row0 = rowb + 4 * wave;

    constexpr unsigned LS = 512, psk = 0, pep = 128, AREA = 6u << 20, NAREA = 32, pfl = H * nb * LS;
    const rsrc_t pr = mkrsrc(a.part, (NAREA + 1) * AREA);
    unsigned vg = ((lane >> 3) * Skv + rowb + (lane & 6) * 64 + (lane & 1) * 8) * 2, dg = lds0 + LDS_SC + wave * 4096;
    u64 t64;
    {
        unsigned vs = ((lane >> 3) * Skv + (rowb & ~63u) + (b & 96) * 4 + (lane & 7) * 8) * 2, v1 = vs + 16 * Skv, v2 = vs + 32 * Skv, v3 = vs + 48 * Skv;
        if (!SJ && wave < 2) vs = vg;
        asm volatile("" : "+v"(vs), "+v"(v1), "+v"(v2), "+v"(v3), "+s"(dg));
        SB();
        asm volatile("s_memrealtime %0\n\ts_mov_b32 m0, %6\n\ts_nop 0\n\tglobal_load_lds_dwordx4 %1, %5\n\t"
                     "s_add_u32 m0, m0, 1024\n\ts_nop 0\n\tglobal_load_lds_dwordx4 %2, %5\n\t"
                     "s_add_u32 m0, m0, 1024\n\ts_nop 0\n\tglobal_load_lds_dwordx4 %3, %5\n\t"
                     "s_add_u32 m0, m0, 1024\n\ts_nop 0\n\tglobal_load_lds_dwordx4 %4, %5\n\ts_waitcnt lgkmcnt(0)"
                     : "=&s"(t64) : "v"(vs), "v"(v1), "v"(v2), "v"(v3), "s"(sp), "s"(dg),
                       "s"(a.qk), "s"(a.qv), "s"(a.k_cos), "s"(a.k_sin), "s"(a.q), "s"(a.q_cos), "s"(a.q_sin), "s"(a.part), "s"(a.out) : "memory", "scc");
    }
    auto gather = [&]() {
        if (F & 2) asm volatile("s_mov_b32 m0, %2\n\ts_nop 0\n\tglobal_load_lds_dwordx4 %0, %1 nt" :: "v"(vg), "s"(sp), "s"(dg) : "memory");
        else asm volatile("s_mov_b32 m0, %2\n\ts_nop 0\n\tglobal_load_lds_dwordx4 %0, %1" :: "v"(vg), "s"(sp), "s"(dg) : "memory");
        vg += 16 * Skv; dg += 1024;
    };
    SB();
    const unsigned tdma = (unsigned)t64;
#define DMA3(V, A, B, P) "s_mov_b32 m0, " B "\n\ts_nop 0\n\t" \
                     "global_load_lds_dwordx4 " V ", " A P "\n\t" \
                     "global_load_lds_dwordx4 " V ", " A " offset:1024" P "\n\t" \
                     "global_load_lds_dwordx4 " V ", " A " offset:2048" P "\n\t"
    asm volatile(DMA3("%0", "%1", "%2", "") "s_mov_b32 m0, %4\n\ts_nop 0\n\tglobal_load_lds_dwordx4 %3, off"
                 :: "v"(lane * 16), "s"(a.q + wave * 1536), "s"(lds0 + LDS_QRAW + wave * 3072),
                    "v"(lane < 48 ? (const unsigned char*)((lane < 24 ? a.q_cos : a.q_sin - DH) + 8 * lane) : a.part + pep), "s"(lds0 + LDS_QT + wave * RECW) : "memory");
    if ((b >> 4) == 1 && wave == 3)
        asm volatile("s_mov_b32 m0, %2\n\ts_mov_b64 exec, 15\n\tglobal_load_lds_dword %0, %1\n\ts_mov_b64 exec, -1" :: "v"((((tdma >> 13) & (NAREA - 1)) + 1) * AREA + lane * 0x160000 + b * 128), "s"(a.part), "s"(lds0 + LDS_PF) : "memory");
    SB();
    constexpr int TH = 4, DL = 32, BIG = 70;
    const int stag = (int)(((sb & ~7) + wave * 2 + (b & 1)) * (TP / 2)) / 256 - TP / 4;
    const unsigned nb16 = -(tdma << 4);
    int sched = ADV * 16 + stag - 4 * (TP / 2);
    int vt = 0;
    int calm = 600 * 16, hush = 0, step = TP / 2, tmin = TMIN / 2, vth = TH, pvm = 0;
    int tru = -4, slip = 0;
    int tgt0, tnow, vm, tf;
    auto pace = [&]() {
        if (DBG == 2 && (F & 16)) tf = (now32() << 4) + nb16;
        if (F & 1)
        asm volatile("s_max_i32 %0, %3, %4\n1:\ts_memrealtime vcc\n\ts_waitcnt lgkmcnt(0)\n\ts_lshl4_add_u32 %1, vcc_lo, %5\n\ts_cmp_lt_i32 %1, %0\n\ts_cbranch_scc1 1b\n\t"
                     "s_getreg_b32 %2, hwreg(HW_REG_IB_STS, 0, 4)" : "=&s"(tgt0), "=&s"(tnow), "=s"(vm) : "s"(sched), "s"(vt), "s"(nb16) : "scc", "vcc", "memory");
        else
        asm volatile("s_max_i32 %0, %3, %4\n\ts_branch 2f\n1:\ts_sleep 1\n2:\ts_memrealtime vcc\n\ts_waitcnt lgkmcnt(0)\n\ts_lshl4_add_u32 %1, vcc_lo, %5\n\ts_cmp_lt_i32 %1, %0\n\ts_cbranch_scc1 1b\n\t"
                     "s_getreg_b32 %2, hwreg(HW_REG_IB_STS, 0, 4)" : "=&s"(tgt0), "=&s"(tnow), "=s"(vm) : "s"(sched), "s"(vt), "s"(nb16) : "scc", "vcc", "memory");
    };
    int nre = 0, far = ((FC >> 8) & 255) * 16;
    auto rebase = [&]() { sched += (RA & 1023) * 16; step -= RA >> 10; calm = tnow + 900 * 16; ++nre; if (DBG == 2) slip += (RA & 1023) * 16; };
    auto steer = [&](bool ctl, int k = 0) {
        sched += step;
        if (!ctl) vt = max(tnow - 128, tgt0) + tmin;
        else {
            vt = tgt0 + tmin;
            if (__builtin_expect(tnow - tgt0 > 128, 0)) {
                asm volatile("");
                vt = tnow - 128 + tmin;
                if (tnow - tgt0 > (tnow > hush ? HD & 255 : HD >> 8) * 16 && tnow > calm) {
                    asm volatile("");
                    rebase();
                    if (FC >> 24) asm volatile("ds_write_b32 %0, %1" :: "v"(lds0 + LDS_CNT + 148), "v"(nre) : "memory");
                }
            }
            if (__builtin_expect(vm > vth, 0)) {
                asm volatile("" : "+s"(pvm));
                if (vt - sched < CAP * 16 || (pvm > TH && vt - sched > BIG * 16)) vt += (vm - TH) * DL;
            }
            if (FC && (((FC >> 20) & 15) >> k & 1)) {
                if (__builtin_expect(vt - sched > far, 0)) {
                    asm volatile("" : "+s"(pvm));
                    if (max(vm, pvm) <= ((FC >> 16) & 15)) vt -= FC & 255;
                    if ((FC >> 24) >> k & 1) {
                        int n;
                        asm volatile("ds_read_b32 %0, %1\n\ts_waitcnt lgkmcnt(0)" : "=v"(n) : "v"(lds0 + LDS_CNT + 148) : "memory");
                        if (__builtin_amdgcn_readfirstlane(n) > nre) rebase();
                    }
                }
            }
            pvm = vm;
            if ((F >> 5) & 7) asm volatile(".rept %0\n\ts_nop 7\n\t.endr" :: "i"(3 * ((F >> 5) & 7)));
        }
        if constexpr (DBG == 2) { *reinterpret_cast<unsigned*>(smem + LDS_TR + 16 + wave * 1024 + (tru & 255) * 4) = ((unsigned)(tnow - (sched - slip - step)) >> 4) | ((F & 8) ? vm << 16 : (F & 16) ? ((tnow - tf) >> 4) << 16 : 0); ++tru; }
    };
    {
        unsigned dst = lds0 + LDS_CS + wave * RECW;
        const unsigned v0 = lane * 16 + row0 * (DH * 2), v2 = v0 + 124 * DH * 2, v1 = lane < 32 ? v0 : v2;
#pragma unroll 1
        for (int j = 0; j < 4; ++j) {
            pace();
            asm volatile("s_mov_b32 m0, %4\n\ts_nop 0\n\tglobal_load_lds_dwordx4 %0, %3 nt\n\tglobal_load_lds_dwordx4 %1, %3 offset:1024 nt\n\tglobal_load_lds_dwordx4 %2, %3 offset:2048 nt"
                         :: "v"(v0), "v"(v1), "v"(v2), "s"((j < 2 ? a.k_cos : a.k_sin) + (j & 1) * 256 * DH), "s"(dst) : "memory");
            if (SJ && j == SJ - 1 && wave < 2) gather();
            steer(false);
            sched += PX & 255;
            dst += 3072;
        }
    }
    if (!SJ && wave < 2) { vg += 16 * Skv; dg += 1024; }
    if (tid < MAXH + 6) *reinterpret_cast<unsigned*>(smem + LDS_CNT + tid * 4) = 0;
    const rsrc_t kr = mkrsrc(a.qk, H * hstride);
    const rsrc_t vr = mkrsrc(a.qv, H * hstride);
    unsigned kvo[4][3];
#pragma unroll
    for (int i = 0; i < 12; ++i) { kvo[i / 3][i % 3] = (row0 + r + i / 3 * 128) * (DH * 4) + c * 16 + i % 3 * 256; asm volatile("" : "+v"(kvo[i / 3][i % 3])); }
    auto issue = [&](HG& g, int k, rsrc_t rs, unsigned so) {
#pragma unroll
        for (int x = 0; x < 3; ++x) g.d[x] = bld128<2>(rs, kvo[k][x], so);
    };
    HG buf[4];
#define PRE(g) SB(); pace(); issue(buf[g], g, kr, 0); SB(); steer(false); SB();
    asm volatile("s_waitcnt vmcnt(%0) lgkmcnt(0)\n\ts_barrier" :: "i"(12 - 3 * SJ) : "memory");
    unsigned tbar = DS ? now32() : 0;
    PRE(0)
    const unsigned qoff = (lane < 48 ? lane : lane - 48) * 4;
    const unsigned* qt = reinterpret_cast<const unsigned*>(smem + LDS_QT + wave * RECW + qoff);
    unsigned qc0 = qt[0], qc1 = qt[HALF / 2], qs0 = qt[DH / 2], qs1 = qt[(DH + HALF) / 2];
    u64 tag;
    {
        const u32x2 e = *reinterpret_cast<const u32x2*>(smem + LDS_QT + wave * RECW + 768);
        tag = (((u64)__builtin_amdgcn_readfirstlane(e[1]) << 32) | __builtin_amdgcn_readfirstlane(e[0])) * 6364136223846793005ull + 1442695040888963407ull;
        asm volatile("" : "+s"(tag));
    }
    const unsigned t32 = tag >> 32;
    unsigned qraw = lds0 + LDS_QRAW + qoff;
    unsigned char* qrot = smem + LDS_Q + wave * (DH * 4) + 2 * qoff;
    unsigned q_lo, q_hi;
    auto getq = [&]() {
        asm volatile("" : "+v"(qraw));
        q_lo = *(const __attribute__((address_space(3))) unsigned*)(size_t)qraw, q_hi = *(const __attribute__((address_space(3))) unsigned*)(size_t)(qraw + HALF * 2);
        qraw += DH * 2;
    };
    auto rotq = [&]() {
        const f32x2 lo = f32x2{bflo(q_lo), bfhi(q_lo)}, hi = f32x2{bflo(q_hi), bfhi(q_hi)};
        const f32x2 y1 = lo * f32x2{bflo(qc0), bfhi(qc0)}, y2 = -hi * f32x2{bflo(qs0), bfhi(qs0)};
        const f32x2 y3 = hi * f32x2{bflo(qc1), bfhi(qc1)}, y4 = lo * f32x2{bflo(qs1), bfhi(qs1)};
        const unsigned x1 = pk_bf16(y1[0], y1[1]), x2 = pk_bf16(y2[0], y2[1]), x3 = pk_bf16(y3[0], y3[1]), x4 = pk_bf16(y4[0], y4[1]);
        const f32x2 z1 = f32x2{bflo(x1), bfhi(x1)} + f32x2{bflo(x2), bfhi(x2)}, z2 = f32x2{bflo(x3), bfhi(x3)} + f32x2{bflo(x4), bfhi(x4)};
        const unsigned l2 = pk_bf16(z1[0], z1[1]), h2 = pk_bf16(z2[0], z2[1]);
        *reinterpret_cast<f32x2*>(qrot) = f32x2{bflo(l2), bfhi(l2)};
        *reinterpret_cast<f32x2*>(qrot + HALF * 4) = f32x2{bflo(h2), bfhi(h2)};
        asm volatile("" ::: "memory");
    };
    f32x4 qa[3], qb[3];
    const unsigned char* qh = smem + LDS_Q + wave * (DH * 4) + 16 * c;
    const unsigned char* qh1 = qh + (c < 8 ? 64 + HALF : 64 - HALF) * 4;
    auto loadq = [&]() {
        asm volatile("" ::: "memory");
        qa[0] = *reinterpret_cast<const f32x4*>(qh); SB();
        qb[0] = *reinterpret_cast<const f32x4*>(qh + HALF * 4); SB();
        qa[1] = *reinterpret_cast<const f32x4*>(qh + 256); SB();
        qb[1] = *reinterpret_cast<const f32x4*>(qh1); SB();
        qa[2] = *reinterpret_cast<const f32x4*>(qh + 512); SB();
        qb[2] = *reinterpret_cast<const f32x4*>(qh + (128 - HALF) * 4);
        asm volatile("" ::: "memory");
    };
    getq(); rotq();
    if ((b >> 3) == 4 && wave == 3) __builtin_amdgcn_raw_buffer_store_b128(u32x4{tdma, t32, tdma, (unsigned)tag}, pr, lane == 0 ? psk + (b & 7) * 16 : 0x80000000u, 0, 16);
    PRE(1)
    f32x2 cc[4][3][2], ss[4][3][2];
    const unsigned char* cb = smem + LDS_CS + wave * RECW + r * (DH * 2) + c * 8;
    asm volatile("s_waitcnt vmcnt(12)" ::: "memory");
#pragma unroll
    for (int k = 0; k < 4; ++k)
#pragma unroll
        for (int x = 0; x < 3; ++x) {
            const u32x2 t = *reinterpret_cast<const u32x2*>(cb + k * 1536 + 128 * x);
            cc[k][x][0] = f32x2{bflo(t[0]), bfhi(t[0])}; cc[k][x][1] = f32x2{bflo(t[1]), bfhi(t[1])};
            asm volatile("" : "+v"(cc[k][x][0]), "+v"(cc[k][x][1]));
        }
    PRE(2)
    loadq();
    const unsigned char* scp = smem + LDS_SC + (c & 1) * 4096 + (4 * (c & 12) + 4 * wave + r) * 2;
    unsigned sc = *reinterpret_cast<const unsigned short*>(scp);
    auto loadsin = [&](int k) {
        const unsigned char* sb1 = cb + (c < 8 ? (64 + HALF) * 2 : (64 - HALF) * 2);
        const unsigned sg = c < 8 ? 0u : 0x80008000u;
        u32x2 t[3];
        t[0] = *reinterpret_cast<const u32x2*>(cb + 6144 + k * 1536 + HALF * 2);
        t[1] = *reinterpret_cast<const u32x2*>(sb1 + 6144 + k * 1536) ^ u32x2{sg, sg};
        t[2] = *reinterpret_cast<const u32x2*>(cb + 6144 + k * 1536 + (128 - HALF) * 2) ^ u32x2{0x80008000u, 0x80008000u};
#pragma unroll
        for (int x = 0; x < 3; ++x) {
            ss[k][x][0] = f32x2{bflo(t[x][0]), bfhi(t[x][0])}; ss[k][x][1] = f32x2{bflo(t[x][1]), bfhi(t[x][1])};
            asm volatile("" : "+v"(ss[k][x][0]), "+v"(ss[k][x][1]));
        }
    };
    asm volatile("s_waitcnt vmcnt(12)" ::: "memory");
    loadsin(0); loadsin(1);
    PRE(3)
    asm volatile("s_waitcnt vmcnt(12)" ::: "memory");
    loadsin(2); loadsin(3);
    unsigned th1 = 0, th4 = 0;
    const unsigned char* prec = smem + LDS_P + wave * (H * 128) + r * 32 + (c & 12);
    unsigned pw_ = lds0 + LDS_P + wave * (H * 128) + r * 32 + (c & 12) + (c & 3) * 128;
    float sm, sp_, sacc = 0.f;
    auto softmax1 = [&]() {
        float u;
        asm volatile("v_max_f32_dpp %0, %2, %2 row_ror:4 row_mask:0xf bank_mask:0xf\n\ts_nop 1\n\tv_max_f32_dpp %0, %0, %0 row_ror:8 row_mask:0xf bank_mask:0xf\n\tv_mov_b32 %1, %0\n\ts_nop 1\n\t"
                     "v_permlane16_swap_b32 %0, %1\n\tv_max_f32 %0, %0, %1\n\tv_mov_b32 %1, %0\n\ts_nop 1\n\tv_permlane32_swap_b32 %0, %1\n\tv_max_f32 %0, %0, %1" : "=&v"(sm), "=&v"(u) : "v"(sacc));
        sp_ = __builtin_amdgcn_exp2f(sacc - sm);
        asm volatile("" : "+v"(sp_));
    };
    auto softmax2 = [&]() {
        float l = sp_ + dpp<0x124>(sp_);
        l = xrow_sum(l + dpp<0x128>(l));
        asm volatile("ds_write2_b32 %0, %1, %2 offset1:4" :: "v"(pw_), "v"(sp_), "v"((c & 4) ? l : sm) : "memory");
        pw_ += 512;
    };

    auto skew = [&](int h) {
        if (wave == 1 && h == 1 + ((b >> 3) & 3))
            asm volatile("s_mov_b32 m0, %2\n\ts_nop 0\n\tglobal_load_lds_dwordx4 %0, %1 sc1" :: "v"(psk + (lane & 7) * 16), "s"(a.part), "s"(lds0 + LDS_SK) : "memory");
        if (h == 6) {
            const u32x2 e = *reinterpret_cast<const u32x2*>(smem + LDS_SK + (lane & 7) * 16);
            const int d = (int)(tdma - e[0]);
            if (__all(e[1] == t32 && d > -400 && d < 400)) {
                int sum = d + __builtin_amdgcn_mov_dpp(d, 0xb1, 0xf, 0xf, true), mn = min(d, __builtin_amdgcn_mov_dpp(d, 0xb1, 0xf, 0xf, true));
                sum += __builtin_amdgcn_mov_dpp(sum, 0x4e, 0xf, 0xf, true); mn = min(mn, __builtin_amdgcn_mov_dpp(mn, 0x4e, 0xf, 0xf, true));
                sum += __builtin_amdgcn_mov_dpp(sum, 0x124, 0xf, 0xf, true); mn = min(mn, __builtin_amdgcn_mov_dpp(mn, 0x124, 0xf, 0xf, true));
                sum = __builtin_amdgcn_readfirstlane(min(sum, 8 * (mn + (SKL & 255))));
                const int adj = (sum * 73 + 4096) >> 13;
                step -= adj; tmin -= adj;
                sched -= 2 * sum - 224 * adj;
            }
        }
    };

    rsrc_t nx = kr;
    unsigned nso = 0, sm4 = 0x11111110;
    unsigned gm = (wave < 2 ? 0x4081u << 1 : 0x204081u << 4) << (((b >> 3) + 2 * wave) & 3), evk = gm * 5 | 0x5e, gn = 0;
    asm volatile("" : "+s"(sm4), "+s"(gm), "+s"(evk));
#pragma unroll 1
    for (int h = 0; h < H; ++h) {
        float scf = bflo(sc);
        asm volatile("" : "+v"(scf) :: "memory");
        SB();
        nso += hstride;
        scp += 128;
        if (__builtin_expect((h & 7) == 7, 0)) {
            const unsigned fa = lds0 + LDS_CNT + 128 + ((h + 1) >> 5) * 8;
            u32x2 n;
            do asm volatile("ds_read_b64 %0, %1\n\ts_waitcnt lgkmcnt(0)" : "=v"(n) : "v"(fa) : "memory");
            while (__any(min(n[0], n[1]) * 8 <= h));
            if (h == H - 1) { nx = vr; nso = 0; scp += 4096; }
        }
        sc = *reinterpret_cast<const unsigned short*>(scp);
        SB();
        float st[2];
#define KSTEP(k) { \
            __builtin_amdgcn_s_waitcnt(0xf79); SB();        \
            const float kz = dpp<0x151 + 4 * (k)>(scf); \
            const f32x2 kz2 = f32x2{kz, kz}; \
            f32x2 a2 = f32x2{0.f, 0.f}; \
            _Pragma("unroll") for (int x = 0; x < 3; ++x) { \
                _Pragma("unroll") for (int e = 0; e < 2; ++e) { \
                    const f32x2 w = __builtin_elementwise_fma(f32x2{qb[x][2 * e], qb[x][2 * e + 1]}, ss[k][x][e], f32x2{qa[x][2 * e], qa[x][2 * e + 1]} * cc[k][x][e]); \
                    const f32x2 kf = f32x2{(float)buf[k].d[x][2 * e], (float)buf[k].d[x][2 * e + 1]} - kz2; \
                    a2 = __builtin_elementwise_fma(kf, w, a2); \
                } \
            } \
            float sa = a2[0] + a2[1]; \
            asm volatile("" : "+v"(sa) :: "memory"); \
            SB(); pace(); issue(buf[k], k, nx, nso); SB(); steer(true, k); SB(); \
              \
            if ((k) < 2) asm volatile("v_add_f32_dpp %0, %1, %1 row_ror:8 row_mask:0xf bank_mask:0xf" : "=v"(st[(k) & 1]) : "v"(sa)); \
            else asm volatile("v_add_f32_dpp %0, %1, %1 row_ror:8 row_mask:0xf bank_mask:0xc" : "+v"(st[(k) & 1]) : "v"(sa)); \
            if ((k) == 0) { getq(); SB(); if (sm4 >> h & 1) softmax1(); SB(); } \
            if ((k) == 1) { rotq(); SB(); } \
            if ((k) == 2) { \
                asm volatile("" : "+s"(sm4)); \
                if (sm4 >> h & 1) softmax2(); \
                SB(); \
                if (__builtin_expect(evk >> h & 1, 0)) { \
                    if (gm >> h & 1) gather(); \
                    if (gm * 4 >> h & 1) *reinterpret_cast<unsigned*>(smem + LDS_CNT + 128 + wave * 4) = ++gn;        \
                    skew(h); \
                } \
                SB(); \
            } \
            if ((k) == 3) { loadq(); SB(); } }
        KSTEP(0) KSTEP(1) KSTEP(2) KSTEP(3)
        asm volatile("v_add_f32_dpp %0, %0, %0 row_shl:4 row_mask:0xf bank_mask:0x5\n\tv_add_f32_dpp %0, %3, %3 row_shr:4 row_mask:0xf bank_mask:0xa\n\t"
                     "v_mul_f32 %1, %4, %1\n\tv_mov_b32_dpp %2, %2 quad_perm:[1,2,3,3] row_mask:0xf bank_mask:0xf\n\t"
                     "v_add_f32_dpp %0, %0, %0 quad_perm:[1,0,3,2] row_mask:0xf bank_mask:0xf\n\ts_nop 1\n\tv_add_f32_dpp %0, %0, %0 quad_perm:[2,3,0,1] row_mask:0xf bank_mask:0xf\n\t"
                     "v_mul_f32_dpp %0, %1, %0 quad_perm:[0,0,0,0] row_mask:0xf bank_mask:0xf\n\tv_cndmask_b32 %2, %2, %0, %5"
                     : "+v"(st[0]), "+v"(scf), "+v"(sacc) : "v"(st[1]), "s"(SCALE_LOG2), "s"(0x8888888888888888ull));
        if (DS) { if (h == 7) th1 = now32(); }
    }
    if (DS) th4 = now32();

    unsigned rl = lds0 + LDS_REC + lane * 16, rh = rl + 2 * RECW;
    unsigned s0, s1, k1;
    asm volatile("" : "=v"(s0), "=v"(s1), "=v"(k1));
    unsigned c7 = 0x70007, c8 = 0xfff8fff8u, c9 = 0x06000000;
    asm volatile("" : "+s"(c7), "+s"(c8), "+s"(c9));
    auto ev_s = [&]() {
        unsigned l = lane;
        asm volatile("" : "+v"(l));
        const unsigned t = tag >> (3 * (l & 14) + 12 * (l & 1));
        s0 = (t & 63) * 0x2001 & 0x70007;
        s1 = (l & 1) ? 0 : (t >> 6 & 63) * 0x2001 & 0x70007;
        k1 = (l & 1) * 0x2001;
        asm volatile("" : "+v"(s0), "+v"(s1), "+v"(k1));
    };
    auto pack = [&](f32x4 v) {
        unsigned h0, h1, m, z;
        asm("v_max3_f32 %2, |%4|, |%5|, |%6|\n\tv_max3_f32 %2, %2, |%7|, %13\n\ts_nop 1\n\tv_max_u32_dpp %2, %2, %2 quad_perm:[1,0,3,2] row_mask:0xf bank_mask:0xf\n\t"
            "v_and_b32 %3, 0x7e000000, %2\n\tv_lshrrev_b32 %2, 25, %2\n\tv_sub_u32 %3, 0x84800000, %3\n\tv_mul_u32_u24 %2, %2, %8\n\t"
            "v_fma_mixlo_f16 %0, %4, %3, 0\n\tv_fma_mixlo_f16 %1, %6, %3, 0\n\tv_fma_mixhi_f16 %0, %5, %3, 0\n\tv_fma_mixhi_f16 %1, %7, %3, 0\n\tv_and_or_b32 %2, %2, %11, %10\n\t"
            "v_pk_add_u16 %0, %0, 4 op_sel_hi:[1,0]\n\tv_pk_add_u16 %1, %1, 4 op_sel_hi:[1,0]\n\tv_and_or_b32 %0, %0, %12, %9\n\tv_and_or_b32 %1, %1, %12, %2"
            : "=&v"(h0), "=&v"(h1), "=&v"(m), "=&v"(z) : "v"(v[0]), "v"(v[1]), "v"(v[2]), "v"(v[3]), "v"(k1), "v"(s0), "v"(s1), "s"(c7), "s"(c8), "s"(c9));
        return u32x2{h0, h1};
    };
#define LDREC(hd) "ds_read_b128 %0, %4 offset:768*" #hd "\n\tds_read_b128 %1, %4 offset:768*" #hd "+24576\n\tds_read_b128 %2, %5 offset:768*" #hd "\n\tds_read_b128 %3, %5 offset:768*" #hd "+24576"
    auto ldrec = [&](unsigned o, f32x4 (&p)[4]) {
        asm volatile(LDREC(0) : "=&v"(p[0]), "=&v"(p[1]), "=&v"(p[2]), "=&v"(p[3]) : "v"(rl + o), "v"(rh + o) : "memory");
    };
    auto add4 = [&](f32x4 (&p)[4]) { return pack((p[0] + p[1]) + (p[2] + p[3])); };
    auto sum4 = [&](unsigned o) {
        f32x4 p[4];
        ldrec(o, p);
        asm volatile("s_waitcnt lgkmcnt(0)" : "+v"(p[0]), "+v"(p[1]), "+v"(p[2]), "+v"(p[3]) :: "memory");
        return add4(p);
    };
    auto wide = [&](u32x2 t) {
        return u32x4{t[0], t[1], (unsigned)__builtin_amdgcn_mov_dpp(t[0], 0xb1, 0xf, 0xf, true), (unsigned)__builtin_amdgcn_mov_dpp(t[1], 0xb1, 0xf, 0xf, true)};
    };
    auto merge = [&](int hd) {
        f32x4 p[4];
        f32x2 ma, mb;
        f32x4 xa, xb;
        asm volatile("ds_read2st64_b64 %0, %2 offset1:8\n\tds_read2st64_b64 %1, %2 offset0:16 offset1:24" : "=&v"(xa), "=&v"(xb) : "v"(lds0 + LDS_P + hd * 128 + 16) : "memory");
        ldrec(hd * (DH * 4), p);
        asm volatile("s_waitcnt lgkmcnt(4)" : "+v"(xa), "+v"(xb) :: "memory");
        const float M = __builtin_amdgcn_fmed3f(__builtin_amdgcn_fmed3f(xa[0], xa[2], __builtin_inff()), __builtin_amdgcn_fmed3f(xb[0], xb[2], __builtin_inff()), __builtin_inff());
        ma = f32x2{__builtin_amdgcn_exp2f(xa[0] - M), __builtin_amdgcn_exp2f(xa[2] - M)};
        mb = f32x2{__builtin_amdgcn_exp2f(xb[0] - M), __builtin_amdgcn_exp2f(xb[2] - M)};
        const float rL = __builtin_amdgcn_rcpf((ma[0] * xa[1] + ma[1] * xa[3]) + (mb[0] * xb[1] + mb[1] * xb[3]));
        ma *= rL; mb *= rL;
        asm volatile("s_waitcnt lgkmcnt(0)" : "+v"(ma), "+v"(mb), "+v"(p[0]), "+v"(p[1]), "+v"(p[2]), "+v"(p[3]) :: "memory");
        return pack(ma[0] * p[0] + ma[1] * p[1] + mb[0] * p[2] + mb[1] * p[3]);
    };
    constexpr unsigned LSTR = 128;
    constexpr int NC = 3 * H;
    const int cn = sb;
    const unsigned hc = 4 * (cn & 7) + ((cn >> 3) & 3);
    unsigned old = 0, area = 0, fo = 0;
    unsigned flv = 0;
    float gw = 1.f, mw = 0.f;
    f32x2 fs[4] = {};
    constexpr int LDS_ML = LDS_QRAW + 2048;
#define BAD(y) ((unsigned)(y)[1] != t32 || (unsigned)(y)[3] != (unsigned)tag)
    auto ev_all = [&](int hd) {
        unsigned n;
        asm volatile("1:\tds_read_b32 %0, %1\n\ts_waitcnt lgkmcnt(0)\n\tv_cmp_ne_u32 vcc, 4, %0\n\ts_cbranch_vccnz 1b" : "=&v"(n) : "v"(lds0 + LDS_CNT + hd * 4) : "memory", "vcc");
    };
    auto quiet = [&]() { vth = 15; hush = vt + 500 * 16; };
    auto ev_ml = [&]() {
        f32x2 ml[4];
#pragma unroll
        for (int w = 0; w < 4; ++w) ml[w] = *reinterpret_cast<const f32x2*>(smem + LDS_P + ((wave + w) & 3) * (H * 128) + (lane & 31) * 128 + 16);
        const float M = fmaxf(fmaxf(ml[0][0], ml[1][0]), fmaxf(ml[2][0], ml[3][0]));
        const float e0 = __builtin_amdgcn_exp2f(ml[0][0] - M);
        const float L = (e0 * ml[0][1] + __builtin_amdgcn_exp2f(ml[1][0] - M) * ml[1][1]) + (__builtin_amdgcn_exp2f(ml[2][0] - M) * ml[2][1] + __builtin_amdgcn_exp2f(ml[3][0] - M) * ml[3][1]);
        flv = ((__float_as_uint(M + __builtin_amdgcn_logf(L)) + 0x80) & 0xffffff00u) | ((unsigned)(tag >> (8 * (lane & 7))) & 255);
        gw = e0 * __builtin_amdgcn_rcpf(L);
        asm volatile("" : "+v"(flv), "+v"(gw));
    };
    auto ev_area = [&]() {
        u32x4 e = *reinterpret_cast<const u32x4*>(smem + LDS_SK);
        if (__builtin_expect(__any(BAD(e)), 0)) {
#pragma unroll 1
            for (int n = 0; n < (1 << 20) && BAD(e); ++n)
                asm volatile("global_load_dwordx4 %0, %1, %2 sc1\n\ts_waitcnt vmcnt(0)" : "=v"(e) : "v"(psk), "s"(a.part) : "memory");
        }
        area = (((__builtin_amdgcn_readfirstlane(e[0]) >> 13) & (NAREA - 1)) + 1) * AREA + 4096;
        asm volatile("" : "+s"(area));
    };
    unsigned eo[5], xo[8], oo, po, wo, ta, tb, tc, ea, txb, tgb;
    u32x4 ev[4];
    f32x4 a0, a1;
    asm volatile("" : "=v"(eo[0]), "=v"(eo[1]), "=v"(eo[2]), "=v"(eo[3]), "=v"(eo[4]), "=v"(xo[0]), "=v"(xo[1]), "=v"(xo[2]), "=v"(xo[3]), "=v"(xo[4]), "=v"(xo[5]), "=v"(xo[6]), "=v"(xo[7]),
                      "=v"(oo), "=v"(po), "=v"(wo), "=v"(ta), "=v"(tb), "=v"(tc), "=v"(ea), "=v"(ev[0]), "=v"(ev[1]), "=v"(ev[2]), "=v"(a0), "=v"(a1));
    txb = nb16 + step + stag - EX * 16 - (sb >> 3) * GS * 4;
    tgb = nb16 + stag - (hc == H - 1 ? HL : HOLD & 255) * 16;
    asm volatile("" : "+s"(txb), "+s"(tgb));
    auto ev_put = [&]() {
        asm volatile("s_mov_b32 exec_hi, 0\n\tbuffer_store_dword %0, %1, %2, %3 offen sc1\n\ts_mov_b32 exec_hi, -1" :: "v"(flv), "v"(lane * 4), "s"(pr), "s"(area + pfl + sb * LSTR) : "memory");
    };
    auto ev_get = [&]() {
        unsigned l = lane;
        asm volatile("" : "+v"(l));
        asm volatile("" : "+s"(area));
        fo = area + pfl + 8 * wave * LSTR + (cn & 7) * 16 + (((l & 56) << 2) + (l & 7)) * LSTR;
        asm volatile("s_mov_b32 m0, %2\n\ts_nop 0\n\tglobal_load_lds_dwordx4 %0, %1 sc1" :: "v"(fo), "s"(a.part), "s"(lds0 + LDS_ML + wave * 1024) : "memory");
    };
    auto ev_w1 = [&]() {
        u32x4 y = *reinterpret_cast<const u32x4*>(smem + LDS_ML + wave * 1024 + lane * 16);
        const unsigned tq = (cn & 1) ? t32 : (unsigned)tag;
#define BADW(y) (__builtin_amdgcn_perm(__builtin_amdgcn_perm((y)[3], (y)[2], 0x0400), __builtin_amdgcn_perm((y)[1], (y)[0], 0x0400), 0x05040100) != tq)
        if (__builtin_expect(__any(BADW(y)), 0)) {
#pragma unroll 1
            for (int n = 0; n < (1 << 20) && BADW(y); ++n)
                asm volatile("global_load_dwordx4 %0, %1, %2 sc1\n\ts_waitcnt vmcnt(0)" : "=v"(y) : "v"(fo), "s"(a.part) : "memory");
        }
        const unsigned se = (cn & 8) ? 0x0706050cu : 0x0302010cu;
        const float m = __uint_as_float((cn & 16) ? __builtin_amdgcn_perm(y[3], y[2], se) : __builtin_amdgcn_perm(y[1], y[0], se));
        float u;
        asm volatile("s_nop 1\n\tv_max_f32_dpp %0, %2, %2 row_ror:8 row_mask:0xf bank_mask:0xf\n\ts_nop 1\n\tv_max_f32_dpp %0, %0, %0 row_ror:4 row_mask:0xf bank_mask:0xf\n\ts_nop 1\n\t"
                     "v_max_f32_dpp %0, %0, %0 row_ror:2 row_mask:0xf bank_mask:0xf\n\ts_nop 1\n\tv_max_f32_dpp %0, %0, %0 row_ror:1 row_mask:0xf bank_mask:0xf\n\tv_mov_b32 %1, %0\n\ts_nop 1\n\t"
                     "v_permlane16_swap_b32 %0, %1\n\ts_nop 0\n\tv_max_f32 %0, %0, %1\n\tv_mov_b32 %1, %0\n\ts_nop 1\n\tv_permlane32_swap_b32 %0, %1\n\ts_nop 0\n\tv_max_f32 %0, %0, %1" : "=&v"(mw), "=&v"(u) : "v"(m));
        fs[0][0] = __builtin_amdgcn_exp2f(m - mw);
        asm volatile("" : "+v"(fs[0]));
    };
    auto ev_w2 = [&]() {
        const f32x2 f = fs[0];
        unsigned bp = (lane >> 3) * 4;
        asm volatile("ds_bpermute_b32 %0, %8, %9\n\tds_bpermute_b32 %1, %8, %9 offset:32\n\tds_bpermute_b32 %2, %8, %9 offset:64\n\tds_bpermute_b32 %3, %8, %9 offset:96\n\t"
                     "ds_bpermute_b32 %4, %8, %9 offset:128\n\tds_bpermute_b32 %5, %8, %9 offset:160\n\tds_bpermute_b32 %6, %8, %9 offset:192\n\tds_bpermute_b32 %7, %8, %9 offset:224"
                     : "=&v"(fs[0][0]), "=&v"(fs[0][1]), "=&v"(fs[1][0]), "=&v"(fs[1][1]), "=&v"(fs[2][0]), "=&v"(fs[2][1]), "=&v"(fs[3][0]), "=&v"(fs[3][1]) : "v"(bp), "v"(f[0]) : "memory");
        *reinterpret_cast<f32x2*>(smem + LDS_QRAW + 1024 + wave * 8) = f32x2{mw, xrow_sum(row16_allsum(f[0]))};
        asm volatile("s_waitcnt lgkmcnt(0)" : "+v"(fs[0]), "+v"(fs[1]), "+v"(fs[2]), "+v"(fs[3]) :: "memory");
    };
#define HERE unsigned l = lane, w = wave, n = sb; asm volatile("" : "+v"(l)); asm volatile("" : "+s"(w), "+s"(n));
    auto ev_a = [&]() {
        HERE
        const unsigned t = (((l >> 5) * nb + n) * LS + (l & 31) * 16) | ((l & 31) + 8) << 26 & 0x80000000u;
        eo[0] = t + 6 * w * nb * LS; eo[1] = eo[0] + 2 * nb * LS; eo[2] = eo[0] + 4 * nb * LS;
        eo[3] = t + (24 + 2 * w) * nb * LS;
        asm volatile("" : "+v"(eo[0]), "+v"(eo[1]), "+v"(eo[2]), "+v"(eo[3]));
    };
    auto ev_a2 = [&]() {
        HERE
        eo[4] = (((H - 1) * nb + n) * LS + l * 8) | l << 31 | (l + 16) << 25 & 0x80000000u;
        eo[3] = w == 3 ? eo[4] - nb * LS : eo[3];
        ea = lds0 + LDS_REC + (6 * w + (l >> 5)) * (DH * 4) + (l & 31) * 16;
        asm volatile("" : "+v"(eo[3]), "+v"(eo[4]), "+v"(ea));
    };
    auto ev_b = [&]() {
        HERE
        xo[0] = (hc * nb + 8 * w + (l >> 3)) * LS + (n >> 5) * 128 + (l & 7) * 16;
#pragma unroll
        for (int i = 1; i < 8; ++i) xo[i] = xo[0] + 32 * i * LS;
        asm volatile("" : "+v"(xo[0]), "+v"(xo[1]), "+v"(xo[2]), "+v"(xo[3]), "+v"(xo[4]), "+v"(xo[5]), "+v"(xo[6]), "+v"(xo[7]));
    };
    auto ev_c = [&]() {
        HERE
        oo = (hc * DH + (n >> 5) * 64 + 8 * (l & 7) + (l >> 3)) * 2;
        po = lds0 + LDS_QRAW + l * 4; wo = po + w * 256;
        asm volatile("" : "+v"(oo), "+v"(po), "+v"(wo));
    };
    auto ev_c2 = [&]() {
        HERE
        const unsigned t = tag >> 6 * (l & 7);
        ta = (t & 63) * 0x2001 & c7; tb = (t >> 6 & 63) * 0x2001 & c7; tc = (t >> 12 & 63) * 0x2001 & c7;
        asm volatile("" : "+v"(ta), "+v"(tb), "+v"(tc));
    };
    auto ev_d = [&]() {
        HERE
        ev_all(24);
        asm volatile("ds_read_b128 %0, %5\n\tds_read_b128 %1, %5 offset:1536\n\tds_read_b128 %2, %5 offset:3072\n\tds_read_b128 %3, %6\n\tds_read_b128 %4, %6 offset:16\n\ts_waitcnt lgkmcnt(0)"
                     : "+v"(ev[0]), "+v"(ev[1]), "+v"(ev[2]), "+v"(a0), "+v"(a1) : "v"(ea), "v"(lds0 + LDS_QRAW + 1024) : "memory");
        ea += (24 - 4 * w) * (DH * 4);
        asm volatile("" : "+v"(ea));
        if (4 * n + w == 1) { asm volatile("s_mov_b64 exec, 1\n\tbuffer_store_dwordx2 %0, off, %1, 0 offset:%2 sc1\n\ts_mov_b64 exec, -1" :: "v"(u32x2{(unsigned)tag, t32}), "s"(pr), "i"(pep) : "memory"); quiet(); }
    };
    auto ev_t = [&]() {
        HERE
        if (w == 1 && (n & (HOLD >> 8 & 255)) == 0) {
            asm volatile("" : "+s"(area));
            asm volatile("s_mov_b32 m0, %2\n\ts_mov_b64 exec, 3\n\tglobal_load_lds_dword %0, %1\n\ts_mov_b64 exec, -1" :: "v"(area + (l << 21) + n * LS + 384), "s"(a.part), "s"(lds0 + LDS_SK) : "memory");
            quiet();
        }
    };
    auto ev_e = [&]() {
        const float M = fmaxf(fmaxf(a0[0], a0[2]), fmaxf(a1[0], a1[2]));
        const float gf = __builtin_amdgcn_exp2f(mw - M) * __builtin_amdgcn_rcpf((__builtin_amdgcn_exp2f(a0[0] - M) * a0[1] + __builtin_amdgcn_exp2f(a0[2] - M) * a0[3]) + (__builtin_amdgcn_exp2f(a1[0] - M) * a1[1] + __builtin_amdgcn_exp2f(a1[2] - M) * a1[3]));
#pragma unroll
        for (int i = 0; i < 4; ++i) fs[i] *= gf;
        asm volatile("" : "+v"(fs[0]), "+v"(fs[1]), "+v"(fs[2]), "+v"(fs[3]));
    };
    constexpr int G1 = 5, G2 = 27;
    const int hput = wave == ((b >> 5) & 3) ? G1 + 2 + ((b >> 3) & 3) : -9, hget = cn < NC ? G1 + 7 + ((cn >> 3) & 7) : -9;
    unsigned ev1 = 2 | 1 << G1 | 15 << (G1 + 2) | 9u << (hget & 31) | 1u << (H - 1), ev2 = 2 | 3 << G1 | 4u << (hget & 31);
    asm volatile("" : "+s"(ev1), "+s"(ev2), "+s"(area));
    float pv = *reinterpret_cast<const float*>(prec);
    f32x2 acc[3][2];
    float y, yn, zn;
    auto prep = [&](int hn) {
        const float s = bflo(sc);
        yn = s * (pv * __uint_as_float(__builtin_amdgcn_readlane(__float_as_uint(gw), hn)));
        float z = yn * dpp<0x55>(s);
        z += dpp<0x124>(z);
        z += dpp<0x128>(z);
        zn = -dpp<0x150>(z);
        asm volatile("" : "+v"(yn), "+v"(zn) :: "memory");
        SB();
        scp += 128;
        sc = *reinterpret_cast<const unsigned short*>(scp);
        prec += 128;
        pv = *reinterpret_cast<const float*>(prec);
        SB();
    };
    prep(0);
#define VCOMP(k) VPART(k, 0, 3)
#define VPART(k, x0, x1) { \
        if ((k) == 0) y = yn; \
        const f32x2 pz = f32x2{dpp<0x150 + 4 * (k)>(y), zn}; \
        _Pragma("unroll") for (int x = x0; x < x1; ++x) \
            _Pragma("unroll") for (int e = 0; e < 2; ++e) { \
                const f32x2 vf = f32x2{(float)buf[k].d[x][2 * e], (float)buf[k].d[x][2 * e + 1]}; \
                if ((k) == 0) asm volatile("v_pk_fma_f32 %0, %1, %2, %1 op_sel:[0,0,1] op_sel_hi:[0,1,1]" : "=v"(acc[x][e]) : "v"(pz), "v"(vf)); \
                else asm volatile("v_pk_fma_f32 %0, %1, %2, %0 op_sel_hi:[0,1,1]" : "+v"(acc[x][e]) : "v"(pz), "v"(vf)); \
            } \
        asm volatile("" ::: "memory"); \
        SB(); }
#define ON(hn, x, f) { int q = (hn) - (x); asm volatile("" : "+s"(q)); if (__builtin_expect(q == 0, 0)) f(); }
#define VSTEP(k, hn) { \
        __builtin_amdgcn_s_waitcnt(0xf79); SB();        \
        VCOMP(k) pace(); issue(buf[k], k, vr, nso); SB(); steer(true, k); SB(); \
        if ((k) == 1) { \
            vth = TH; asm volatile("" : "+s"(vth)); \
            if (__builtin_expect(ev1 >> (hn) & 1, 0)) { \
                if ((hn) == 1) { softmax1(); softmax2(); }        \
                if ((hn) == G1) ev_all(0);        \
                if ((hn) == hput) { ev_put(); quiet(); } \
                if ((hn) == hget) { ev_get(); quiet(); } \
                if ((hn) == hget + 3) ev_w2(); \
                if ((hn) == H - 1) tmin = 0; \
            } \
            SB(); \
            if (__builtin_expect((hn) >= G2 - 4, 0)) { ON(hn, G2 - 4, ev_a) ON(hn, G2 - 3, ev_a2) ON(hn, G2 - 2, ev_b) ON(hn, G2 - 1, ev_c) ON(hn, G2, ev_d) ON(hn, G2 + 1, ev_e) ON(hn, G2 + 2, ev_c2) if (HOLD >> 16) ON(hn, HOLD >> 16, ev_t) \
                if ((hn) == (PX >> 16)) far = ((PX >> 8) & 255) * 16;        \
                if ((SKL >> 8) && (hn) >= (SKL >> 8)) vth = 15; \
                asm volatile("" : "+s"(far), "+s"(vth)); } \
            SB(); \
        } \
        if ((k) == 2) { \
            if (__builtin_expect(ev2 >> (hn) & 1, 0)) { \
                ON(hn, 1, ev_s) \
                if ((hn) == G1) ev_ml(); \
                if ((hn) == G1 + 1) ev_area(); \
                if ((hn) == hget + 2) ev_w1(); \
            } \
            SB(); \
            prep(hn); \
        } }
    unsigned ra = lds0 + LDS_REC + wave * RECW + (4 * c + r) * 4, ca = lds0 + LDS_CNT, one = 1;
    asm volatile("" : "+v"(ra), "+v"(ca), "+v"(one));
    auto record = [&]() {
        float w[3];
        f32x2 u[3];
#pragma unroll
        for (int x = 0; x < 3; ++x) { swap32(acc[x][0], acc[x][1]); SB(); }
#pragma unroll
        for (int x = 0; x < 3; ++x) { u[x] = acc[x][0] + acc[x][1]; SB(); }
#pragma unroll
        for (int x = 0; x < 3; ++x) {
            auto t = __builtin_amdgcn_permlane16_swap(__float_as_uint(u[x][0]), __float_as_uint(u[x][1]), false, false);
            u[x] = f32x2{__uint_as_float(t[0]), __uint_as_float(t[1])};
            SB();
        }
#pragma unroll
        for (int x = 0; x < 3; ++x) w[x] = u[x][0] + u[x][1];
        asm volatile("ds_write2st64_b32 %1, %2, %3 offset1:1\n\tds_write_b32 %1, %4 offset:512\n\ts_mov_b64 exec, 1\n\tds_add_rtn_u32 %0, %5, %6\n\ts_mov_b64 exec, -1"
                     : "+v"(old) : "v"(ra), "v"(w[0]), "v"(w[1]), "v"(w[2]), "v"(ca), "v"(one) : "memory");
        SB();
    };
#pragma unroll 1
    for (int h = 0; h < H - 1; ++h) {
        nso += hstride;
        VSTEP(0, h + 1)
        asm volatile("s_waitcnt lgkmcnt(0)" : "+v"(old) :: "memory");
        if (__builtin_amdgcn_readfirstlane(old) == 3) *reinterpret_cast<u32x2*>(smem + LDS_REC + (h - 1) * (DH * 4) + lane * 8) = __builtin_expect(h > G1, 1) ? sum4((h - 1) * (DH * 4)) : merge(h - 1);
        SB();
        VSTEP(1, h + 1) VSTEP(2, h + 1) VSTEP(3, h + 1)
        record();
        ra += DH * 4; ca += 4;
        asm volatile("" : "+v"(ra), "+v"(ca));
    }
    if (DBG == 4 && (F >> 13) == 5) { tbar = now32(); asm volatile("" : "+s"(tbar)); }
    asm volatile("s_branch .Lkv_t%c0\n\t.p2align 12,,3327\n.Lkv_t%c0:" :: "i"(ID));
    if (DBG == 4 && (F >> 13) == 4) { tbar = now32(); asm volatile("" : "+s"(tbar)); }
    unsigned tx = txb - sched, tgt = tgb - sched;
    asm volatile("ds_write_b32 %0, %1 offset:36" :: "v"(ca + 4 * wave), "v"(tx) : "memory");
    VCOMP(0)
    asm volatile("s_waitcnt lgkmcnt(0)\n\ts_barrier" ::: "memory");
    if (DBG == 4 && (F >> 13) == 3) { tbar = now32(); asm volatile("" : "+s"(tbar)); }
    f32x4 p[4];
    i32x4 tw;
    asm volatile(LDREC(30) "\n\tds_read_b128 %6, %8\n\tds_read_b128 %7, %9 offset:36" : "=&v"(p[0]), "=&v"(p[1]), "=&v"(p[2]), "=&v"(p[3]), "+v"(rl), "+v"(rh), "=&v"(ev[3]), "=&v"(tw) : "v"(ea), "v"(ca) : "memory");
    SB();
    VCOMP(1)
    asm volatile("s_waitcnt lgkmcnt(0)" : "+v"(p[0]), "+v"(p[1]), "+v"(p[2]), "+v"(p[3]), "+v"(ev[3]), "+v"(tw) :: "memory");
    if (!(F & 4)) {
        tw -= (int)tx;
        const int d = __builtin_amdgcn_readfirstlane(min(min(tw[0], tw[1]), min(tw[2], tw[3])));
        tx += d; tgt += d;
    }
    if (wave == 3) ev[3] = wide(add4(p));
    int lx = hc == H - 1 ? ((SL >> 24) & 15) * ((SL >> 16) & 255) : 0;
    asm volatile("" : "+s"(lx));
    asm volatile("" : "+v"(ev[3]));
    asm volatile("" : "+s"(tx), "+s"(tgt), "+s"(area));
    SB();
    VCOMP(2)
    if (DBG == 4 && (F >> 13) == 1) { tbar = now32(); asm volatile("" : "+s"(tbar)); }
    auto until = [&](unsigned t) {
        int d;
        asm volatile("1:\ts_memrealtime vcc\n\ts_waitcnt lgkmcnt(0)\n\ts_lshl4_add_u32 %0, vcc_lo, %1\n\ts_cmp_lt_i32 %0, 0\n\ts_cbranch_scc1 1b" : "=&s"(d) : "s"(t) : "memory", "vcc", "scc");
        return d;
    };
    const rsrc_t orr = mkrsrc(a.out, H * DH * 2);
    asm volatile("" :: "s"(orr), "s"(pr));
    until(tx);
    constexpr int XP = (F & 4096) ? ((F & 4) ? 3 : 2) : 0;
    VPART(3, 0, XP)
    if ((F & 256) && XP < 3) asm volatile("" : "+v"(buf[3].d[2]));
    SB();
#pragma unroll
    for (int i = 0; i < 4; ++i) { __builtin_amdgcn_raw_buffer_store_b128(ev[i], pr, eo[i], area, 16); SB(); }
    if (DBG == 4 && !(F >> 13)) tbar = now32();
    SB();
    VPART(3, XP, 3)
    if (DBG == 4 && (F >> 13) == 6) { tbar = now32(); asm volatile("" : "+s"(tbar)); }
    record();
    asm volatile(LDREC(31) "\n\ts_waitcnt lgkmcnt(4)" : "=&v"(p[0]), "=&v"(p[1]), "=&v"(p[2]), "=&v"(p[3]), "+v"(rl), "+v"(rh), "+v"(old) :: "memory");
    if (F & 2048) {
        int last = __builtin_amdgcn_readfirstlane(old);
        asm volatile("" : "+s"(last));
        asm volatile("s_waitcnt lgkmcnt(0)" : "+v"(p[0]), "+v"(p[1]), "+v"(p[2]), "+v"(p[3]) :: "memory");
        const u32x4 t = wide(add4(p));
        asm volatile("s_cmp_eq_u32 %4, 3\n\ts_cbranch_scc0 1f\n\tbuffer_store_dwordx4 %0, %1, %2, %3 offen sc1\n1:\ts_nop 1" :: "v"(t), "v"(eo[4]), "s"(pr), "s"(area), "s"(last) : "memory", "scc");
    } else {
    const bool last = __builtin_amdgcn_readfirstlane(old) == 3;
    if (__builtin_expect(last, 1)) {
        asm volatile("s_waitcnt lgkmcnt(0)" : "+v"(p[0]), "+v"(p[1]), "+v"(p[2]), "+v"(p[3]) :: "memory");
        __builtin_amdgcn_raw_buffer_store_b128(wide(add4(p)), pr, eo[4], area, 16);
    }
    asm volatile("s_waitcnt lgkmcnt(0)" :: "v"(p[0]), "v"(p[1]), "v"(p[2]), "v"(p[3]) : "memory");
    }
    unsigned tend = DS ? now32() : 0;
    unsigned tfl = 0, tpl = 0;

    if (cn < NC) {
        i32x4 x[8];
        float o[8];
        int cm = -138, kd = 0x200004, rounds = 0;
        unsigned bad, mk;
        asm volatile("" : "+v"(cm));
        asm volatile("" : "+s"(kd), "+s"(rounds));
        int late = until(tgt) + lx;
        if (DS) tfl = now32();
        if (DBG == 4) th4 = 0;
#define XL(i) x[i] = bld128<16 + (SL >> 28)>(pr, xo[i], area); SB();
#define XS(i) XL(i) asm volatile("s_cmp_lt_i32 %0, %1\n\ts_cbranch_scc0 1f\n\ts_sleep %2\n1:" :: "s"(late), "i"(((SL >> 16) & 255) * ((i) + 1)), "i"((SL >> (2 * (i))) & 3) : "scc"); SB();
#pragma unroll 1
        do {
            XS(0) XS(1) XS(2) XS(3) XS(4) XS(5) XS(6) XL(7)
            if (DBG == 4 && !th4) th4 = now32();
            late = 1 << 30;
            mk = ++rounds < (1 << 20) ? 0x70007 : 0;
            asm volatile("" : "+s"(mk), "+s"(late), "+s"(rounds));
            SB();
#pragma unroll
            for (int i = 0; i < 8; ++i) {
                float w;
                int x0, x1, x2, x3;
#define MIX(a, b, x, z) "\n\tv_fma_mix_f32 %" #a ", %9, %" #x ", " z(a) " op_sel_hi:[0,1,0]\n\tv_fma_mix_f32 %" #b ", %9, %" #x ", " z(b) " op_sel:[0,1,0] op_sel_hi:[0,1,0]"
#define ROW(z) "v_and_b32 %9, 0x70007, %23\n\tv_xor_b32 %13, %23, %9\n\tv_dot2_i32_i16 %9, %9, %18, %19\n\tv_xor_b32 %10, %20, %15\n\tv_xor_b32 %11, %21, %16\n\tv_xor_b32 %12, %22, %17\n\t" \
               "v_ldexp_f32 %9, %14, %9\n\tv_or3_b32 %8, " z(8) ", %10, %11" MIX(0, 1, 10, z) MIX(2, 3, 11, z) "\n\tv_or_b32 %8, %8, %12" MIX(4, 5, 12, z) MIX(6, 7, 13, z)
#define ZERO(a) "0"
#define SAME(a) "%" #a
                if (i == 0) asm volatile(ROW(ZERO) : "=&v"(o[0]), "=&v"(o[1]), "=&v"(o[2]), "=&v"(o[3]), "=&v"(o[4]), "=&v"(o[5]), "=&v"(o[6]), "=&v"(o[7]), "=&v"(bad), "=&v"(w),
                                           "=&v"(x0), "=&v"(x1), "=&v"(x2), "=&v"(x3) : "v"(fs[i >> 1][i & 1]), "v"(ta), "v"(tb), "v"(tc), "s"(kd), "v"(cm),
                                    "v"(x[i][0]), "v"(x[i][1]), "v"(x[i][2]), "v"(x[i][3]));
                else asm volatile(ROW(SAME) : "+v"(o[0]), "+v"(o[1]), "+v"(o[2]), "+v"(o[3]), "+v"(o[4]), "+v"(o[5]), "+v"(o[6]), "+v"(o[7]), "+v"(bad), "=&v"(w),
                                    "=&v"(x0), "=&v"(x1), "=&v"(x2), "=&v"(x3) : "v"(fs[i >> 1][i & 1]), "v"(ta), "v"(tb), "v"(tc), "s"(kd), "v"(cm),
                                    "v"(x[i][0]), "v"(x[i][1]), "v"(x[i][2]), "v"(x[i][3]));
                if (DBG == 6 && __any(((x0 | x1 | x2) & 0x70007) != 0) && !th1) th4 |= 1 << i;
                SB();
            }
            if (DBG == 6) th1 = 1;
        } while (__builtin_expect(__builtin_amdgcn_ballot_w64((bad & mk) != 0) != 0, 0));
        if (DS) tpl = now32();
        f32x2 u0 = f32x2{o[0], o[1]}, u1 = f32x2{o[2], o[3]}, u2 = f32x2{o[4], o[5]}, u3 = f32x2{o[6], o[7]};
        swap32(u0, u2); swap32(u1, u3);
        SB();
        u0 += u2; u1 += u3;
        swap16(u0, u1);
        u0 += u1;
        f32x2 p0, p1;
        asm volatile("s_nop 0\n\ts_nop 0\n\tv_add_f32_dpp %3, %3, %3 row_ror:8 row_mask:0xf bank_mask:0x3\n\tv_add_f32_dpp %3, %4, %4 row_ror:8 row_mask:0xf bank_mask:0xc\n\t"
                     "ds_write_b32 %5, %3\n\ts_mov_b64 exec, 1\n\tds_add_rtn_u32 %2, %7, %8 offset:20\n\ts_mov_b64 exec, -1\n\t"
                     "ds_read2st64_b32 %0, %6 offset1:1\n\tds_read2st64_b32 %1, %6 offset0:2 offset1:3\n\ts_waitcnt lgkmcnt(2)"
                     : "=&v"(p0), "=&v"(p1), "+v"(old), "+v"(u0[0]) : "v"(u0[1]), "v"(wo), "v"(po), "v"(ca), "v"(one) : "memory");
        if (F & 2048) {
            int last = __builtin_amdgcn_readfirstlane(old);
            asm volatile("" : "+s"(last));
            asm volatile("s_waitcnt lgkmcnt(0)" : "+v"(p0), "+v"(p1) :: "memory");
            p0 += p1;
            const float t = p0[0] + p0[1];
            asm volatile("s_cmp_eq_u32 %3, 3\n\ts_cbranch_scc0 1f\n\tbuffer_store_short %0, %1, %2, 0 offen\n1:" :: "v"(pk_bf16(t, t)), "v"(oo), "s"(orr), "s"(last) : "memory", "scc");
        } else
        if (__builtin_expect(__builtin_amdgcn_readfirstlane(old) == 3, 1)) {
            asm volatile("s_waitcnt lgkmcnt(0)" : "+v"(p0), "+v"(p1) :: "memory");
            p0 += p1;
            const float t = p0[0] + p0[1];
            __builtin_amdgcn_raw_buffer_store_b16((unsigned short)pk_bf16(t, t), orr, oo, 0, 0);
        }
        asm volatile(".Lkv_b%c0:" :: "i"(ID));
    }
    if (DBG) {
        const unsigned tc1 = now32();
        const unsigned fm = th4;
        if (!DS) tend = tbar = th4 = tfl = tpl = tc1;
        if (DBG == 6) tfl = tpl = fm;
        if (DBG == 5) tbar = tdma;
        const unsigned tl16 = sched - nb16 - stag - step;
        u64* stp = reinterpret_cast<u64*>(a.part + STAMP_OFF) + (size_t)((DBG == 4 || DBG == 24 || DBG == 6 ? cn : b) * 4 + wave) * 8;

        if (lane == 0) { stp[0] = tstart; stp[1] = tbar; stp[2] = DBG >= 4 ? tend - (((tend << 4) - tl16) >> 4) : th1; stp[3] = DBG == 4 && cn >= NC ? tend : th4; stp[4] = tend; stp[5] = cn < NC || DBG == 6 ? tfl : tend; stp[6] = cn < NC || DBG == 6 ? tpl : tend; stp[7] = tc1; }
        if (DBG == 2) *reinterpret_cast<u32x4*>(a.part + STAMP_OFF + 65536 + (b * 4 + wave) * 1024 + lane * 16) = *reinterpret_cast<const u32x4*>(smem + LDS_TR + 16 + wave * 1024 + lane * 16);
    }
}

template <int TP, int ADV, int PX, int TMIN, int CAP, int HD, int RA, int SKL, int FC, int F, int EX, int GS, int HOLD, int HL, int SL, int ID, int DBG>
static void launch91(void* const* d_in, const int* in_sizes, void* d_out, void* d_ws, hipStream_t stream) {
    typedef const unsigned short* P;
    Args a;
    a.qk = (const int*)d_in[1]; a.qv = (const int*)d_in[4];
    a.k_cos = (P)d_in[9]; a.k_sin = (P)d_in[10];
    a.q = (P)d_in[0]; a.q_cos = (P)d_in[7]; a.q_sin = (P)d_in[8];
    a.sc[0] = (P)d_in[2]; a.sc[1] = (P)d_in[3]; a.sc[2] = (P)d_in[5]; a.sc[3] = (P)d_in[6];
    a.part = (unsigned char*)d_ws;
    a.out = (unsigned short*)d_out;
    kv91_kernel<TP, ADV, PX, TMIN, CAP, HD, RA, SKL, FC, F, EX, GS, HOLD, HL, SL, ID, DBG><<<Skv / RW, 256, 0, stream>>>(a);
}

extern "C" void kernel_launch(void* const* d_in, const int* in_sizes, int n_in, void* d_out, int out_size, void* d_ws, size_t ws_size, hipStream_t stream) {
    launch91<79*16+8,220,40,78*16+2,28,70+256*110,240,24,32+256*50+65536*3+(15<<20)+(2<<24),257,56,2,78,86,0x2999+(70<<16)+(1<<24),0,0>(d_in, in_sizes, d_out, d_ws, stream);
}
```
